```python
import torch

def _softmax(x, axis=-1):
    return torch.softmax(x, dim=axis)
def _gelu(x):
    return torch.nn.functional.gelu(x, approximate="tanh")
def _silu(x):
    return torch.nn.functional.silu(x)
def _sigmoid(x):
    return torch.sigmoid(x)
def _layernorm(x, axis=-1, eps=1e-5):
    m = x.mean(dim=axis, keepdim=True)
    v = x.var(dim=axis, keepdim=True, unbiased=False)
    return (x - m) / torch.sqrt(v + eps)
def _scatter_add(tgt, idx, src, axis=0):
    return tgt.clone().index_add_(axis, idx, src)
def _segment_sum(src, seg, n):
    out = torch.zeros((n,) + src.shape[1:], dtype=src.dtype)
    return out.index_add_(0, seg, src)
def _pad(x, pw):
    flat = tuple(v for pair in reversed(pw) for v in pair)
    return torch.nn.functional.pad(x, flat)

kernel_name = 'concat_heads__mha_block_cached__mha_block_cached__near_ridge'

def setup_inputs():

    _w = (lambda a: (lambda t: t.to(torch.bfloat16) if t.is_floating_point() else t)(torch.as_tensor(a)))
    def _A_setup():
        B, Sq, D = 8, 112, 512
        H, Sk, Dh = 32, 512, 128
        return {
            "x": _w((lambda *s: torch.randn(*s, dtype=torch.bfloat16) * 0.8)(B, Sq, D)),
            "k_cache": _w((lambda *s: torch.randn(*s, dtype=torch.bfloat16) * 0.8)(B, H, Sk, Dh)),
            "v_cache": _w((lambda *s: torch.randn(*s, dtype=torch.bfloat16) * 0.05)(B, H, Sk, Dh)),
            "wq": _w((lambda *s: torch.randn(*s, dtype=torch.bfloat16) * 0.8)(D, H * Dh) * (D ** -0.5)),
            "wo": _w((lambda *s: torch.randn(*s, dtype=torch.bfloat16) * 0.05)(H * Dh, D) * ((H * Dh) ** -0.5)),
        }
    _A_in = _A_setup()

    def _B_setup():
        B, Sq, D = 8, 112, 512
        H, Sk, Dh = 32, 512, 128
        return {
            "x": _w((lambda *s: torch.randn(*s, dtype=torch.bfloat16) * 0.8)(B, Sq, D)),
            "k_cache": _w((lambda *s: torch.randn(*s, dtype=torch.bfloat16) * 0.8)(B, H, Sk, Dh)),
            "v_cache": _w((lambda *s: torch.randn(*s, dtype=torch.bfloat16) * 0.05)(B, H, Sk, Dh)),
            "wq": _w((lambda *s: torch.randn(*s, dtype=torch.bfloat16) * 0.8)(D, H * Dh) * (D ** -0.5)),
            "wo": _w((lambda *s: torch.randn(*s, dtype=torch.bfloat16) * 0.05)(H * Dh, D) * ((H * Dh) ** -0.5)),
        }
    _B_in = _B_setup()

    return {
        "A_x": _A_in["x"],
        "A_k_cache": _A_in["k_cache"],
        "A_v_cache": _A_in["v_cache"],
        "A_wq": _A_in["wq"],
        "A_wo": _A_in["wo"],
        "B_x": _B_in["x"],
        "B_k_cache": _B_in["k_cache"],
        "B_v_cache": _B_in["v_cache"],
        "B_wq": _B_in["wq"],
        "B_wo": _B_in["wo"],
    }

def reference(A_x, A_k_cache, A_v_cache, A_wq, A_wo, B_x, B_k_cache, B_v_cache, B_wq, B_wo):
    def _A_ref(x, k_cache, v_cache, wq, wo):
        B, Sq, D = x.shape
        H, Dh = 32, 128
        q = (lambda x, *d: x.permute(*d))((x @ wq).reshape(B, Sq, H, Dh), 0, 2, 1, 3)
        scores = torch.einsum("bhqd,bhkd->bhqk", q, k_cache) * (Dh ** -0.5)
        p = _softmax(scores, axis=-1)
        o = torch.einsum("bhqk,bhkd->bhqd", p, v_cache)
        return (lambda x, *d: x.permute(*d))(o, 0, 2, 1, 3).reshape(B, Sq, H * Dh) @ wo

    def _B_ref(x, k_cache, v_cache, wq, wo):
        B, Sq, D = x.shape
        H, Dh = 32, 128
        q = (lambda x, *d: x.permute(*d))((x @ wq).reshape(B, Sq, H, Dh), 0, 2, 1, 3)
        scores = torch.einsum("bhqd,bhkd->bhqk", q, k_cache) * (Dh ** -0.5)
        p = _softmax(scores, axis=-1)
        o = torch.einsum("bhqk,bhkd->bhqd", p, v_cache)
        return (lambda x, *d: x.permute(*d))(o, 0, 2, 1, 3).reshape(B, Sq, H * Dh) @ wo

    a = _A_ref(A_x, A_k_cache, A_v_cache, A_wq, A_wo)
    b = _B_ref(B_x, B_k_cache, B_v_cache, B_wq, B_wo)
    return torch.concatenate([a, b], axis=-1)
```

```cpp
#include <hip/hip_runtime.h>
#include <hip/hip_bf16.h>
typedef unsigned short u16;
__device__ __forceinline__ float bf2f(u16 v){ return __uint_as_float(((unsigned)v)<<16); }
__device__ __forceinline__ u16 f2bf(float f){ unsigned u=__float_as_uint(f); u += 0x7fff + ((u>>16)&1); return (u16)(u>>16); }
constexpr int B=8,SQ=112,D=512,H=32,SK=512,DH=128;
__global__ void qproj(const u16* x,const u16* wq,u16* q){
  int n=blockIdx.x*256+threadIdx.x; int m=blockIdx.y;
  float acc=0; for(int k=0;k<D;k++) acc+=bf2f(x[m*D+k])*bf2f(wq[(size_t)k*H*DH+n]);
  q[(size_t)m*H*DH+n]=f2bf(acc);
}
__global__ void attn(const u16* q,const u16* k,const u16* v,u16* o){
  __shared__ float qs[DH]; __shared__ float s[SK]; __shared__ float red[2];
  int row=blockIdx.x, h=blockIdx.y; int b=row/SQ; int t=threadIdx.x;
  qs[t]=bf2f(q[(size_t)row*H*DH+h*DH+t]); __syncthreads();
  const u16* kb=k+(size_t)(b*H+h)*SK*DH; const u16* vb=v+(size_t)(b*H+h)*SK*DH;
  for(int j=t;j<SK;j+=128){ float a=0; for(int d=0;d<DH;d++) a+=qs[d]*bf2f(kb[j*DH+d]); s[j]=a*0.08838834764831845f; }
  __syncthreads();
  if(t==0){ float m=-1e30f; for(int j=0;j<SK;j++) m=fmaxf(m,s[j]); float l=0; for(int j=0;j<SK;j++) l+=__expf(s[j]-m); red[0]=m; red[1]=l; }
  __syncthreads();
  float m=red[0], il=1.f/red[1]; float a=0;
  for(int j=0;j<SK;j++) a+=__expf(s[j]-m)*il*bf2f(vb[j*DH+t]);
  o[(size_t)row*H*DH+h*DH+t]=f2bf(a);
}
__global__ void oproj(const u16* o,const u16* wo,u16* out,int coloff){
  int n=blockIdx.x*256+threadIdx.x; int m=blockIdx.y;
  float acc=0; for(int k=0;k<H*DH;k++) acc+=bf2f(o[(size_t)m*H*DH+k])*bf2f(wo[(size_t)k*D+n]);
  out[(size_t)m*2*D+coloff+n]=f2bf(acc);
}
extern "C" void kernel_launch(void* const* d_in, const int* in_sizes, int n_in, void* d_out, int out_size, void* d_ws, size_t ws_size, hipStream_t stream){
  u16* q=(u16*)d_ws; u16* o=q+(size_t)B*SQ*H*DH;
  for(int br=0;br<2;br++){
    const u16* x=(const u16*)d_in[br*5+0]; const u16* k=(const u16*)d_in[br*5+1]; const u16* v=(const u16*)d_in[br*5+2];
    const u16* wq=(const u16*)d_in[br*5+3]; const u16* wo=(const u16*)d_in[br*5+4];
    qproj<<<dim3(H*DH/256,B*SQ),256,0,stream>>>(x,wq,q);
    attn<<<dim3(B*SQ,H),128,0,stream>>>(q,k,v,o);
    oproj<<<dim3(D/256,B*SQ),256,0,stream>>>(o,wo,(u16*)d_out,br*D);
  }
}
```

```cpp
#include <hip/hip_runtime.h>
#include <hip/hip_bf16.h>

struct FusedArgs { const char* x[2]; const char* k[2]; const char* v[2]; const char* wq[2]; const char* wo[2]; char* ws; char* out; };

__global__ __launch_bounds__(512, 2) void fused_mha_kernel(FusedArgs a) {
  __shared__ __attribute__((aligned(1024))) char lds[163840];
  const unsigned wg = blockIdx.x, xcd = wg & 7u, j = wg >> 3, br = xcd >> 2, oct = xcd & 3u, b = j >> 2, qi = j & 3u, g = br * 8u + b;
  const unsigned wid = __builtin_amdgcn_readfirstlane(threadIdx.x >> 6);
  const char* xp = br ? a.x[1] : a.x[0]; const char* kp = br ? a.k[1] : a.k[0]; const char* vp = br ? a.v[1] : a.v[0];
  const char* wqp = br ? a.wq[1] : a.wq[0]; const char* wop = br ? a.wo[1] : a.wo[0];
  const unsigned cb = qi ^ 2u;
  const unsigned cbo = cb * 256u, octo = (oct & 1u) * 64u + ((oct >> 1) << 7);
  const size_t kvo = (size_t)(b * 32u + oct * 8u) * 131072u;
  auto srdx  = __builtin_amdgcn_make_buffer_rsrc((void*)(xp + (size_t)b * 114688u), 0, 114688, 0x00020000);
  auto srdk  = __builtin_amdgcn_make_buffer_rsrc((void*)(kp + kvo - 1024), 0, 8 * 131072 + 1024, 0x00020000);
  auto srdv  = __builtin_amdgcn_make_buffer_rsrc((void*)(vp + kvo - 1024), 0, 8 * 131072 + 1024, 0x00020000);
  auto srdwq = __builtin_amdgcn_make_buffer_rsrc((void*)(wqp + oct * 2048u - 1024), 0, 4194304 + 1024 - oct * 2048u, 0x00020000);
  auto srdwo = __builtin_amdgcn_make_buffer_rsrc((void*)(wop + oct * 1048576u + cbo - 1024), 0, 1048576 + 1024 - cbo, 0x00020000);
  auto srdws = __builtin_amdgcn_make_buffer_rsrc((void*)a.ws, 0, 33554432, 0x00020000);
  auto srdout = __builtin_amdgcn_make_buffer_rsrc((void*)(a.out + (size_t)b * 229376u + br * 1024u + cbo + octo), 0, 111 * 2048 + 64, 0x00020000);
  const unsigned obase = 0u + (xcd * 8u + b) * 262144u;
  const unsigned xbase = 16777216u + (g * 4u + qi) * 131072u;
  const unsigned fbase = 25165824u + g * 32768u;
  const unsigned f1base = fbase + oct * 4096u, f2base = fbase + 16384u + qi * 2048u;
  const unsigned rot = (b + 2u * qi) & 7u;
  const unsigned rotq = (b + 2u * qi) & 7u;
  const unsigned htab = (qi & 2u ? (qi & 1u ? 0x2461357u : 0x20643175u) : (qi & 1u ? 0x46025713u : 0x64207531u));
  const unsigned vsel = (b ^ qi) & 1u;
  const unsigned sdn = (b ^ qi) & 1u;
  const char* eptr = a.ws + fbase + 0x6000;
  const unsigned ldsb = (unsigned)(size_t)(__attribute__((address_space(3))) char*)lds;
  asm volatile(
    "START_%=:\n\t"
    "s_load_dwordx2 s[44:45], %[eptr], 0x0\n\t"
    "v_mbcnt_lo_u32_b32 v253, -1, 0\n\t"
    "v_mbcnt_hi_u32_b32 v253, -1, v253\n\t"
    "s_and_b32 s50, %[wid], 3\n\t"
    "s_lshr_b32 s51, %[wid], 2\n\t"
    "s_lshl_b32 s47, s50, 12\n\t"
    "s_add_u32 s47, s47, %[lds]\n\t"
    "s_and_b32 s48, %[htab], 7\n\t"
    "s_lshl_b32 s48, s48, 17\n\t"
    "s_bfe_u32 s49, %[htab], 0x30010\n\t"
    "s_lshl_b32 s49, s49, 17\n\t"
    "s_lshl_b32 s57, %[rotq], 7\n\t"
    "s_lshl_b32 s58, %[rotq], 19\n\t"
    "s_lshl_b32 s59, %[rot], 14\n\t"
    "s_cmp_eq_u32 s51, 0\n\t"
    "s_cbranch_scc1 G0_%=\n\t"
    "s_mov_b32 s52, %[sdn]\n\t"
    "SD1_%=:\n\t"
    "s_sub_u32 s52, s52, 1\n\t"
    "s_cbranch_scc1 SD0_%=\n\t"
    "s_sleep 20\n\t"
    "s_branch SD1_%=\n\t"
    "SD0_%=:\n\t"
    "v_bfe_u32 v254, v253, 2, 3\n\t"
    "v_lshlrev_b32 v134, 13, v254\n\t"
    "v_bfe_u32 v254, v253, 5, 1\n\t"
    "v_lshl_add_u32 v134, v254, 6, v134\n\t"
    "v_bfe_u32 v254, v253, 0, 2\n\t"
    "v_lshl_add_u32 v134, v254, 4, v134\n\t"
    "s_and_b32 s52, s50, 1\n\t"
    "s_lshl_b32 s52, s52, 5\n\t"
    "v_xor_b32 v134, s52, v134\n\t"
    "s_lshl_b32 s52, s50, 17\n\t"
    "s_add_u32 s52, s52, 0x400\n\t"
    "v_add_u32 v134, s52, v134\n\t"
    "v_add_u32 v135, 0xfffffc80, v134\n\t"
    "v_add_u32 v136, 0xf800, v134\n\t"
    "v_add_u32 v137, 0xf480, v134\n\t"
    "s_add_u32 m0, s47, 0x0\n\t"
    "s_lshr_b32 s46, s48, 9\n\t"
    "s_add_u32 s46, s46, s58\n\t"
    "s_add_u32 s46, s46, 0x0\n\t"
    "s_and_b32 s46, s46, 0x3fffff\n\t"
    "buffer_load_dwordx4 v134, %[srdwq], s46 offen lds\n\t"
    "buffer_load_dwordx4 v135, %[srdwq], s46 offen offset:1024 lds\n\t"
    "buffer_load_dwordx4 v136, %[srdwq], s46 offen offset:2048 lds\n\t"
    "buffer_load_dwordx4 v137, %[srdwq], s46 offen offset:3072 lds\n\t"
    "v_bfe_u32 v254, v253, 2, 3\n\t"
    "v_lshlrev_b32 v138, 10, v254\n\t"
    "v_bfe_u32 v254, v253, 5, 1\n\t"
    "v_lshl_add_u32 v138, v254, 6, v138\n\t"
    "v_bfe_u32 v254, v253, 0, 2\n\t"
    "v_lshl_add_u32 v138, v254, 4, v138\n\t"
    "s_lshl_b32 s52, s50, 15\n\t"
    "v_add_u32 v138, s52, v138\n\t"
    "v_xor_b32 v139, 16, v138\n\t"
    "v_add_u32 v140, 0x3800, v138\n\t"
    "v_add_u32 v141, 0x5400, v139\n\t"
    "v_add_u32 v139, 0x1c00, v139\n\t"
    "s_add_u32 m0, s47, 0x4000\n\t"
    "s_add_u32 s46, s57, 0x0\n\t"
    "s_and_b32 s46, s46, 0x3ff\n\t"
    "buffer_load_dwordx4 v138, %[srdx], s46 offen lds\n\t"
    "buffer_load_dwordx4 v139, %[srdx], s46 offen offset:1024 lds\n\t"
    "buffer_load_dwordx4 v140, %[srdx], s46 offen offset:2048 lds\n\t"
    "buffer_load_dwordx4 v141, %[srdx], s46 offen offset:3072 lds\n\t"
    "s_add_u32 m0, s47, 0x8000\n\t"
    "s_lshr_b32 s46, s48, 9\n\t"
    "s_add_u32 s46, s46, s58\n\t"
    "s_add_u32 s46, s46, 0x80000\n\t"
    "s_and_b32 s46, s46, 0x3fffff\n\t"
    "buffer_load_dwordx4 v134, %[srdwq], s46 offen lds\n\t"
    "buffer_load_dwordx4 v135, %[srdwq], s46 offen offset:1024 lds\n\t"
    "buffer_load_dwordx4 v136, %[srdwq], s46 offen offset:2048 lds\n\t"
    "buffer_load_dwordx4 v137, %[srdwq], s46 offen offset:3072 lds\n\t"
    "s_add_u32 m0, s47, 0xc000\n\t"
    "s_add_u32 s46, s57, 0x80\n\t"
    "s_and_b32 s46, s46, 0x3ff\n\t"
    "buffer_load_dwordx4 v138, %[srdx], s46 offen lds\n\t"
    "buffer_load_dwordx4 v139, %[srdx], s46 offen offset:1024 lds\n\t"
    "buffer_load_dwordx4 v140, %[srdx], s46 offen offset:2048 lds\n\t"
    "buffer_load_dwordx4 v141, %[srdx], s46 offen offset:3072 lds\n\t"
    "s_add_u32 m0, s47, 0x10000\n\t"
    "s_lshr_b32 s46, s49, 9\n\t"
    "s_add_u32 s46, s46, s58\n\t"
    "s_add_u32 s46, s46, 0x0\n\t"
    "s_and_b32 s46, s46, 0x3fffff\n\t"
    "buffer_load_dwordx4 v134, %[srdwq], s46 offen lds\n\t"
    "buffer_load_dwordx4 v135, %[srdwq], s46 offen offset:1024 lds\n\t"
    "buffer_load_dwordx4 v136, %[srdwq], s46 offen offset:2048 lds\n\t"
    "buffer_load_dwordx4 v137, %[srdwq], s46 offen offset:3072 lds\n\t"
    "v_bfe_u32 v254, v253, 2, 3\n\t"
    "v_lshlrev_b32 v128, 8, v254\n\t"
    "v_bfe_u32 v254, v253, 5, 1\n\t"
    "v_lshl_add_u32 v128, v254, 6, v128\n\t"
    "v_bfe_u32 v254, v253, 0, 2\n\t"
    "v_lshl_add_u32 v128, v254, 4, v128\n\t"
    "s_lshl_b32 s52, s50, 12\n\t"
    "s_add_u32 s52, s52, 0x400\n\t"
    "v_add_u32 v128, s52, v128\n\t"
    "v_add_u32 v129, 0xfffffc80, v128\n\t"
    "v_xor_b32 v130, 16, v128\n\t"
    "v_add_u32 v131, 0xfffffc80, v130\n\t"
    "v_mov_b32 v132, v128\n\t"
    "v_bfe_u32 v254, v253, 4, 1\n\t"
    "v_lshlrev_b32 v254, 5, v254\n\t"
    "v_xor_b32 v132, v132, v254\n\t"
    "v_add_u32 v133, 0xfffffc80, v132\n\t"
    "s_lshl_b32 s42, %[qi], 17\n\t"
    "s_or_b32 s43, s42, 0x80000\n\t"
    "s_cmp_eq_u32 s50, 3\n\t"
    "s_mov_b32 s60, 0xc1c00000\n\t"
    "s_cselect_b32 s60, 0xff800000, s60\n\t"
    "s_cselect_b32 s61, 0x80000000, 0\n\t"
    "v_mov_b32 v248, %[lds]\n\t"
    "v_bfe_u32 v254, v253, 4, 1\n\t"
    "v_lshl_add_u32 v248, v254, 12, v248\n\t"
    "v_bfe_u32 v254, v253, 5, 1\n\t"
    "v_lshl_add_u32 v248, v254, 11, v248\n\t"
    "v_bfe_u32 v254, v253, 2, 2\n\t"
    "v_lshl_add_u32 v248, v254, 6, v248\n\t"
    "v_bfe_u32 v254, v253, 4, 1\n\t"
    "v_lshl_add_u32 v248, v254, 5, v248\n\t"
    "v_bfe_u32 v254, v253, 0, 2\n\t"
    "v_lshl_add_u32 v248, v254, 3, v248\n\t"
    "v_xor_b32 v249, 32, v248\n\t"
    "v_mov_b32 v250, s47\n\t"
    "v_bfe_u32 v254, v253, 3, 1\n\t"
    "v_lshl_add_u32 v250, v254, 10, v250\n\t"
    "v_bfe_u32 v254, v253, 0, 3\n\t"
    "v_lshl_add_u32 v250, v254, 6, v250\n\t"
    "v_bfe_u32 v254, v253, 4, 1\n\t"
    "v_lshl_add_u32 v250, v254, 5, v250\n\t"
    "v_bfe_u32 v254, v253, 5, 1\n\t"
    "v_lshl_add_u32 v250, v254, 4, v250\n\t"
    "v_bfe_u32 v254, v253, 3, 1\n\t"
    "v_lshlrev_b32 v254, 4, v254\n\t"
    "v_xor_b32 v250, v250, v254\n\t"
    "s_waitcnt lgkmcnt(0)\n\t"
    "s_add_u32 s44, s44, 0x7f4a7c15\n\t"
    "s_addc_u32 s45, s45, 0x9e3779b9\n\t"
    "s_setprio 1\n\t"
    "s_cmp_eq_u32 %[vsel], 1\n\t"
    "s_cbranch_scc1 G1V0_%=\n\t"
    "s_waitcnt vmcnt(12)\n\t"
    "s_barrier\n\t"
    "s_add_u32 m0, s47, 0x14000\n\t"
    "s_lshr_b32 s46, s48, 9\n\t"
    "s_add_u32 s46, s46, s58\n\t"
    "s_add_u32 s46, s46, 0x100000\n\t"
    "s_and_b32 s46, s46, 0x3fffff\n\t"
    "buffer_load_dwordx4 v134, %[srdwq], s46 offen lds\n\t"
    "buffer_load_dwordx4 v135, %[srdwq], s46 offen offset:1024 lds\n\t"
    "buffer_load_dwordx4 v136, %[srdwq], s46 offen offset:2048 lds\n\t"
    "buffer_load_dwordx4 v137, %[srdwq], s46 offen offset:3072 lds\n\t"
    "ds_read_b128 v[0:3], v250 offset:16384\n\t"
    "ds_read_b128 v[4:7], v250 offset:18432\n\t"
    "ds_read_b128 v[8:11], v250 offset:16896\n\t"
    "ds_read_b128 v[12:15], v250 offset:18944\n\t"
    "s_add_u32 m0, s47, 0x18000\n\t"
    "s_add_u32 s46, s57, 0x100\n\t"
    "s_and_b32 s46, s46, 0x3ff\n\t"
    "buffer_load_dwordx4 v138, %[srdx], s46 offen lds\n\t"
    "buffer_load_dwordx4 v139, %[srdx], s46 offen offset:1024 lds\n\t"
    "buffer_load_dwordx4 v140, %[srdx], s46 offen offset:2048 lds\n\t"
    "buffer_load_dwordx4 v141, %[srdx], s46 offen offset:3072 lds\n\t"
    "s_waitcnt vmcnt(12)\n\t"
    "s_waitcnt lgkmcnt(0)\n\t"
    "s_barrier\n\t"
    "s_add_u32 m0, s47, 0x1c000\n\t"
    "s_lshr_b32 s46, s48, 9\n\t"
    "s_add_u32 s46, s46, s58\n\t"
    "s_add_u32 s46, s46, 0x180000\n\t"
    "s_and_b32 s46, s46, 0x3fffff\n\t"
    "buffer_load_dwordx4 v134, %[srdwq], s46 offen lds\n\t"
    "buffer_load_dwordx4 v135, %[srdwq], s46 offen offset:1024 lds\n\t"
    "buffer_load_dwordx4 v136, %[srdwq], s46 offen offset:2048 lds\n\t"
    "buffer_load_dwordx4 v137, %[srdwq], s46 offen offset:3072 lds\n\t"
    "ds_read_b128 v[16:19], v250 offset:49152\n\t"
    "ds_read_b128 v[20:23], v250 offset:51200\n\t"
    "ds_read_b128 v[24:27], v250 offset:49664\n\t"
    "ds_read_b128 v[28:31], v250 offset:51712\n\t"
    "s_add_u32 m0, s47, 0x20000\n\t"
    "s_add_u32 s46, s57, 0x180\n\t"
    "s_and_b32 s46, s46, 0x3ff\n\t"
    "buffer_load_dwordx4 v138, %[srdx], s46 offen lds\n\t"
    "buffer_load_dwordx4 v139, %[srdx], s46 offen offset:1024 lds\n\t"
    "buffer_load_dwordx4 v140, %[srdx], s46 offen offset:2048 lds\n\t"
    "buffer_load_dwordx4 v141, %[srdx], s46 offen offset:3072 lds\n\t"
    "s_add_u32 m0, s47, 0x24000\n\t"
    "s_add_u32 s46, s59, 0x0\n\t"
    "s_and_b32 s46, s46, 0x1ffff\n\t"
    "s_or_b32 s46, s46, s48\n\t"
    "s_waitcnt vmcnt(12)\n\t"
    "buffer_load_dwordx4 v128, %[srdk], s46 offen nt lds\n\t"
    "s_waitcnt vmcnt(12)\n\t"
    "buffer_load_dwordx4 v129, %[srdk], s46 offen offset:1024 nt lds\n\t"
    "s_waitcnt vmcnt(12)\n\t"
    "buffer_load_dwordx4 v130, %[srdk], s46 offen offset:2048 nt lds\n\t"
    "s_waitcnt vmcnt(12)\n\t"
    "buffer_load_dwordx4 v131, %[srdk], s46 offen offset:3072 nt lds\n\t"
    "s_add_u32 m0, s47, 0x0\n\t"
    "s_add_u32 s46, s59, 0x4000\n\t"
    "s_and_b32 s46, s46, 0x1ffff\n\t"
    "s_or_b32 s46, s46, s48\n\t"
    "s_waitcnt vmcnt(12)\n\t"
    "buffer_load_dwordx4 v128, %[srdk], s46 offen nt lds\n\t"
    "s_waitcnt vmcnt(12)\n\t"
    "buffer_load_dwordx4 v129, %[srdk], s46 offen offset:1024 nt lds\n\t"
    "s_waitcnt vmcnt(12)\n\t"
    "buffer_load_dwordx4 v130, %[srdk], s46 offen offset:2048 nt lds\n\t"
    "s_waitcnt vmcnt(12)\n\t"
    "buffer_load_dwordx4 v131, %[srdk], s46 offen offset:3072 nt lds\n\t"
    "s_add_u32 m0, s47, 0x4000\n\t"
    "s_add_u32 s46, s59, 0x8000\n\t"
    "s_and_b32 s46, s46, 0x1ffff\n\t"
    "s_or_b32 s46, s46, s48\n\t"
    "s_waitcnt vmcnt(12)\n\t"
    "buffer_load_dwordx4 v128, %[srdk], s46 offen nt lds\n\t"
    "s_waitcnt vmcnt(12)\n\t"
    "buffer_load_dwordx4 v129, %[srdk], s46 offen offset:1024 nt lds\n\t"
    "s_waitcnt vmcnt(12)\n\t"
    "buffer_load_dwordx4 v130, %[srdk], s46 offen offset:2048 nt lds\n\t"
    "s_waitcnt vmcnt(12)\n\t"
    "buffer_load_dwordx4 v131, %[srdk], s46 offen offset:3072 nt lds\n\t"
    "s_waitcnt lgkmcnt(0)\n\t"
    "s_barrier\n\t"
    "s_add_u32 m0, s47, 0x8000\n\t"
    "s_lshr_b32 s46, s48, 9\n\t"
    "s_add_u32 s46, s46, s58\n\t"
    "s_add_u32 s46, s46, 0x200000\n\t"
    "s_and_b32 s46, s46, 0x3fffff\n\t"
    "buffer_load_dwordx4 v134, %[srdwq], s46 offen lds\n\t"
    "buffer_load_dwordx4 v135, %[srdwq], s46 offen offset:1024 lds\n\t"
    "buffer_load_dwordx4 v136, %[srdwq], s46 offen offset:2048 lds\n\t"
    "buffer_load_dwordx4 v137, %[srdwq], s46 offen offset:3072 lds\n\t"
    "v_add_u32 v250, 0x10000, v250\n\t"
    "ds_read_b128 v[32:35], v250 offset:32768\n\t"
    "ds_read_b128 v[36:39], v250 offset:34816\n\t"
    "ds_read_b128 v[40:43], v250 offset:33280\n\t"
    "ds_read_b128 v[44:47], v250 offset:35328\n\t"
    "s_add_u32 m0, s47, 0xc000\n\t"
    "s_add_u32 s46, s57, 0x200\n\t"
    "s_and_b32 s46, s46, 0x3ff\n\t"
    "buffer_load_dwordx4 v138, %[srdx], s46 offen lds\n\t"
    "buffer_load_dwordx4 v139, %[srdx], s46 offen offset:1024 lds\n\t"
    "buffer_load_dwordx4 v140, %[srdx], s46 offen offset:2048 lds\n\t"
    "buffer_load_dwordx4 v141, %[srdx], s46 offen offset:3072 lds\n\t"
    "s_waitcnt vmcnt(20)\n\t"
    "s_waitcnt lgkmcnt(0)\n\t"
    "s_barrier\n\t"
    "s_add_u32 m0, s47, 0x14000\n\t"
    "s_lshr_b32 s46, s48, 9\n\t"
    "s_add_u32 s46, s46, s58\n\t"
    "s_add_u32 s46, s46, 0x280000\n\t"
    "s_and_b32 s46, s46, 0x3fffff\n\t"
    "buffer_load_dwordx4 v134, %[srdwq], s46 offen lds\n\t"
    "buffer_load_dwordx4 v135, %[srdwq], s46 offen offset:1024 lds\n\t"
    "buffer_load_dwordx4 v136, %[srdwq], s46 offen offset:2048 lds\n\t"
    "buffer_load_dwordx4 v137, %[srdwq], s46 offen offset:3072 lds\n\t"
    "v_add_u32 v250, 0x10000, v250\n\t"
    "ds_read_b128 v[48:51], v250 offset:0\n\t"
    "ds_read_b128 v[52:55], v250 offset:2048\n\t"
    "ds_read_b128 v[56:59], v250 offset:512\n\t"
    "ds_read_b128 v[60:63], v250 offset:2560\n\t"
    "s_add_u32 m0, s47, 0x18000\n\t"
    "s_add_u32 s46, s57, 0x280\n\t"
    "s_and_b32 s46, s46, 0x3ff\n\t"
    "buffer_load_dwordx4 v138, %[srdx], s46 offen lds\n\t"
    "buffer_load_dwordx4 v139, %[srdx], s46 offen offset:1024 lds\n\t"
    "buffer_load_dwordx4 v140, %[srdx], s46 offen offset:2048 lds\n\t"
    "buffer_load_dwordx4 v141, %[srdx], s46 offen offset:3072 lds\n\t"
    "s_waitcnt vmcnt(8)\n\t"
    "s_waitcnt lgkmcnt(0)\n\t"
    "s_barrier\n\t"
    "s_add_u32 m0, s47, 0x1c000\n\t"
    "s_lshr_b32 s46, s48, 9\n\t"
    "s_add_u32 s46, s46, s58\n\t"
    "s_add_u32 s46, s46, 0x300000\n\t"
    "s_and_b32 s46, s46, 0x3fffff\n\t"
    "buffer_load_dwordx4 v134, %[srdwq], s46 offen lds\n\t"
    "buffer_load_dwordx4 v135, %[srdwq], s46 offen offset:1024 lds\n\t"
    "buffer_load_dwordx4 v136, %[srdwq], s46 offen offset:2048 lds\n\t"
    "buffer_load_dwordx4 v137, %[srdwq], s46 offen offset:3072 lds\n\t"
    "v_add_u32 v250, 0xfffe0000, v250\n\t"
    "ds_read_b128 v[64:67], v250 offset:49152\n\t"
    "ds_read_b128 v[68:71], v250 offset:51200\n\t"
    "ds_read_b128 v[72:75], v250 offset:49664\n\t"
    "ds_read_b128 v[76:79], v250 offset:51712\n\t"
    "s_add_u32 m0, s47, 0x20000\n\t"
    "s_add_u32 s46, s57, 0x300\n\t"
    "s_and_b32 s46, s46, 0x3ff\n\t"
    "buffer_load_dwordx4 v138, %[srdx], s46 offen lds\n\t"
    "buffer_load_dwordx4 v139, %[srdx], s46 offen offset:1024 lds\n\t"
    "buffer_load_dwordx4 v140, %[srdx], s46 offen offset:2048 lds\n\t"
    "buffer_load_dwordx4 v141, %[srdx], s46 offen offset:3072 lds\n\t"
    "s_waitcnt vmcnt(8)\n\t"
    "s_waitcnt lgkmcnt(0)\n\t"
    "s_barrier\n\t"
    "s_add_u32 m0, s47, 0x8000\n\t"
    "s_lshr_b32 s46, s48, 9\n\t"
    "s_add_u32 s46, s46, s58\n\t"
    "s_add_u32 s46, s46, 0x380000\n\t"
    "s_and_b32 s46, s46, 0x3fffff\n\t"
    "buffer_load_dwordx4 v134, %[srdwq], s46 offen lds\n\t"
    "buffer_load_dwordx4 v135, %[srdwq], s46 offen offset:1024 lds\n\t"
    "buffer_load_dwordx4 v136, %[srdwq], s46 offen offset:2048 lds\n\t"
    "buffer_load_dwordx4 v137, %[srdwq], s46 offen offset:3072 lds\n\t"
    "v_add_u32 v250, 0x10000, v250\n\t"
    "ds_read_b128 v[80:83], v250 offset:32768\n\t"
    "ds_read_b128 v[84:87], v250 offset:34816\n\t"
    "ds_read_b128 v[88:91], v250 offset:33280\n\t"
    "ds_read_b128 v[92:95], v250 offset:35328\n\t"
    "s_add_u32 m0, s47, 0xc000\n\t"
    "s_add_u32 s46, s57, 0x380\n\t"
    "s_and_b32 s46, s46, 0x3ff\n\t"
    "buffer_load_dwordx4 v138, %[srdx], s46 offen lds\n\t"
    "buffer_load_dwordx4 v139, %[srdx], s46 offen offset:1024 lds\n\t"
    "buffer_load_dwordx4 v140, %[srdx], s46 offen offset:2048 lds\n\t"
    "buffer_load_dwordx4 v141, %[srdx], s46 offen offset:3072 lds\n\t"
    "s_waitcnt vmcnt(8)\n\t"
    "s_waitcnt lgkmcnt(0)\n\t"
    "s_barrier\n\t"
    "s_add_u32 m0, s47, 0x14000\n\t"
    "s_add_u32 s46, s59, 0xc000\n\t"
    "s_and_b32 s46, s46, 0x1ffff\n\t"
    "s_or_b32 s46, s46, s48\n\t"
    "buffer_load_dwordx4 v128, %[srdk], s46 offen nt lds\n\t"
    "buffer_load_dwordx4 v129, %[srdk], s46 offen offset:1024 nt lds\n\t"
    "buffer_load_dwordx4 v130, %[srdk], s46 offen offset:2048 nt lds\n\t"
    "buffer_load_dwordx4 v131, %[srdk], s46 offen offset:3072 nt lds\n\t"
    "v_add_u32 v250, 0x10000, v250\n\t"
    "ds_read_b128 v[96:99], v250 offset:0\n\t"
    "ds_read_b128 v[100:103], v250 offset:2048\n\t"
    "ds_read_b128 v[104:107], v250 offset:512\n\t"
    "ds_read_b128 v[108:111], v250 offset:2560\n\t"
    "s_add_u32 m0, s47, 0x18000\n\t"
    "s_add_u32 s46, s59, 0x10000\n\t"
    "s_and_b32 s46, s46, 0x1ffff\n\t"
    "s_or_b32 s46, s46, s48\n\t"
    "buffer_load_dwordx4 v128, %[srdk], s46 offen nt lds\n\t"
    "s_waitcnt vmcnt(12)\n\t"
    "buffer_load_dwordx4 v129, %[srdk], s46 offen offset:1024 nt lds\n\t"
    "s_waitcnt vmcnt(12)\n\t"
    "buffer_load_dwordx4 v130, %[srdk], s46 offen offset:2048 nt lds\n\t"
    "s_waitcnt vmcnt(12)\n\t"
    "buffer_load_dwordx4 v131, %[srdk], s46 offen offset:3072 nt lds\n\t"
    "G1V_%=:\n\t"
    "s_cmp_eq_u32 s50, 3\n\t"
    "s_cbranch_scc1 W3S1_%=\n\t"
    "s_waitcnt vmcnt(8)\n\t"
    "s_waitcnt lgkmcnt(0)\n\t"
    "s_barrier\n\t"
    "s_add_u32 m0, s47, 0x1c000\n\t"
    "s_add_u32 s46, s59, 0x14000\n\t"
    "s_and_b32 s46, s46, 0x1ffff\n\t"
    "s_or_b32 s46, s46, s48\n\t"
    "buffer_load_dwordx4 v128, %[srdk], s46 offen nt lds\n\t"
    "buffer_load_dwordx4 v129, %[srdk], s46 offen offset:1024 nt lds\n\t"
    "buffer_load_dwordx4 v130, %[srdk], s46 offen offset:2048 nt lds\n\t"
    "buffer_load_dwordx4 v131, %[srdk], s46 offen offset:3072 nt lds\n\t"
    "v_add_u32 v250, 0xfffe0000, v250\n\t"
    "ds_read_b128 v[112:115], v250 offset:49152\n\t"
    "ds_read_b128 v[116:119], v250 offset:51200\n\t"
    "ds_read_b128 v[120:123], v250 offset:49664\n\t"
    "ds_read_b128 v[124:127], v250 offset:51712\n\t"
    "v_add_u32 v248, 0x10000, v248\n\t"
    "v_add_u32 v249, 0x10000, v249\n\t"
    "ds_read_b64_tr_b16 v[216:217], v248 offset:0\n\t"
    "ds_read_b64_tr_b16 v[218:219], v248 offset:256\n\t"
    "ds_read_b64_tr_b16 v[220:221], v248 offset:8192\n\t"
    "ds_read_b64_tr_b16 v[222:223], v248 offset:8448\n\t"
    "ds_read_b64_tr_b16 v[224:225], v249 offset:0\n\t"
    "ds_read_b64_tr_b16 v[226:227], v249 offset:256\n\t"
    "ds_read_b64_tr_b16 v[228:229], v249 offset:8192\n\t"
    "ds_read_b64_tr_b16 v[230:231], v249 offset:8448\n\t"
    "ds_read_b64_tr_b16 v[232:233], v248 offset:512\n\t"
    "ds_read_b64_tr_b16 v[234:235], v248 offset:768\n\t"
    "ds_read_b64_tr_b16 v[236:237], v248 offset:8704\n\t"
    "ds_read_b64_tr_b16 v[238:239], v248 offset:8960\n\t"
    "ds_read_b64_tr_b16 v[240:241], v249 offset:512\n\t"
    "ds_read_b64_tr_b16 v[242:243], v249 offset:768\n\t"
    "ds_read_b64_tr_b16 v[244:245], v249 offset:8704\n\t"
    "ds_read_b64_tr_b16 v[246:247], v249 offset:8960\n\t"
    "s_waitcnt lgkmcnt(12)\n\t"
    "v_mfma_f32_16x16x32_bf16 v[152:155], v[216:219], v[0:3], 0\n\t"
    "v_mfma_f32_16x16x32_bf16 v[152:155], v[220:223], v[8:11], v[152:155]\n\t"
    "v_mfma_f32_16x16x32_bf16 v[160:163], v[220:223], v[12:15], 0\n\t"
    "v_mfma_f32_16x16x32_bf16 v[160:163], v[216:219], v[4:7], v[160:163]\n\t"
    "s_waitcnt lgkmcnt(8)\n\t"
    "v_mfma_f32_16x16x32_bf16 v[156:159], v[224:227], v[0:3], 0\n\t"
    "v_mfma_f32_16x16x32_bf16 v[156:159], v[228:231], v[8:11], v[156:159]\n\t"
    "v_mfma_f32_16x16x32_bf16 v[164:167], v[228:231], v[12:15], 0\n\t"
    "v_mfma_f32_16x16x32_bf16 v[164:167], v[224:227], v[4:7], v[164:167]\n\t"
    "s_waitcnt lgkmcnt(0)\n\t"
    "s_barrier\n\t"
    "s_add_u32 m0, s47, 0x20000\n\t"
    "s_lshr_b32 s46, s49, 9\n\t"
    "s_add_u32 s46, s46, s58\n\t"
    "s_add_u32 s46, s46, 0x80000\n\t"
    "s_and_b32 s46, s46, 0x3fffff\n\t"
    "buffer_load_dwordx4 v134, %[srdwq], s46 offen lds\n\t"
    "buffer_load_dwordx4 v135, %[srdwq], s46 offen offset:1024 lds\n\t"
    "buffer_load_dwordx4 v136, %[srdwq], s46 offen offset:2048 lds\n\t"
    "buffer_load_dwordx4 v137, %[srdwq], s46 offen offset:3072 lds\n\t"
    "s_add_u32 m0, s47, 0x8000\n\t"
    "s_add_u32 s46, s59, 0x18000\n\t"
    "s_and_b32 s46, s46, 0x1ffff\n\t"
    "s_or_b32 s46, s46, s48\n\t"
    "s_waitcnt vmcnt(12)\n\t"
    "buffer_load_dwordx4 v128, %[srdk], s46 offen nt lds\n\t"
    "s_waitcnt vmcnt(12)\n\t"
    "buffer_load_dwordx4 v129, %[srdk], s46 offen offset:1024 nt lds\n\t"
    "s_waitcnt vmcnt(12)\n\t"
    "buffer_load_dwordx4 v130, %[srdk], s46 offen offset:2048 nt lds\n\t"
    "s_waitcnt vmcnt(12)\n\t"
    "buffer_load_dwordx4 v131, %[srdk], s46 offen offset:3072 nt lds\n\t"
    "s_add_u32 m0, s47, 0xc000\n\t"
    "s_add_u32 s46, s59, 0x1c000\n\t"
    "s_and_b32 s46, s46, 0x1ffff\n\t"
    "s_or_b32 s46, s46, s48\n\t"
    "s_waitcnt vmcnt(12)\n\t"
    "buffer_load_dwordx4 v128, %[srdk], s46 offen nt lds\n\t"
    "ds_read_b64_tr_b16 v[216:217], v248 offset:1024\n\t"
    "ds_read_b64_tr_b16 v[218:219], v248 offset:1280\n\t"
    "v_mfma_f32_16x16x32_bf16 v[168:171], v[232:235], v[0:3], 0\n\t"
    "ds_read_b64_tr_b16 v[220:221], v248 offset:9216\n\t"
    "ds_read_b64_tr_b16 v[222:223], v248 offset:9472\n\t"
    "v_mfma_f32_16x16x32_bf16 v[168:171], v[236:239], v[8:11], v[168:171]\n\t"
    "ds_read_b64_tr_b16 v[224:225], v249 offset:1024\n\t"
    "ds_read_b64_tr_b16 v[226:227], v249 offset:1280\n\t"
    "v_mfma_f32_16x16x32_bf16 v[176:179], v[236:239], v[12:15], 0\n\t"
    "ds_read_b64_tr_b16 v[228:229], v249 offset:9216\n\t"
    "ds_read_b64_tr_b16 v[230:231], v249 offset:9472\n\t"
    "v_mfma_f32_16x16x32_bf16 v[176:179], v[232:235], v[4:7], v[176:179]\n\t"
    "ds_read_b64_tr_b16 v[232:233], v248 offset:1536\n\t"
    "v_mfma_f32_16x16x32_bf16 v[172:175], v[240:243], v[0:3], 0\n\t"
    "ds_read_b64_tr_b16 v[234:235], v248 offset:1792\n\t"
    "v_mfma_f32_16x16x32_bf16 v[172:175], v[244:247], v[8:11], v[172:175]\n\t"
    "ds_read_b64_tr_b16 v[236:237], v248 offset:9728\n\t"
    "v_mfma_f32_16x16x32_bf16 v[180:183], v[244:247], v[12:15], 0\n\t"
    "ds_read_b64_tr_b16 v[238:239], v248 offset:9984\n\t"
    "v_mfma_f32_16x16x32_bf16 v[180:183], v[240:243], v[4:7], v[180:183]\n\t"
    "ds_read_b64_tr_b16 v[240:241], v249 offset:1536\n\t"
    "s_waitcnt lgkmcnt(9)\n\t"
    "v_mfma_f32_16x16x32_bf16 v[184:187], v[216:219], v[0:3], 0\n\t"
    "ds_read_b64_tr_b16 v[242:243], v249 offset:1792\n\t"
    "v_mfma_f32_16x16x32_bf16 v[184:187], v[220:223], v[8:11], v[184:187]\n\t"
    "ds_read_b64_tr_b16 v[244:245], v249 offset:9728\n\t"
    "v_mfma_f32_16x16x32_bf16 v[192:195], v[220:223], v[12:15], 0\n\t"
    "ds_read_b64_tr_b16 v[246:247], v249 offset:9984\n\t"
    "v_mfma_f32_16x16x32_bf16 v[192:195], v[216:219], v[4:7], v[192:195]\n\t"
    "s_waitcnt lgkmcnt(8)\n\t"
    "v_mfma_f32_16x16x32_bf16 v[188:191], v[224:227], v[0:3], 0\n\t"
    "v_mfma_f32_16x16x32_bf16 v[188:191], v[228:231], v[8:11], v[188:191]\n\t"
    "v_mfma_f32_16x16x32_bf16 v[196:199], v[228:231], v[12:15], 0\n\t"
    "v_mfma_f32_16x16x32_bf16 v[196:199], v[224:227], v[4:7], v[196:199]\n\t"
    "s_waitcnt vmcnt(5)\n\t"
    "s_waitcnt lgkmcnt(0)\n\t"
    "s_barrier\n\t"
    "s_add_u32 m0, s47, 0xc000\n\t"
    "s_add_u32 s46, s59, 0x1c000\n\t"
    "s_and_b32 s46, s46, 0x1ffff\n\t"
    "s_or_b32 s46, s46, s48\n\t"
    "buffer_load_dwordx4 v129, %[srdk], s46 offen offset:1024 nt lds\n\t"
    "buffer_load_dwordx4 v130, %[srdk], s46 offen offset:2048 nt lds\n\t"
    "buffer_load_dwordx4 v131, %[srdk], s46 offen offset:3072 nt lds\n\t"
    "s_add_u32 m0, s47, 0x10000\n\t"
    "s_add_u32 s46, s59, 0x0\n\t"
    "s_and_b32 s46, s46, 0x1ffff\n\t"
    "s_or_b32 s46, s46, s48\n\t"
    "buffer_load_dwordx4 v132, %[srdv], s46 offen nt lds\n\t"
    "v_add_u32 v248, 0x10000, v248\n\t"
    "v_add_u32 v249, 0x10000, v249\n\t"
    "ds_read_b64_tr_b16 v[216:217], v248 offset:0\n\t"
    "v_mfma_f32_16x16x32_bf16 v[200:203], v[232:235], v[0:3], 0\n\t"
    "ds_read_b64_tr_b16 v[218:219], v248 offset:256\n\t"
    "ds_read_b64_tr_b16 v[220:221], v248 offset:8192\n\t"
    "ds_read_b64_tr_b16 v[222:223], v248 offset:8448\n\t"
    "v_mfma_f32_16x16x32_bf16 v[200:203], v[236:239], v[8:11], v[200:203]\n\t"
    "ds_read_b64_tr_b16 v[224:225], v249 offset:0\n\t"
    "ds_read_b64_tr_b16 v[226:227], v249 offset:256\n\t"
    "v_mfma_f32_16x16x32_bf16 v[208:211], v[236:239], v[12:15], 0\n\t"
    "ds_read_b64_tr_b16 v[228:229], v249 offset:8192\n\t"
    "ds_read_b64_tr_b16 v[230:231], v249 offset:8448\n\t"
    "v_mfma_f32_16x16x32_bf16 v[208:211], v[232:235], v[4:7], v[208:211]\n\t"
    "ds_read_b64_tr_b16 v[232:233], v248 offset:512\n\t"
    "v_mfma_f32_16x16x32_bf16 v[204:207], v[240:243], v[0:3], 0\n\t"
    "ds_read_b64_tr_b16 v[234:235], v248 offset:768\n\t"
    "v_mfma_f32_16x16x32_bf16 v[204:207], v[244:247], v[8:11], v[204:207]\n\t"
    "ds_read_b64_tr_b16 v[236:237], v248 offset:8704\n\t"
    "v_mfma_f32_16x16x32_bf16 v[212:215], v[244:247], v[12:15], 0\n\t"
    "ds_read_b64_tr_b16 v[238:239], v248 offset:8960\n\t"
    "v_mfma_f32_16x16x32_bf16 v[212:215], v[240:243], v[4:7], v[212:215]\n\t"
    "ds_read_b64_tr_b16 v[240:241], v249 offset:512\n\t"
    "s_waitcnt lgkmcnt(9)\n\t"
    "v_mfma_f32_16x16x32_bf16 v[152:155], v[216:219], v[16:19], v[152:155]\n\t"
    "ds_read_b64_tr_b16 v[242:243], v249 offset:768\n\t"
    "v_mfma_f32_16x16x32_bf16 v[152:155], v[220:223], v[24:27], v[152:155]\n\t"
    "ds_read_b64_tr_b16 v[244:245], v249 offset:8704\n\t"
    "v_mfma_f32_16x16x32_bf16 v[160:163], v[220:223], v[28:31], v[160:163]\n\t"
    "ds_read_b64_tr_b16 v[246:247], v249 offset:8960\n\t"
    "v_mfma_f32_16x16x32_bf16 v[160:163], v[216:219], v[20:23], v[160:163]\n\t"
    "s_waitcnt lgkmcnt(8)\n\t"
    "v_mfma_f32_16x16x32_bf16 v[156:159], v[224:227], v[16:19], v[156:159]\n\t"
    "v_mfma_f32_16x16x32_bf16 v[156:159], v[228:231], v[24:27], v[156:159]\n\t"
    "v_mfma_f32_16x16x32_bf16 v[164:167], v[228:231], v[28:31], v[164:167]\n\t"
    "v_mfma_f32_16x16x32_bf16 v[164:167], v[224:227], v[20:23], v[164:167]\n\t"
    "s_waitcnt lgkmcnt(0)\n\t"
    "s_barrier\n\t"
    "s_add_u32 m0, s47, 0x24000\n\t"
    "s_lshr_b32 s46, s49, 9\n\t"
    "s_add_u32 s46, s46, s58\n\t"
    "s_add_u32 s46, s46, 0x100000\n\t"
    "s_and_b32 s46, s46, 0x3fffff\n\t"
    "buffer_load_dwordx4 v134, %[srdwq], s46 offen lds\n\t"
    "buffer_load_dwordx4 v135, %[srdwq], s46 offen offset:1024 lds\n\t"
    "buffer_load_dwordx4 v136, %[srdwq], s46 offen offset:2048 lds\n\t"
    "buffer_load_dwordx4 v137, %[srdwq], s46 offen offset:3072 lds\n\t"
    "s_add_u32 m0, s47, 0x10000\n\t"
    "s_add_u32 s46, s59, 0x0\n\t"
    "s_and_b32 s46, s46, 0x1ffff\n\t"
    "s_or_b32 s46, s46, s48\n\t"
    "s_waitcnt vmcnt(12)\n\t"
    "buffer_load_dwordx4 v133, %[srdv], s46 offen offset:1024 nt lds\n\t"
    "s_waitcnt vmcnt(12)\n\t"
    "buffer_load_dwordx4 v132, %[srdv], s46 offen offset:2048 nt lds\n\t"
    "s_waitcnt vmcnt(12)\n\t"
    "buffer_load_dwordx4 v133, %[srdv], s46 offen offset:3072 nt lds\n\t"
    "s_add_u32 m0, s47, 0x0\n\t"
    "s_add_u32 s46, s59, 0x4000\n\t"
    "s_and_b32 s46, s46, 0x1ffff\n\t"
    "s_or_b32 s46, s46, s48\n\t"
    "s_waitcnt vmcnt(12)\n\t"
    "buffer_load_dwordx4 v132, %[srdv], s46 offen nt lds\n\t"
    "s_waitcnt vmcnt(12)\n\t"
    "buffer_load_dwordx4 v133, %[srdv], s46 offen offset:1024 nt lds\n\t"
    "ds_read_b64_tr_b16 v[216:217], v248 offset:1024\n\t"
    "ds_read_b64_tr_b16 v[218:219], v248 offset:1280\n\t"
    "v_mfma_f32_16x16x32_bf16 v[168:171], v[232:235], v[16:19], v[168:171]\n\t"
    "ds_read_b64_tr_b16 v[220:221], v248 offset:9216\n\t"
    "ds_read_b64_tr_b16 v[222:223], v248 offset:9472\n\t"
    "v_mfma_f32_16x16x32_bf16 v[168:171], v[236:239], v[24:27], v[168:171]\n\t"
    "ds_read_b64_tr_b16 v[224:225], v249 offset:1024\n\t"
    "ds_read_b64_tr_b16 v[226:227], v249 offset:1280\n\t"
    "v_mfma_f32_16x16x32_bf16 v[176:179], v[236:239], v[28:31], v[176:179]\n\t"
    "ds_read_b64_tr_b16 v[228:229], v249 offset:9216\n\t"
    "ds_read_b64_tr_b16 v[230:231], v249 offset:9472\n\t"
    "v_mfma_f32_16x16x32_bf16 v[176:179], v[232:235], v[20:23], v[176:179]\n\t"
    "ds_read_b64_tr_b16 v[232:233], v248 offset:1536\n\t"
    "v_mfma_f32_16x16x32_bf16 v[172:175], v[240:243], v[16:19], v[172:175]\n\t"
    "ds_read_b64_tr_b16 v[234:235], v248 offset:1792\n\t"
    "v_mfma_f32_16x16x32_bf16 v[172:175], v[244:247], v[24:27], v[172:175]\n\t"
    "ds_read_b64_tr_b16 v[236:237], v248 offset:9728\n\t"
    "v_mfma_f32_16x16x32_bf16 v[180:183], v[244:247], v[28:31], v[180:183]\n\t"
    "ds_read_b64_tr_b16 v[238:239], v248 offset:9984\n\t"
    "v_mfma_f32_16x16x32_bf16 v[180:183], v[240:243], v[20:23], v[180:183]\n\t"
    "ds_read_b64_tr_b16 v[240:241], v249 offset:1536\n\t"
    "s_waitcnt lgkmcnt(9)\n\t"
    "v_mfma_f32_16x16x32_bf16 v[184:187], v[216:219], v[16:19], v[184:187]\n\t"
    "ds_read_b64_tr_b16 v[242:243], v249 offset:1792\n\t"
    "v_mfma_f32_16x16x32_bf16 v[184:187], v[220:223], v[24:27], v[184:187]\n\t"
    "ds_read_b64_tr_b16 v[244:245], v249 offset:9728\n\t"
    "v_mfma_f32_16x16x32_bf16 v[192:195], v[220:223], v[28:31], v[192:195]\n\t"
    "ds_read_b64_tr_b16 v[246:247], v249 offset:9984\n\t"
    "v_mfma_f32_16x16x32_bf16 v[192:195], v[216:219], v[20:23], v[192:195]\n\t"
    "s_waitcnt lgkmcnt(8)\n\t"
    "v_mfma_f32_16x16x32_bf16 v[188:191], v[224:227], v[16:19], v[188:191]\n\t"
    "v_mfma_f32_16x16x32_bf16 v[188:191], v[228:231], v[24:27], v[188:191]\n\t"
    "v_mfma_f32_16x16x32_bf16 v[196:199], v[228:231], v[28:31], v[196:199]\n\t"
    "v_mfma_f32_16x16x32_bf16 v[196:199], v[224:227], v[20:23], v[196:199]\n\t"
    "s_waitcnt vmcnt(5)\n\t"
    "s_waitcnt lgkmcnt(0)\n\t"
    "s_barrier\n\t"
    "s_add_u32 m0, s47, 0x0\n\t"
    "s_add_u32 s46, s59, 0x4000\n\t"
    "s_and_b32 s46, s46, 0x1ffff\n\t"
    "s_or_b32 s46, s46, s48\n\t"
    "buffer_load_dwordx4 v132, %[srdv], s46 offen offset:2048 nt lds\n\t"
    "buffer_load_dwordx4 v133, %[srdv], s46 offen offset:3072 nt lds\n\t"
    "s_add_u32 m0, s47, 0x4000\n\t"
    "s_add_u32 s46, s59, 0x8000\n\t"
    "s_and_b32 s46, s46, 0x1ffff\n\t"
    "s_or_b32 s46, s46, s48\n\t"
    "buffer_load_dwordx4 v132, %[srdv], s46 offen nt lds\n\t"
    "buffer_load_dwordx4 v133, %[srdv], s46 offen offset:1024 nt lds\n\t"
    "ds_read_b64_tr_b16 v[216:217], v248 offset:16384\n\t"
    "ds_read_b64_tr_b16 v[218:219], v248 offset:16640\n\t"
    "v_mfma_f32_16x16x32_bf16 v[200:203], v[232:235], v[16:19], v[200:203]\n\t"
    "ds_read_b64_tr_b16 v[220:221], v248 offset:24576\n\t"
    "ds_read_b64_tr_b16 v[222:223], v248 offset:24832\n\t"
    "v_mfma_f32_16x16x32_bf16 v[200:203], v[236:239], v[24:27], v[200:203]\n\t"
    "ds_read_b64_tr_b16 v[224:225], v249 offset:16384\n\t"
    "ds_read_b64_tr_b16 v[226:227], v249 offset:16640\n\t"
    "v_mfma_f32_16x16x32_bf16 v[208:211], v[236:239], v[28:31], v[208:211]\n\t"
    "ds_read_b64_tr_b16 v[228:229], v249 offset:24576\n\t"
    "ds_read_b64_tr_b16 v[230:231], v249 offset:24832\n\t"
    "v_mfma_f32_16x16x32_bf16 v[208:211], v[232:235], v[20:23], v[208:211]\n\t"
    "ds_read_b64_tr_b16 v[232:233], v248 offset:16896\n\t"
    "v_mfma_f32_16x16x32_bf16 v[204:207], v[240:243], v[16:19], v[204:207]\n\t"
    "ds_read_b64_tr_b16 v[234:235], v248 offset:17152\n\t"
    "v_mfma_f32_16x16x32_bf16 v[204:207], v[244:247], v[24:27], v[204:207]\n\t"
    "ds_read_b64_tr_b16 v[236:237], v248 offset:25088\n\t"
    "v_mfma_f32_16x16x32_bf16 v[212:215], v[244:247], v[28:31], v[212:215]\n\t"
    "ds_read_b64_tr_b16 v[238:239], v248 offset:25344\n\t"
    "v_mfma_f32_16x16x32_bf16 v[212:215], v[240:243], v[20:23], v[212:215]\n\t"
    "ds_read_b64_tr_b16 v[240:241], v249 offset:16896\n\t"
    "s_waitcnt lgkmcnt(9)\n\t"
    "v_mfma_f32_16x16x32_bf16 v[152:155], v[216:219], v[32:35], v[152:155]\n\t"
    "ds_read_b64_tr_b16 v[242:243], v249 offset:17152\n\t"
    "v_mfma_f32_16x16x32_bf16 v[152:155], v[220:223], v[40:43], v[152:155]\n\t"
    "ds_read_b64_tr_b16 v[244:245], v249 offset:25088\n\t"
    "v_mfma_f32_16x16x32_bf16 v[160:163], v[220:223], v[44:47], v[160:163]\n\t"
    "ds_read_b64_tr_b16 v[246:247], v249 offset:25344\n\t"
    "v_mfma_f32_16x16x32_bf16 v[160:163], v[216:219], v[36:39], v[160:163]\n\t"
    "s_waitcnt lgkmcnt(8)\n\t"
    "v_mfma_f32_16x16x32_bf16 v[156:159], v[224:227], v[32:35], v[156:159]\n\t"
    "v_mfma_f32_16x16x32_bf16 v[156:159], v[228:231], v[40:43], v[156:159]\n\t"
    "v_mfma_f32_16x16x32_bf16 v[164:167], v[228:231], v[44:47], v[164:167]\n\t"
    "v_mfma_f32_16x16x32_bf16 v[164:167], v[224:227], v[36:39], v[164:167]\n\t"
    "s_waitcnt lgkmcnt(0)\n\t"
    "s_barrier\n\t"
    "s_add_u32 m0, s47, 0x20000\n\t"
    "s_lshr_b32 s46, s49, 9\n\t"
    "s_add_u32 s46, s46, s58\n\t"
    "s_add_u32 s46, s46, 0x180000\n\t"
    "s_and_b32 s46, s46, 0x3fffff\n\t"
    "buffer_load_dwordx4 v134, %[srdwq], s46 offen lds\n\t"
    "buffer_load_dwordx4 v135, %[srdwq], s46 offen offset:1024 lds\n\t"
    "buffer_load_dwordx4 v136, %[srdwq], s46 offen offset:2048 lds\n\t"
    "buffer_load_dwordx4 v137, %[srdwq], s46 offen offset:3072 lds\n\t"
    "s_add_u32 m0, s47, 0x4000\n\t"
    "s_add_u32 s46, s59, 0x8000\n\t"
    "s_and_b32 s46, s46, 0x1ffff\n\t"
    "s_or_b32 s46, s46, s48\n\t"
    "s_waitcnt vmcnt(12)\n\t"
    "buffer_load_dwordx4 v132, %[srdv], s46 offen offset:2048 nt lds\n\t"
    "s_waitcnt vmcnt(12)\n\t"
    "buffer_load_dwordx4 v133, %[srdv], s46 offen offset:3072 nt lds\n\t"
    "s_add_u32 m0, s47, 0x14000\n\t"
    "s_add_u32 s46, s59, 0xc000\n\t"
    "s_and_b32 s46, s46, 0x1ffff\n\t"
    "s_or_b32 s46, s46, s48\n\t"
    "s_waitcnt vmcnt(12)\n\t"
    "buffer_load_dwordx4 v132, %[srdv], s46 offen nt lds\n\t"
    "s_waitcnt vmcnt(12)\n\t"
    "buffer_load_dwordx4 v133, %[srdv], s46 offen offset:1024 nt lds\n\t"
    "s_waitcnt vmcnt(12)\n\t"
    "buffer_load_dwordx4 v132, %[srdv], s46 offen offset:2048 nt lds\n\t"
    "ds_read_b64_tr_b16 v[216:217], v248 offset:17408\n\t"
    "ds_read_b64_tr_b16 v[218:219], v248 offset:17664\n\t"
    "v_mfma_f32_16x16x32_bf16 v[168:171], v[232:235], v[32:35], v[168:171]\n\t"
    "ds_read_b64_tr_b16 v[220:221], v248 offset:25600\n\t"
    "ds_read_b64_tr_b16 v[222:223], v248 offset:25856\n\t"
    "v_mfma_f32_16x16x32_bf16 v[168:171], v[236:239], v[40:43], v[168:171]\n\t"
    "ds_read_b64_tr_b16 v[224:225], v249 offset:17408\n\t"
    "ds_read_b64_tr_b16 v[226:227], v249 offset:17664\n\t"
    "v_mfma_f32_16x16x32_bf16 v[176:179], v[236:239], v[44:47], v[176:179]\n\t"
    "ds_read_b64_tr_b16 v[228:229], v249 offset:25600\n\t"
    "ds_read_b64_tr_b16 v[230:231], v249 offset:25856\n\t"
    "v_mfma_f32_16x16x32_bf16 v[176:179], v[232:235], v[36:39], v[176:179]\n\t"
    "ds_read_b64_tr_b16 v[232:233], v248 offset:17920\n\t"
    "v_mfma_f32_16x16x32_bf16 v[172:175], v[240:243], v[32:35], v[172:175]\n\t"
    "ds_read_b64_tr_b16 v[234:235], v248 offset:18176\n\t"
    "v_mfma_f32_16x16x32_bf16 v[172:175], v[244:247], v[40:43], v[172:175]\n\t"
    "ds_read_b64_tr_b16 v[236:237], v248 offset:26112\n\t"
    "v_mfma_f32_16x16x32_bf16 v[180:183], v[244:247], v[44:47], v[180:183]\n\t"
    "ds_read_b64_tr_b16 v[238:239], v248 offset:26368\n\t"
    "v_mfma_f32_16x16x32_bf16 v[180:183], v[240:243], v[36:39], v[180:183]\n\t"
    "ds_read_b64_tr_b16 v[240:241], v249 offset:17920\n\t"
    "s_waitcnt lgkmcnt(9)\n\t"
    "v_mfma_f32_16x16x32_bf16 v[184:187], v[216:219], v[32:35], v[184:187]\n\t"
    "ds_read_b64_tr_b16 v[242:243], v249 offset:18176\n\t"
    "v_mfma_f32_16x16x32_bf16 v[184:187], v[220:223], v[40:43], v[184:187]\n\t"
    "ds_read_b64_tr_b16 v[244:245], v249 offset:26112\n\t"
    "v_mfma_f32_16x16x32_bf16 v[192:195], v[220:223], v[44:47], v[192:195]\n\t"
    "ds_read_b64_tr_b16 v[246:247], v249 offset:26368\n\t"
    "v_mfma_f32_16x16x32_bf16 v[192:195], v[216:219], v[36:39], v[192:195]\n\t"
    "s_waitcnt lgkmcnt(8)\n\t"
    "v_mfma_f32_16x16x32_bf16 v[188:191], v[224:227], v[32:35], v[188:191]\n\t"
    "v_mfma_f32_16x16x32_bf16 v[188:191], v[228:231], v[40:43], v[188:191]\n\t"
    "v_mfma_f32_16x16x32_bf16 v[196:199], v[228:231], v[44:47], v[196:199]\n\t"
    "v_mfma_f32_16x16x32_bf16 v[196:199], v[224:227], v[36:39], v[196:199]\n\t"
    "s_waitcnt vmcnt(5)\n\t"
    "s_waitcnt lgkmcnt(0)\n\t"
    "s_barrier\n\t"
    "s_add_u32 m0, s47, 0x14000\n\t"
    "s_add_u32 s46, s59, 0xc000\n\t"
    "s_and_b32 s46, s46, 0x1ffff\n\t"
    "s_or_b32 s46, s46, s48\n\t"
    "buffer_load_dwordx4 v133, %[srdv], s46 offen offset:3072 nt lds\n\t"
    "s_add_u32 m0, s47, 0x18000\n\t"
    "s_add_u32 s46, s59, 0x10000\n\t"
    "s_and_b32 s46, s46, 0x1ffff\n\t"
    "s_or_b32 s46, s46, s48\n\t"
    "buffer_load_dwordx4 v132, %[srdv], s46 offen nt lds\n\t"
    "buffer_load_dwordx4 v133, %[srdv], s46 offen offset:1024 nt lds\n\t"
    "buffer_load_dwordx4 v132, %[srdv], s46 offen offset:2048 nt lds\n\t"
    "ds_read_b64_tr_b16 v[216:217], v248 offset:0\n\t"
    "ds_read_b64_tr_b16 v[218:219], v248 offset:256\n\t"
    "v_mfma_f32_16x16x32_bf16 v[200:203], v[232:235], v[32:35], v[200:203]\n\t"
    "ds_read_b64_tr_b16 v[220:221], v248 offset:8192\n\t"
    "ds_read_b64_tr_b16 v[222:223], v248 offset:8448\n\t"
    "v_mfma_f32_16x16x32_bf16 v[200:203], v[236:239], v[40:43], v[200:203]\n\t"
    "ds_read_b64_tr_b16 v[224:225], v249 offset:0\n\t"
    "ds_read_b64_tr_b16 v[226:227], v249 offset:256\n\t"
    "v_mfma_f32_16x16x32_bf16 v[208:211], v[236:239], v[44:47], v[208:211]\n\t"
    "ds_read_b64_tr_b16 v[228:229], v249 offset:8192\n\t"
    "ds_read_b64_tr_b16 v[230:231], v249 offset:8448\n\t"
    "v_mfma_f32_16x16x32_bf16 v[208:211], v[232:235], v[36:39], v[208:211]\n\t"
    "ds_read_b64_tr_b16 v[232:233], v248 offset:512\n\t"
    "v_mfma_f32_16x16x32_bf16 v[204:207], v[240:243], v[32:35], v[204:207]\n\t"
    "ds_read_b64_tr_b16 v[234:235], v248 offset:768\n\t"
    "v_mfma_f32_16x16x32_bf16 v[204:207], v[244:247], v[40:43], v[204:207]\n\t"
    "ds_read_b64_tr_b16 v[236:237], v248 offset:8704\n\t"
    "v_mfma_f32_16x16x32_bf16 v[212:215], v[244:247], v[44:47], v[212:215]\n\t"
    "ds_read_b64_tr_b16 v[238:239], v248 offset:8960\n\t"
    "v_mfma_f32_16x16x32_bf16 v[212:215], v[240:243], v[36:39], v[212:215]\n\t"
    "ds_read_b64_tr_b16 v[240:241], v249 offset:512\n\t"
    "s_waitcnt lgkmcnt(9)\n\t"
    "v_mfma_f32_16x16x32_bf16 v[152:155], v[216:219], v[48:51], v[152:155]\n\t"
    "ds_read_b64_tr_b16 v[242:243], v249 offset:768\n\t"
    "v_mfma_f32_16x16x32_bf16 v[152:155], v[220:223], v[56:59], v[152:155]\n\t"
    "ds_read_b64_tr_b16 v[244:245], v249 offset:8704\n\t"
    "v_mfma_f32_16x16x32_bf16 v[160:163], v[220:223], v[60:63], v[160:163]\n\t"
    "ds_read_b64_tr_b16 v[246:247], v249 offset:8960\n\t"
    "v_mfma_f32_16x16x32_bf16 v[160:163], v[216:219], v[52:55], v[160:163]\n\t"
    "s_waitcnt lgkmcnt(8)\n\t"
    "v_mfma_f32_16x16x32_bf16 v[156:159], v[224:227], v[48:51], v[156:159]\n\t"
    "v_mfma_f32_16x16x32_bf16 v[156:159], v[228:231], v[56:59], v[156:159]\n\t"
    "v_mfma_f32_16x16x32_bf16 v[164:167], v[228:231], v[60:63], v[164:167]\n\t"
    "v_mfma_f32_16x16x32_bf16 v[164:167], v[224:227], v[52:55], v[164:167]\n\t"
    "s_waitcnt lgkmcnt(0)\n\t"
    "s_barrier\n\t"
    "s_add_u32 m0, s47, 0x24000\n\t"
    "s_lshr_b32 s46, s49, 9\n\t"
    "s_add_u32 s46, s46, s58\n\t"
    "s_add_u32 s46, s46, 0x200000\n\t"
    "s_and_b32 s46, s46, 0x3fffff\n\t"
    "buffer_load_dwordx4 v134, %[srdwq], s46 offen lds\n\t"
    "buffer_load_dwordx4 v135, %[srdwq], s46 offen offset:1024 lds\n\t"
    "buffer_load_dwordx4 v136, %[srdwq], s46 offen offset:2048 lds\n\t"
    "buffer_load_dwordx4 v137, %[srdwq], s46 offen offset:3072 lds\n\t"
    "s_add_u32 m0, s47, 0x18000\n\t"
    "s_add_u32 s46, s59, 0x10000\n\t"
    "s_and_b32 s46, s46, 0x1ffff\n\t"
    "s_or_b32 s46, s46, s48\n\t"
    "s_waitcnt vmcnt(12)\n\t"
    "buffer_load_dwordx4 v133, %[srdv], s46 offen offset:3072 nt lds\n\t"
    "s_add_u32 m0, s47, 0x1c000\n\t"
    "s_add_u32 s46, s59, 0x14000\n\t"
    "s_and_b32 s46, s46, 0x1ffff\n\t"
    "s_or_b32 s46, s46, s48\n\t"
    "s_waitcnt vmcnt(12)\n\t"
    "buffer_load_dwordx4 v132, %[srdv], s46 offen nt lds\n\t"
    "s_waitcnt vmcnt(12)\n\t"
    "buffer_load_dwordx4 v133, %[srdv], s46 offen offset:1024 nt lds\n\t"
    "s_waitcnt vmcnt(12)\n\t"
    "buffer_load_dwordx4 v132, %[srdv], s46 offen offset:2048 nt lds\n\t"
    "ds_read_b64_tr_b16 v[216:217], v248 offset:1024\n\t"
    "ds_read_b64_tr_b16 v[218:219], v248 offset:1280\n\t"
    "v_mfma_f32_16x16x32_bf16 v[168:171], v[232:235], v[48:51], v[168:171]\n\t"
    "ds_read_b64_tr_b16 v[220:221], v248 offset:9216\n\t"
    "ds_read_b64_tr_b16 v[222:223], v248 offset:9472\n\t"
    "v_mfma_f32_16x16x32_bf16 v[168:171], v[236:239], v[56:59], v[168:171]\n\t"
    "ds_read_b64_tr_b16 v[224:225], v249 offset:1024\n\t"
    "ds_read_b64_tr_b16 v[226:227], v249 offset:1280\n\t"
    "v_mfma_f32_16x16x32_bf16 v[176:179], v[236:239], v[60:63], v[176:179]\n\t"
    "ds_read_b64_tr_b16 v[228:229], v249 offset:9216\n\t"
    "ds_read_b64_tr_b16 v[230:231], v249 offset:9472\n\t"
    "v_mfma_f32_16x16x32_bf16 v[176:179], v[232:235], v[52:55], v[176:179]\n\t"
    "ds_read_b64_tr_b16 v[232:233], v248 offset:1536\n\t"
    "v_mfma_f32_16x16x32_bf16 v[172:175], v[240:243], v[48:51], v[172:175]\n\t"
    "ds_read_b64_tr_b16 v[234:235], v248 offset:1792\n\t"
    "v_mfma_f32_16x16x32_bf16 v[172:175], v[244:247], v[56:59], v[172:175]\n\t"
    "ds_read_b64_tr_b16 v[236:237], v248 offset:9728\n\t"
    "v_mfma_f32_16x16x32_bf16 v[180:183], v[244:247], v[60:63], v[180:183]\n\t"
    "ds_read_b64_tr_b16 v[238:239], v248 offset:9984\n\t"
    "v_mfma_f32_16x16x32_bf16 v[180:183], v[240:243], v[52:55], v[180:183]\n\t"
    "ds_read_b64_tr_b16 v[240:241], v249 offset:1536\n\t"
    "s_waitcnt lgkmcnt(9)\n\t"
    "v_mfma_f32_16x16x32_bf16 v[184:187], v[216:219], v[48:51], v[184:187]\n\t"
    "ds_read_b64_tr_b16 v[242:243], v249 offset:1792\n\t"
    "v_mfma_f32_16x16x32_bf16 v[184:187], v[220:223], v[56:59], v[184:187]\n\t"
    "ds_read_b64_tr_b16 v[244:245], v249 offset:9728\n\t"
    "v_mfma_f32_16x16x32_bf16 v[192:195], v[220:223], v[60:63], v[192:195]\n\t"
    "ds_read_b64_tr_b16 v[246:247], v249 offset:9984\n\t"
    "v_mfma_f32_16x16x32_bf16 v[192:195], v[216:219], v[52:55], v[192:195]\n\t"
    "s_waitcnt lgkmcnt(8)\n\t"
    "v_mfma_f32_16x16x32_bf16 v[188:191], v[224:227], v[48:51], v[188:191]\n\t"
    "v_mfma_f32_16x16x32_bf16 v[188:191], v[228:231], v[56:59], v[188:191]\n\t"
    "v_mfma_f32_16x16x32_bf16 v[196:199], v[228:231], v[60:63], v[196:199]\n\t"
    "v_mfma_f32_16x16x32_bf16 v[196:199], v[224:227], v[52:55], v[196:199]\n\t"
    "s_waitcnt vmcnt(4)\n\t"
    "s_waitcnt lgkmcnt(0)\n\t"
    "s_barrier\n\t"
    "s_add_u32 m0, s47, 0x1c000\n\t"
    "s_add_u32 s46, s59, 0x14000\n\t"
    "s_and_b32 s46, s46, 0x1ffff\n\t"
    "s_or_b32 s46, s46, s48\n\t"
    "buffer_load_dwordx4 v133, %[srdv], s46 offen offset:3072 nt lds\n\t"
    "s_add_u32 m0, s47, 0x8000\n\t"
    "s_add_u32 s46, s59, 0x18000\n\t"
    "s_and_b32 s46, s46, 0x1ffff\n\t"
    "s_or_b32 s46, s46, s48\n\t"
    "buffer_load_dwordx4 v132, %[srdv], s46 offen nt lds\n\t"
    "buffer_load_dwordx4 v133, %[srdv], s46 offen offset:1024 nt lds\n\t"
    "buffer_load_dwordx4 v132, %[srdv], s46 offen offset:2048 nt lds\n\t"
    "buffer_load_dwordx4 v133, %[srdv], s46 offen offset:3072 nt lds\n\t"
    "ds_read_b64_tr_b16 v[216:217], v248 offset:16384\n\t"
    "ds_read_b64_tr_b16 v[218:219], v248 offset:16640\n\t"
    "v_mfma_f32_16x16x32_bf16 v[200:203], v[232:235], v[48:51], v[200:203]\n\t"
    "ds_read_b64_tr_b16 v[220:221], v248 offset:24576\n\t"
    "ds_read_b64_tr_b16 v[222:223], v248 offset:24832\n\t"
    "v_mfma_f32_16x16x32_bf16 v[200:203], v[236:239], v[56:59], v[200:203]\n\t"
    "ds_read_b64_tr_b16 v[224:225], v249 offset:16384\n\t"
    "ds_read_b64_tr_b16 v[226:227], v249 offset:16640\n\t"
    "v_mfma_f32_16x16x32_bf16 v[208:211], v[236:239], v[60:63], v[208:211]\n\t"
    "ds_read_b64_tr_b16 v[228:229], v249 offset:24576\n\t"
    "ds_read_b64_tr_b16 v[230:231], v249 offset:24832\n\t"
    "v_mfma_f32_16x16x32_bf16 v[208:211], v[232:235], v[52:55], v[208:211]\n\t"
    "ds_read_b64_tr_b16 v[232:233], v248 offset:16896\n\t"
    "v_mfma_f32_16x16x32_bf16 v[204:207], v[240:243], v[48:51], v[204:207]\n\t"
    "ds_read_b64_tr_b16 v[234:235], v248 offset:17152\n\t"
    "v_mfma_f32_16x16x32_bf16 v[204:207], v[244:247], v[56:59], v[204:207]\n\t"
    "ds_read_b64_tr_b16 v[236:237], v248 offset:25088\n\t"
    "v_mfma_f32_16x16x32_bf16 v[212:215], v[244:247], v[60:63], v[212:215]\n\t"
    "ds_read_b64_tr_b16 v[238:239], v248 offset:25344\n\t"
    "v_mfma_f32_16x16x32_bf16 v[212:215], v[240:243], v[52:55], v[212:215]\n\t"
    "ds_read_b64_tr_b16 v[240:241], v249 offset:16896\n\t"
    "s_waitcnt lgkmcnt(9)\n\t"
    "v_mfma_f32_16x16x32_bf16 v[152:155], v[216:219], v[64:67], v[152:155]\n\t"
    "ds_read_b64_tr_b16 v[242:243], v249 offset:17152\n\t"
    "v_mfma_f32_16x16x32_bf16 v[152:155], v[220:223], v[72:75], v[152:155]\n\t"
    "ds_read_b64_tr_b16 v[244:245], v249 offset:25088\n\t"
    "v_mfma_f32_16x16x32_bf16 v[160:163], v[220:223], v[76:79], v[160:163]\n\t"
    "ds_read_b64_tr_b16 v[246:247], v249 offset:25344\n\t"
    "v_mfma_f32_16x16x32_bf16 v[160:163], v[216:219], v[68:71], v[160:163]\n\t"
    "s_waitcnt lgkmcnt(8)\n\t"
    "v_mfma_f32_16x16x32_bf16 v[156:159], v[224:227], v[64:67], v[156:159]\n\t"
    "v_mfma_f32_16x16x32_bf16 v[156:159], v[228:231], v[72:75], v[156:159]\n\t"
    "v_mfma_f32_16x16x32_bf16 v[164:167], v[228:231], v[76:79], v[164:167]\n\t"
    "v_mfma_f32_16x16x32_bf16 v[164:167], v[224:227], v[68:71], v[164:167]\n\t"
    "s_waitcnt lgkmcnt(0)\n\t"
    "s_barrier\n\t"
    "s_add_u32 m0, s47, 0x20000\n\t"
    "s_lshr_b32 s46, s49, 9\n\t"
    "s_add_u32 s46, s46, s58\n\t"
    "s_add_u32 s46, s46, 0x280000\n\t"
    "s_and_b32 s46, s46, 0x3fffff\n\t"
    "buffer_load_dwordx4 v134, %[srdwq], s46 offen lds\n\t"
    "buffer_load_dwordx4 v135, %[srdwq], s46 offen offset:1024 lds\n\t"
    "buffer_load_dwordx4 v136, %[srdwq], s46 offen offset:2048 lds\n\t"
    "buffer_load_dwordx4 v137, %[srdwq], s46 offen offset:3072 lds\n\t"
    "s_add_u32 m0, s47, 0xc000\n\t"
    "s_add_u32 s46, s59, 0x1c000\n\t"
    "s_and_b32 s46, s46, 0x1ffff\n\t"
    "s_or_b32 s46, s46, s48\n\t"
    "s_waitcnt vmcnt(12)\n\t"
    "buffer_load_dwordx4 v132, %[srdv], s46 offen nt lds\n\t"
    "s_waitcnt vmcnt(12)\n\t"
    "buffer_load_dwordx4 v133, %[srdv], s46 offen offset:1024 nt lds\n\t"
    "s_waitcnt vmcnt(12)\n\t"
    "buffer_load_dwordx4 v132, %[srdv], s46 offen offset:2048 nt lds\n\t"
    "s_waitcnt vmcnt(12)\n\t"
    "buffer_load_dwordx4 v133, %[srdv], s46 offen offset:3072 nt lds\n\t"
    "ds_read_b64_tr_b16 v[216:217], v248 offset:17408\n\t"
    "ds_read_b64_tr_b16 v[218:219], v248 offset:17664\n\t"
    "v_mfma_f32_16x16x32_bf16 v[168:171], v[232:235], v[64:67], v[168:171]\n\t"
    "ds_read_b64_tr_b16 v[220:221], v248 offset:25600\n\t"
    "ds_read_b64_tr_b16 v[222:223], v248 offset:25856\n\t"
    "v_mfma_f32_16x16x32_bf16 v[168:171], v[236:239], v[72:75], v[168:171]\n\t"
    "ds_read_b64_tr_b16 v[224:225], v249 offset:17408\n\t"
    "ds_read_b64_tr_b16 v[226:227], v249 offset:17664\n\t"
    "v_mfma_f32_16x16x32_bf16 v[176:179], v[236:239], v[76:79], v[176:179]\n\t"
    "ds_read_b64_tr_b16 v[228:229], v249 offset:25600\n\t"
    "ds_read_b64_tr_b16 v[230:231], v249 offset:25856\n\t"
    "v_mfma_f32_16x16x32_bf16 v[176:179], v[232:235], v[68:71], v[176:179]\n\t"
    "ds_read_b64_tr_b16 v[232:233], v248 offset:17920\n\t"
    "v_mfma_f32_16x16x32_bf16 v[172:175], v[240:243], v[64:67], v[172:175]\n\t"
    "ds_read_b64_tr_b16 v[234:235], v248 offset:18176\n\t"
    "v_mfma_f32_16x16x32_bf16 v[172:175], v[244:247], v[72:75], v[172:175]\n\t"
    "ds_read_b64_tr_b16 v[236:237], v248 offset:26112\n\t"
    "v_mfma_f32_16x16x32_bf16 v[180:183], v[244:247], v[76:79], v[180:183]\n\t"
    "ds_read_b64_tr_b16 v[238:239], v248 offset:26368\n\t"
    "v_mfma_f32_16x16x32_bf16 v[180:183], v[240:243], v[68:71], v[180:183]\n\t"
    "ds_read_b64_tr_b16 v[240:241], v249 offset:17920\n\t"
    "s_waitcnt lgkmcnt(9)\n\t"
    "v_mfma_f32_16x16x32_bf16 v[184:187], v[216:219], v[64:67], v[184:187]\n\t"
    "ds_read_b64_tr_b16 v[242:243], v249 offset:18176\n\t"
    "v_mfma_f32_16x16x32_bf16 v[184:187], v[220:223], v[72:75], v[184:187]\n\t"
    "ds_read_b64_tr_b16 v[244:245], v249 offset:26112\n\t"
    "v_mfma_f32_16x16x32_bf16 v[192:195], v[220:223], v[76:79], v[192:195]\n\t"
    "ds_read_b64_tr_b16 v[246:247], v249 offset:26368\n\t"
    "v_mfma_f32_16x16x32_bf16 v[192:195], v[216:219], v[68:71], v[192:195]\n\t"
    "s_waitcnt lgkmcnt(8)\n\t"
    "v_mfma_f32_16x16x32_bf16 v[188:191], v[224:227], v[64:67], v[188:191]\n\t"
    "v_mfma_f32_16x16x32_bf16 v[188:191], v[228:231], v[72:75], v[188:191]\n\t"
    "v_mfma_f32_16x16x32_bf16 v[196:199], v[228:231], v[76:79], v[196:199]\n\t"
    "v_mfma_f32_16x16x32_bf16 v[196:199], v[224:227], v[68:71], v[196:199]\n\t"
    "s_waitcnt vmcnt(4)\n\t"
    "s_waitcnt lgkmcnt(0)\n\t"
    "s_barrier\n\t"
    "s_add_u32 m0, s47, 0x10000\n\t"
    "s_add_u32 s46, s59, 0x0\n\t"
    "s_and_b32 s46, s46, 0x1ffff\n\t"
    "s_or_b32 s46, s46, s49\n\t"
    "buffer_load_dwordx4 v128, %[srdk], s46 offen nt lds\n\t"
    "buffer_load_dwordx4 v129, %[srdk], s46 offen offset:1024 nt lds\n\t"
    "buffer_load_dwordx4 v130, %[srdk], s46 offen offset:2048 nt lds\n\t"
    "buffer_load_dwordx4 v131, %[srdk], s46 offen offset:3072 nt lds\n\t"
    "ds_read_b64_tr_b16 v[216:217], v248 offset:0\n\t"
    "ds_read_b64_tr_b16 v[218:219], v248 offset:256\n\t"
    "v_mfma_f32_16x16x32_bf16 v[200:203], v[232:235], v[64:67], v[200:203]\n\t"
    "ds_read_b64_tr_b16 v[220:221], v248 offset:8192\n\t"
    "ds_read_b64_tr_b16 v[222:223], v248 offset:8448\n\t"
    "v_mfma_f32_16x16x32_bf16 v[200:203], v[236:239], v[72:75], v[200:203]\n\t"
    "ds_read_b64_tr_b16 v[224:225], v249 offset:0\n\t"
    "ds_read_b64_tr_b16 v[226:227], v249 offset:256\n\t"
    "v_mfma_f32_16x16x32_bf16 v[208:211], v[236:239], v[76:79], v[208:211]\n\t"
    "ds_read_b64_tr_b16 v[228:229], v249 offset:8192\n\t"
    "ds_read_b64_tr_b16 v[230:231], v249 offset:8448\n\t"
    "v_mfma_f32_16x16x32_bf16 v[208:211], v[232:235], v[68:71], v[208:211]\n\t"
    "ds_read_b64_tr_b16 v[232:233], v248 offset:512\n\t"
    "v_mfma_f32_16x16x32_bf16 v[204:207], v[240:243], v[64:67], v[204:207]\n\t"
    "ds_read_b64_tr_b16 v[234:235], v248 offset:768\n\t"
    "v_mfma_f32_16x16x32_bf16 v[204:207], v[244:247], v[72:75], v[204:207]\n\t"
    "ds_read_b64_tr_b16 v[236:237], v248 offset:8704\n\t"
    "v_mfma_f32_16x16x32_bf16 v[212:215], v[244:247], v[76:79], v[212:215]\n\t"
    "ds_read_b64_tr_b16 v[238:239], v248 offset:8960\n\t"
    "v_mfma_f32_16x16x32_bf16 v[212:215], v[240:243], v[68:71], v[212:215]\n\t"
    "ds_read_b64_tr_b16 v[240:241], v249 offset:512\n\t"
    "s_waitcnt lgkmcnt(9)\n\t"
    "v_mfma_f32_16x16x32_bf16 v[152:155], v[216:219], v[80:83], v[152:155]\n\t"
    "ds_read_b64_tr_b16 v[242:243], v249 offset:768\n\t"
    "v_mfma_f32_16x16x32_bf16 v[152:155], v[220:223], v[88:91], v[152:155]\n\t"
    "ds_read_b64_tr_b16 v[244:245], v249 offset:8704\n\t"
    "v_mfma_f32_16x16x32_bf16 v[160:163], v[220:223], v[92:95], v[160:163]\n\t"
    "ds_read_b64_tr_b16 v[246:247], v249 offset:8960\n\t"
    "v_mfma_f32_16x16x32_bf16 v[160:163], v[216:219], v[84:87], v[160:163]\n\t"
    "s_waitcnt lgkmcnt(8)\n\t"
    "v_mfma_f32_16x16x32_bf16 v[156:159], v[224:227], v[80:83], v[156:159]\n\t"
    "v_mfma_f32_16x16x32_bf16 v[156:159], v[228:231], v[88:91], v[156:159]\n\t"
    "v_mfma_f32_16x16x32_bf16 v[164:167], v[228:231], v[92:95], v[164:167]\n\t"
    "v_mfma_f32_16x16x32_bf16 v[164:167], v[224:227], v[84:87], v[164:167]\n\t"
    "s_waitcnt lgkmcnt(0)\n\t"
    "s_barrier\n\t"
    "s_add_u32 m0, s47, 0x24000\n\t"
    "s_lshr_b32 s46, s49, 9\n\t"
    "s_add_u32 s46, s46, s58\n\t"
    "s_add_u32 s46, s46, 0x300000\n\t"
    "s_and_b32 s46, s46, 0x3fffff\n\t"
    "buffer_load_dwordx4 v134, %[srdwq], s46 offen lds\n\t"
    "buffer_load_dwordx4 v135, %[srdwq], s46 offen offset:1024 lds\n\t"
    "buffer_load_dwordx4 v136, %[srdwq], s46 offen offset:2048 lds\n\t"
    "buffer_load_dwordx4 v137, %[srdwq], s46 offen offset:3072 lds\n\t"
    "s_add_u32 m0, s47, 0x0\n\t"
    "s_add_u32 s46, s59, 0x4000\n\t"
    "s_and_b32 s46, s46, 0x1ffff\n\t"
    "s_or_b32 s46, s46, s49\n\t"
    "buffer_load_dwordx4 v128, %[srdk], s46 offen nt lds\n\t"
    "s_waitcnt vmcnt(12)\n\t"
    "buffer_load_dwordx4 v129, %[srdk], s46 offen offset:1024 nt lds\n\t"
    "s_waitcnt vmcnt(12)\n\t"
    "buffer_load_dwordx4 v130, %[srdk], s46 offen offset:2048 nt lds\n\t"
    "s_waitcnt vmcnt(12)\n\t"
    "buffer_load_dwordx4 v131, %[srdk], s46 offen offset:3072 nt lds\n\t"
    "ds_read_b64_tr_b16 v[216:217], v248 offset:1024\n\t"
    "ds_read_b64_tr_b16 v[218:219], v248 offset:1280\n\t"
    "v_mfma_f32_16x16x32_bf16 v[168:171], v[232:235], v[80:83], v[168:171]\n\t"
    "ds_read_b64_tr_b16 v[220:221], v248 offset:9216\n\t"
    "ds_read_b64_tr_b16 v[222:223], v248 offset:9472\n\t"
    "v_mfma_f32_16x16x32_bf16 v[168:171], v[236:239], v[88:91], v[168:171]\n\t"
    "ds_read_b64_tr_b16 v[224:225], v249 offset:1024\n\t"
    "ds_read_b64_tr_b16 v[226:227], v249 offset:1280\n\t"
    "v_mfma_f32_16x16x32_bf16 v[176:179], v[236:239], v[92:95], v[176:179]\n\t"
    "ds_read_b64_tr_b16 v[228:229], v249 offset:9216\n\t"
    "ds_read_b64_tr_b16 v[230:231], v249 offset:9472\n\t"
    "v_mfma_f32_16x16x32_bf16 v[176:179], v[232:235], v[84:87], v[176:179]\n\t"
    "ds_read_b64_tr_b16 v[232:233], v248 offset:1536\n\t"
    "v_mfma_f32_16x16x32_bf16 v[172:175], v[240:243], v[80:83], v[172:175]\n\t"
    "ds_read_b64_tr_b16 v[234:235], v248 offset:1792\n\t"
    "v_mfma_f32_16x16x32_bf16 v[172:175], v[244:247], v[88:91], v[172:175]\n\t"
    "ds_read_b64_tr_b16 v[236:237], v248 offset:9728\n\t"
    "v_mfma_f32_16x16x32_bf16 v[180:183], v[244:247], v[92:95], v[180:183]\n\t"
    "ds_read_b64_tr_b16 v[238:239], v248 offset:9984\n\t"
    "v_mfma_f32_16x16x32_bf16 v[180:183], v[240:243], v[84:87], v[180:183]\n\t"
    "ds_read_b64_tr_b16 v[240:241], v249 offset:1536\n\t"
    "s_waitcnt lgkmcnt(9)\n\t"
    "v_mfma_f32_16x16x32_bf16 v[184:187], v[216:219], v[80:83], v[184:187]\n\t"
    "ds_read_b64_tr_b16 v[242:243], v249 offset:1792\n\t"
    "v_mfma_f32_16x16x32_bf16 v[184:187], v[220:223], v[88:91], v[184:187]\n\t"
    "ds_read_b64_tr_b16 v[244:245], v249 offset:9728\n\t"
    "v_mfma_f32_16x16x32_bf16 v[192:195], v[220:223], v[92:95], v[192:195]\n\t"
    "ds_read_b64_tr_b16 v[246:247], v249 offset:9984\n\t"
    "v_mfma_f32_16x16x32_bf16 v[192:195], v[216:219], v[84:87], v[192:195]\n\t"
    "s_waitcnt lgkmcnt(8)\n\t"
    "v_mfma_f32_16x16x32_bf16 v[188:191], v[224:227], v[80:83], v[188:191]\n\t"
    "v_mfma_f32_16x16x32_bf16 v[188:191], v[228:231], v[88:91], v[188:191]\n\t"
    "v_mfma_f32_16x16x32_bf16 v[196:199], v[228:231], v[92:95], v[196:199]\n\t"
    "v_mfma_f32_16x16x32_bf16 v[196:199], v[224:227], v[84:87], v[196:199]\n\t"
    "s_waitcnt vmcnt(4)\n\t"
    "s_waitcnt lgkmcnt(0)\n\t"
    "s_barrier\n\t"
    "s_add_u32 m0, s47, 0x4000\n\t"
    "s_add_u32 s46, s59, 0x8000\n\t"
    "s_and_b32 s46, s46, 0x1ffff\n\t"
    "s_or_b32 s46, s46, s49\n\t"
    "buffer_load_dwordx4 v128, %[srdk], s46 offen nt lds\n\t"
    "buffer_load_dwordx4 v129, %[srdk], s46 offen offset:1024 nt lds\n\t"
    "buffer_load_dwordx4 v130, %[srdk], s46 offen offset:2048 nt lds\n\t"
    "buffer_load_dwordx4 v131, %[srdk], s46 offen offset:3072 nt lds\n\t"
    "ds_read_b64_tr_b16 v[216:217], v248 offset:16384\n\t"
    "ds_read_b64_tr_b16 v[218:219], v248 offset:16640\n\t"
    "v_mfma_f32_16x16x32_bf16 v[200:203], v[232:235], v[80:83], v[200:203]\n\t"
    "ds_read_b64_tr_b16 v[220:221], v248 offset:24576\n\t"
    "ds_read_b64_tr_b16 v[222:223], v248 offset:24832\n\t"
    "v_mfma_f32_16x16x32_bf16 v[200:203], v[236:239], v[88:91], v[200:203]\n\t"
    "ds_read_b64_tr_b16 v[224:225], v249 offset:16384\n\t"
    "ds_read_b64_tr_b16 v[226:227], v249 offset:16640\n\t"
    "v_mfma_f32_16x16x32_bf16 v[208:211], v[236:239], v[92:95], v[208:211]\n\t"
    "ds_read_b64_tr_b16 v[228:229], v249 offset:24576\n\t"
    "ds_read_b64_tr_b16 v[230:231], v249 offset:24832\n\t"
    "v_mfma_f32_16x16x32_bf16 v[208:211], v[232:235], v[84:87], v[208:211]\n\t"
    "ds_read_b64_tr_b16 v[232:233], v248 offset:16896\n\t"
    "v_mfma_f32_16x16x32_bf16 v[204:207], v[240:243], v[80:83], v[204:207]\n\t"
    "ds_read_b64_tr_b16 v[234:235], v248 offset:17152\n\t"
    "v_mfma_f32_16x16x32_bf16 v[204:207], v[244:247], v[88:91], v[204:207]\n\t"
    "ds_read_b64_tr_b16 v[236:237], v248 offset:25088\n\t"
    "v_mfma_f32_16x16x32_bf16 v[212:215], v[244:247], v[92:95], v[212:215]\n\t"
    "ds_read_b64_tr_b16 v[238:239], v248 offset:25344\n\t"
    "v_mfma_f32_16x16x32_bf16 v[212:215], v[240:243], v[84:87], v[212:215]\n\t"
    "ds_read_b64_tr_b16 v[240:241], v249 offset:16896\n\t"
    "s_waitcnt lgkmcnt(9)\n\t"
    "v_mfma_f32_16x16x32_bf16 v[152:155], v[216:219], v[96:99], v[152:155]\n\t"
    "ds_read_b64_tr_b16 v[242:243], v249 offset:17152\n\t"
    "v_mfma_f32_16x16x32_bf16 v[152:155], v[220:223], v[104:107], v[152:155]\n\t"
    "ds_read_b64_tr_b16 v[244:245], v249 offset:25088\n\t"
    "v_mfma_f32_16x16x32_bf16 v[160:163], v[220:223], v[108:111], v[160:163]\n\t"
    "ds_read_b64_tr_b16 v[246:247], v249 offset:25344\n\t"
    "v_mfma_f32_16x16x32_bf16 v[160:163], v[216:219], v[100:103], v[160:163]\n\t"
    "s_waitcnt lgkmcnt(8)\n\t"
    "v_mfma_f32_16x16x32_bf16 v[156:159], v[224:227], v[96:99], v[156:159]\n\t"
    "v_mfma_f32_16x16x32_bf16 v[156:159], v[228:231], v[104:107], v[156:159]\n\t"
    "v_mfma_f32_16x16x32_bf16 v[164:167], v[228:231], v[108:111], v[164:167]\n\t"
    "v_mfma_f32_16x16x32_bf16 v[164:167], v[224:227], v[100:103], v[164:167]\n\t"
    "s_waitcnt lgkmcnt(0)\n\t"
    "s_barrier\n\t"
    "s_add_u32 m0, s47, 0x20000\n\t"
    "s_lshr_b32 s46, s49, 9\n\t"
    "s_add_u32 s46, s46, s58\n\t"
    "s_add_u32 s46, s46, 0x380000\n\t"
    "s_and_b32 s46, s46, 0x3fffff\n\t"
    "buffer_load_dwordx4 v134, %[srdwq], s46 offen lds\n\t"
    "buffer_load_dwordx4 v135, %[srdwq], s46 offen offset:1024 lds\n\t"
    "buffer_load_dwordx4 v136, %[srdwq], s46 offen offset:2048 lds\n\t"
    "buffer_load_dwordx4 v137, %[srdwq], s46 offen offset:3072 lds\n\t"
    "s_add_u32 m0, s47, 0x14000\n\t"
    "s_add_u32 s46, s59, 0xc000\n\t"
    "s_and_b32 s46, s46, 0x1ffff\n\t"
    "s_or_b32 s46, s46, s49\n\t"
    "buffer_load_dwordx4 v128, %[srdk], s46 offen nt lds\n\t"
    "s_waitcnt vmcnt(12)\n\t"
    "buffer_load_dwordx4 v129, %[srdk], s46 offen offset:1024 nt lds\n\t"
    "s_waitcnt vmcnt(12)\n\t"
    "buffer_load_dwordx4 v130, %[srdk], s46 offen offset:2048 nt lds\n\t"
    "s_waitcnt vmcnt(12)\n\t"
    "buffer_load_dwordx4 v131, %[srdk], s46 offen offset:3072 nt lds\n\t"
    "ds_read_b64_tr_b16 v[216:217], v248 offset:17408\n\t"
    "ds_read_b64_tr_b16 v[218:219], v248 offset:17664\n\t"
    "v_mfma_f32_16x16x32_bf16 v[168:171], v[232:235], v[96:99], v[168:171]\n\t"
    "ds_read_b64_tr_b16 v[220:221], v248 offset:25600\n\t"
    "ds_read_b64_tr_b16 v[222:223], v248 offset:25856\n\t"
    "v_mfma_f32_16x16x32_bf16 v[168:171], v[236:239], v[104:107], v[168:171]\n\t"
    "ds_read_b64_tr_b16 v[224:225], v249 offset:17408\n\t"
    "ds_read_b64_tr_b16 v[226:227], v249 offset:17664\n\t"
    "v_mfma_f32_16x16x32_bf16 v[176:179], v[236:239], v[108:111], v[176:179]\n\t"
    "ds_read_b64_tr_b16 v[228:229], v249 offset:25600\n\t"
    "ds_read_b64_tr_b16 v[230:231], v249 offset:25856\n\t"
    "v_mfma_f32_16x16x32_bf16 v[176:179], v[232:235], v[100:103], v[176:179]\n\t"
    "ds_read_b64_tr_b16 v[232:233], v248 offset:17920\n\t"
    "v_mfma_f32_16x16x32_bf16 v[172:175], v[240:243], v[96:99], v[172:175]\n\t"
    "ds_read_b64_tr_b16 v[234:235], v248 offset:18176\n\t"
    "v_mfma_f32_16x16x32_bf16 v[172:175], v[244:247], v[104:107], v[172:175]\n\t"
    "ds_read_b64_tr_b16 v[236:237], v248 offset:26112\n\t"
    "v_mfma_f32_16x16x32_bf16 v[180:183], v[244:247], v[108:111], v[180:183]\n\t"
    "ds_read_b64_tr_b16 v[238:239], v248 offset:26368\n\t"
    "v_mfma_f32_16x16x32_bf16 v[180:183], v[240:243], v[100:103], v[180:183]\n\t"
    "ds_read_b64_tr_b16 v[240:241], v249 offset:17920\n\t"
    "s_waitcnt lgkmcnt(9)\n\t"
    "v_mfma_f32_16x16x32_bf16 v[184:187], v[216:219], v[96:99], v[184:187]\n\t"
    "ds_read_b64_tr_b16 v[242:243], v249 offset:18176\n\t"
    "v_mfma_f32_16x16x32_bf16 v[184:187], v[220:223], v[104:107], v[184:187]\n\t"
    "ds_read_b64_tr_b16 v[244:245], v249 offset:26112\n\t"
    "v_mfma_f32_16x16x32_bf16 v[192:195], v[220:223], v[108:111], v[192:195]\n\t"
    "ds_read_b64_tr_b16 v[246:247], v249 offset:26368\n\t"
    "v_mfma_f32_16x16x32_bf16 v[192:195], v[216:219], v[100:103], v[192:195]\n\t"
    "s_waitcnt lgkmcnt(8)\n\t"
    "v_mfma_f32_16x16x32_bf16 v[188:191], v[224:227], v[96:99], v[188:191]\n\t"
    "v_mfma_f32_16x16x32_bf16 v[188:191], v[228:231], v[104:107], v[188:191]\n\t"
    "v_mfma_f32_16x16x32_bf16 v[196:199], v[228:231], v[108:111], v[196:199]\n\t"
    "v_mfma_f32_16x16x32_bf16 v[196:199], v[224:227], v[100:103], v[196:199]\n\t"
    "s_waitcnt vmcnt(4)\n\t"
    "s_waitcnt lgkmcnt(0)\n\t"
    "s_barrier\n\t"
    "s_add_u32 m0, s47, 0x18000\n\t"
    "s_add_u32 s46, s59, 0x10000\n\t"
    "s_and_b32 s46, s46, 0x1ffff\n\t"
    "s_or_b32 s46, s46, s49\n\t"
    "buffer_load_dwordx4 v128, %[srdk], s46 offen nt lds\n\t"
    "buffer_load_dwordx4 v129, %[srdk], s46 offen offset:1024 nt lds\n\t"
    "buffer_load_dwordx4 v130, %[srdk], s46 offen offset:2048 nt lds\n\t"
    "buffer_load_dwordx4 v131, %[srdk], s46 offen offset:3072 nt lds\n\t"
    "ds_read_b64_tr_b16 v[216:217], v248 offset:0\n\t"
    "ds_read_b64_tr_b16 v[218:219], v248 offset:256\n\t"
    "v_mfma_f32_16x16x32_bf16 v[200:203], v[232:235], v[96:99], v[200:203]\n\t"
    "ds_read_b64_tr_b16 v[220:221], v248 offset:8192\n\t"
    "ds_read_b64_tr_b16 v[222:223], v248 offset:8448\n\t"
    "v_mfma_f32_16x16x32_bf16 v[200:203], v[236:239], v[104:107], v[200:203]\n\t"
    "ds_read_b64_tr_b16 v[224:225], v249 offset:0\n\t"
    "ds_read_b64_tr_b16 v[226:227], v249 offset:256\n\t"
    "v_mfma_f32_16x16x32_bf16 v[208:211], v[236:239], v[108:111], v[208:211]\n\t"
    "ds_read_b64_tr_b16 v[228:229], v249 offset:8192\n\t"
    "ds_read_b64_tr_b16 v[230:231], v249 offset:8448\n\t"
    "v_mfma_f32_16x16x32_bf16 v[208:211], v[232:235], v[100:103], v[208:211]\n\t"
    "ds_read_b64_tr_b16 v[232:233], v248 offset:512\n\t"
    "v_mfma_f32_16x16x32_bf16 v[204:207], v[240:243], v[96:99], v[204:207]\n\t"
    "ds_read_b64_tr_b16 v[234:235], v248 offset:768\n\t"
    "v_mfma_f32_16x16x32_bf16 v[204:207], v[244:247], v[104:107], v[204:207]\n\t"
    "ds_read_b64_tr_b16 v[236:237], v248 offset:8704\n\t"
    "v_mfma_f32_16x16x32_bf16 v[212:215], v[244:247], v[108:111], v[212:215]\n\t"
    "ds_read_b64_tr_b16 v[238:239], v248 offset:8960\n\t"
    "v_mfma_f32_16x16x32_bf16 v[212:215], v[240:243], v[100:103], v[212:215]\n\t"
    "ds_read_b64_tr_b16 v[240:241], v249 offset:512\n\t"
    "s_waitcnt lgkmcnt(9)\n\t"
    "v_mfma_f32_16x16x32_bf16 v[152:155], v[216:219], v[112:115], v[152:155]\n\t"
    "ds_read_b64_tr_b16 v[242:243], v249 offset:768\n\t"
    "v_mfma_f32_16x16x32_bf16 v[152:155], v[220:223], v[120:123], v[152:155]\n\t"
    "ds_read_b64_tr_b16 v[244:245], v249 offset:8704\n\t"
    "v_mfma_f32_16x16x32_bf16 v[160:163], v[220:223], v[124:127], v[160:163]\n\t"
    "ds_read_b64_tr_b16 v[246:247], v249 offset:8960\n\t"
    "v_mfma_f32_16x16x32_bf16 v[160:163], v[216:219], v[116:119], v[160:163]\n\t"
    "s_waitcnt lgkmcnt(8)\n\t"
    "v_mfma_f32_16x16x32_bf16 v[156:159], v[224:227], v[112:115], v[156:159]\n\t"
    "v_mfma_f32_16x16x32_bf16 v[156:159], v[228:231], v[120:123], v[156:159]\n\t"
    "v_mfma_f32_16x16x32_bf16 v[164:167], v[228:231], v[124:127], v[164:167]\n\t"
    "v_mfma_f32_16x16x32_bf16 v[164:167], v[224:227], v[116:119], v[164:167]\n\t"
    "s_waitcnt lgkmcnt(0)\n\t"
    "s_barrier\n\t"
    "s_add_u32 m0, s47, 0x24000\n\t"
    "s_add_u32 s46, s59, 0x14000\n\t"
    "s_and_b32 s46, s46, 0x1ffff\n\t"
    "s_or_b32 s46, s46, s49\n\t"
    "buffer_load_dwordx4 v128, %[srdk], s46 offen nt lds\n\t"
    "buffer_load_dwordx4 v129, %[srdk], s46 offen offset:1024 nt lds\n\t"
    "buffer_load_dwordx4 v130, %[srdk], s46 offen offset:2048 nt lds\n\t"
    "buffer_load_dwordx4 v131, %[srdk], s46 offen offset:3072 nt lds\n\t"
    "ds_read_b64_tr_b16 v[216:217], v248 offset:1024\n\t"
    "ds_read_b64_tr_b16 v[218:219], v248 offset:1280\n\t"
    "v_mfma_f32_16x16x32_bf16 v[168:171], v[232:235], v[112:115], v[168:171]\n\t"
    "ds_read_b64_tr_b16 v[220:221], v248 offset:9216\n\t"
    "ds_read_b64_tr_b16 v[222:223], v248 offset:9472\n\t"
    "v_mfma_f32_16x16x32_bf16 v[168:171], v[236:239], v[120:123], v[168:171]\n\t"
    "ds_read_b64_tr_b16 v[224:225], v249 offset:1024\n\t"
    "ds_read_b64_tr_b16 v[226:227], v249 offset:1280\n\t"
    "v_mul_f32 v152, 0x3e0293ee, v152\n\t"
    "v_mul_f32 v153, 0x3e0293ee, v153\n\t"
    "v_mfma_f32_16x16x32_bf16 v[176:179], v[236:239], v[124:127], v[176:179]\n\t"
    "ds_read_b64_tr_b16 v[228:229], v249 offset:9216\n\t"
    "ds_read_b64_tr_b16 v[230:231], v249 offset:9472\n\t"
    "v_cvt_pk_bf16_f32 v152, v152, v153\n\t"
    "v_mul_f32 v154, 0x3e0293ee, v154\n\t"
    "v_mfma_f32_16x16x32_bf16 v[176:179], v[232:235], v[116:119], v[176:179]\n\t"
    "ds_read_b64_tr_b16 v[232:233], v248 offset:1536\n\t"
    "v_mul_f32 v155, 0x3e0293ee, v155\n\t"
    "v_cvt_pk_bf16_f32 v153, v154, v155\n\t"
    "v_mul_f32 v156, 0x3e0293ee, v156\n\t"
    "v_mfma_f32_16x16x32_bf16 v[172:175], v[240:243], v[112:115], v[172:175]\n\t"
    "ds_read_b64_tr_b16 v[234:235], v248 offset:1792\n\t"
    "v_mul_f32 v157, 0x3e0293ee, v157\n\t"
    "v_cvt_pk_bf16_f32 v154, v156, v157\n\t"
    "v_mul_f32 v158, 0x3e0293ee, v158\n\t"
    "v_mfma_f32_16x16x32_bf16 v[172:175], v[244:247], v[120:123], v[172:175]\n\t"
    "ds_read_b64_tr_b16 v[236:237], v248 offset:9728\n\t"
    "v_mul_f32 v159, 0x3e0293ee, v159\n\t"
    "v_cvt_pk_bf16_f32 v155, v158, v159\n\t"
    "v_mul_f32 v160, 0x3e0293ee, v160\n\t"
    "v_mfma_f32_16x16x32_bf16 v[180:183], v[244:247], v[124:127], v[180:183]\n\t"
    "ds_read_b64_tr_b16 v[238:239], v248 offset:9984\n\t"
    "v_mul_f32 v161, 0x3e0293ee, v161\n\t"
    "v_cvt_pk_bf16_f32 v156, v160, v161\n\t"
    "v_mul_f32 v162, 0x3e0293ee, v162\n\t"
    "v_mfma_f32_16x16x32_bf16 v[180:183], v[240:243], v[116:119], v[180:183]\n\t"
    "ds_read_b64_tr_b16 v[240:241], v249 offset:1536\n\t"
    "v_mul_f32 v163, 0x3e0293ee, v163\n\t"
    "v_cvt_pk_bf16_f32 v157, v162, v163\n\t"
    "v_mul_f32 v164, 0x3e0293ee, v164\n\t"
    "s_waitcnt lgkmcnt(9)\n\t"
    "v_mfma_f32_16x16x32_bf16 v[184:187], v[216:219], v[112:115], v[184:187]\n\t"
    "ds_read_b64_tr_b16 v[242:243], v249 offset:1792\n\t"
    "v_mul_f32 v165, 0x3e0293ee, v165\n\t"
    "v_cvt_pk_bf16_f32 v158, v164, v165\n\t"
    "v_mul_f32 v166, 0x3e0293ee, v166\n\t"
    "v_mfma_f32_16x16x32_bf16 v[184:187], v[220:223], v[120:123], v[184:187]\n\t"
    "ds_read_b64_tr_b16 v[244:245], v249 offset:9728\n\t"
    "v_mul_f32 v167, 0x3e0293ee, v167\n\t"
    "v_cvt_pk_bf16_f32 v159, v166, v167\n\t"
    "v_permlane16_swap_b32 v152, v154\n\t"
    "v_mfma_f32_16x16x32_bf16 v[192:195], v[220:223], v[124:127], v[192:195]\n\t"
    "ds_read_b64_tr_b16 v[246:247], v249 offset:9984\n\t"
    "v_permlane16_swap_b32 v153, v155\n\t"
    "v_permlane16_swap_b32 v156, v158\n\t"
    "v_permlane16_swap_b32 v157, v159\n\t"
    "v_mfma_f32_16x16x32_bf16 v[192:195], v[216:219], v[116:119], v[192:195]\n\t"
    "v_mul_f32 v168, 0x3e0293ee, v168\n\t"
    "v_mul_f32 v169, 0x3e0293ee, v169\n\t"
    "v_cvt_pk_bf16_f32 v160, v168, v169\n\t"
    "v_mul_f32 v170, 0x3e0293ee, v170\n\t"
    "s_waitcnt lgkmcnt(8)\n\t"
    "v_mfma_f32_16x16x32_bf16 v[188:191], v[224:227], v[112:115], v[188:191]\n\t"
    "v_mul_f32 v171, 0x3e0293ee, v171\n\t"
    "v_cvt_pk_bf16_f32 v161, v170, v171\n\t"
    "v_mul_f32 v172, 0x3e0293ee, v172\n\t"
    "v_mul_f32 v173, 0x3e0293ee, v173\n\t"
    "v_mfma_f32_16x16x32_bf16 v[188:191], v[228:231], v[120:123], v[188:191]\n\t"
    "v_cvt_pk_bf16_f32 v162, v172, v173\n\t"
    "v_mul_f32 v174, 0x3e0293ee, v174\n\t"
    "v_mul_f32 v175, 0x3e0293ee, v175\n\t"
    "v_cvt_pk_bf16_f32 v163, v174, v175\n\t"
    "v_mfma_f32_16x16x32_bf16 v[196:199], v[228:231], v[124:127], v[196:199]\n\t"
    "v_mul_f32 v176, 0x3e0293ee, v176\n\t"
    "v_mul_f32 v177, 0x3e0293ee, v177\n\t"
    "v_cvt_pk_bf16_f32 v164, v176, v177\n\t"
    "v_mul_f32 v178, 0x3e0293ee, v178\n\t"
    "v_mfma_f32_16x16x32_bf16 v[196:199], v[224:227], v[116:119], v[196:199]\n\t"
    "s_waitcnt lgkmcnt(0)\n\t"
    "s_barrier\n\t"
    "s_add_u32 m0, s47, 0x1c000\n\t"
    "s_add_u32 s46, s59, 0x18000\n\t"
    "s_and_b32 s46, s46, 0x1ffff\n\t"
    "s_or_b32 s46, s46, s49\n\t"
    "buffer_load_dwordx4 v128, %[srdk], s46 offen nt lds\n\t"
    "s_waitcnt vmcnt(12)\n\t"
    "buffer_load_dwordx4 v129, %[srdk], s46 offen offset:1024 nt lds\n\t"
    "s_waitcnt vmcnt(12)\n\t"
    "buffer_load_dwordx4 v130, %[srdk], s46 offen offset:2048 nt lds\n\t"
    "s_waitcnt vmcnt(12)\n\t"
    "buffer_load_dwordx4 v131, %[srdk], s46 offen offset:3072 nt lds\n\t"
    "v_mul_f32 v179, 0x3e0293ee, v179\n\t"
    "v_cvt_pk_bf16_f32 v165, v178, v179\n\t"
    "v_mul_f32 v180, 0x3e0293ee, v180\n\t"
    "v_mul_f32 v181, 0x3e0293ee, v181\n\t"
    "v_mfma_f32_16x16x32_bf16 v[200:203], v[232:235], v[112:115], v[200:203]\n\t"
    "v_cvt_pk_bf16_f32 v166, v180, v181\n\t"
    "v_mul_f32 v182, 0x3e0293ee, v182\n\t"
    "v_mul_f32 v183, 0x3e0293ee, v183\n\t"
    "v_cvt_pk_bf16_f32 v167, v182, v183\n\t"
    "v_mfma_f32_16x16x32_bf16 v[200:203], v[236:239], v[120:123], v[200:203]\n\t"
    "v_permlane16_swap_b32 v160, v162\n\t"
    "v_permlane16_swap_b32 v161, v163\n\t"
    "v_permlane16_swap_b32 v164, v166\n\t"
    "v_permlane16_swap_b32 v165, v167\n\t"
    "v_mfma_f32_16x16x32_bf16 v[208:211], v[236:239], v[124:127], v[208:211]\n\t"
    "v_mul_f32 v184, 0x3e0293ee, v184\n\t"
    "v_mul_f32 v185, 0x3e0293ee, v185\n\t"
    "v_cvt_pk_bf16_f32 v168, v184, v185\n\t"
    "v_mul_f32 v186, 0x3e0293ee, v186\n\t"
    "v_mfma_f32_16x16x32_bf16 v[208:211], v[232:235], v[116:119], v[208:211]\n\t"
    "v_mul_f32 v187, 0x3e0293ee, v187\n\t"
    "v_cvt_pk_bf16_f32 v169, v186, v187\n\t"
    "v_mul_f32 v188, 0x3e0293ee, v188\n\t"
    "v_mul_f32 v189, 0x3e0293ee, v189\n\t"
    "v_mfma_f32_16x16x32_bf16 v[204:207], v[240:243], v[112:115], v[204:207]\n\t"
    "v_cvt_pk_bf16_f32 v170, v188, v189\n\t"
    "v_mul_f32 v190, 0x3e0293ee, v190\n\t"
    "v_mul_f32 v191, 0x3e0293ee, v191\n\t"
    "v_cvt_pk_bf16_f32 v171, v190, v191\n\t"
    "v_mfma_f32_16x16x32_bf16 v[204:207], v[244:247], v[120:123], v[204:207]\n\t"
    "v_mul_f32 v192, 0x3e0293ee, v192\n\t"
    "v_mul_f32 v193, 0x3e0293ee, v193\n\t"
    "v_cvt_pk_bf16_f32 v172, v192, v193\n\t"
    "v_mul_f32 v194, 0x3e0293ee, v194\n\t"
    "v_mfma_f32_16x16x32_bf16 v[212:215], v[244:247], v[124:127], v[212:215]\n\t"
    "v_mul_f32 v195, 0x3e0293ee, v195\n\t"
    "v_cvt_pk_bf16_f32 v173, v194, v195\n\t"
    "v_mul_f32 v196, 0x3e0293ee, v196\n\t"
    "v_mul_f32 v197, 0x3e0293ee, v197\n\t"
    "v_mfma_f32_16x16x32_bf16 v[212:215], v[240:243], v[116:119], v[212:215]\n\t"
    "v_cvt_pk_bf16_f32 v174, v196, v197\n\t"
    "v_mul_f32 v198, 0x3e0293ee, v198\n\t"
    "v_mul_f32 v199, 0x3e0293ee, v199\n\t"
    "v_cvt_pk_bf16_f32 v175, v198, v199\n\t"
    "v_permlane16_swap_b32 v168, v170\n\t"
    "v_permlane16_swap_b32 v169, v171\n\t"
    "v_permlane16_swap_b32 v172, v174\n\t"
    "v_permlane16_swap_b32 v173, v175\n\t"
    "v_mul_f32 v200, 0x3e0293ee, v200\n\t"
    "v_mul_f32 v201, 0x3e0293ee, v201\n\t"
    "v_cvt_pk_bf16_f32 v176, v200, v201\n\t"
    "v_mul_f32 v202, 0x3e0293ee, v202\n\t"
    "v_mul_f32 v203, 0x3e0293ee, v203\n\t"
    "v_cvt_pk_bf16_f32 v177, v202, v203\n\t"
    "v_mul_f32 v204, 0x3e0293ee, v204\n\t"
    "v_mul_f32 v205, 0x3e0293ee, v205\n\t"
    "v_cvt_pk_bf16_f32 v178, v204, v205\n\t"
    "v_mul_f32 v206, 0x3e0293ee, v206\n\t"
    "v_mul_f32 v207, 0x3e0293ee, v207\n\t"
    "v_cvt_pk_bf16_f32 v179, v206, v207\n\t"
    "v_mul_f32 v208, 0x3e0293ee, v208\n\t"
    "v_mul_f32 v209, 0x3e0293ee, v209\n\t"
    "v_cvt_pk_bf16_f32 v180, v208, v209\n\t"
    "v_mul_f32 v210, 0x3e0293ee, v210\n\t"
    "v_mul_f32 v211, 0x3e0293ee, v211\n\t"
    "v_cvt_pk_bf16_f32 v181, v210, v211\n\t"
    "v_mul_f32 v212, 0x3e0293ee, v212\n\t"
    "v_mul_f32 v213, 0x3e0293ee, v213\n\t"
    "v_cvt_pk_bf16_f32 v182, v212, v213\n\t"
    "v_mul_f32 v214, 0x3e0293ee, v214\n\t"
    "v_mul_f32 v215, 0x3e0293ee, v215\n\t"
    "v_cvt_pk_bf16_f32 v183, v214, v215\n\t"
    "v_permlane16_swap_b32 v176, v178\n\t"
    "v_permlane16_swap_b32 v177, v179\n\t"
    "v_permlane16_swap_b32 v180, v182\n\t"
    "v_permlane16_swap_b32 v181, v183\n\t"
    "v_mov_b32 v250, %[lds]\n\t"
    "v_bfe_u32 v254, v253, 3, 1\n\t"
    "v_lshl_add_u32 v250, v254, 11, v250\n\t"
    "v_bfe_u32 v254, v253, 0, 3\n\t"
    "v_lshl_add_u32 v250, v254, 6, v250\n\t"
    "v_bfe_u32 v254, v253, 4, 1\n\t"
    "v_lshl_add_u32 v250, v254, 5, v250\n\t"
    "v_bfe_u32 v254, v253, 5, 1\n\t"
    "v_lshl_add_u32 v250, v254, 4, v250\n\t"
    "v_bfe_u32 v254, v253, 3, 1\n\t"
    "v_lshlrev_b32 v254, 4, v254\n\t"
    "v_xor_b32 v250, v250, v254\n\t"
    "s_setprio 0\n\t"
    "s_barrier\n\t"
    "v_add_u32 v250, 0x10000, v250\n\t"
    "ds_read_b128 v[216:219], v250 offset:0\n\t"
    "ds_read_b128 v[220:223], v250 offset:512\n\t"
    "ds_read_b128 v[224:227], v250 offset:1024\n\t"
    "ds_read_b128 v[228:231], v250 offset:1536\n\t"
    "ds_read_b128 v[232:235], v250 offset:4096\n\t"
    "ds_read_b128 v[236:239], v250 offset:4608\n\t"
    "ds_read_b128 v[240:243], v250 offset:5120\n\t"
    "ds_read_b128 v[244:247], v250 offset:5632\n\t"
    "s_waitcnt lgkmcnt(6)\n\t"
    "v_mfma_f32_16x16x32_bf16 v[120:123], v[216:219], v[152:155], 0\n\t"
    "v_mfma_f32_16x16x32_bf16 v[120:123], v[220:223], v[160:163], v[120:123]\n\t"
    "s_waitcnt lgkmcnt(4)\n\t"
    "v_mfma_f32_16x16x32_bf16 v[120:123], v[224:227], v[168:171], v[120:123]\n\t"
    "v_mfma_f32_16x16x32_bf16 v[120:123], v[228:231], v[176:179], v[120:123]\n\t"
    "v_mfma_f32_16x16x32_bf16 v[128:131], v[228:231], v[180:183], 0\n\t"
    "v_mfma_f32_16x16x32_bf16 v[128:131], v[224:227], v[172:175], v[128:131]\n\t"
    "v_mfma_f32_16x16x32_bf16 v[128:131], v[220:223], v[164:167], v[128:131]\n\t"
    "v_mfma_f32_16x16x32_bf16 v[128:131], v[216:219], v[156:159], v[128:131]\n\t"
    "ds_read_b128 v[216:219], v250 offset:8192\n\t"
    "s_waitcnt lgkmcnt(3)\n\t"
    "v_mfma_f32_16x16x32_bf16 v[124:127], v[232:235], v[152:155], 0\n\t"
    "ds_read_b128 v[220:223], v250 offset:8704\n\t"
    "v_mfma_f32_16x16x32_bf16 v[124:127], v[236:239], v[160:163], v[124:127]\n\t"
    "ds_read_b128 v[224:227], v250 offset:9216\n\t"
    "s_waitcnt lgkmcnt(3)\n\t"
    "v_mfma_f32_16x16x32_bf16 v[124:127], v[240:243], v[168:171], v[124:127]\n\t"
    "ds_read_b128 v[228:231], v250 offset:9728\n\t"
    "v_mfma_f32_16x16x32_bf16 v[124:127], v[244:247], v[176:179], v[124:127]\n\t"
    "v_mfma_f32_16x16x32_bf16 v[132:135], v[244:247], v[180:183], 0\n\t"
    "v_mfma_f32_16x16x32_bf16 v[132:135], v[240:243], v[172:175], v[132:135]\n\t"
    "v_mfma_f32_16x16x32_bf16 v[132:135], v[236:239], v[164:167], v[132:135]\n\t"
    "v_mfma_f32_16x16x32_bf16 v[132:135], v[232:235], v[156:159], v[132:135]\n\t"
    "ds_read_b128 v[232:235], v250 offset:12288\n\t"
    "s_waitcnt lgkmcnt(3)\n\t"
    "v_mfma_f32_16x16x32_bf16 v[104:107], v[216:219], v[152:155], 0\n\t"
    "ds_read_b128 v[236:239], v250 offset:12800\n\t"
    "v_mfma_f32_16x16x32_bf16 v[104:107], v[220:223], v[160:163], v[104:107]\n\t"
    "ds_read_b128 v[240:243], v250 offset:13312\n\t"
    "v_max3_f32 v192, v120, v121, v122\n\t"
    "v_max3_f32 v193, v128, v129, v130\n\t"
    "v_max3_f32 v192, v192, v123, v124\n\t"
    "s_waitcnt lgkmcnt(3)\n\t"
    "v_mfma_f32_16x16x32_bf16 v[104:107], v[224:227], v[168:171], v[104:107]\n\t"
    "ds_read_b128 v[244:247], v250 offset:13824\n\t"
    "v_max3_f32 v193, v193, v131, v132\n\t"
    "v_max3_f32 v192, v192, v125, v126\n\t"
    "v_max3_f32 v193, v193, v133, v134\n\t"
    "v_mfma_f32_16x16x32_bf16 v[104:107], v[228:231], v[176:179], v[104:107]\n\t"
    "v_max_f32 v192, v192, v127\n\t"
    "v_max_f32 v193, v193, v135\n\t"
    "v_mov_b32 v194, v192\n\t"
    "v_mov_b32 v195, v193\n\t"
    "v_mfma_f32_16x16x32_bf16 v[112:115], v[228:231], v[180:183], 0\n\t"
    "v_permlane32_swap_b32 v192, v194\n\t"
    "v_permlane32_swap_b32 v193, v195\n\t"
    "v_max_f32 v192, v192, v194\n\t"
    "v_max_f32 v193, v193, v195\n\t"
    "v_mfma_f32_16x16x32_bf16 v[112:115], v[224:227], v[172:175], v[112:115]\n\t"
    "v_mov_b32 v194, v192\n\t"
    "v_mov_b32 v195, v193\n\t"
    "s_nop 0\n\t"
    "v_permlane16_swap_b32 v192, v194\n\t"
    "v_permlane16_swap_b32 v193, v195\n\t"
    "v_mfma_f32_16x16x32_bf16 v[112:115], v[220:223], v[164:167], v[112:115]\n\t"
    "v_max_f32 v192, v192, v194\n\t"
    "v_max_f32 v193, v193, v195\n\t"
    "v_mov_b32 v251, 0\n\t"
    "v_mov_b32 v252, 0\n\t"
    "v_mfma_f32_16x16x32_bf16 v[112:115], v[216:219], v[156:159], v[112:115]\n\t"
    "s_waitcnt lgkmcnt(0)\n\t"
    "s_barrier\n\t"
    "v_mov_b32 v248, %[lds]\n\t"
    "v_bfe_u32 v254, v253, 5, 1\n\t"
    "v_sub_f32 v184, 0xc1c00000, v192\n\t"
    "v_sub_f32 v188, s60, v193\n\t"
    "v_mfma_f32_16x16x32_bf16 v[108:111], v[232:235], v[152:155], 0\n\t"
    "v_lshl_add_u32 v248, v254, 11, v248\n\t"
    "v_bfe_u32 v254, v253, 2, 3\n\t"
    "v_mov_b32 v185, v184\n\t"
    "v_mov_b32 v186, v184\n\t"
    "v_mfma_f32_16x16x32_bf16 v[108:111], v[236:239], v[160:163], v[108:111]\n\t"
    "v_lshl_add_u32 v248, v254, 6, v248\n\t"
    "v_bfe_u32 v254, v253, 4, 1\n\t"
    "v_mov_b32 v187, v184\n\t"
    "v_mov_b32 v189, v188\n\t"
    "v_mfma_f32_16x16x32_bf16 v[108:111], v[240:243], v[168:171], v[108:111]\n\t"
    "v_lshl_add_u32 v248, v254, 5, v248\n\t"
    "v_bfe_u32 v254, v253, 0, 2\n\t"
    "v_mov_b32 v190, v188\n\t"
    "v_mov_b32 v191, v188\n\t"
    "v_mfma_f32_16x16x32_bf16 v[108:111], v[244:247], v[176:179], v[108:111]\n\t"
    "v_lshl_add_u32 v248, v254, 3, v248\n\t"
    "v_xor_b32 v249, 32, v248\n\t"
    "v_add_f32 v120, v184, v120\n\t"
    "v_add_f32 v121, v184, v121\n\t"
    "v_mfma_f32_16x16x32_bf16 v[116:119], v[244:247], v[180:183], 0\n\t"
    "v_add_u32 v250, 0xffff0000, v250\n\t"
    "ds_read_b128 v[216:219], v250 offset:0\n\t"
    "v_add_f32 v122, v184, v122\n\t"
    "v_add_f32 v123, v184, v123\n\t"
    "v_mfma_f32_16x16x32_bf16 v[116:119], v[240:243], v[172:175], v[116:119]\n\t"
    "ds_read_b128 v[220:223], v250 offset:512\n\t"
    "ds_read_b128 v[224:227], v250 offset:1024\n\t"
    "v_add_f32 v124, v184, v124\n\t"
    "v_add_f32 v125, v184, v125\n\t"
    "v_mfma_f32_16x16x32_bf16 v[116:119], v[236:239], v[164:167], v[116:119]\n\t"
    "ds_read_b128 v[228:231], v250 offset:1536\n\t"
    "v_add_f32 v126, v184, v126\n\t"
    "v_add_f32 v127, v184, v127\n\t"
    "v_add_f32 v128, v188, v128\n\t"
    "v_mfma_f32_16x16x32_bf16 v[116:119], v[232:235], v[156:159], v[116:119]\n\t"
    "ds_read_b128 v[232:235], v250 offset:4096\n\t"
    "v_add_f32 v129, v188, v129\n\t"
    "v_add_f32 v130, v188, v130\n\t"
    "v_add_f32 v131, v188, v131\n\t"
    "s_waitcnt lgkmcnt(3)\n\t"
    "v_mfma_f32_16x16x32_bf16 v[136:139], v[216:219], v[152:155], v[184:187]\n\t"
    "ds_read_b128 v[236:239], v250 offset:4608\n\t"
    "v_add_f32 v132, v188, v132\n\t"
    "v_add_f32 v133, v188, v133\n\t"
    "v_add_f32 v134, v188, v134\n\t"
    "v_mfma_f32_16x16x32_bf16 v[136:139], v[220:223], v[160:163], v[136:139]\n\t"
    "ds_read_b128 v[240:243], v250 offset:5120\n\t"
    "v_add_f32 v135, v188, v135\n\t"
    "v_exp_f32 v120, v120\n\t"
    "s_waitcnt lgkmcnt(3)\n\t"
    "v_mfma_f32_16x16x32_bf16 v[136:139], v[224:227], v[168:171], v[136:139]\n\t"
    "ds_read_b128 v[244:247], v250 offset:5632\n\t"
    "v_exp_f32 v121, v121\n\t"
    "v_exp_f32 v122, v122\n\t"
    "v_mfma_f32_16x16x32_bf16 v[136:139], v[228:231], v[176:179], v[136:139]\n\t"
    "v_exp_f32 v123, v123\n\t"
    "v_cvt_pk_bf16_f32 v0, v120, v121\n\t"
    "v_exp_f32 v124, v124\n\t"
    "v_mfma_f32_16x16x32_bf16 v[144:147], v[228:231], v[180:183], v[188:191]\n\t"
    "v_exp_f32 v125, v125\n\t"
    "v_cvt_pk_bf16_f32 v1, v122, v123\n\t"
    "v_exp_f32 v126, v126\n\t"
    "v_mfma_f32_16x16x32_bf16 v[144:147], v[224:227], v[172:175], v[144:147]\n\t"
    "v_exp_f32 v127, v127\n\t"
    "v_cvt_pk_bf16_f32 v2, v124, v125\n\t"
    "v_or3_b32 v251, v251, v0, v1\n\t"
    "v_mfma_f32_16x16x32_bf16 v[144:147], v[220:223], v[164:167], v[144:147]\n\t"
    "v_exp_f32 v128, v128\n\t"
    "v_exp_f32 v129, v129\n\t"
    "v_mfma_f32_16x16x32_bf16 v[144:147], v[216:219], v[156:159], v[144:147]\n\t"
    "ds_read_b128 v[216:219], v250 offset:8192\n\t"
    "v_cvt_pk_bf16_f32 v3, v126, v127\n\t"
    "v_exp_f32 v130, v130\n\t"
    "s_waitcnt lgkmcnt(3)\n\t"
    "v_mfma_f32_16x16x32_bf16 v[140:143], v[232:235], v[152:155], v[184:187]\n\t"
    "ds_read_b128 v[220:223], v250 offset:8704\n\t"
    "v_exp_f32 v131, v131\n\t"
    "v_cvt_pk_bf16_f32 v4, v128, v129\n\t"
    "v_mfma_f32_16x16x32_bf16 v[140:143], v[236:239], v[160:163], v[140:143]\n\t"
    "ds_read_b128 v[224:227], v250 offset:9216\n\t"
    "v_or3_b32 v251, v251, v2, v3\n\t"
    "v_exp_f32 v132, v132\n\t"
    "s_waitcnt lgkmcnt(3)\n\t"
    "v_mfma_f32_16x16x32_bf16 v[140:143], v[240:243], v[168:171], v[140:143]\n\t"
    "ds_read_b128 v[228:231], v250 offset:9728\n\t"
    "v_exp_f32 v133, v133\n\t"
    "v_cvt_pk_bf16_f32 v5, v130, v131\n\t"
    "v_mfma_f32_16x16x32_bf16 v[140:143], v[244:247], v[176:179], v[140:143]\n\t"
    "v_exp_f32 v134, v134\n\t"
    "v_exp_f32 v135, v135\n\t"
    "v_mfma_f32_16x16x32_bf16 v[148:151], v[244:247], v[180:183], v[188:191]\n\t"
    "v_cvt_pk_bf16_f32 v6, v132, v133\n\t"
    "v_or3_b32 v251, v251, v4, v5\n\t"
    "v_cvt_pk_bf16_f32 v7, v134, v135\n\t"
    "v_or3_b32 v251, v251, v6, v7\n\t"
    "v_mfma_f32_16x16x32_bf16 v[148:151], v[240:243], v[172:175], v[148:151]\n\t"
    "v_add_f32 v104, v184, v104\n\t"
    "v_add_f32 v105, v184, v105\n\t"
    "v_add_f32 v106, v184, v106\n\t"
    "v_add_f32 v107, v184, v107\n\t"
    "v_mfma_f32_16x16x32_bf16 v[148:151], v[236:239], v[164:167], v[148:151]\n\t"
    "v_add_f32 v108, v184, v108\n\t"
    "v_add_f32 v109, v184, v109\n\t"
    "v_add_f32 v110, v184, v110\n\t"
    "v_add_f32 v111, v184, v111\n\t"
    "v_mfma_f32_16x16x32_bf16 v[148:151], v[232:235], v[156:159], v[148:151]\n\t"
    "ds_read_b128 v[232:235], v250 offset:12288\n\t"
    "v_add_f32 v112, v188, v112\n\t"
    "v_add_f32 v113, v188, v113\n\t"
    "v_add_f32 v114, v188, v114\n\t"
    "s_waitcnt lgkmcnt(3)\n\t"
    "v_mfma_f32_16x16x32_bf16 v[120:123], v[216:219], v[152:155], v[184:187]\n\t"
    "ds_read_b128 v[236:239], v250 offset:12800\n\t"
    "v_add_f32 v115, v188, v115\n\t"
    "v_add_f32 v116, v188, v116\n\t"
    "v_add_f32 v117, v188, v117\n\t"
    "v_mfma_f32_16x16x32_bf16 v[120:123], v[220:223], v[160:163], v[120:123]\n\t"
    "ds_read_b128 v[240:243], v250 offset:13312\n\t"
    "v_add_f32 v118, v188, v118\n\t"
    "v_add_f32 v119, v188, v119\n\t"
    "v_exp_f32 v104, v104\n\t"
    "s_waitcnt lgkmcnt(3)\n\t"
    "v_mfma_f32_16x16x32_bf16 v[120:123], v[224:227], v[168:171], v[120:123]\n\t"
    "ds_read_b128 v[244:247], v250 offset:13824\n\t"
    "v_exp_f32 v105, v105\n\t"
    "v_exp_f32 v106, v106\n\t"
    "v_mfma_f32_16x16x32_bf16 v[120:123], v[228:231], v[176:179], v[120:123]\n\t"
    "v_exp_f32 v107, v107\n\t"
    "v_cvt_pk_bf16_f32 v8, v104, v105\n\t"
    "v_exp_f32 v108, v108\n\t"
    "v_mfma_f32_16x16x32_bf16 v[128:131], v[228:231], v[180:183], v[188:191]\n\t"
    "v_exp_f32 v109, v109\n\t"
    "v_cvt_pk_bf16_f32 v9, v106, v107\n\t"
    "v_exp_f32 v110, v110\n\t"
    "v_mfma_f32_16x16x32_bf16 v[128:131], v[224:227], v[172:175], v[128:131]\n\t"
    "v_exp_f32 v111, v111\n\t"
    "v_cvt_pk_bf16_f32 v10, v108, v109\n\t"
    "v_or3_b32 v251, v251, v8, v9\n\t"
    "v_mfma_f32_16x16x32_bf16 v[128:131], v[220:223], v[164:167], v[128:131]\n\t"
    "v_exp_f32 v112, v112\n\t"
    "v_exp_f32 v113, v113\n\t"
    "v_mfma_f32_16x16x32_bf16 v[128:131], v[216:219], v[156:159], v[128:131]\n\t"
    "s_waitcnt lgkmcnt(0)\n\t"
    "s_barrier\n\t"
    "ds_read_b128 v[216:219], v250 offset:16384\n\t"
    "v_cvt_pk_bf16_f32 v11, v110, v111\n\t"
    "v_exp_f32 v114, v114\n\t"
    "v_mfma_f32_16x16x32_bf16 v[124:127], v[232:235], v[152:155], v[184:187]\n\t"
    "ds_read_b128 v[220:223], v250 offset:16896\n\t"
    "v_exp_f32 v115, v115\n\t"
    "v_cvt_pk_bf16_f32 v12, v112, v113\n\t"
    "v_mfma_f32_16x16x32_bf16 v[124:127], v[236:239], v[160:163], v[124:127]\n\t"
    "ds_read_b128 v[224:227], v250 offset:17408\n\t"
    "v_or3_b32 v251, v251, v10, v11\n\t"
    "v_exp_f32 v116, v116\n\t"
    "v_mfma_f32_16x16x32_bf16 v[124:127], v[240:243], v[168:171], v[124:127]\n\t"
    "ds_read_b128 v[228:231], v250 offset:17920\n\t"
    "v_exp_f32 v117, v117\n\t"
    "v_cvt_pk_bf16_f32 v13, v114, v115\n\t"
    "v_mfma_f32_16x16x32_bf16 v[124:127], v[244:247], v[176:179], v[124:127]\n\t"
    "v_exp_f32 v118, v118\n\t"
    "v_exp_f32 v119, v119\n\t"
    "v_mfma_f32_16x16x32_bf16 v[132:135], v[244:247], v[180:183], v[188:191]\n\t"
    "v_cvt_pk_bf16_f32 v14, v116, v117\n\t"
    "v_or3_b32 v251, v251, v12, v13\n\t"
    "v_cvt_pk_bf16_f32 v15, v118, v119\n\t"
    "v_or3_b32 v251, v251, v14, v15\n\t"
    "v_mfma_f32_16x16x32_bf16 v[132:135], v[240:243], v[172:175], v[132:135]\n\t"
    "v_exp_f32 v136, v136\n\t"
    "v_exp_f32 v137, v137\n\t"
    "v_mfma_f32_16x16x32_bf16 v[132:135], v[236:239], v[164:167], v[132:135]\n\t"
    "v_exp_f32 v138, v138\n\t"
    "v_exp_f32 v139, v139\n\t"
    "v_mfma_f32_16x16x32_bf16 v[132:135], v[232:235], v[156:159], v[132:135]\n\t"
    "ds_read_b128 v[232:235], v250 offset:20480\n\t"
    "v_cvt_pk_bf16_f32 v16, v136, v137\n\t"
    "v_exp_f32 v140, v140\n\t"
    "s_waitcnt lgkmcnt(3)\n\t"
    "v_mfma_f32_16x16x32_bf16 v[104:107], v[216:219], v[152:155], v[184:187]\n\t"
    "ds_read_b128 v[236:239], v250 offset:20992\n\t"
    "v_exp_f32 v141, v141\n\t"
    "v_cvt_pk_bf16_f32 v17, v138, v139\n\t"
    "v_mfma_f32_16x16x32_bf16 v[104:107], v[220:223], v[160:163], v[104:107]\n\t"
    "ds_read_b128 v[240:243], v250 offset:21504\n\t"
    "v_exp_f32 v142, v142\n\t"
    "v_exp_f32 v143, v143\n\t"
    "s_waitcnt lgkmcnt(3)\n\t"
    "v_mfma_f32_16x16x32_bf16 v[104:107], v[224:227], v[168:171], v[104:107]\n\t"
    "ds_read_b128 v[244:247], v250 offset:22016\n\t"
    "v_cvt_pk_bf16_f32 v18, v140, v141\n\t"
    "v_or3_b32 v251, v251, v16, v17\n\t"
    "v_exp_f32 v144, v144\n\t"
    "v_mfma_f32_16x16x32_bf16 v[104:107], v[228:231], v[176:179], v[104:107]\n\t"
    "v_exp_f32 v145, v145\n\t"
    "v_cvt_pk_bf16_f32 v19, v142, v143\n\t"
    "v_exp_f32 v146, v146\n\t"
    "v_mfma_f32_16x16x32_bf16 v[112:115], v[228:231], v[180:183], v[188:191]\n\t"
    "v_exp_f32 v147, v147\n\t"
    "v_cvt_pk_bf16_f32 v20, v144, v145\n\t"
    "v_or3_b32 v251, v251, v18, v19\n\t"
    "v_mfma_f32_16x16x32_bf16 v[112:115], v[224:227], v[172:175], v[112:115]\n\t"
    "v_exp_f32 v148, v148\n\t"
    "v_exp_f32 v149, v149\n\t"
    "v_mfma_f32_16x16x32_bf16 v[112:115], v[220:223], v[164:167], v[112:115]\n\t"
    "v_cvt_pk_bf16_f32 v21, v146, v147\n\t"
    "v_exp_f32 v150, v150\n\t"
    "v_exp_f32 v151, v151\n\t"
    "v_mfma_f32_16x16x32_bf16 v[112:115], v[216:219], v[156:159], v[112:115]\n\t"
    "ds_read_b128 v[216:219], v250 offset:24576\n\t"
    "v_cvt_pk_bf16_f32 v22, v148, v149\n\t"
    "v_or3_b32 v251, v251, v20, v21\n\t"
    "v_cvt_pk_bf16_f32 v23, v150, v151\n\t"
    "s_waitcnt lgkmcnt(3)\n\t"
    "v_mfma_f32_16x16x32_bf16 v[108:111], v[232:235], v[152:155], v[184:187]\n\t"
    "ds_read_b128 v[220:223], v250 offset:25088\n\t"
    "v_or3_b32 v251, v251, v22, v23\n\t"
    "v_exp_f32 v120, v120\n\t"
    "v_mfma_f32_16x16x32_bf16 v[108:111], v[236:239], v[160:163], v[108:111]\n\t"
    "ds_read_b128 v[224:227], v250 offset:25600\n\t"
    "v_exp_f32 v121, v121\n\t"
    "v_exp_f32 v122, v122\n\t"
    "s_waitcnt lgkmcnt(3)\n\t"
    "v_mfma_f32_16x16x32_bf16 v[108:111], v[240:243], v[168:171], v[108:111]\n\t"
    "ds_read_b128 v[228:231], v250 offset:26112\n\t"
    "v_exp_f32 v123, v123\n\t"
    "v_cvt_pk_bf16_f32 v24, v120, v121\n\t"
    "v_mfma_f32_16x16x32_bf16 v[108:111], v[244:247], v[176:179], v[108:111]\n\t"
    "v_exp_f32 v124, v124\n\t"
    "v_exp_f32 v125, v125\n\t"
    "v_mfma_f32_16x16x32_bf16 v[116:119], v[244:247], v[180:183], v[188:191]\n\t"
    "v_cvt_pk_bf16_f32 v25, v122, v123\n\t"
    "v_exp_f32 v126, v126\n\t"
    "v_exp_f32 v127, v127\n\t"
    "v_mfma_f32_16x16x32_bf16 v[116:119], v[240:243], v[172:175], v[116:119]\n\t"
    "v_cvt_pk_bf16_f32 v26, v124, v125\n\t"
    "v_or3_b32 v251, v251, v24, v25\n\t"
    "v_exp_f32 v128, v128\n\t"
    "v_mfma_f32_16x16x32_bf16 v[116:119], v[236:239], v[164:167], v[116:119]\n\t"
    "v_exp_f32 v129, v129\n\t"
    "v_cvt_pk_bf16_f32 v27, v126, v127\n\t"
    "v_exp_f32 v130, v130\n\t"
    "v_mfma_f32_16x16x32_bf16 v[116:119], v[232:235], v[156:159], v[116:119]\n\t"
    "ds_read_b128 v[232:235], v250 offset:28672\n\t"
    "v_exp_f32 v131, v131\n\t"
    "v_cvt_pk_bf16_f32 v28, v128, v129\n\t"
    "s_waitcnt lgkmcnt(3)\n\t"
    "v_mfma_f32_16x16x32_bf16 v[136:139], v[216:219], v[152:155], v[184:187]\n\t"
    "ds_read_b128 v[236:239], v250 offset:29184\n\t"
    "v_or3_b32 v251, v251, v26, v27\n\t"
    "v_exp_f32 v132, v132\n\t"
    "v_mfma_f32_16x16x32_bf16 v[136:139], v[220:223], v[160:163], v[136:139]\n\t"
    "ds_read_b128 v[240:243], v250 offset:29696\n\t"
    "v_exp_f32 v133, v133\n\t"
    "v_cvt_pk_bf16_f32 v29, v130, v131\n\t"
    "s_waitcnt lgkmcnt(3)\n\t"
    "v_mfma_f32_16x16x32_bf16 v[136:139], v[224:227], v[168:171], v[136:139]\n\t"
    "ds_read_b128 v[244:247], v250 offset:30208\n\t"
    "v_exp_f32 v134, v134\n\t"
    "v_exp_f32 v135, v135\n\t"
    "v_mfma_f32_16x16x32_bf16 v[136:139], v[228:231], v[176:179], v[136:139]\n\t"
    "v_cvt_pk_bf16_f32 v30, v132, v133\n\t"
    "v_or3_b32 v251, v251, v28, v29\n\t"
    "v_cvt_pk_bf16_f32 v31, v134, v135\n\t"
    "v_or3_b32 v251, v251, v30, v31\n\t"
    "v_mfma_f32_16x16x32_bf16 v[144:147], v[228:231], v[180:183], v[188:191]\n\t"
    "v_exp_f32 v104, v104\n\t"
    "v_exp_f32 v105, v105\n\t"
    "v_mfma_f32_16x16x32_bf16 v[144:147], v[224:227], v[172:175], v[144:147]\n\t"
    "v_exp_f32 v106, v106\n\t"
    "v_exp_f32 v107, v107\n\t"
    "v_mfma_f32_16x16x32_bf16 v[144:147], v[220:223], v[164:167], v[144:147]\n\t"
    "v_cvt_pk_bf16_f32 v32, v104, v105\n\t"
    "v_exp_f32 v108, v108\n\t"
    "v_exp_f32 v109, v109\n\t"
    "v_mfma_f32_16x16x32_bf16 v[144:147], v[216:219], v[156:159], v[144:147]\n\t"
    "s_waitcnt vmcnt(12)\n\t"
    "s_waitcnt lgkmcnt(0)\n\t"
    "s_barrier\n\t"
    "v_add_u32 v250, 0x10000, v250\n\t"
    "v_cvt_pk_bf16_f32 v33, v106, v107\n\t"
    "v_exp_f32 v110, v110\n\t"
    "v_mfma_f32_16x16x32_bf16 v[140:143], v[232:235], v[152:155], v[184:187]\n\t"
    "ds_read_b128 v[216:219], v250 offset:16384\n\t"
    "v_exp_f32 v111, v111\n\t"
    "v_cvt_pk_bf16_f32 v34, v108, v109\n\t"
    "v_mfma_f32_16x16x32_bf16 v[140:143], v[236:239], v[160:163], v[140:143]\n\t"
    "ds_read_b128 v[220:223], v250 offset:16896\n\t"
    "v_or3_b32 v251, v251, v32, v33\n\t"
    "v_exp_f32 v112, v112\n\t"
    "v_mfma_f32_16x16x32_bf16 v[140:143], v[240:243], v[168:171], v[140:143]\n\t"
    "ds_read_b128 v[224:227], v250 offset:17408\n\t"
    "v_exp_f32 v113, v113\n\t"
    "v_cvt_pk_bf16_f32 v35, v110, v111\n\t"
    "v_mfma_f32_16x16x32_bf16 v[140:143], v[244:247], v[176:179], v[140:143]\n\t"
    "ds_read_b128 v[228:231], v250 offset:17920\n\t"
    "v_exp_f32 v114, v114\n\t"
    "v_exp_f32 v115, v115\n\t"
    "v_mfma_f32_16x16x32_bf16 v[148:151], v[244:247], v[180:183], v[188:191]\n\t"
    "v_cvt_pk_bf16_f32 v36, v112, v113\n\t"
    "v_or3_b32 v251, v251, v34, v35\n\t"
    "v_exp_f32 v116, v116\n\t"
    "v_mfma_f32_16x16x32_bf16 v[148:151], v[240:243], v[172:175], v[148:151]\n\t"
    "v_exp_f32 v117, v117\n\t"
    "v_cvt_pk_bf16_f32 v37, v114, v115\n\t"
    "v_exp_f32 v118, v118\n\t"
    "v_mfma_f32_16x16x32_bf16 v[148:151], v[236:239], v[164:167], v[148:151]\n\t"
    "v_exp_f32 v119, v119\n\t"
    "v_cvt_pk_bf16_f32 v38, v116, v117\n\t"
    "v_or3_b32 v251, v251, v36, v37\n\t"
    "v_mfma_f32_16x16x32_bf16 v[148:151], v[232:235], v[156:159], v[148:151]\n\t"
    "ds_read_b128 v[232:235], v250 offset:20480\n\t"
    "v_cvt_pk_bf16_f32 v39, v118, v119\n\t"
    "v_or3_b32 v251, v251, v38, v39\n\t"
    "s_waitcnt lgkmcnt(3)\n\t"
    "v_mfma_f32_16x16x32_bf16 v[120:123], v[216:219], v[152:155], v[184:187]\n\t"
    "ds_read_b128 v[236:239], v250 offset:20992\n\t"
    "v_mfma_f32_16x16x32_bf16 v[120:123], v[220:223], v[160:163], v[120:123]\n\t"
    "ds_read_b128 v[240:243], v250 offset:21504\n\t"
    "v_exp_f32 v136, v136\n\t"
    "v_exp_f32 v137, v137\n\t"
    "s_waitcnt lgkmcnt(3)\n\t"
    "v_mfma_f32_16x16x32_bf16 v[120:123], v[224:227], v[168:171], v[120:123]\n\t"
    "ds_read_b128 v[244:247], v250 offset:22016\n\t"
    "v_exp_f32 v138, v138\n\t"
    "v_exp_f32 v139, v139\n\t"
    "v_mfma_f32_16x16x32_bf16 v[120:123], v[228:231], v[176:179], v[120:123]\n\t"
    "v_cvt_pk_bf16_f32 v40, v136, v137\n\t"
    "v_exp_f32 v140, v140\n\t"
    "v_exp_f32 v141, v141\n\t"
    "v_mfma_f32_16x16x32_bf16 v[128:131], v[228:231], v[180:183], v[188:191]\n\t"
    "v_cvt_pk_bf16_f32 v41, v138, v139\n\t"
    "v_exp_f32 v142, v142\n\t"
    "v_exp_f32 v143, v143\n\t"
    "v_mfma_f32_16x16x32_bf16 v[128:131], v[224:227], v[172:175], v[128:131]\n\t"
    "v_cvt_pk_bf16_f32 v42, v140, v141\n\t"
    "v_or3_b32 v251, v251, v40, v41\n\t"
    "v_exp_f32 v144, v144\n\t"
    "v_mfma_f32_16x16x32_bf16 v[128:131], v[220:223], v[164:167], v[128:131]\n\t"
    "v_exp_f32 v145, v145\n\t"
    "v_cvt_pk_bf16_f32 v43, v142, v143\n\t"
    "v_exp_f32 v146, v146\n\t"
    "v_mfma_f32_16x16x32_bf16 v[128:131], v[216:219], v[156:159], v[128:131]\n\t"
    "ds_read_b128 v[216:219], v250 offset:24576\n\t"
    "v_exp_f32 v147, v147\n\t"
    "v_cvt_pk_bf16_f32 v44, v144, v145\n\t"
    "s_waitcnt lgkmcnt(3)\n\t"
    "v_mfma_f32_16x16x32_bf16 v[124:127], v[232:235], v[152:155], v[184:187]\n\t"
    "ds_read_b128 v[220:223], v250 offset:25088\n\t"
    "v_or3_b32 v251, v251, v42, v43\n\t"
    "v_exp_f32 v148, v148\n\t"
    "v_mfma_f32_16x16x32_bf16 v[124:127], v[236:239], v[160:163], v[124:127]\n\t"
    "ds_read_b128 v[224:227], v250 offset:25600\n\t"
    "v_exp_f32 v149, v149\n\t"
    "v_cvt_pk_bf16_f32 v45, v146, v147\n\t"
    "s_waitcnt lgkmcnt(3)\n\t"
    "v_mfma_f32_16x16x32_bf16 v[124:127], v[240:243], v[168:171], v[124:127]\n\t"
    "ds_read_b128 v[228:231], v250 offset:26112\n\t"
    "v_exp_f32 v150, v150\n\t"
    "v_exp_f32 v151, v151\n\t"
    "v_mfma_f32_16x16x32_bf16 v[124:127], v[244:247], v[176:179], v[124:127]\n\t"
    "v_cvt_pk_bf16_f32 v46, v148, v149\n\t"
    "v_or3_b32 v251, v251, v44, v45\n\t"
    "v_cvt_pk_bf16_f32 v47, v150, v151\n\t"
    "v_or3_b32 v251, v251, v46, v47\n\t"
    "v_mfma_f32_16x16x32_bf16 v[132:135], v[244:247], v[180:183], v[188:191]\n\t"
    "v_mfma_f32_16x16x32_bf16 v[132:135], v[240:243], v[172:175], v[132:135]\n\t"
    "v_mfma_f32_16x16x32_bf16 v[132:135], v[236:239], v[164:167], v[132:135]\n\t"
    "v_mfma_f32_16x16x32_bf16 v[132:135], v[232:235], v[156:159], v[132:135]\n\t"
    "ds_read_b128 v[232:235], v250 offset:28672\n\t"
    "s_waitcnt lgkmcnt(3)\n\t"
    "v_mfma_f32_16x16x32_bf16 v[104:107], v[216:219], v[152:155], v[184:187]\n\t"
    "ds_read_b128 v[236:239], v250 offset:29184\n\t"
    "v_mfma_f32_16x16x32_bf16 v[104:107], v[220:223], v[160:163], v[104:107]\n\t"
    "ds_read_b128 v[240:243], v250 offset:29696\n\t"
    "v_exp_f32 v120, v120\n\t"
    "v_exp_f32 v121, v121\n\t"
    "s_waitcnt lgkmcnt(3)\n\t"
    "v_mfma_f32_16x16x32_bf16 v[104:107], v[224:227], v[168:171], v[104:107]\n\t"
    "ds_read_b128 v[244:247], v250 offset:30208\n\t"
    "v_exp_f32 v122, v122\n\t"
    "v_exp_f32 v123, v123\n\t"
    "v_mfma_f32_16x16x32_bf16 v[104:107], v[228:231], v[176:179], v[104:107]\n\t"
    "v_cvt_pk_bf16_f32 v48, v120, v121\n\t"
    "v_exp_f32 v124, v124\n\t"
    "v_exp_f32 v125, v125\n\t"
    "v_mfma_f32_16x16x32_bf16 v[112:115], v[228:231], v[180:183], v[188:191]\n\t"
    "v_cvt_pk_bf16_f32 v49, v122, v123\n\t"
    "v_exp_f32 v126, v126\n\t"
    "v_exp_f32 v127, v127\n\t"
    "v_mfma_f32_16x16x32_bf16 v[112:115], v[224:227], v[172:175], v[112:115]\n\t"
    "v_cvt_pk_bf16_f32 v50, v124, v125\n\t"
    "v_or3_b32 v251, v251, v48, v49\n\t"
    "v_exp_f32 v128, v128\n\t"
    "v_mfma_f32_16x16x32_bf16 v[112:115], v[220:223], v[164:167], v[112:115]\n\t"
    "v_exp_f32 v129, v129\n\t"
    "v_cvt_pk_bf16_f32 v51, v126, v127\n\t"
    "v_exp_f32 v130, v130\n\t"
    "v_mfma_f32_16x16x32_bf16 v[112:115], v[216:219], v[156:159], v[112:115]\n\t"
    "s_waitcnt vmcnt(8)\n\t"
    "s_waitcnt lgkmcnt(0)\n\t"
    "s_barrier\n\t"
    "ds_read_b128 v[216:219], v250 offset:32768\n\t"
    "v_exp_f32 v131, v131\n\t"
    "v_cvt_pk_bf16_f32 v52, v128, v129\n\t"
    "v_mfma_f32_16x16x32_bf16 v[108:111], v[232:235], v[152:155], v[184:187]\n\t"
    "ds_read_b128 v[220:223], v250 offset:33280\n\t"
    "v_or3_b32 v251, v251, v50, v51\n\t"
    "v_exp_f32 v132, v132\n\t"
    "v_mfma_f32_16x16x32_bf16 v[108:111], v[236:239], v[160:163], v[108:111]\n\t"
    "ds_read_b128 v[224:227], v250 offset:33792\n\t"
    "v_exp_f32 v133, v133\n\t"
    "v_cvt_pk_bf16_f32 v53, v130, v131\n\t"
    "v_mfma_f32_16x16x32_bf16 v[108:111], v[240:243], v[168:171], v[108:111]\n\t"
    "ds_read_b128 v[228:231], v250 offset:34304\n\t"
    "v_exp_f32 v134, v134\n\t"
    "v_exp_f32 v135, v135\n\t"
    "v_mfma_f32_16x16x32_bf16 v[108:111], v[244:247], v[176:179], v[108:111]\n\t"
    "v_cvt_pk_bf16_f32 v54, v132, v133\n\t"
    "v_or3_b32 v251, v251, v52, v53\n\t"
    "v_cvt_pk_bf16_f32 v55, v134, v135\n\t"
    "v_or3_b32 v251, v251, v54, v55\n\t"
    "v_mfma_f32_16x16x32_bf16 v[116:119], v[244:247], v[180:183], v[188:191]\n\t"
    "v_mfma_f32_16x16x32_bf16 v[116:119], v[240:243], v[172:175], v[116:119]\n\t"
    "v_mfma_f32_16x16x32_bf16 v[116:119], v[236:239], v[164:167], v[116:119]\n\t"
    "v_mfma_f32_16x16x32_bf16 v[116:119], v[232:235], v[156:159], v[116:119]\n\t"
    "ds_read_b128 v[232:235], v250 offset:36864\n\t"
    "s_waitcnt lgkmcnt(3)\n\t"
    "v_mfma_f32_16x16x32_bf16 v[136:139], v[216:219], v[152:155], v[184:187]\n\t"
    "ds_read_b128 v[236:239], v250 offset:37376\n\t"
    "v_mfma_f32_16x16x32_bf16 v[136:139], v[220:223], v[160:163], v[136:139]\n\t"
    "ds_read_b128 v[240:243], v250 offset:37888\n\t"
    "v_exp_f32 v104, v104\n\t"
    "v_exp_f32 v105, v105\n\t"
    "s_waitcnt lgkmcnt(3)\n\t"
    "v_mfma_f32_16x16x32_bf16 v[136:139], v[224:227], v[168:171], v[136:139]\n\t"
    "ds_read_b128 v[244:247], v250 offset:38400\n\t"
    "v_exp_f32 v106, v106\n\t"
    "v_exp_f32 v107, v107\n\t"
    "v_mfma_f32_16x16x32_bf16 v[136:139], v[228:231], v[176:179], v[136:139]\n\t"
    "v_cvt_pk_bf16_f32 v56, v104, v105\n\t"
    "v_exp_f32 v108, v108\n\t"
    "v_exp_f32 v109, v109\n\t"
    "v_mfma_f32_16x16x32_bf16 v[144:147], v[228:231], v[180:183], v[188:191]\n\t"
    "v_cvt_pk_bf16_f32 v57, v106, v107\n\t"
    "v_exp_f32 v110, v110\n\t"
    "v_exp_f32 v111, v111\n\t"
    "v_mfma_f32_16x16x32_bf16 v[144:147], v[224:227], v[172:175], v[144:147]\n\t"
    "v_cvt_pk_bf16_f32 v58, v108, v109\n\t"
    "v_or3_b32 v251, v251, v56, v57\n\t"
    "v_exp_f32 v112, v112\n\t"
    "v_mfma_f32_16x16x32_bf16 v[144:147], v[220:223], v[164:167], v[144:147]\n\t"
    "v_exp_f32 v113, v113\n\t"
    "v_cvt_pk_bf16_f32 v59, v110, v111\n\t"
    "v_exp_f32 v114, v114\n\t"
    "v_mfma_f32_16x16x32_bf16 v[144:147], v[216:219], v[156:159], v[144:147]\n\t"
    "ds_read_b128 v[216:219], v250 offset:40960\n\t"
    "v_exp_f32 v115, v115\n\t"
    "v_cvt_pk_bf16_f32 v60, v112, v113\n\t"
    "s_waitcnt lgkmcnt(3)\n\t"
    "v_mfma_f32_16x16x32_bf16 v[140:143], v[232:235], v[152:155], v[184:187]\n\t"
    "ds_read_b128 v[220:223], v250 offset:41472\n\t"
    "v_or3_b32 v251, v251, v58, v59\n\t"
    "v_exp_f32 v116, v116\n\t"
    "v_mfma_f32_16x16x32_bf16 v[140:143], v[236:239], v[160:163], v[140:143]\n\t"
    "ds_read_b128 v[224:227], v250 offset:41984\n\t"
    "v_exp_f32 v117, v117\n\t"
    "v_cvt_pk_bf16_f32 v61, v114, v115\n\t"
    "s_waitcnt lgkmcnt(3)\n\t"
    "v_mfma_f32_16x16x32_bf16 v[140:143], v[240:243], v[168:171], v[140:143]\n\t"
    "ds_read_b128 v[228:231], v250 offset:42496\n\t"
    "v_exp_f32 v118, v118\n\t"
    "v_exp_f32 v119, v119\n\t"
    "v_mfma_f32_16x16x32_bf16 v[140:143], v[244:247], v[176:179], v[140:143]\n\t"
    "v_cvt_pk_bf16_f32 v62, v116, v117\n\t"
    "v_or3_b32 v251, v251, v60, v61\n\t"
    "v_cvt_pk_bf16_f32 v63, v118, v119\n\t"
    "v_or3_b32 v251, v251, v62, v63\n\t"
    "v_mfma_f32_16x16x32_bf16 v[148:151], v[244:247], v[180:183], v[188:191]\n\t"
    "v_mfma_f32_16x16x32_bf16 v[148:151], v[240:243], v[172:175], v[148:151]\n\t"
    "v_mfma_f32_16x16x32_bf16 v[148:151], v[236:239], v[164:167], v[148:151]\n\t"
    "v_mfma_f32_16x16x32_bf16 v[148:151], v[232:235], v[156:159], v[148:151]\n\t"
    "ds_read_b128 v[232:235], v250 offset:45056\n\t"
    "s_waitcnt lgkmcnt(3)\n\t"
    "v_mfma_f32_16x16x32_bf16 v[120:123], v[216:219], v[152:155], v[184:187]\n\t"
    "ds_read_b128 v[236:239], v250 offset:45568\n\t"
    "v_mfma_f32_16x16x32_bf16 v[120:123], v[220:223], v[160:163], v[120:123]\n\t"
    "ds_read_b128 v[240:243], v250 offset:46080\n\t"
    "v_exp_f32 v136, v136\n\t"
    "v_exp_f32 v137, v137\n\t"
    "s_waitcnt lgkmcnt(3)\n\t"
    "v_mfma_f32_16x16x32_bf16 v[120:123], v[224:227], v[168:171], v[120:123]\n\t"
    "ds_read_b128 v[244:247], v250 offset:46592\n\t"
    "v_exp_f32 v138, v138\n\t"
    "v_exp_f32 v139, v139\n\t"
    "v_mfma_f32_16x16x32_bf16 v[120:123], v[228:231], v[176:179], v[120:123]\n\t"
    "v_cvt_pk_bf16_f32 v64, v136, v137\n\t"
    "v_exp_f32 v140, v140\n\t"
    "v_exp_f32 v141, v141\n\t"
    "v_mfma_f32_16x16x32_bf16 v[128:131], v[228:231], v[180:183], v[188:191]\n\t"
    "v_cvt_pk_bf16_f32 v65, v138, v139\n\t"
    "v_exp_f32 v142, v142\n\t"
    "v_exp_f32 v143, v143\n\t"
    "v_mfma_f32_16x16x32_bf16 v[128:131], v[224:227], v[172:175], v[128:131]\n\t"
    "v_cvt_pk_bf16_f32 v66, v140, v141\n\t"
    "v_or3_b32 v251, v251, v64, v65\n\t"
    "v_exp_f32 v144, v144\n\t"
    "v_mfma_f32_16x16x32_bf16 v[128:131], v[220:223], v[164:167], v[128:131]\n\t"
    "v_exp_f32 v145, v145\n\t"
    "v_cvt_pk_bf16_f32 v67, v142, v143\n\t"
    "v_exp_f32 v146, v146\n\t"
    "v_mfma_f32_16x16x32_bf16 v[128:131], v[216:219], v[156:159], v[128:131]\n\t"
    "s_waitcnt vmcnt(4)\n\t"
    "s_waitcnt lgkmcnt(0)\n\t"
    "s_barrier\n\t"
    "v_add_u32 v250, 0x10000, v250\n\t"
    "v_exp_f32 v147, v147\n\t"
    "v_cvt_pk_bf16_f32 v68, v144, v145\n\t"
    "v_mfma_f32_16x16x32_bf16 v[124:127], v[232:235], v[152:155], v[184:187]\n\t"
    "ds_read_b128 v[216:219], v250 offset:16384\n\t"
    "v_or3_b32 v251, v251, v66, v67\n\t"
    "v_exp_f32 v148, v148\n\t"
    "v_mfma_f32_16x16x32_bf16 v[124:127], v[236:239], v[160:163], v[124:127]\n\t"
    "ds_read_b128 v[220:223], v250 offset:16896\n\t"
    "v_exp_f32 v149, v149\n\t"
    "v_cvt_pk_bf16_f32 v69, v146, v147\n\t"
    "v_mfma_f32_16x16x32_bf16 v[124:127], v[240:243], v[168:171], v[124:127]\n\t"
    "ds_read_b128 v[224:227], v250 offset:17408\n\t"
    "v_exp_f32 v150, v150\n\t"
    "v_exp_f32 v151, v151\n\t"
    "v_mfma_f32_16x16x32_bf16 v[124:127], v[244:247], v[176:179], v[124:127]\n\t"
    "ds_read_b128 v[228:231], v250 offset:17920\n\t"
    "v_cvt_pk_bf16_f32 v70, v148, v149\n\t"
    "v_or3_b32 v251, v251, v68, v69\n\t"
    "v_cvt_pk_bf16_f32 v71, v150, v151\n\t"
    "v_mfma_f32_16x16x32_bf16 v[132:135], v[244:247], v[180:183], v[188:191]\n\t"
    "v_or3_b32 v251, v251, v70, v71\n\t"
    "v_mfma_f32_16x16x32_bf16 v[132:135], v[240:243], v[172:175], v[132:135]\n\t"
    "v_mfma_f32_16x16x32_bf16 v[132:135], v[236:239], v[164:167], v[132:135]\n\t"
    "v_mfma_f32_16x16x32_bf16 v[132:135], v[232:235], v[156:159], v[132:135]\n\t"
    "ds_read_b128 v[232:235], v250 offset:20480\n\t"
    "s_waitcnt lgkmcnt(3)\n\t"
    "v_mfma_f32_16x16x32_bf16 v[104:107], v[216:219], v[152:155], v[184:187]\n\t"
    "ds_read_b128 v[236:239], v250 offset:20992\n\t"
    "v_mfma_f32_16x16x32_bf16 v[104:107], v[220:223], v[160:163], v[104:107]\n\t"
    "ds_read_b128 v[240:243], v250 offset:21504\n\t"
    "v_exp_f32 v120, v120\n\t"
    "v_exp_f32 v121, v121\n\t"
    "s_waitcnt lgkmcnt(3)\n\t"
    "v_mfma_f32_16x16x32_bf16 v[104:107], v[224:227], v[168:171], v[104:107]\n\t"
    "ds_read_b128 v[244:247], v250 offset:22016\n\t"
    "v_exp_f32 v122, v122\n\t"
    "v_exp_f32 v123, v123\n\t"
    "v_mfma_f32_16x16x32_bf16 v[104:107], v[228:231], v[176:179], v[104:107]\n\t"
    "v_cvt_pk_bf16_f32 v72, v120, v121\n\t"
    "v_exp_f32 v124, v124\n\t"
    "v_exp_f32 v125, v125\n\t"
    "v_mfma_f32_16x16x32_bf16 v[112:115], v[228:231], v[180:183], v[188:191]\n\t"
    "v_cvt_pk_bf16_f32 v73, v122, v123\n\t"
    "v_exp_f32 v126, v126\n\t"
    "v_exp_f32 v127, v127\n\t"
    "v_mfma_f32_16x16x32_bf16 v[112:115], v[224:227], v[172:175], v[112:115]\n\t"
    "v_cvt_pk_bf16_f32 v74, v124, v125\n\t"
    "v_or3_b32 v251, v251, v72, v73\n\t"
    "v_exp_f32 v128, v128\n\t"
    "v_mfma_f32_16x16x32_bf16 v[112:115], v[220:223], v[164:167], v[112:115]\n\t"
    "v_exp_f32 v129, v129\n\t"
    "v_cvt_pk_bf16_f32 v75, v126, v127\n\t"
    "v_exp_f32 v130, v130\n\t"
    "v_mfma_f32_16x16x32_bf16 v[112:115], v[216:219], v[156:159], v[112:115]\n\t"
    "ds_read_b128 v[216:219], v250 offset:24576\n\t"
    "v_exp_f32 v131, v131\n\t"
    "v_cvt_pk_bf16_f32 v76, v128, v129\n\t"
    "s_waitcnt lgkmcnt(3)\n\t"
    "v_mfma_f32_16x16x32_bf16 v[108:111], v[232:235], v[152:155], v[184:187]\n\t"
    "ds_read_b128 v[220:223], v250 offset:25088\n\t"
    "v_or3_b32 v251, v251, v74, v75\n\t"
    "v_exp_f32 v132, v132\n\t"
    "v_mfma_f32_16x16x32_bf16 v[108:111], v[236:239], v[160:163], v[108:111]\n\t"
    "ds_read_b128 v[224:227], v250 offset:25600\n\t"
    "v_exp_f32 v133, v133\n\t"
    "v_cvt_pk_bf16_f32 v77, v130, v131\n\t"
    "s_waitcnt lgkmcnt(3)\n\t"
    "v_mfma_f32_16x16x32_bf16 v[108:111], v[240:243], v[168:171], v[108:111]\n\t"
    "ds_read_b128 v[228:231], v250 offset:26112\n\t"
    "v_exp_f32 v134, v134\n\t"
    "v_exp_f32 v135, v135\n\t"
    "v_mfma_f32_16x16x32_bf16 v[108:111], v[244:247], v[176:179], v[108:111]\n\t"
    "v_cvt_pk_bf16_f32 v78, v132, v133\n\t"
    "v_or3_b32 v251, v251, v76, v77\n\t"
    "v_cvt_pk_bf16_f32 v79, v134, v135\n\t"
    "v_or3_b32 v251, v251, v78, v79\n\t"
    "v_mfma_f32_16x16x32_bf16 v[116:119], v[244:247], v[180:183], v[188:191]\n\t"
    "v_mfma_f32_16x16x32_bf16 v[116:119], v[240:243], v[172:175], v[116:119]\n\t"
    "v_mfma_f32_16x16x32_bf16 v[116:119], v[236:239], v[164:167], v[116:119]\n\t"
    "v_mfma_f32_16x16x32_bf16 v[116:119], v[232:235], v[156:159], v[116:119]\n\t"
    "ds_read_b128 v[232:235], v250 offset:28672\n\t"
    "s_waitcnt lgkmcnt(3)\n\t"
    "v_mfma_f32_16x16x32_bf16 v[136:139], v[216:219], v[152:155], v[184:187]\n\t"
    "ds_read_b128 v[236:239], v250 offset:29184\n\t"
    "v_mfma_f32_16x16x32_bf16 v[136:139], v[220:223], v[160:163], v[136:139]\n\t"
    "ds_read_b128 v[240:243], v250 offset:29696\n\t"
    "v_exp_f32 v104, v104\n\t"
    "v_exp_f32 v105, v105\n\t"
    "s_waitcnt lgkmcnt(3)\n\t"
    "v_mfma_f32_16x16x32_bf16 v[136:139], v[224:227], v[168:171], v[136:139]\n\t"
    "ds_read_b128 v[244:247], v250 offset:30208\n\t"
    "v_exp_f32 v106, v106\n\t"
    "v_exp_f32 v107, v107\n\t"
    "v_mfma_f32_16x16x32_bf16 v[136:139], v[228:231], v[176:179], v[136:139]\n\t"
    "v_cvt_pk_bf16_f32 v80, v104, v105\n\t"
    "v_exp_f32 v108, v108\n\t"
    "v_exp_f32 v109, v109\n\t"
    "v_mfma_f32_16x16x32_bf16 v[144:147], v[228:231], v[180:183], v[188:191]\n\t"
    "v_cvt_pk_bf16_f32 v81, v106, v107\n\t"
    "v_exp_f32 v110, v110\n\t"
    "v_exp_f32 v111, v111\n\t"
    "v_mfma_f32_16x16x32_bf16 v[144:147], v[224:227], v[172:175], v[144:147]\n\t"
    "v_cvt_pk_bf16_f32 v82, v108, v109\n\t"
    "v_or3_b32 v251, v251, v80, v81\n\t"
    "v_exp_f32 v112, v112\n\t"
    "v_mfma_f32_16x16x32_bf16 v[144:147], v[220:223], v[164:167], v[144:147]\n\t"
    "v_exp_f32 v113, v113\n\t"
    "v_cvt_pk_bf16_f32 v83, v110, v111\n\t"
    "v_exp_f32 v114, v114\n\t"
    "v_mfma_f32_16x16x32_bf16 v[144:147], v[216:219], v[156:159], v[144:147]\n\t"
    "s_waitcnt vmcnt(0)\n\t"
    "s_waitcnt lgkmcnt(0)\n\t"
    "s_barrier\n\t"
    "v_add_u32 v250, 0xffff0000, v250\n\t"
    "v_exp_f32 v115, v115\n\t"
    "v_cvt_pk_bf16_f32 v84, v112, v113\n\t"
    "v_mfma_f32_16x16x32_bf16 v[140:143], v[232:235], v[152:155], v[184:187]\n\t"
    "ds_read_b128 v[216:219], v250 offset:49152\n\t"
    "v_or3_b32 v251, v251, v82, v83\n\t"
    "v_exp_f32 v116, v116\n\t"
    "v_mfma_f32_16x16x32_bf16 v[140:143], v[236:239], v[160:163], v[140:143]\n\t"
    "ds_read_b128 v[220:223], v250 offset:49664\n\t"
    "v_exp_f32 v117, v117\n\t"
    "v_cvt_pk_bf16_f32 v85, v114, v115\n\t"
    "v_mfma_f32_16x16x32_bf16 v[140:143], v[240:243], v[168:171], v[140:143]\n\t"
    "ds_read_b128 v[224:227], v250 offset:50176\n\t"
    "v_exp_f32 v118, v118\n\t"
    "v_exp_f32 v119, v119\n\t"
    "v_mfma_f32_16x16x32_bf16 v[140:143], v[244:247], v[176:179], v[140:143]\n\t"
    "ds_read_b128 v[228:231], v250 offset:50688\n\t"
    "v_cvt_pk_bf16_f32 v86, v116, v117\n\t"
    "v_or3_b32 v251, v251, v84, v85\n\t"
    "v_cvt_pk_bf16_f32 v87, v118, v119\n\t"
    "v_mfma_f32_16x16x32_bf16 v[148:151], v[244:247], v[180:183], v[188:191]\n\t"
    "v_or3_b32 v251, v251, v86, v87\n\t"
    "v_mfma_f32_16x16x32_bf16 v[148:151], v[240:243], v[172:175], v[148:151]\n\t"
    "v_mfma_f32_16x16x32_bf16 v[148:151], v[236:239], v[164:167], v[148:151]\n\t"
    "v_mfma_f32_16x16x32_bf16 v[148:151], v[232:235], v[156:159], v[148:151]\n\t"
    "ds_read_b128 v[232:235], v250 offset:53248\n\t"
    "s_waitcnt lgkmcnt(3)\n\t"
    "v_mfma_f32_16x16x32_bf16 v[120:123], v[216:219], v[152:155], v[184:187]\n\t"
    "ds_read_b128 v[236:239], v250 offset:53760\n\t"
    "v_mfma_f32_16x16x32_bf16 v[120:123], v[220:223], v[160:163], v[120:123]\n\t"
    "ds_read_b128 v[240:243], v250 offset:54272\n\t"
    "v_exp_f32 v136, v136\n\t"
    "v_exp_f32 v137, v137\n\t"
    "s_waitcnt lgkmcnt(3)\n\t"
    "v_mfma_f32_16x16x32_bf16 v[120:123], v[224:227], v[168:171], v[120:123]\n\t"
    "ds_read_b128 v[244:247], v250 offset:54784\n\t"
    "v_exp_f32 v138, v138\n\t"
    "v_exp_f32 v139, v139\n\t"
    "v_mfma_f32_16x16x32_bf16 v[120:123], v[228:231], v[176:179], v[120:123]\n\t"
    "v_cvt_pk_bf16_f32 v88, v136, v137\n\t"
    "v_exp_f32 v140, v140\n\t"
    "v_exp_f32 v141, v141\n\t"
    "v_mfma_f32_16x16x32_bf16 v[128:131], v[228:231], v[180:183], v[188:191]\n\t"
    "v_cvt_pk_bf16_f32 v89, v138, v139\n\t"
    "v_exp_f32 v142, v142\n\t"
    "v_exp_f32 v143, v143\n\t"
    "v_mfma_f32_16x16x32_bf16 v[128:131], v[224:227], v[172:175], v[128:131]\n\t"
    "v_cvt_pk_bf16_f32 v90, v140, v141\n\t"
    "v_or3_b32 v251, v251, v88, v89\n\t"
    "v_exp_f32 v144, v144\n\t"
    "v_mfma_f32_16x16x32_bf16 v[128:131], v[220:223], v[164:167], v[128:131]\n\t"
    "v_exp_f32 v145, v145\n\t"
    "v_cvt_pk_bf16_f32 v91, v142, v143\n\t"
    "v_exp_f32 v146, v146\n\t"
    "v_mfma_f32_16x16x32_bf16 v[128:131], v[216:219], v[156:159], v[128:131]\n\t"
    "ds_read_b128 v[216:219], v250 offset:57344\n\t"
    "v_exp_f32 v147, v147\n\t"
    "v_cvt_pk_bf16_f32 v92, v144, v145\n\t"
    "s_waitcnt lgkmcnt(3)\n\t"
    "v_mfma_f32_16x16x32_bf16 v[124:127], v[232:235], v[152:155], v[184:187]\n\t"
    "ds_read_b128 v[220:223], v250 offset:57856\n\t"
    "v_or3_b32 v251, v251, v90, v91\n\t"
    "v_exp_f32 v148, v148\n\t"
    "v_mfma_f32_16x16x32_bf16 v[124:127], v[236:239], v[160:163], v[124:127]\n\t"
    "ds_read_b128 v[224:227], v250 offset:58368\n\t"
    "v_exp_f32 v149, v149\n\t"
    "v_cvt_pk_bf16_f32 v93, v146, v147\n\t"
    "s_waitcnt lgkmcnt(3)\n\t"
    "v_mfma_f32_16x16x32_bf16 v[124:127], v[240:243], v[168:171], v[124:127]\n\t"
    "ds_read_b128 v[228:231], v250 offset:58880\n\t"
    "v_exp_f32 v150, v150\n\t"
    "v_exp_f32 v151, v151\n\t"
    "v_mfma_f32_16x16x32_bf16 v[124:127], v[244:247], v[176:179], v[124:127]\n\t"
    "v_cvt_pk_bf16_f32 v94, v148, v149\n\t"
    "v_or3_b32 v251, v251, v92, v93\n\t"
    "v_cvt_pk_bf16_f32 v95, v150, v151\n\t"
    "v_or3_b32 v251, v251, v94, v95\n\t"
    "v_mfma_f32_16x16x32_bf16 v[132:135], v[244:247], v[180:183], v[188:191]\n\t"
    "v_mfma_f32_16x16x32_bf16 v[132:135], v[240:243], v[172:175], v[132:135]\n\t"
    "v_mfma_f32_16x16x32_bf16 v[132:135], v[236:239], v[164:167], v[132:135]\n\t"
    "v_mfma_f32_16x16x32_bf16 v[132:135], v[232:235], v[156:159], v[132:135]\n\t"
    "ds_read_b128 v[232:235], v250 offset:61440\n\t"
    "s_waitcnt lgkmcnt(3)\n\t"
    "v_mfma_f32_16x16x32_bf16 v[104:107], v[216:219], v[152:155], v[184:187]\n\t"
    "ds_read_b128 v[236:239], v250 offset:61952\n\t"
    "v_mfma_f32_16x16x32_bf16 v[104:107], v[220:223], v[160:163], v[104:107]\n\t"
    "ds_read_b128 v[240:243], v250 offset:62464\n\t"
    "v_exp_f32 v120, v120\n\t"
    "v_exp_f32 v121, v121\n\t"
    "s_waitcnt lgkmcnt(3)\n\t"
    "v_mfma_f32_16x16x32_bf16 v[104:107], v[224:227], v[168:171], v[104:107]\n\t"
    "ds_read_b128 v[244:247], v250 offset:62976\n\t"
    "v_exp_f32 v122, v122\n\t"
    "v_exp_f32 v123, v123\n\t"
    "v_mfma_f32_16x16x32_bf16 v[104:107], v[228:231], v[176:179], v[104:107]\n\t"
    "v_cvt_pk_bf16_f32 v96, v120, v121\n\t"
    "v_exp_f32 v124, v124\n\t"
    "v_exp_f32 v125, v125\n\t"
    "v_mfma_f32_16x16x32_bf16 v[112:115], v[228:231], v[180:183], v[188:191]\n\t"
    "v_cvt_pk_bf16_f32 v97, v122, v123\n\t"
    "v_exp_f32 v126, v126\n\t"
    "v_exp_f32 v127, v127\n\t"
    "v_mfma_f32_16x16x32_bf16 v[112:115], v[224:227], v[172:175], v[112:115]\n\t"
    "v_cvt_pk_bf16_f32 v98, v124, v125\n\t"
    "v_or3_b32 v251, v251, v96, v97\n\t"
    "v_exp_f32 v128, v128\n\t"
    "v_mfma_f32_16x16x32_bf16 v[112:115], v[220:223], v[164:167], v[112:115]\n\t"
    "v_exp_f32 v129, v129\n\t"
    "v_cvt_pk_bf16_f32 v99, v126, v127\n\t"
    "v_exp_f32 v130, v130\n\t"
    "v_mfma_f32_16x16x32_bf16 v[112:115], v[216:219], v[156:159], v[112:115]\n\t"
    "s_waitcnt lgkmcnt(0)\n\t"
    "s_barrier\n\t"
    "v_add_u32 v250, 0xffff0000, v250\n\t"
    "v_exp_f32 v131, v131\n\t"
    "v_cvt_pk_bf16_f32 v100, v128, v129\n\t"
    "v_mfma_f32_16x16x32_bf16 v[108:111], v[232:235], v[152:155], v[184:187]\n\t"
    "ds_read_b128 v[216:219], v250 offset:32768\n\t"
    "v_or3_b32 v251, v251, v98, v99\n\t"
    "v_exp_f32 v132, v132\n\t"
    "v_mfma_f32_16x16x32_bf16 v[108:111], v[236:239], v[160:163], v[108:111]\n\t"
    "ds_read_b128 v[220:223], v250 offset:33280\n\t"
    "v_exp_f32 v133, v133\n\t"
    "v_cvt_pk_bf16_f32 v101, v130, v131\n\t"
    "v_mfma_f32_16x16x32_bf16 v[108:111], v[240:243], v[168:171], v[108:111]\n\t"
    "ds_read_b128 v[224:227], v250 offset:33792\n\t"
    "v_exp_f32 v134, v134\n\t"
    "v_exp_f32 v135, v135\n\t"
    "v_mfma_f32_16x16x32_bf16 v[108:111], v[244:247], v[176:179], v[108:111]\n\t"
    "ds_read_b128 v[228:231], v250 offset:34304\n\t"
    "v_cvt_pk_bf16_f32 v102, v132, v133\n\t"
    "v_or3_b32 v251, v251, v100, v101\n\t"
    "v_cvt_pk_bf16_f32 v103, v134, v135\n\t"
    "v_mfma_f32_16x16x32_bf16 v[116:119], v[244:247], v[180:183], v[188:191]\n\t"
    "v_or3_b32 v251, v251, v102, v103\n\t"
    "v_mfma_f32_16x16x32_bf16 v[116:119], v[240:243], v[172:175], v[116:119]\n\t"
    "v_mfma_f32_16x16x32_bf16 v[116:119], v[236:239], v[164:167], v[116:119]\n\t"
    "v_mfma_f32_16x16x32_bf16 v[116:119], v[232:235], v[156:159], v[116:119]\n\t"
    "ds_read_b128 v[232:235], v250 offset:36864\n\t"
    "s_waitcnt lgkmcnt(3)\n\t"
    "v_mfma_f32_16x16x32_bf16 v[136:139], v[216:219], v[152:155], v[184:187]\n\t"
    "ds_read_b128 v[236:239], v250 offset:37376\n\t"
    "v_mfma_f32_16x16x32_bf16 v[136:139], v[220:223], v[160:163], v[136:139]\n\t"
    "ds_read_b128 v[240:243], v250 offset:37888\n\t"
    "v_exp_f32 v104, v104\n\t"
    "v_exp_f32 v105, v105\n\t"
    "s_waitcnt lgkmcnt(3)\n\t"
    "v_mfma_f32_16x16x32_bf16 v[136:139], v[224:227], v[168:171], v[136:139]\n\t"
    "ds_read_b128 v[244:247], v250 offset:38400\n\t"
    "v_exp_f32 v106, v106\n\t"
    "v_exp_f32 v107, v107\n\t"
    "v_mfma_f32_16x16x32_bf16 v[136:139], v[228:231], v[176:179], v[136:139]\n\t"
    "v_cvt_pk_bf16_f32 v104, v104, v105\n\t"
    "v_exp_f32 v108, v108\n\t"
    "v_exp_f32 v109, v109\n\t"
    "v_mfma_f32_16x16x32_bf16 v[144:147], v[228:231], v[180:183], v[188:191]\n\t"
    "v_cvt_pk_bf16_f32 v105, v106, v107\n\t"
    "v_exp_f32 v110, v110\n\t"
    "v_exp_f32 v111, v111\n\t"
    "v_mfma_f32_16x16x32_bf16 v[144:147], v[224:227], v[172:175], v[144:147]\n\t"
    "v_cvt_pk_bf16_f32 v106, v108, v109\n\t"
    "v_or3_b32 v251, v251, v104, v105\n\t"
    "v_exp_f32 v112, v112\n\t"
    "v_mfma_f32_16x16x32_bf16 v[144:147], v[220:223], v[164:167], v[144:147]\n\t"
    "v_exp_f32 v113, v113\n\t"
    "v_cvt_pk_bf16_f32 v107, v110, v111\n\t"
    "v_exp_f32 v114, v114\n\t"
    "v_mfma_f32_16x16x32_bf16 v[144:147], v[216:219], v[156:159], v[144:147]\n\t"
    "ds_read_b128 v[216:219], v250 offset:40960\n\t"
    "v_exp_f32 v115, v115\n\t"
    "v_cvt_pk_bf16_f32 v108, v112, v113\n\t"
    "s_waitcnt lgkmcnt(3)\n\t"
    "v_mfma_f32_16x16x32_bf16 v[140:143], v[232:235], v[152:155], v[184:187]\n\t"
    "ds_read_b128 v[220:223], v250 offset:41472\n\t"
    "v_or3_b32 v251, v251, v106, v107\n\t"
    "v_exp_f32 v116, v116\n\t"
    "v_mfma_f32_16x16x32_bf16 v[140:143], v[236:239], v[160:163], v[140:143]\n\t"
    "ds_read_b128 v[224:227], v250 offset:41984\n\t"
    "v_exp_f32 v117, v117\n\t"
    "v_cvt_pk_bf16_f32 v109, v114, v115\n\t"
    "s_waitcnt lgkmcnt(3)\n\t"
    "v_mfma_f32_16x16x32_bf16 v[140:143], v[240:243], v[168:171], v[140:143]\n\t"
    "ds_read_b128 v[228:231], v250 offset:42496\n\t"
    "v_exp_f32 v118, v118\n\t"
    "v_exp_f32 v119, v119\n\t"
    "v_mfma_f32_16x16x32_bf16 v[140:143], v[244:247], v[176:179], v[140:143]\n\t"
    "v_cvt_pk_bf16_f32 v110, v116, v117\n\t"
    "v_or3_b32 v251, v251, v108, v109\n\t"
    "v_cvt_pk_bf16_f32 v111, v118, v119\n\t"
    "v_or3_b32 v251, v251, v110, v111\n\t"
    "v_mfma_f32_16x16x32_bf16 v[148:151], v[244:247], v[180:183], v[188:191]\n\t"
    "v_mfma_f32_16x16x32_bf16 v[148:151], v[240:243], v[172:175], v[148:151]\n\t"
    "v_mfma_f32_16x16x32_bf16 v[148:151], v[236:239], v[164:167], v[148:151]\n\t"
    "v_mfma_f32_16x16x32_bf16 v[148:151], v[232:235], v[156:159], v[148:151]\n\t"
    "ds_read_b128 v[232:235], v250 offset:45056\n\t"
    "s_waitcnt lgkmcnt(3)\n\t"
    "v_mfma_f32_16x16x32_bf16 v[120:123], v[216:219], v[152:155], v[184:187]\n\t"
    "ds_read_b128 v[236:239], v250 offset:45568\n\t"
    "v_mfma_f32_16x16x32_bf16 v[120:123], v[220:223], v[160:163], v[120:123]\n\t"
    "ds_read_b128 v[240:243], v250 offset:46080\n\t"
    "v_exp_f32 v136, v136\n\t"
    "v_exp_f32 v137, v137\n\t"
    "s_waitcnt lgkmcnt(3)\n\t"
    "v_mfma_f32_16x16x32_bf16 v[120:123], v[224:227], v[168:171], v[120:123]\n\t"
    "ds_read_b128 v[244:247], v250 offset:46592\n\t"
    "v_exp_f32 v138, v138\n\t"
    "v_exp_f32 v139, v139\n\t"
    "v_mfma_f32_16x16x32_bf16 v[120:123], v[228:231], v[176:179], v[120:123]\n\t"
    "v_cvt_pk_bf16_f32 v112, v136, v137\n\t"
    "v_exp_f32 v140, v140\n\t"
    "v_exp_f32 v141, v141\n\t"
    "v_mfma_f32_16x16x32_bf16 v[128:131], v[228:231], v[180:183], v[188:191]\n\t"
    "v_cvt_pk_bf16_f32 v113, v138, v139\n\t"
    "v_exp_f32 v142, v142\n\t"
    "v_exp_f32 v143, v143\n\t"
    "v_mfma_f32_16x16x32_bf16 v[128:131], v[224:227], v[172:175], v[128:131]\n\t"
    "v_cvt_pk_bf16_f32 v114, v140, v141\n\t"
    "v_or3_b32 v251, v251, v112, v113\n\t"
    "v_exp_f32 v144, v144\n\t"
    "v_mfma_f32_16x16x32_bf16 v[128:131], v[220:223], v[164:167], v[128:131]\n\t"
    "v_exp_f32 v145, v145\n\t"
    "v_cvt_pk_bf16_f32 v115, v142, v143\n\t"
    "v_exp_f32 v146, v146\n\t"
    "v_mfma_f32_16x16x32_bf16 v[128:131], v[216:219], v[156:159], v[128:131]\n\t"
    "s_waitcnt lgkmcnt(0)\n\t"
    "s_barrier\n\t"
    "v_add_u32 v248, 0x20000, v248\n\t"
    "v_add_u32 v249, 0x20000, v249\n\t"
    "v_exp_f32 v147, v147\n\t"
    "v_mfma_f32_16x16x32_bf16 v[124:127], v[232:235], v[152:155], v[184:187]\n\t"
    "ds_read_b64_tr_b16 v[216:217], v248 offset:0\n\t"
    "ds_read_b64_tr_b16 v[218:219], v248 offset:4096\n\t"
    "v_cvt_pk_bf16_f32 v116, v144, v145\n\t"
    "v_or3_b32 v251, v251, v114, v115\n\t"
    "v_mfma_f32_16x16x32_bf16 v[124:127], v[236:239], v[160:163], v[124:127]\n\t"
    "ds_read_b64_tr_b16 v[220:221], v248 offset:8192\n\t"
    "v_exp_f32 v148, v148\n\t"
    "v_exp_f32 v149, v149\n\t"
    "v_mfma_f32_16x16x32_bf16 v[124:127], v[240:243], v[168:171], v[124:127]\n\t"
    "ds_read_b64_tr_b16 v[222:223], v248 offset:12288\n\t"
    "v_cvt_pk_bf16_f32 v117, v146, v147\n\t"
    "v_exp_f32 v150, v150\n\t"
    "v_mfma_f32_16x16x32_bf16 v[124:127], v[244:247], v[176:179], v[124:127]\n\t"
    "ds_read_b64_tr_b16 v[224:225], v249 offset:0\n\t"
    "v_exp_f32 v151, v151\n\t"
    "v_cvt_pk_bf16_f32 v118, v148, v149\n\t"
    "v_mfma_f32_16x16x32_bf16 v[132:135], v[244:247], v[180:183], v[188:191]\n\t"
    "ds_read_b64_tr_b16 v[226:227], v249 offset:4096\n\t"
    "v_or3_b32 v251, v251, v116, v117\n\t"
    "v_cvt_pk_bf16_f32 v119, v150, v151\n\t"
    "v_or3_b32 v251, v251, v118, v119\n\t"
    "v_mfma_f32_16x16x32_bf16 v[132:135], v[240:243], v[172:175], v[132:135]\n\t"
    "ds_read_b64_tr_b16 v[228:229], v249 offset:8192\n\t"
    "v_mfma_f32_16x16x32_bf16 v[132:135], v[236:239], v[164:167], v[132:135]\n\t"
    "ds_read_b64_tr_b16 v[230:231], v249 offset:12288\n\t"
    "v_mfma_f32_16x16x32_bf16 v[132:135], v[232:235], v[156:159], v[132:135]\n\t"
    "ds_read_b64_tr_b16 v[232:233], v248 offset:512\n\t"
    "v_exp_f32 v120, v120\n\t"
    "v_exp_f32 v121, v121\n\t"
    "v_exp_f32 v122, v122\n\t"
    "v_exp_f32 v123, v123\n\t"
    "v_cvt_pk_bf16_f32 v120, v120, v121\n\t"
    "v_exp_f32 v124, v124\n\t"
    "v_exp_f32 v125, v125\n\t"
    "v_cvt_pk_bf16_f32 v121, v122, v123\n\t"
    "v_exp_f32 v126, v126\n\t"
    "v_exp_f32 v127, v127\n\t"
    "v_cvt_pk_bf16_f32 v122, v124, v125\n\t"
    "v_or3_b32 v251, v251, v120, v121\n\t"
    "v_exp_f32 v128, v128\n\t"
    "v_exp_f32 v129, v129\n\t"
    "v_cvt_pk_bf16_f32 v123, v126, v127\n\t"
    "v_exp_f32 v130, v130\n\t"
    "v_exp_f32 v131, v131\n\t"
    "v_cvt_pk_bf16_f32 v124, v128, v129\n\t"
    "v_or3_b32 v251, v251, v122, v123\n\t"
    "v_exp_f32 v132, v132\n\t"
    "v_exp_f32 v133, v133\n\t"
    "v_cvt_pk_bf16_f32 v125, v130, v131\n\t"
    "v_exp_f32 v134, v134\n\t"
    "v_exp_f32 v135, v135\n\t"
    "v_cvt_pk_bf16_f32 v126, v132, v133\n\t"
    "v_or3_b32 v251, v251, v124, v125\n\t"
    "v_cvt_pk_bf16_f32 v127, v134, v135\n\t"
    "v_or3_b32 v251, v251, v126, v127\n\t"
    "v_and_b32 v251, 0x40004000, v251\n\t"
    "v_cmp_ne_u32 vcc, 0, v251\n\t"
    "s_nop 3\n\t"
    "s_cmp_lg_u64 vcc, 0\n\t"
    "s_cbranch_scc1 FB%=\n\t"
    "FBRET1_%=:\n\t"
    "v_and_b32 v138, 3, v253\n\t"
    "v_cmp_eq_u32 vcc, 0, v138\n\t"
    "v_mov_b32 v139, 0x3f803f80\n\t"
    "v_mov_b32 v140, 0\n\t"
    "v_cndmask_b32 v138, v140, v139, vcc\n\t"
    "v_mov_b32 v139, v138\n\t"
    "v_mov_b32 v140, v138\n\t"
    "v_mov_b32 v141, v138\n\t"
    "s_waitcnt lgkmcnt(5)\n\t"
    "v_mfma_f32_16x16x32_bf16 v[152:155], v[216:219], v[0:3], 0\n\t"
    "ds_read_b64_tr_b16 v[234:235], v248 offset:4608\n\t"
    "ds_read_b64_tr_b16 v[236:237], v248 offset:8704\n\t"
    "ds_read_b64_tr_b16 v[238:239], v248 offset:12800\n\t"
    "v_mfma_f32_16x16x32_bf16 v[152:155], v[220:223], v[8:11], v[152:155]\n\t"
    "ds_read_b64_tr_b16 v[240:241], v249 offset:512\n\t"
    "ds_read_b64_tr_b16 v[242:243], v249 offset:4608\n\t"
    "v_mfma_f32_16x16x32_bf16 v[160:163], v[220:223], v[12:15], 0\n\t"
    "ds_read_b64_tr_b16 v[244:245], v249 offset:8704\n\t"
    "ds_read_b64_tr_b16 v[246:247], v249 offset:12800\n\t"
    "v_mfma_f32_16x16x32_bf16 v[160:163], v[216:219], v[4:7], v[160:163]\n\t"
    "v_mfma_f32_16x16x32_bf16 v[144:147], v[138:141], v[0:3], 0\n\t"
    "ds_read_b64_tr_b16 v[216:217], v248 offset:1024\n\t"
    "s_waitcnt lgkmcnt(9)\n\t"
    "v_mfma_f32_16x16x32_bf16 v[156:159], v[224:227], v[0:3], 0\n\t"
    "ds_read_b64_tr_b16 v[218:219], v248 offset:5120\n\t"
    "v_mfma_f32_16x16x32_bf16 v[156:159], v[228:231], v[8:11], v[156:159]\n\t"
    "ds_read_b64_tr_b16 v[220:221], v248 offset:9216\n\t"
    "v_mfma_f32_16x16x32_bf16 v[164:167], v[228:231], v[12:15], 0\n\t"
    "ds_read_b64_tr_b16 v[222:223], v248 offset:13312\n\t"
    "v_mfma_f32_16x16x32_bf16 v[164:167], v[224:227], v[4:7], v[164:167]\n\t"
    "v_lshlrev_b32 v142, 4, v253\n\t"
    "s_lshl_b32 s52, s50, 12\n\t"
    "v_mfma_f32_16x16x32_bf16 v[148:151], v[138:141], v[4:7], 0\n\t"
    "ds_read_b64_tr_b16 v[224:225], v249 offset:1024\n\t"
    "v_add_u32 v142, s52, v142\n\t"
    "s_waitcnt lgkmcnt(9)\n\t"
    "v_mfma_f32_16x16x32_bf16 v[168:171], v[232:235], v[0:3], 0\n\t"
    "ds_read_b64_tr_b16 v[226:227], v249 offset:5120\n\t"
    "v_or_b32 v143, s61, v142\n\t"
    "v_mfma_f32_16x16x32_bf16 v[168:171], v[236:239], v[8:11], v[168:171]\n\t"
    "ds_read_b64_tr_b16 v[228:229], v249 offset:9216\n\t"
    "v_bfe_u32 v254, v253, 2, 3\n\t"
    "v_mfma_f32_16x16x32_bf16 v[176:179], v[236:239], v[12:15], 0\n\t"
    "ds_read_b64_tr_b16 v[230:231], v249 offset:13312\n\t"
    "v_lshlrev_b32 v134, 10, v254\n\t"
    "v_mfma_f32_16x16x32_bf16 v[176:179], v[232:235], v[4:7], v[176:179]\n\t"
    "ds_read_b64_tr_b16 v[232:233], v248 offset:1536\n\t"
    "v_bfe_u32 v254, v253, 5, 1\n\t"
    "s_waitcnt lgkmcnt(9)\n\t"
    "v_mfma_f32_16x16x32_bf16 v[172:175], v[240:243], v[0:3], 0\n\t"
    "ds_read_b64_tr_b16 v[234:235], v248 offset:5632\n\t"
    "v_lshl_add_u32 v134, v254, 6, v134\n\t"
    "v_mfma_f32_16x16x32_bf16 v[172:175], v[244:247], v[8:11], v[172:175]\n\t"
    "ds_read_b64_tr_b16 v[236:237], v248 offset:9728\n\t"
    "v_bfe_u32 v254, v253, 0, 2\n\t"
    "v_mfma_f32_16x16x32_bf16 v[180:183], v[244:247], v[12:15], 0\n\t"
    "ds_read_b64_tr_b16 v[238:239], v248 offset:13824\n\t"
    "v_lshl_add_u32 v134, v254, 4, v134\n\t"
    "v_mfma_f32_16x16x32_bf16 v[180:183], v[240:243], v[4:7], v[180:183]\n\t"
    "v_bfe_u32 v254, v253, 4, 1\n\t"
    "v_lshlrev_b32 v254, 5, v254\n\t"
    "v_mfma_f32_16x16x32_bf16 v[144:147], v[138:141], v[8:11], v[144:147]\n\t"
    "ds_read_b64_tr_b16 v[240:241], v249 offset:1536\n\t"
    "v_xor_b32 v134, v134, v254\n\t"
    "s_waitcnt lgkmcnt(9)\n\t"
    "v_mfma_f32_16x16x32_bf16 v[184:187], v[216:219], v[0:3], 0\n\t"
    "ds_read_b64_tr_b16 v[242:243], v249 offset:5632\n\t"
    "v_xor_b32 v134, %[octo], v134\n\t"
    "v_mfma_f32_16x16x32_bf16 v[184:187], v[220:223], v[8:11], v[184:187]\n\t"
    "ds_read_b64_tr_b16 v[244:245], v249 offset:9728\n\t"
    "s_lshl_b32 s52, s50, 14\n\t"
    "v_mfma_f32_16x16x32_bf16 v[192:195], v[220:223], v[12:15], 0\n\t"
    "ds_read_b64_tr_b16 v[246:247], v249 offset:13824\n\t"
    "v_add_u32 v134, s52, v134\n\t"
    "v_mfma_f32_16x16x32_bf16 v[192:195], v[216:219], v[4:7], v[192:195]\n\t"
    "v_xor_b32 v135, 0x80, v134\n\t"
    "v_add_u32 v136, 0x1c00, v134\n\t"
    "s_waitcnt lgkmcnt(8)\n\t"
    "v_mfma_f32_16x16x32_bf16 v[188:191], v[224:227], v[0:3], 0\n\t"
    "v_add_u32 v137, 0x1800, v135\n\t"
    "v_add_u32 v134, 0x400, v134\n\t"
    "v_mfma_f32_16x16x32_bf16 v[188:191], v[228:231], v[8:11], v[188:191]\n\t"
    "v_mfma_f32_16x16x32_bf16 v[196:199], v[228:231], v[12:15], 0\n\t"
    "v_mfma_f32_16x16x32_bf16 v[196:199], v[224:227], v[4:7], v[196:199]\n\t"
    "v_mfma_f32_16x16x32_bf16 v[148:151], v[138:141], v[12:15], v[148:151]\n\t"
    "s_waitcnt lgkmcnt(0)\n\t"
    "s_barrier\n\t"
    "v_add_u32 v248, 0xfffe0000, v248\n\t"
    "v_add_u32 v249, 0xfffe0000, v249\n\t"
    "ds_read_b64_tr_b16 v[216:217], v248 offset:49152\n\t"
    "v_mfma_f32_16x16x32_bf16 v[200:203], v[232:235], v[0:3], 0\n\t"
    "ds_read_b64_tr_b16 v[218:219], v248 offset:53248\n\t"
    "ds_read_b64_tr_b16 v[220:221], v248 offset:57344\n\t"
    "ds_read_b64_tr_b16 v[222:223], v248 offset:61440\n\t"
    "v_mfma_f32_16x16x32_bf16 v[200:203], v[236:239], v[8:11], v[200:203]\n\t"
    "ds_read_b64_tr_b16 v[224:225], v249 offset:49152\n\t"
    "ds_read_b64_tr_b16 v[226:227], v249 offset:53248\n\t"
    "v_mfma_f32_16x16x32_bf16 v[208:211], v[236:239], v[12:15], 0\n\t"
    "ds_read_b64_tr_b16 v[228:229], v249 offset:57344\n\t"
    "ds_read_b64_tr_b16 v[230:231], v249 offset:61440\n\t"
    "v_mfma_f32_16x16x32_bf16 v[208:211], v[232:235], v[4:7], v[208:211]\n\t"
    "v_mfma_f32_16x16x32_bf16 v[144:147], v[138:141], v[16:19], v[144:147]\n\t"
    "ds_read_b64_tr_b16 v[232:233], v248 offset:49664\n\t"
    "v_mfma_f32_16x16x32_bf16 v[204:207], v[240:243], v[0:3], 0\n\t"
    "ds_read_b64_tr_b16 v[234:235], v248 offset:53760\n\t"
    "v_mfma_f32_16x16x32_bf16 v[204:207], v[244:247], v[8:11], v[204:207]\n\t"
    "ds_read_b64_tr_b16 v[236:237], v248 offset:57856\n\t"
    "v_mfma_f32_16x16x32_bf16 v[212:215], v[244:247], v[12:15], 0\n\t"
    "ds_read_b64_tr_b16 v[238:239], v248 offset:61952\n\t"
    "v_mfma_f32_16x16x32_bf16 v[212:215], v[240:243], v[4:7], v[212:215]\n\t"
    "ds_read_b64_tr_b16 v[240:241], v249 offset:49664\n\t"
    "s_waitcnt lgkmcnt(9)\n\t"
    "v_mfma_f32_16x16x32_bf16 v[152:155], v[216:219], v[16:19], v[152:155]\n\t"
    "ds_read_b64_tr_b16 v[242:243], v249 offset:53760\n\t"
    "v_mfma_f32_16x16x32_bf16 v[152:155], v[220:223], v[24:27], v[152:155]\n\t"
    "ds_read_b64_tr_b16 v[244:245], v249 offset:57856\n\t"
    "v_mfma_f32_16x16x32_bf16 v[160:163], v[220:223], v[28:31], v[160:163]\n\t"
    "ds_read_b64_tr_b16 v[246:247], v249 offset:61952\n\t"
    "v_mfma_f32_16x16x32_bf16 v[160:163], v[216:219], v[20:23], v[160:163]\n\t"
    "v_mfma_f32_16x16x32_bf16 v[148:151], v[138:141], v[20:23], v[148:151]\n\t"
    "ds_read_b64_tr_b16 v[216:217], v248 offset:50176\n\t"
    "s_waitcnt lgkmcnt(9)\n\t"
    "v_mfma_f32_16x16x32_bf16 v[156:159], v[224:227], v[16:19], v[156:159]\n\t"
    "ds_read_b64_tr_b16 v[218:219], v248 offset:54272\n\t"
    "v_mfma_f32_16x16x32_bf16 v[156:159], v[228:231], v[24:27], v[156:159]\n\t"
    "ds_read_b64_tr_b16 v[220:221], v248 offset:58368\n\t"
    "v_mfma_f32_16x16x32_bf16 v[164:167], v[228:231], v[28:31], v[164:167]\n\t"
    "ds_read_b64_tr_b16 v[222:223], v248 offset:62464\n\t"
    "v_mfma_f32_16x16x32_bf16 v[164:167], v[224:227], v[20:23], v[164:167]\n\t"
    "ds_read_b64_tr_b16 v[224:225], v249 offset:50176\n\t"
    "s_waitcnt lgkmcnt(9)\n\t"
    "v_mfma_f32_16x16x32_bf16 v[168:171], v[232:235], v[16:19], v[168:171]\n\t"
    "ds_read_b64_tr_b16 v[226:227], v249 offset:54272\n\t"
    "v_mfma_f32_16x16x32_bf16 v[168:171], v[236:239], v[24:27], v[168:171]\n\t"
    "ds_read_b64_tr_b16 v[228:229], v249 offset:58368\n\t"
    "v_mfma_f32_16x16x32_bf16 v[176:179], v[236:239], v[28:31], v[176:179]\n\t"
    "ds_read_b64_tr_b16 v[230:231], v249 offset:62464\n\t"
    "v_mfma_f32_16x16x32_bf16 v[176:179], v[232:235], v[20:23], v[176:179]\n\t"
    "v_mfma_f32_16x16x32_bf16 v[144:147], v[138:141], v[24:27], v[144:147]\n\t"
    "ds_read_b64_tr_b16 v[232:233], v248 offset:50688\n\t"
    "s_waitcnt lgkmcnt(9)\n\t"
    "v_mfma_f32_16x16x32_bf16 v[172:175], v[240:243], v[16:19], v[172:175]\n\t"
    "ds_read_b64_tr_b16 v[234:235], v248 offset:54784\n\t"
    "v_mfma_f32_16x16x32_bf16 v[172:175], v[244:247], v[24:27], v[172:175]\n\t"
    "ds_read_b64_tr_b16 v[236:237], v248 offset:58880\n\t"
    "v_mfma_f32_16x16x32_bf16 v[180:183], v[244:247], v[28:31], v[180:183]\n\t"
    "ds_read_b64_tr_b16 v[238:239], v248 offset:62976\n\t"
    "v_mfma_f32_16x16x32_bf16 v[180:183], v[240:243], v[20:23], v[180:183]\n\t"
    "v_mfma_f32_16x16x32_bf16 v[148:151], v[138:141], v[28:31], v[148:151]\n\t"
    "ds_read_b64_tr_b16 v[240:241], v249 offset:50688\n\t"
    "s_waitcnt lgkmcnt(9)\n\t"
    "v_mfma_f32_16x16x32_bf16 v[184:187], v[216:219], v[16:19], v[184:187]\n\t"
    "ds_read_b64_tr_b16 v[242:243], v249 offset:54784\n\t"
    "v_mfma_f32_16x16x32_bf16 v[184:187], v[220:223], v[24:27], v[184:187]\n\t"
    "ds_read_b64_tr_b16 v[244:245], v249 offset:58880\n\t"
    "v_mfma_f32_16x16x32_bf16 v[192:195], v[220:223], v[28:31], v[192:195]\n\t"
    "ds_read_b64_tr_b16 v[246:247], v249 offset:62976\n\t"
    "v_mfma_f32_16x16x32_bf16 v[192:195], v[216:219], v[20:23], v[192:195]\n\t"
    "s_waitcnt lgkmcnt(8)\n\t"
    "v_mfma_f32_16x16x32_bf16 v[188:191], v[224:227], v[16:19], v[188:191]\n\t"
    "v_mfma_f32_16x16x32_bf16 v[188:191], v[228:231], v[24:27], v[188:191]\n\t"
    "v_mfma_f32_16x16x32_bf16 v[196:199], v[228:231], v[28:31], v[196:199]\n\t"
    "v_mfma_f32_16x16x32_bf16 v[196:199], v[224:227], v[20:23], v[196:199]\n\t"
    "v_mfma_f32_16x16x32_bf16 v[144:147], v[138:141], v[32:35], v[144:147]\n\t"
    "s_waitcnt lgkmcnt(0)\n\t"
    "s_barrier\n\t"
    "v_add_u32 v248, 0x10000, v248\n\t"
    "v_add_u32 v249, 0x10000, v249\n\t"
    "ds_read_b64_tr_b16 v[216:217], v248 offset:0\n\t"
    "v_mfma_f32_16x16x32_bf16 v[200:203], v[232:235], v[16:19], v[200:203]\n\t"
    "ds_read_b64_tr_b16 v[218:219], v248 offset:4096\n\t"
    "ds_read_b64_tr_b16 v[220:221], v248 offset:8192\n\t"
    "ds_read_b64_tr_b16 v[222:223], v248 offset:12288\n\t"
    "v_mfma_f32_16x16x32_bf16 v[200:203], v[236:239], v[24:27], v[200:203]\n\t"
    "ds_read_b64_tr_b16 v[224:225], v249 offset:0\n\t"
    "ds_read_b64_tr_b16 v[226:227], v249 offset:4096\n\t"
    "v_mfma_f32_16x16x32_bf16 v[208:211], v[236:239], v[28:31], v[208:211]\n\t"
    "ds_read_b64_tr_b16 v[228:229], v249 offset:8192\n\t"
    "ds_read_b64_tr_b16 v[230:231], v249 offset:12288\n\t"
    "v_mfma_f32_16x16x32_bf16 v[208:211], v[232:235], v[20:23], v[208:211]\n\t"
    "ds_read_b64_tr_b16 v[232:233], v248 offset:512\n\t"
    "v_mfma_f32_16x16x32_bf16 v[204:207], v[240:243], v[16:19], v[204:207]\n\t"
    "ds_read_b64_tr_b16 v[234:235], v248 offset:4608\n\t"
    "v_mfma_f32_16x16x32_bf16 v[204:207], v[244:247], v[24:27], v[204:207]\n\t"
    "ds_read_b64_tr_b16 v[236:237], v248 offset:8704\n\t"
    "v_mfma_f32_16x16x32_bf16 v[212:215], v[244:247], v[28:31], v[212:215]\n\t"
    "ds_read_b64_tr_b16 v[238:239], v248 offset:12800\n\t"
    "v_mfma_f32_16x16x32_bf16 v[212:215], v[240:243], v[20:23], v[212:215]\n\t"
    "v_mfma_f32_16x16x32_bf16 v[148:151], v[138:141], v[36:39], v[148:151]\n\t"
    "ds_read_b64_tr_b16 v[240:241], v249 offset:512\n\t"
    "s_waitcnt lgkmcnt(9)\n\t"
    "v_mfma_f32_16x16x32_bf16 v[152:155], v[216:219], v[32:35], v[152:155]\n\t"
    "ds_read_b64_tr_b16 v[242:243], v249 offset:4608\n\t"
    "v_mfma_f32_16x16x32_bf16 v[152:155], v[220:223], v[40:43], v[152:155]\n\t"
    "ds_read_b64_tr_b16 v[244:245], v249 offset:8704\n\t"
    "v_mfma_f32_16x16x32_bf16 v[160:163], v[220:223], v[44:47], v[160:163]\n\t"
    "ds_read_b64_tr_b16 v[246:247], v249 offset:12800\n\t"
    "v_mfma_f32_16x16x32_bf16 v[160:163], v[216:219], v[36:39], v[160:163]\n\t"
    "v_mfma_f32_16x16x32_bf16 v[144:147], v[138:141], v[40:43], v[144:147]\n\t"
    "ds_read_b64_tr_b16 v[216:217], v248 offset:1024\n\t"
    "s_waitcnt lgkmcnt(9)\n\t"
    "v_mfma_f32_16x16x32_bf16 v[156:159], v[224:227], v[32:35], v[156:159]\n\t"
    "ds_read_b64_tr_b16 v[218:219], v248 offset:5120\n\t"
    "v_mfma_f32_16x16x32_bf16 v[156:159], v[228:231], v[40:43], v[156:159]\n\t"
    "ds_read_b64_tr_b16 v[220:221], v248 offset:9216\n\t"
    "v_mfma_f32_16x16x32_bf16 v[164:167], v[228:231], v[44:47], v[164:167]\n\t"
    "ds_read_b64_tr_b16 v[222:223], v248 offset:13312\n\t"
    "v_mfma_f32_16x16x32_bf16 v[164:167], v[224:227], v[36:39], v[164:167]\n\t"
    "ds_read_b64_tr_b16 v[224:225], v249 offset:1024\n\t"
    "s_waitcnt lgkmcnt(9)\n\t"
    "v_mfma_f32_16x16x32_bf16 v[168:171], v[232:235], v[32:35], v[168:171]\n\t"
    "ds_read_b64_tr_b16 v[226:227], v249 offset:5120\n\t"
    "v_mfma_f32_16x16x32_bf16 v[168:171], v[236:239], v[40:43], v[168:171]\n\t"
    "ds_read_b64_tr_b16 v[228:229], v249 offset:9216\n\t"
    "v_mfma_f32_16x16x32_bf16 v[176:179], v[236:239], v[44:47], v[176:179]\n\t"
    "ds_read_b64_tr_b16 v[230:231], v249 offset:13312\n\t"
    "v_mfma_f32_16x16x32_bf16 v[176:179], v[232:235], v[36:39], v[176:179]\n\t"
    "v_mfma_f32_16x16x32_bf16 v[148:151], v[138:141], v[44:47], v[148:151]\n\t"
    "ds_read_b64_tr_b16 v[232:233], v248 offset:1536\n\t"
    "s_waitcnt lgkmcnt(9)\n\t"
    "v_mfma_f32_16x16x32_bf16 v[172:175], v[240:243], v[32:35], v[172:175]\n\t"
    "ds_read_b64_tr_b16 v[234:235], v248 offset:5632\n\t"
    "v_mfma_f32_16x16x32_bf16 v[172:175], v[244:247], v[40:43], v[172:175]\n\t"
    "ds_read_b64_tr_b16 v[236:237], v248 offset:9728\n\t"
    "v_mfma_f32_16x16x32_bf16 v[180:183], v[244:247], v[44:47], v[180:183]\n\t"
    "ds_read_b64_tr_b16 v[238:239], v248 offset:13824\n\t"
    "v_mfma_f32_16x16x32_bf16 v[180:183], v[240:243], v[36:39], v[180:183]\n\t"
    "ds_read_b64_tr_b16 v[240:241], v249 offset:1536\n\t"
    "s_waitcnt lgkmcnt(9)\n\t"
    "v_mfma_f32_16x16x32_bf16 v[184:187], v[216:219], v[32:35], v[184:187]\n\t"
    "ds_read_b64_tr_b16 v[242:243], v249 offset:5632\n\t"
    "v_mfma_f32_16x16x32_bf16 v[184:187], v[220:223], v[40:43], v[184:187]\n\t"
    "ds_read_b64_tr_b16 v[244:245], v249 offset:9728\n\t"
    "v_mfma_f32_16x16x32_bf16 v[192:195], v[220:223], v[44:47], v[192:195]\n\t"
    "ds_read_b64_tr_b16 v[246:247], v249 offset:13824\n\t"
    "v_mfma_f32_16x16x32_bf16 v[192:195], v[216:219], v[36:39], v[192:195]\n\t"
    "v_mfma_f32_16x16x32_bf16 v[144:147], v[138:141], v[48:51], v[144:147]\n\t"
    "s_waitcnt lgkmcnt(8)\n\t"
    "v_mfma_f32_16x16x32_bf16 v[188:191], v[224:227], v[32:35], v[188:191]\n\t"
    "v_mfma_f32_16x16x32_bf16 v[188:191], v[228:231], v[40:43], v[188:191]\n\t"
    "v_mfma_f32_16x16x32_bf16 v[196:199], v[228:231], v[44:47], v[196:199]\n\t"
    "v_mfma_f32_16x16x32_bf16 v[196:199], v[224:227], v[36:39], v[196:199]\n\t"
    "v_mfma_f32_16x16x32_bf16 v[148:151], v[138:141], v[52:55], v[148:151]\n\t"
    "s_waitcnt lgkmcnt(0)\n\t"
    "s_barrier\n\t"
    "v_add_u32 v248, 0xffff0000, v248\n\t"
    "v_add_u32 v249, 0xffff0000, v249\n\t"
    "ds_read_b64_tr_b16 v[216:217], v248 offset:0\n\t"
    "v_mfma_f32_16x16x32_bf16 v[200:203], v[232:235], v[32:35], v[200:203]\n\t"
    "ds_read_b64_tr_b16 v[218:219], v248 offset:4096\n\t"
    "ds_read_b64_tr_b16 v[220:221], v248 offset:8192\n\t"
    "ds_read_b64_tr_b16 v[222:223], v248 offset:12288\n\t"
    "v_mfma_f32_16x16x32_bf16 v[200:203], v[236:239], v[40:43], v[200:203]\n\t"
    "ds_read_b64_tr_b16 v[224:225], v249 offset:0\n\t"
    "ds_read_b64_tr_b16 v[226:227], v249 offset:4096\n\t"
    "v_mfma_f32_16x16x32_bf16 v[208:211], v[236:239], v[44:47], v[208:211]\n\t"
    "ds_read_b64_tr_b16 v[228:229], v249 offset:8192\n\t"
    "ds_read_b64_tr_b16 v[230:231], v249 offset:12288\n\t"
    "v_mfma_f32_16x16x32_bf16 v[208:211], v[232:235], v[36:39], v[208:211]\n\t"
    "ds_read_b64_tr_b16 v[232:233], v248 offset:512\n\t"
    "v_mfma_f32_16x16x32_bf16 v[204:207], v[240:243], v[32:35], v[204:207]\n\t"
    "ds_read_b64_tr_b16 v[234:235], v248 offset:4608\n\t"
    "v_mfma_f32_16x16x32_bf16 v[204:207], v[244:247], v[40:43], v[204:207]\n\t"
    "ds_read_b64_tr_b16 v[236:237], v248 offset:8704\n\t"
    "v_mfma_f32_16x16x32_bf16 v[212:215], v[244:247], v[44:47], v[212:215]\n\t"
    "ds_read_b64_tr_b16 v[238:239], v248 offset:12800\n\t"
    "v_mfma_f32_16x16x32_bf16 v[212:215], v[240:243], v[36:39], v[212:215]\n\t"
    "v_mfma_f32_16x16x32_bf16 v[144:147], v[138:141], v[56:59], v[144:147]\n\t"
    "ds_read_b64_tr_b16 v[240:241], v249 offset:512\n\t"
    "s_waitcnt lgkmcnt(9)\n\t"
    "v_mfma_f32_16x16x32_bf16 v[152:155], v[216:219], v[48:51], v[152:155]\n\t"
    "ds_read_b64_tr_b16 v[242:243], v249 offset:4608\n\t"
    "v_mfma_f32_16x16x32_bf16 v[152:155], v[220:223], v[56:59], v[152:155]\n\t"
    "ds_read_b64_tr_b16 v[244:245], v249 offset:8704\n\t"
    "v_mfma_f32_16x16x32_bf16 v[160:163], v[220:223], v[60:63], v[160:163]\n\t"
    "ds_read_b64_tr_b16 v[246:247], v249 offset:12800\n\t"
    "v_mfma_f32_16x16x32_bf16 v[160:163], v[216:219], v[52:55], v[160:163]\n\t"
    "ds_read_b64_tr_b16 v[216:217], v248 offset:1024\n\t"
    "s_waitcnt lgkmcnt(9)\n\t"
    "v_mfma_f32_16x16x32_bf16 v[156:159], v[224:227], v[48:51], v[156:159]\n\t"
    "ds_read_b64_tr_b16 v[218:219], v248 offset:5120\n\t"
    "v_mfma_f32_16x16x32_bf16 v[156:159], v[228:231], v[56:59], v[156:159]\n\t"
    "ds_read_b64_tr_b16 v[220:221], v248 offset:9216\n\t"
    "v_mfma_f32_16x16x32_bf16 v[164:167], v[228:231], v[60:63], v[164:167]\n\t"
    "ds_read_b64_tr_b16 v[222:223], v248 offset:13312\n\t"
    "v_mfma_f32_16x16x32_bf16 v[164:167], v[224:227], v[52:55], v[164:167]\n\t"
    "v_mfma_f32_16x16x32_bf16 v[148:151], v[138:141], v[60:63], v[148:151]\n\t"
    "ds_read_b64_tr_b16 v[224:225], v249 offset:1024\n\t"
    "s_waitcnt lgkmcnt(9)\n\t"
    "v_mfma_f32_16x16x32_bf16 v[168:171], v[232:235], v[48:51], v[168:171]\n\t"
    "ds_read_b64_tr_b16 v[226:227], v249 offset:5120\n\t"
    "v_mfma_f32_16x16x32_bf16 v[168:171], v[236:239], v[56:59], v[168:171]\n\t"
    "ds_read_b64_tr_b16 v[228:229], v249 offset:9216\n\t"
    "v_mfma_f32_16x16x32_bf16 v[176:179], v[236:239], v[60:63], v[176:179]\n\t"
    "ds_read_b64_tr_b16 v[230:231], v249 offset:13312\n\t"
    "v_mfma_f32_16x16x32_bf16 v[176:179], v[232:235], v[52:55], v[176:179]\n\t"
    "ds_read_b64_tr_b16 v[232:233], v248 offset:1536\n\t"
    "s_waitcnt lgkmcnt(9)\n\t"
    "v_mfma_f32_16x16x32_bf16 v[172:175], v[240:243], v[48:51], v[172:175]\n\t"
    "ds_read_b64_tr_b16 v[234:235], v248 offset:5632\n\t"
    "v_mfma_f32_16x16x32_bf16 v[172:175], v[244:247], v[56:59], v[172:175]\n\t"
    "ds_read_b64_tr_b16 v[236:237], v248 offset:9728\n\t"
    "v_mfma_f32_16x16x32_bf16 v[180:183], v[244:247], v[60:63], v[180:183]\n\t"
    "ds_read_b64_tr_b16 v[238:239], v248 offset:13824\n\t"
    "v_mfma_f32_16x16x32_bf16 v[180:183], v[240:243], v[52:55], v[180:183]\n\t"
    "v_mfma_f32_16x16x32_bf16 v[144:147], v[138:141], v[64:67], v[144:147]\n\t"
    "ds_read_b64_tr_b16 v[240:241], v249 offset:1536\n\t"
    "s_waitcnt lgkmcnt(9)\n\t"
    "v_mfma_f32_16x16x32_bf16 v[184:187], v[216:219], v[48:51], v[184:187]\n\t"
    "ds_read_b64_tr_b16 v[242:243], v249 offset:5632\n\t"
    "v_mfma_f32_16x16x32_bf16 v[184:187], v[220:223], v[56:59], v[184:187]\n\t"
    "ds_read_b64_tr_b16 v[244:245], v249 offset:9728\n\t"
    "v_mfma_f32_16x16x32_bf16 v[192:195], v[220:223], v[60:63], v[192:195]\n\t"
    "ds_read_b64_tr_b16 v[246:247], v249 offset:13824\n\t"
    "v_mfma_f32_16x16x32_bf16 v[192:195], v[216:219], v[52:55], v[192:195]\n\t"
    "v_mfma_f32_16x16x32_bf16 v[148:151], v[138:141], v[68:71], v[148:151]\n\t"
    "s_waitcnt lgkmcnt(8)\n\t"
    "v_mfma_f32_16x16x32_bf16 v[188:191], v[224:227], v[48:51], v[188:191]\n\t"
    "v_mfma_f32_16x16x32_bf16 v[188:191], v[228:231], v[56:59], v[188:191]\n\t"
    "v_mfma_f32_16x16x32_bf16 v[196:199], v[228:231], v[60:63], v[196:199]\n\t"
    "v_mfma_f32_16x16x32_bf16 v[196:199], v[224:227], v[52:55], v[196:199]\n\t"
    "s_waitcnt lgkmcnt(0)\n\t"
    "s_barrier\n\t"
    "v_add_u32 v248, 0x10000, v248\n\t"
    "v_add_u32 v249, 0x10000, v249\n\t"
    "ds_read_b64_tr_b16 v[216:217], v248 offset:16384\n\t"
    "v_mfma_f32_16x16x32_bf16 v[200:203], v[232:235], v[48:51], v[200:203]\n\t"
    "ds_read_b64_tr_b16 v[218:219], v248 offset:20480\n\t"
    "ds_read_b64_tr_b16 v[220:221], v248 offset:24576\n\t"
    "ds_read_b64_tr_b16 v[222:223], v248 offset:28672\n\t"
    "v_mfma_f32_16x16x32_bf16 v[200:203], v[236:239], v[56:59], v[200:203]\n\t"
    "ds_read_b64_tr_b16 v[224:225], v249 offset:16384\n\t"
    "ds_read_b64_tr_b16 v[226:227], v249 offset:20480\n\t"
    "v_mfma_f32_16x16x32_bf16 v[208:211], v[236:239], v[60:63], v[208:211]\n\t"
    "ds_read_b64_tr_b16 v[228:229], v249 offset:24576\n\t"
    "ds_read_b64_tr_b16 v[230:231], v249 offset:28672\n\t"
    "v_mfma_f32_16x16x32_bf16 v[208:211], v[232:235], v[52:55], v[208:211]\n\t"
    "v_mfma_f32_16x16x32_bf16 v[144:147], v[138:141], v[72:75], v[144:147]\n\t"
    "ds_read_b64_tr_b16 v[232:233], v248 offset:16896\n\t"
    "v_mfma_f32_16x16x32_bf16 v[204:207], v[240:243], v[48:51], v[204:207]\n\t"
    "ds_read_b64_tr_b16 v[234:235], v248 offset:20992\n\t"
    "v_mfma_f32_16x16x32_bf16 v[204:207], v[244:247], v[56:59], v[204:207]\n\t"
    "ds_read_b64_tr_b16 v[236:237], v248 offset:25088\n\t"
    "v_mfma_f32_16x16x32_bf16 v[212:215], v[244:247], v[60:63], v[212:215]\n\t"
    "ds_read_b64_tr_b16 v[238:239], v248 offset:29184\n\t"
    "v_mfma_f32_16x16x32_bf16 v[212:215], v[240:243], v[52:55], v[212:215]\n\t"
    "ds_read_b64_tr_b16 v[240:241], v249 offset:16896\n\t"
    "s_waitcnt lgkmcnt(9)\n\t"
    "v_mfma_f32_16x16x32_bf16 v[152:155], v[216:219], v[64:67], v[152:155]\n\t"
    "ds_read_b64_tr_b16 v[242:243], v249 offset:20992\n\t"
    "v_mfma_f32_16x16x32_bf16 v[152:155], v[220:223], v[72:75], v[152:155]\n\t"
    "ds_read_b64_tr_b16 v[244:245], v249 offset:25088\n\t"
    "v_mfma_f32_16x16x32_bf16 v[160:163], v[220:223], v[76:79], v[160:163]\n\t"
    "ds_read_b64_tr_b16 v[246:247], v249 offset:29184\n\t"
    "v_mfma_f32_16x16x32_bf16 v[160:163], v[216:219], v[68:71], v[160:163]\n\t"
    "v_mfma_f32_16x16x32_bf16 v[148:151], v[138:141], v[76:79], v[148:151]\n\t"
    "ds_read_b64_tr_b16 v[216:217], v248 offset:17408\n\t"
    "s_waitcnt lgkmcnt(9)\n\t"
    "v_mfma_f32_16x16x32_bf16 v[156:159], v[224:227], v[64:67], v[156:159]\n\t"
    "ds_read_b64_tr_b16 v[218:219], v248 offset:21504\n\t"
    "v_mfma_f32_16x16x32_bf16 v[156:159], v[228:231], v[72:75], v[156:159]\n\t"
    "ds_read_b64_tr_b16 v[220:221], v248 offset:25600\n\t"
    "v_mfma_f32_16x16x32_bf16 v[164:167], v[228:231], v[76:79], v[164:167]\n\t"
    "ds_read_b64_tr_b16 v[222:223], v248 offset:29696\n\t"
    "v_mfma_f32_16x16x32_bf16 v[164:167], v[224:227], v[68:71], v[164:167]\n\t"
    "v_mfma_f32_16x16x32_bf16 v[144:147], v[138:141], v[80:83], v[144:147]\n\t"
    "ds_read_b64_tr_b16 v[224:225], v249 offset:17408\n\t"
    "s_waitcnt lgkmcnt(9)\n\t"
    "v_mfma_f32_16x16x32_bf16 v[168:171], v[232:235], v[64:67], v[168:171]\n\t"
    "ds_read_b64_tr_b16 v[226:227], v249 offset:21504\n\t"
    "v_mfma_f32_16x16x32_bf16 v[168:171], v[236:239], v[72:75], v[168:171]\n\t"
    "ds_read_b64_tr_b16 v[228:229], v249 offset:25600\n\t"
    "v_mfma_f32_16x16x32_bf16 v[176:179], v[236:239], v[76:79], v[176:179]\n\t"
    "ds_read_b64_tr_b16 v[230:231], v249 offset:29696\n\t"
    "v_mfma_f32_16x16x32_bf16 v[176:179], v[232:235], v[68:71], v[176:179]\n\t"
    "ds_read_b64_tr_b16 v[232:233], v248 offset:17920\n\t"
    "s_waitcnt lgkmcnt(9)\n\t"
    "v_mfma_f32_16x16x32_bf16 v[172:175], v[240:243], v[64:67], v[172:175]\n\t"
    "ds_read_b64_tr_b16 v[234:235], v248 offset:22016\n\t"
    "v_mfma_f32_16x16x32_bf16 v[172:175], v[244:247], v[72:75], v[172:175]\n\t"
    "ds_read_b64_tr_b16 v[236:237], v248 offset:26112\n\t"
    "v_mfma_f32_16x16x32_bf16 v[180:183], v[244:247], v[76:79], v[180:183]\n\t"
    "ds_read_b64_tr_b16 v[238:239], v248 offset:30208\n\t"
    "v_mfma_f32_16x16x32_bf16 v[180:183], v[240:243], v[68:71], v[180:183]\n\t"
    "v_mfma_f32_16x16x32_bf16 v[148:151], v[138:141], v[84:87], v[148:151]\n\t"
    "ds_read_b64_tr_b16 v[240:241], v249 offset:17920\n\t"
    "s_waitcnt lgkmcnt(9)\n\t"
    "v_mfma_f32_16x16x32_bf16 v[184:187], v[216:219], v[64:67], v[184:187]\n\t"
    "ds_read_b64_tr_b16 v[242:243], v249 offset:22016\n\t"
    "v_mfma_f32_16x16x32_bf16 v[184:187], v[220:223], v[72:75], v[184:187]\n\t"
    "ds_read_b64_tr_b16 v[244:245], v249 offset:26112\n\t"
    "v_mfma_f32_16x16x32_bf16 v[192:195], v[220:223], v[76:79], v[192:195]\n\t"
    "ds_read_b64_tr_b16 v[246:247], v249 offset:30208\n\t"
    "v_mfma_f32_16x16x32_bf16 v[192:195], v[216:219], v[68:71], v[192:195]\n\t"
    "s_waitcnt lgkmcnt(8)\n\t"
    "v_mfma_f32_16x16x32_bf16 v[188:191], v[224:227], v[64:67], v[188:191]\n\t"
    "v_mfma_f32_16x16x32_bf16 v[188:191], v[228:231], v[72:75], v[188:191]\n\t"
    "v_mfma_f32_16x16x32_bf16 v[196:199], v[228:231], v[76:79], v[196:199]\n\t"
    "v_mfma_f32_16x16x32_bf16 v[196:199], v[224:227], v[68:71], v[196:199]\n\t"
    "v_mfma_f32_16x16x32_bf16 v[144:147], v[138:141], v[88:91], v[144:147]\n\t"
    "s_waitcnt lgkmcnt(0)\n\t"
    "s_barrier\n\t"
    "ds_read_b64_tr_b16 v[216:217], v248 offset:32768\n\t"
    "ds_read_b64_tr_b16 v[218:219], v248 offset:36864\n\t"
    "v_mfma_f32_16x16x32_bf16 v[200:203], v[232:235], v[64:67], v[200:203]\n\t"
    "ds_read_b64_tr_b16 v[220:221], v248 offset:40960\n\t"
    "ds_read_b64_tr_b16 v[222:223], v248 offset:45056\n\t"
    "v_mfma_f32_16x16x32_bf16 v[200:203], v[236:239], v[72:75], v[200:203]\n\t"
    "ds_read_b64_tr_b16 v[224:225], v249 offset:32768\n\t"
    "ds_read_b64_tr_b16 v[226:227], v249 offset:36864\n\t"
    "v_mfma_f32_16x16x32_bf16 v[208:211], v[236:239], v[76:79], v[208:211]\n\t"
    "ds_read_b64_tr_b16 v[228:229], v249 offset:40960\n\t"
    "ds_read_b64_tr_b16 v[230:231], v249 offset:45056\n\t"
    "v_mfma_f32_16x16x32_bf16 v[208:211], v[232:235], v[68:71], v[208:211]\n\t"
    "v_mfma_f32_16x16x32_bf16 v[148:151], v[138:141], v[92:95], v[148:151]\n\t"
    "ds_read_b64_tr_b16 v[232:233], v248 offset:33280\n\t"
    "v_mfma_f32_16x16x32_bf16 v[204:207], v[240:243], v[64:67], v[204:207]\n\t"
    "ds_read_b64_tr_b16 v[234:235], v248 offset:37376\n\t"
    "v_mfma_f32_16x16x32_bf16 v[204:207], v[244:247], v[72:75], v[204:207]\n\t"
    "ds_read_b64_tr_b16 v[236:237], v248 offset:41472\n\t"
    "v_mfma_f32_16x16x32_bf16 v[212:215], v[244:247], v[76:79], v[212:215]\n\t"
    "ds_read_b64_tr_b16 v[238:239], v248 offset:45568\n\t"
    "v_mfma_f32_16x16x32_bf16 v[212:215], v[240:243], v[68:71], v[212:215]\n\t"
    "ds_read_b64_tr_b16 v[240:241], v249 offset:33280\n\t"
    "s_waitcnt lgkmcnt(9)\n\t"
    "v_mfma_f32_16x16x32_bf16 v[152:155], v[216:219], v[80:83], v[152:155]\n\t"
    "ds_read_b64_tr_b16 v[242:243], v249 offset:37376\n\t"
    "v_mfma_f32_16x16x32_bf16 v[152:155], v[220:223], v[88:91], v[152:155]\n\t"
    "ds_read_b64_tr_b16 v[244:245], v249 offset:41472\n\t"
    "v_mfma_f32_16x16x32_bf16 v[160:163], v[220:223], v[92:95], v[160:163]\n\t"
    "ds_read_b64_tr_b16 v[246:247], v249 offset:45568\n\t"
    "v_mfma_f32_16x16x32_bf16 v[160:163], v[216:219], v[84:87], v[160:163]\n\t"
    "v_mfma_f32_16x16x32_bf16 v[144:147], v[138:141], v[96:99], v[144:147]\n\t"
    "ds_read_b64_tr_b16 v[216:217], v248 offset:33792\n\t"
    "s_waitcnt lgkmcnt(9)\n\t"
    "v_mfma_f32_16x16x32_bf16 v[156:159], v[224:227], v[80:83], v[156:159]\n\t"
    "ds_read_b64_tr_b16 v[218:219], v248 offset:37888\n\t"
    "v_mfma_f32_16x16x32_bf16 v[156:159], v[228:231], v[88:91], v[156:159]\n\t"
    "ds_read_b64_tr_b16 v[220:221], v248 offset:41984\n\t"
    "v_mfma_f32_16x16x32_bf16 v[164:167], v[228:231], v[92:95], v[164:167]\n\t"
    "ds_read_b64_tr_b16 v[222:223], v248 offset:46080\n\t"
    "v_mfma_f32_16x16x32_bf16 v[164:167], v[224:227], v[84:87], v[164:167]\n\t"
    "ds_read_b64_tr_b16 v[224:225], v249 offset:33792\n\t"
    "s_waitcnt lgkmcnt(9)\n\t"
    "v_mfma_f32_16x16x32_bf16 v[168:171], v[232:235], v[80:83], v[168:171]\n\t"
    "ds_read_b64_tr_b16 v[226:227], v249 offset:37888\n\t"
    "v_mfma_f32_16x16x32_bf16 v[168:171], v[236:239], v[88:91], v[168:171]\n\t"
    "ds_read_b64_tr_b16 v[228:229], v249 offset:41984\n\t"
    "v_mfma_f32_16x16x32_bf16 v[176:179], v[236:239], v[92:95], v[176:179]\n\t"
    "ds_read_b64_tr_b16 v[230:231], v249 offset:46080\n\t"
    "v_mfma_f32_16x16x32_bf16 v[176:179], v[232:235], v[84:87], v[176:179]\n\t"
    "v_mfma_f32_16x16x32_bf16 v[148:151], v[138:141], v[100:103], v[148:151]\n\t"
    "ds_read_b64_tr_b16 v[232:233], v248 offset:34304\n\t"
    "s_waitcnt lgkmcnt(9)\n\t"
    "v_mfma_f32_16x16x32_bf16 v[172:175], v[240:243], v[80:83], v[172:175]\n\t"
    "ds_read_b64_tr_b16 v[234:235], v248 offset:38400\n\t"
    "v_mfma_f32_16x16x32_bf16 v[172:175], v[244:247], v[88:91], v[172:175]\n\t"
    "ds_read_b64_tr_b16 v[236:237], v248 offset:42496\n\t"
    "v_mfma_f32_16x16x32_bf16 v[180:183], v[244:247], v[92:95], v[180:183]\n\t"
    "ds_read_b64_tr_b16 v[238:239], v248 offset:46592\n\t"
    "v_mfma_f32_16x16x32_bf16 v[180:183], v[240:243], v[84:87], v[180:183]\n\t"
    "v_mfma_f32_16x16x32_bf16 v[144:147], v[138:141], v[104:107], v[144:147]\n\t"
    "ds_read_b64_tr_b16 v[240:241], v249 offset:34304\n\t"
    "s_waitcnt lgkmcnt(9)\n\t"
    "v_mfma_f32_16x16x32_bf16 v[184:187], v[216:219], v[80:83], v[184:187]\n\t"
    "ds_read_b64_tr_b16 v[242:243], v249 offset:38400\n\t"
    "v_mfma_f32_16x16x32_bf16 v[184:187], v[220:223], v[88:91], v[184:187]\n\t"
    "ds_read_b64_tr_b16 v[244:245], v249 offset:42496\n\t"
    "v_mfma_f32_16x16x32_bf16 v[192:195], v[220:223], v[92:95], v[192:195]\n\t"
    "ds_read_b64_tr_b16 v[246:247], v249 offset:46592\n\t"
    "v_mfma_f32_16x16x32_bf16 v[192:195], v[216:219], v[84:87], v[192:195]\n\t"
    "s_waitcnt lgkmcnt(8)\n\t"
    "v_mfma_f32_16x16x32_bf16 v[188:191], v[224:227], v[80:83], v[188:191]\n\t"
    "v_mfma_f32_16x16x32_bf16 v[188:191], v[228:231], v[88:91], v[188:191]\n\t"
    "v_mfma_f32_16x16x32_bf16 v[196:199], v[228:231], v[92:95], v[196:199]\n\t"
    "v_mfma_f32_16x16x32_bf16 v[196:199], v[224:227], v[84:87], v[196:199]\n\t"
    "v_mfma_f32_16x16x32_bf16 v[148:151], v[138:141], v[108:111], v[148:151]\n\t"
    "s_waitcnt lgkmcnt(0)\n\t"
    "s_barrier\n\t"
    "v_add_u32 v248, 0xffff0000, v248\n\t"
    "v_add_u32 v249, 0xffff0000, v249\n\t"
    "ds_read_b64_tr_b16 v[216:217], v248 offset:32768\n\t"
    "v_mfma_f32_16x16x32_bf16 v[200:203], v[232:235], v[80:83], v[200:203]\n\t"
    "ds_read_b64_tr_b16 v[218:219], v248 offset:36864\n\t"
    "ds_read_b64_tr_b16 v[220:221], v248 offset:40960\n\t"
    "ds_read_b64_tr_b16 v[222:223], v248 offset:45056\n\t"
    "v_mfma_f32_16x16x32_bf16 v[200:203], v[236:239], v[88:91], v[200:203]\n\t"
    "ds_read_b64_tr_b16 v[224:225], v249 offset:32768\n\t"
    "ds_read_b64_tr_b16 v[226:227], v249 offset:36864\n\t"
    "v_mfma_f32_16x16x32_bf16 v[208:211], v[236:239], v[92:95], v[208:211]\n\t"
    "ds_read_b64_tr_b16 v[228:229], v249 offset:40960\n\t"
    "ds_read_b64_tr_b16 v[230:231], v249 offset:45056\n\t"
    "v_mfma_f32_16x16x32_bf16 v[208:211], v[232:235], v[84:87], v[208:211]\n\t"
    "ds_read_b64_tr_b16 v[232:233], v248 offset:33280\n\t"
    "v_mfma_f32_16x16x32_bf16 v[204:207], v[240:243], v[80:83], v[204:207]\n\t"
    "ds_read_b64_tr_b16 v[234:235], v248 offset:37376\n\t"
    "v_mfma_f32_16x16x32_bf16 v[204:207], v[244:247], v[88:91], v[204:207]\n\t"
    "ds_read_b64_tr_b16 v[236:237], v248 offset:41472\n\t"
    "v_mfma_f32_16x16x32_bf16 v[212:215], v[244:247], v[92:95], v[212:215]\n\t"
    "ds_read_b64_tr_b16 v[238:239], v248 offset:45568\n\t"
    "v_mfma_f32_16x16x32_bf16 v[212:215], v[240:243], v[84:87], v[212:215]\n\t"
    "v_mfma_f32_16x16x32_bf16 v[144:147], v[138:141], v[112:115], v[144:147]\n\t"
    "ds_read_b64_tr_b16 v[240:241], v249 offset:33280\n\t"
    "s_waitcnt lgkmcnt(9)\n\t"
    "v_mfma_f32_16x16x32_bf16 v[152:155], v[216:219], v[96:99], v[152:155]\n\t"
    "ds_read_b64_tr_b16 v[242:243], v249 offset:37376\n\t"
    "v_mfma_f32_16x16x32_bf16 v[152:155], v[220:223], v[104:107], v[152:155]\n\t"
    "ds_read_b64_tr_b16 v[244:245], v249 offset:41472\n\t"
    "v_mfma_f32_16x16x32_bf16 v[160:163], v[220:223], v[108:111], v[160:163]\n\t"
    "ds_read_b64_tr_b16 v[246:247], v249 offset:45568\n\t"
    "v_mfma_f32_16x16x32_bf16 v[160:163], v[216:219], v[100:103], v[160:163]\n\t"
    "v_mfma_f32_16x16x32_bf16 v[148:151], v[138:141], v[116:119], v[148:151]\n\t"
    "ds_read_b64_tr_b16 v[216:217], v248 offset:33792\n\t"
    "s_waitcnt lgkmcnt(9)\n\t"
    "v_mfma_f32_16x16x32_bf16 v[156:159], v[224:227], v[96:99], v[156:159]\n\t"
    "ds_read_b64_tr_b16 v[218:219], v248 offset:37888\n\t"
    "v_mfma_f32_16x16x32_bf16 v[156:159], v[228:231], v[104:107], v[156:159]\n\t"
    "ds_read_b64_tr_b16 v[220:221], v248 offset:41984\n\t"
    "v_mfma_f32_16x16x32_bf16 v[164:167], v[228:231], v[108:111], v[164:167]\n\t"
    "ds_read_b64_tr_b16 v[222:223], v248 offset:46080\n\t"
    "v_mfma_f32_16x16x32_bf16 v[164:167], v[224:227], v[100:103], v[164:167]\n\t"
    "ds_read_b64_tr_b16 v[224:225], v249 offset:33792\n\t"
    "s_waitcnt lgkmcnt(9)\n\t"
    "v_mfma_f32_16x16x32_bf16 v[168:171], v[232:235], v[96:99], v[168:171]\n\t"
    "ds_read_b64_tr_b16 v[226:227], v249 offset:37888\n\t"
    "v_mfma_f32_16x16x32_bf16 v[168:171], v[236:239], v[104:107], v[168:171]\n\t"
    "ds_read_b64_tr_b16 v[228:229], v249 offset:41984\n\t"
    "v_mfma_f32_16x16x32_bf16 v[176:179], v[236:239], v[108:111], v[176:179]\n\t"
    "ds_read_b64_tr_b16 v[230:231], v249 offset:46080\n\t"
    "v_mfma_f32_16x16x32_bf16 v[176:179], v[232:235], v[100:103], v[176:179]\n\t"
    "v_mfma_f32_16x16x32_bf16 v[144:147], v[138:141], v[120:123], v[144:147]\n\t"
    "ds_read_b64_tr_b16 v[232:233], v248 offset:34304\n\t"
    "s_waitcnt lgkmcnt(9)\n\t"
    "v_mfma_f32_16x16x32_bf16 v[172:175], v[240:243], v[96:99], v[172:175]\n\t"
    "ds_read_b64_tr_b16 v[234:235], v248 offset:38400\n\t"
    "v_mfma_f32_16x16x32_bf16 v[172:175], v[244:247], v[104:107], v[172:175]\n\t"
    "ds_read_b64_tr_b16 v[236:237], v248 offset:42496\n\t"
    "v_mfma_f32_16x16x32_bf16 v[180:183], v[244:247], v[108:111], v[180:183]\n\t"
    "ds_read_b64_tr_b16 v[238:239], v248 offset:46592\n\t"
    "v_mfma_f32_16x16x32_bf16 v[180:183], v[240:243], v[100:103], v[180:183]\n\t"
    "ds_read_b64_tr_b16 v[240:241], v249 offset:34304\n\t"
    "s_waitcnt lgkmcnt(9)\n\t"
    "v_mfma_f32_16x16x32_bf16 v[184:187], v[216:219], v[96:99], v[184:187]\n\t"
    "ds_read_b64_tr_b16 v[242:243], v249 offset:38400\n\t"
    "v_mfma_f32_16x16x32_bf16 v[184:187], v[220:223], v[104:107], v[184:187]\n\t"
    "ds_read_b64_tr_b16 v[244:245], v249 offset:42496\n\t"
    "v_mfma_f32_16x16x32_bf16 v[192:195], v[220:223], v[108:111], v[192:195]\n\t"
    "ds_read_b64_tr_b16 v[246:247], v249 offset:46592\n\t"
    "v_mfma_f32_16x16x32_bf16 v[192:195], v[216:219], v[100:103], v[192:195]\n\t"
    "v_mfma_f32_16x16x32_bf16 v[148:151], v[138:141], v[124:127], v[148:151]\n\t"
    "s_waitcnt lgkmcnt(8)\n\t"
    "v_mfma_f32_16x16x32_bf16 v[188:191], v[224:227], v[96:99], v[188:191]\n\t"
    "v_mfma_f32_16x16x32_bf16 v[188:191], v[228:231], v[104:107], v[188:191]\n\t"
    "v_mfma_f32_16x16x32_bf16 v[196:199], v[228:231], v[108:111], v[196:199]\n\t"
    "v_mfma_f32_16x16x32_bf16 v[196:199], v[224:227], v[100:103], v[196:199]\n\t"
    "s_waitcnt lgkmcnt(0)\n\t"
    "s_barrier\n\t"
    "s_setprio 2\n\t"
    "ds_read_b64_tr_b16 v[216:217], v248 offset:49152\n\t"
    "ds_read_b64_tr_b16 v[218:219], v248 offset:53248\n\t"
    "v_mfma_f32_16x16x32_bf16 v[200:203], v[232:235], v[96:99], v[200:203]\n\t"
    "ds_read_b64_tr_b16 v[220:221], v248 offset:57344\n\t"
    "ds_read_b64_tr_b16 v[222:223], v248 offset:61440\n\t"
    "v_mfma_f32_16x16x32_bf16 v[200:203], v[236:239], v[104:107], v[200:203]\n\t"
    "ds_read_b64_tr_b16 v[224:225], v249 offset:49152\n\t"
    "ds_read_b64_tr_b16 v[226:227], v249 offset:53248\n\t"
    "v_mfma_f32_16x16x32_bf16 v[208:211], v[236:239], v[108:111], v[208:211]\n\t"
    "ds_read_b64_tr_b16 v[228:229], v249 offset:57344\n\t"
    "ds_read_b64_tr_b16 v[230:231], v249 offset:61440\n\t"
    "v_mfma_f32_16x16x32_bf16 v[208:211], v[232:235], v[100:103], v[208:211]\n\t"
    "ds_read_b64_tr_b16 v[232:233], v248 offset:49664\n\t"
    "v_mfma_f32_16x16x32_bf16 v[204:207], v[240:243], v[96:99], v[204:207]\n\t"
    "ds_read_b64_tr_b16 v[234:235], v248 offset:53760\n\t"
    "v_mfma_f32_16x16x32_bf16 v[204:207], v[244:247], v[104:107], v[204:207]\n\t"
    "ds_read_b64_tr_b16 v[236:237], v248 offset:57856\n\t"
    "v_mfma_f32_16x16x32_bf16 v[212:215], v[244:247], v[108:111], v[212:215]\n\t"
    "ds_read_b64_tr_b16 v[238:239], v248 offset:61952\n\t"
    "v_mfma_f32_16x16x32_bf16 v[212:215], v[240:243], v[100:103], v[212:215]\n\t"
    "ds_read_b64_tr_b16 v[240:241], v249 offset:49664\n\t"
    "v_max_f32 v252, 0xda24260, v148\n\t"
    "s_waitcnt lgkmcnt(9)\n\t"
    "v_mfma_f32_16x16x32_bf16 v[152:155], v[216:219], v[112:115], v[152:155]\n\t"
    "ds_read_b64_tr_b16 v[242:243], v249 offset:53760\n\t"
    "v_rcp_f32 v251, v144\n\t"
    "v_rcp_f32 v252, v252\n\t"
    "v_mfma_f32_16x16x32_bf16 v[152:155], v[220:223], v[120:123], v[152:155]\n\t"
    "ds_read_b64_tr_b16 v[244:245], v249 offset:57856\n\t"
    "v_mfma_f32_16x16x32_bf16 v[160:163], v[220:223], v[124:127], v[160:163]\n\t"
    "ds_read_b64_tr_b16 v[246:247], v249 offset:61952\n\t"
    "v_mfma_f32_16x16x32_bf16 v[160:163], v[216:219], v[116:119], v[160:163]\n\t"
    "ds_read_b64_tr_b16 v[216:217], v248 offset:50176\n\t"
    "v_lshlrev_b32 v145, 5, v253\n\t"
    "v_cmp_gt_u32 vcc, 4, v253\n\t"
    "v_mov_b32 v254, 0x80000000\n\t"
    "s_waitcnt lgkmcnt(9)\n\t"
    "v_mfma_f32_16x16x32_bf16 v[156:159], v[224:227], v[112:115], v[156:159]\n\t"
    "ds_read_b64_tr_b16 v[218:219], v248 offset:54272\n\t"
    "v_cndmask_b32 v145, v254, v145, vcc\n\t"
    "v_mov_b32 v144, 0\n\t"
    "v_cmp_eq_u32 vcc, 0, v253\n\t"
    "v_mfma_f32_16x16x32_bf16 v[156:159], v[228:231], v[120:123], v[156:159]\n\t"
    "ds_read_b64_tr_b16 v[220:221], v248 offset:58368\n\t"
    "v_mov_b32 v254, 0x80000000\n\t"
    "v_cndmask_b32 v144, v254, v144, vcc\n\t"
    "v_mov_b32 v148, s44\n\t"
    "v_mfma_f32_16x16x32_bf16 v[164:167], v[228:231], v[124:127], v[164:167]\n\t"
    "ds_read_b64_tr_b16 v[222:223], v248 offset:62464\n\t"
    "v_mov_b32 v149, s45\n\t"
    "v_mfma_f32_16x16x32_bf16 v[164:167], v[224:227], v[116:119], v[164:167]\n\t"
    "ds_read_b64_tr_b16 v[224:225], v249 offset:50176\n\t"
    "s_waitcnt lgkmcnt(9)\n\t"
    "v_mfma_f32_16x16x32_bf16 v[168:171], v[232:235], v[112:115], v[168:171]\n\t"
    "ds_read_b64_tr_b16 v[226:227], v249 offset:54272\n\t"
    "v_mfma_f32_16x16x32_bf16 v[168:171], v[236:239], v[120:123], v[168:171]\n\t"
    "ds_read_b64_tr_b16 v[228:229], v249 offset:58368\n\t"
    "v_mul_f32 v152, v251, v152\n\t"
    "v_mul_f32 v153, v251, v153\n\t"
    "v_cvt_pk_bf16_f32 v0, v152, v153\n\t"
    "v_mfma_f32_16x16x32_bf16 v[176:179], v[236:239], v[124:127], v[176:179]\n\t"
    "ds_read_b64_tr_b16 v[230:231], v249 offset:62464\n\t"
    "v_mul_f32 v154, v251, v154\n\t"
    "v_mul_f32 v155, v251, v155\n\t"
    "v_cvt_pk_bf16_f32 v1, v154, v155\n\t"
    "v_mfma_f32_16x16x32_bf16 v[176:179], v[232:235], v[116:119], v[176:179]\n\t"
    "ds_read_b64_tr_b16 v[232:233], v248 offset:50688\n\t"
    "v_mul_f32 v156, v251, v156\n\t"
    "v_mul_f32 v157, v251, v157\n\t"
    "v_cvt_pk_bf16_f32 v2, v156, v157\n\t"
    "s_waitcnt lgkmcnt(9)\n\t"
    "v_mfma_f32_16x16x32_bf16 v[172:175], v[240:243], v[112:115], v[172:175]\n\t"
    "ds_read_b64_tr_b16 v[234:235], v248 offset:54784\n\t"
    "v_mul_f32 v158, v251, v158\n\t"
    "v_mul_f32 v159, v251, v159\n\t"
    "v_cvt_pk_bf16_f32 v3, v158, v159\n\t"
    "v_mfma_f32_16x16x32_bf16 v[172:175], v[244:247], v[120:123], v[172:175]\n\t"
    "ds_read_b64_tr_b16 v[236:237], v248 offset:58880\n\t"
    "v_mul_f32 v160, v252, v160\n\t"
    "v_mul_f32 v161, v252, v161\n\t"
    "v_cvt_pk_bf16_f32 v4, v160, v161\n\t"
    "v_mfma_f32_16x16x32_bf16 v[180:183], v[244:247], v[124:127], v[180:183]\n\t"
    "ds_read_b64_tr_b16 v[238:239], v248 offset:62976\n\t"
    "v_mul_f32 v162, v252, v162\n\t"
    "v_mul_f32 v163, v252, v163\n\t"
    "v_cvt_pk_bf16_f32 v5, v162, v163\n\t"
    "v_mfma_f32_16x16x32_bf16 v[180:183], v[240:243], v[116:119], v[180:183]\n\t"
    "ds_read_b64_tr_b16 v[240:241], v249 offset:50688\n\t"
    "v_mul_f32 v164, v252, v164\n\t"
    "v_mul_f32 v165, v252, v165\n\t"
    "v_cvt_pk_bf16_f32 v6, v164, v165\n\t"
    "s_waitcnt lgkmcnt(9)\n\t"
    "v_mfma_f32_16x16x32_bf16 v[184:187], v[216:219], v[112:115], v[184:187]\n\t"
    "ds_read_b64_tr_b16 v[242:243], v249 offset:54784\n\t"
    "v_mul_f32 v166, v252, v166\n\t"
    "v_mul_f32 v167, v252, v167\n\t"
    "v_cvt_pk_bf16_f32 v7, v166, v167\n\t"
    "v_mfma_f32_16x16x32_bf16 v[184:187], v[220:223], v[120:123], v[184:187]\n\t"
    "ds_read_b64_tr_b16 v[244:245], v249 offset:58880\n\t"
    "s_add_u32 s54, s47, 0x14000\n\t"
    "v_lshl_add_u32 v146, v253, 4, s54\n\t"
    "ds_write_b128 v146, v[0:3] offset:0\n\t"
    "v_mfma_f32_16x16x32_bf16 v[192:195], v[220:223], v[124:127], v[192:195]\n\t"
    "ds_read_b64_tr_b16 v[246:247], v249 offset:62976\n\t"
    "ds_write_b128 v146, v[4:7] offset:1024\n\t"
    "s_xor_b32 s53, s43, 0x0\n\t"
    "v_mfma_f32_16x16x32_bf16 v[192:195], v[216:219], v[116:119], v[192:195]\n\t"
    "s_lshr_b32 s53, s53, 2\n\t"
    "s_add_u32 s53, s53, %[obase]\n\t"
    "buffer_store_dwordx4 v[0:3], v142, %[srdws], s53 offen sc1\n\t"
    "s_waitcnt lgkmcnt(10)\n\t"
    "v_mfma_f32_16x16x32_bf16 v[188:191], v[224:227], v[112:115], v[188:191]\n\t"
    "buffer_store_dwordx4 v[4:7], v143, %[srdws], s53 offen offset:1024 sc1\n\t"
    "v_mul_f32 v168, v251, v168\n\t"
    "v_mfma_f32_16x16x32_bf16 v[188:191], v[228:231], v[120:123], v[188:191]\n\t"
    "v_mul_f32 v169, v251, v169\n\t"
    "v_cvt_pk_bf16_f32 v8, v168, v169\n\t"
    "v_mul_f32 v170, v251, v170\n\t"
    "v_mul_f32 v171, v251, v171\n\t"
    "v_mfma_f32_16x16x32_bf16 v[196:199], v[228:231], v[124:127], v[196:199]\n\t"
    "v_cvt_pk_bf16_f32 v9, v170, v171\n\t"
    "v_mul_f32 v172, v251, v172\n\t"
    "v_mul_f32 v173, v251, v173\n\t"
    "v_cvt_pk_bf16_f32 v10, v172, v173\n\t"
    "v_mfma_f32_16x16x32_bf16 v[196:199], v[224:227], v[116:119], v[196:199]\n\t"
    "v_mul_f32 v174, v251, v174\n\t"
    "v_mul_f32 v175, v251, v175\n\t"
    "v_cvt_pk_bf16_f32 v11, v174, v175\n\t"
    "v_mul_f32 v176, v252, v176\n\t"
    "s_waitcnt lgkmcnt(6)\n\t"
    "v_mfma_f32_16x16x32_bf16 v[200:203], v[232:235], v[112:115], v[200:203]\n\t"
    "v_mul_f32 v177, v252, v177\n\t"
    "v_cvt_pk_bf16_f32 v12, v176, v177\n\t"
    "v_mul_f32 v178, v252, v178\n\t"
    "v_mul_f32 v179, v252, v179\n\t"
    "v_mfma_f32_16x16x32_bf16 v[200:203], v[236:239], v[120:123], v[200:203]\n\t"
    "v_cvt_pk_bf16_f32 v13, v178, v179\n\t"
    "v_mul_f32 v180, v252, v180\n\t"
    "v_mul_f32 v181, v252, v181\n\t"
    "v_cvt_pk_bf16_f32 v14, v180, v181\n\t"
    "v_mfma_f32_16x16x32_bf16 v[208:211], v[236:239], v[124:127], v[208:211]\n\t"
    "v_mul_f32 v182, v252, v182\n\t"
    "v_mul_f32 v183, v252, v183\n\t"
    "v_cvt_pk_bf16_f32 v15, v182, v183\n\t"
    "ds_write_b128 v146, v[8:11] offset:2048\n\t"
    "v_mfma_f32_16x16x32_bf16 v[208:211], v[232:235], v[116:119], v[208:211]\n\t"
    "ds_write_b128 v146, v[12:15] offset:3072\n\t"
    "buffer_store_dwordx4 v[8:11], v142, %[srdws], s53 offen offset:2048 sc1\n\t"
    "s_waitcnt lgkmcnt(3)\n\t"
    "v_mfma_f32_16x16x32_bf16 v[204:207], v[240:243], v[112:115], v[204:207]\n\t"
    "buffer_store_dwordx4 v[12:15], v143, %[srdws], s53 offen offset:3072 sc1\n\t"
    "v_mul_f32 v184, v251, v184\n\t"
    "v_mfma_f32_16x16x32_bf16 v[204:207], v[244:247], v[120:123], v[204:207]\n\t"
    "v_mul_f32 v185, v251, v185\n\t"
    "v_cvt_pk_bf16_f32 v16, v184, v185\n\t"
    "v_mul_f32 v186, v251, v186\n\t"
    "v_mul_f32 v187, v251, v187\n\t"
    "v_mfma_f32_16x16x32_bf16 v[212:215], v[244:247], v[124:127], v[212:215]\n\t"
    "v_cvt_pk_bf16_f32 v17, v186, v187\n\t"
    "v_mul_f32 v188, v251, v188\n\t"
    "v_mul_f32 v189, v251, v189\n\t"
    "v_cvt_pk_bf16_f32 v18, v188, v189\n\t"
    "v_mfma_f32_16x16x32_bf16 v[212:215], v[240:243], v[116:119], v[212:215]\n\t"
    "v_mul_f32 v190, v251, v190\n\t"
    "v_mul_f32 v191, v251, v191\n\t"
    "v_cvt_pk_bf16_f32 v19, v190, v191\n\t"
    "v_mul_f32 v192, v252, v192\n\t"
    "v_mul_f32 v193, v252, v193\n\t"
    "v_cvt_pk_bf16_f32 v20, v192, v193\n\t"
    "v_mul_f32 v194, v252, v194\n\t"
    "v_mul_f32 v195, v252, v195\n\t"
    "v_cvt_pk_bf16_f32 v21, v194, v195\n\t"
    "v_mul_f32 v196, v252, v196\n\t"
    "v_mul_f32 v197, v252, v197\n\t"
    "v_cvt_pk_bf16_f32 v22, v196, v197\n\t"
    "v_mul_f32 v198, v252, v198\n\t"
    "v_mul_f32 v199, v252, v199\n\t"
    "v_cvt_pk_bf16_f32 v23, v198, v199\n\t"
    "s_add_u32 s54, s47, 0x18000\n\t"
    "v_lshl_add_u32 v146, v253, 4, s54\n\t"
    "ds_write_b128 v146, v[16:19] offset:0\n\t"
    "ds_write_b128 v146, v[20:23] offset:1024\n\t"
    "s_xor_b32 s53, s43, 0x10000\n\t"
    "s_lshr_b32 s53, s53, 2\n\t"
    "s_add_u32 s53, s53, %[obase]\n\t"
    "buffer_store_dwordx4 v[16:19], v142, %[srdws], s53 offen sc1\n\t"
    "buffer_store_dwordx4 v[20:23], v143, %[srdws], s53 offen offset:1024 sc1\n\t"
    "v_mul_f32 v200, v251, v200\n\t"
    "v_mul_f32 v201, v251, v201\n\t"
    "v_cvt_pk_bf16_f32 v24, v200, v201\n\t"
    "v_mul_f32 v202, v251, v202\n\t"
    "v_mul_f32 v203, v251, v203\n\t"
    "v_cvt_pk_bf16_f32 v25, v202, v203\n\t"
    "v_mul_f32 v204, v251, v204\n\t"
    "v_mul_f32 v205, v251, v205\n\t"
    "v_cvt_pk_bf16_f32 v26, v204, v205\n\t"
    "v_mul_f32 v206, v251, v206\n\t"
    "v_mul_f32 v207, v251, v207\n\t"
    "v_cvt_pk_bf16_f32 v27, v206, v207\n\t"
    "v_mul_f32 v208, v252, v208\n\t"
    "v_mul_f32 v209, v252, v209\n\t"
    "v_cvt_pk_bf16_f32 v28, v208, v209\n\t"
    "v_mul_f32 v210, v252, v210\n\t"
    "v_mul_f32 v211, v252, v211\n\t"
    "v_cvt_pk_bf16_f32 v29, v210, v211\n\t"
    "v_mul_f32 v212, v252, v212\n\t"
    "v_mul_f32 v213, v252, v213\n\t"
    "v_cvt_pk_bf16_f32 v30, v212, v213\n\t"
    "v_mul_f32 v214, v252, v214\n\t"
    "v_mul_f32 v215, v252, v215\n\t"
    "v_cvt_pk_bf16_f32 v31, v214, v215\n\t"
    "ds_write_b128 v146, v[24:27] offset:2048\n\t"
    "ds_write_b128 v146, v[28:31] offset:3072\n\t"
    "buffer_store_dwordx4 v[24:27], v142, %[srdws], s53 offen offset:2048 sc1\n\t"
    "buffer_store_dwordx4 v[28:31], v143, %[srdws], s53 offen offset:3072 sc1\n\t"
    "s_waitcnt lgkmcnt(0)\n\t"
    "s_barrier\n\t"
    "s_add_u32 m0, s47, 0x4000\n\t"
    "s_bfe_u32 s46, %[htab], 0x30004\n\t"
    "s_lshl_b32 s46, s46, 17\n\t"
    "s_or_b32 s46, s46, 0x10000\n\t"
    "buffer_load_dwordx4 v134, %[srdwo], s46 offen lds\n\t"
    "buffer_load_dwordx4 v135, %[srdwo], s46 offen offset:1024 lds\n\t"
    "buffer_load_dwordx4 v136, %[srdwo], s46 offen offset:2048 lds\n\t"
    "buffer_load_dwordx4 v137, %[srdwo], s46 offen offset:3072 lds\n\t"
    "s_add_u32 m0, s47, 0x24000\n\t"
    "s_bfe_u32 s46, %[htab], 0x30008\n\t"
    "s_lshl_b32 s46, s46, 17\n\t"
    "buffer_load_dwordx4 v134, %[srdwo], s46 offen lds\n\t"
    "buffer_load_dwordx4 v135, %[srdwo], s46 offen offset:1024 lds\n\t"
    "buffer_load_dwordx4 v136, %[srdwo], s46 offen offset:2048 lds\n\t"
    "buffer_load_dwordx4 v137, %[srdwo], s46 offen offset:3072 lds\n\t"
    "s_add_u32 m0, s47, 0xc000\n\t"
    "s_bfe_u32 s46, %[htab], 0x30008\n\t"
    "s_lshl_b32 s46, s46, 17\n\t"
    "s_or_b32 s46, s46, 0x10000\n\t"
    "buffer_load_dwordx4 v134, %[srdwo], s46 offen lds\n\t"
    "buffer_load_dwordx4 v135, %[srdwo], s46 offen offset:1024 lds\n\t"
    "buffer_load_dwordx4 v136, %[srdwo], s46 offen offset:2048 lds\n\t"
    "buffer_load_dwordx4 v137, %[srdwo], s46 offen offset:3072 lds\n\t"
    "s_barrier\n\t"
    "s_lshl_b32 s55, s50, 3\n\t"
    "s_add_u32 s55, s55, %[f1base]\n\t"
    "s_lshr_b32 s52, s43, 12\n\t"
    "s_add_u32 s55, s55, s52\n\t"
    "s_waitcnt vmcnt(12)\n\t"
    "buffer_store_dwordx2 v[148:149], v144, %[srdws], s55 offen sc1\n\t"
    "s_lshl_b32 s55, s50, 3\n\t"
    "s_add_u32 s55, s55, %[f1base]\n\t"
    "s_add_u32 s55, s55, 0x80\n\t"
    "s_add_u32 m0, s47, 0x20000\n\t"
    "s_bfe_u32 s46, %[htab], 0x3000c\n\t"
    "s_lshl_b32 s46, s46, 17\n\t"
    "buffer_load_dwordx4 v134, %[srdwo], s46 offen lds\n\t"
    "buffer_load_dwordx4 v135, %[srdwo], s46 offen offset:1024 lds\n\t"
    "buffer_load_dwordx4 v136, %[srdwo], s46 offen offset:2048 lds\n\t"
    "buffer_load_dwordx4 v137, %[srdwo], s46 offen offset:3072 lds\n\t"
    "s_waitcnt vmcnt(13)\n\t"
    "s_barrier\n\t"
    "s_add_u32 m0, s47, 0x14000\n\t"
    "s_xor_b32 s46, s42, 0x50000\n\t"
    "s_lshr_b32 s46, s46, 2\n\t"
    "s_add_u32 s46, s46, %[obase]\n\t"
    "buffer_load_dwordx4 v142, %[srdws], s46 offen sc1 lds\n\t"
    "buffer_load_dwordx4 v143, %[srdws], s46 offen offset:1024 sc1 lds\n\t"
    "buffer_load_dwordx4 v142, %[srdws], s46 offen offset:2048 sc1 lds\n\t"
    "buffer_load_dwordx4 v143, %[srdws], s46 offen offset:3072 sc1 lds\n\t"
    "s_add_u32 m0, s47, 0x18000\n\t"
    "s_xor_b32 s46, s42, 0x60000\n\t"
    "s_lshr_b32 s46, s46, 2\n\t"
    "s_add_u32 s46, s46, %[obase]\n\t"
    "buffer_load_dwordx4 v142, %[srdws], s46 offen sc1 lds\n\t"
    "buffer_load_dwordx4 v143, %[srdws], s46 offen offset:1024 sc1 lds\n\t"
    "buffer_load_dwordx4 v142, %[srdws], s46 offen offset:2048 sc1 lds\n\t"
    "buffer_load_dwordx4 v143, %[srdws], s46 offen offset:3072 sc1 lds\n\t"
    "s_waitcnt vmcnt(4)\n\t"
    "s_barrier\n\t"
    "buffer_load_dwordx2 v[150:151], v145, %[srdws], s55 offen sc1\n\t"
    "s_add_u32 m0, s47, 0x8000\n\t"
    "s_xor_b32 s46, s42, 0x70000\n\t"
    "s_lshr_b32 s46, s46, 2\n\t"
    "s_add_u32 s46, s46, %[obase]\n\t"
    "buffer_load_dwordx4 v142, %[srdws], s46 offen sc1 lds\n\t"
    "buffer_load_dwordx4 v143, %[srdws], s46 offen offset:1024 sc1 lds\n\t"
    "buffer_load_dwordx4 v142, %[srdws], s46 offen offset:2048 sc1 lds\n\t"
    "buffer_load_dwordx4 v143, %[srdws], s46 offen offset:3072 sc1 lds\n\t"
    "s_add_u32 m0, s47, 0x4000\n\t"
    "s_bfe_u32 s46, %[htab], 0x3000c\n\t"
    "s_lshl_b32 s46, s46, 17\n\t"
    "s_or_b32 s46, s46, 0x10000\n\t"
    "buffer_load_dwordx4 v134, %[srdwo], s46 offen lds\n\t"
    "buffer_load_dwordx4 v135, %[srdwo], s46 offen offset:1024 lds\n\t"
    "buffer_load_dwordx4 v136, %[srdwo], s46 offen offset:2048 lds\n\t"
    "buffer_load_dwordx4 v137, %[srdwo], s46 offen offset:3072 lds\n\t"
    "s_add_u32 m0, s47, 0x10000\n\t"
    "s_bfe_u32 s46, %[htab], 0x30014\n\t"
    "s_lshl_b32 s46, s46, 17\n\t"
    "buffer_load_dwordx4 v134, %[srdwo], s46 offen lds\n\t"
    "buffer_load_dwordx4 v135, %[srdwo], s46 offen offset:1024 lds\n\t"
    "buffer_load_dwordx4 v136, %[srdwo], s46 offen offset:2048 lds\n\t"
    "buffer_load_dwordx4 v137, %[srdwo], s46 offen offset:3072 lds\n\t"
    "s_mov_b32 s54, 2000000\n\t"
    "s_waitcnt vmcnt(12)\n\t"
    "v_cmp_ne_u64 vcc, s[44:45], v[150:151]\n\t"
    "s_nop 3\n\t"
    "s_and_b64 vcc, vcc, 15\n\t"
    "s_cbranch_scc0 PB_done%=\n\t"
    "PB_loop%=:\n\t"
    "buffer_load_dwordx2 v[150:151], v145, %[srdws], s55 offen sc1\n\t"
    "s_sub_u32 s54, s54, 1\n\t"
    "s_cbranch_scc1 PB_tmo%=\n\t"
    "s_waitcnt vmcnt(0)\n\t"
    "v_cmp_ne_u64 vcc, s[44:45], v[150:151]\n\t"
    "s_nop 3\n\t"
    "s_and_b64 vcc, vcc, 15\n\t"
    "s_cbranch_scc1 PB_loop%=\n\t"
    "PB_tmo%=:\n\t"
    "s_waitcnt vmcnt(0)\n\t"
    "PB_done%=:\n\t"
    "s_add_u32 m0, s47, 0x0\n\t"
    "s_xor_b32 s46, s43, 0x20000\n\t"
    "s_lshr_b32 s46, s46, 2\n\t"
    "s_add_u32 s46, s46, %[obase]\n\t"
    "buffer_load_dwordx4 v142, %[srdws], s46 offen sc1 lds\n\t"
    "buffer_load_dwordx4 v143, %[srdws], s46 offen offset:1024 sc1 lds\n\t"
    "buffer_load_dwordx4 v142, %[srdws], s46 offen offset:2048 sc1 lds\n\t"
    "buffer_load_dwordx4 v143, %[srdws], s46 offen offset:3072 sc1 lds\n\t"
    "s_waitcnt vmcnt(8)\n\t"
    "s_barrier\n\t"
    "s_add_u32 m0, s47, 0x24000\n\t"
    "s_bfe_u32 s46, %[htab], 0x30014\n\t"
    "s_lshl_b32 s46, s46, 17\n\t"
    "s_or_b32 s46, s46, 0x10000\n\t"
    "buffer_load_dwordx4 v134, %[srdwo], s46 offen lds\n\t"
    "buffer_load_dwordx4 v135, %[srdwo], s46 offen offset:1024 lds\n\t"
    "buffer_load_dwordx4 v136, %[srdwo], s46 offen offset:2048 lds\n\t"
    "buffer_load_dwordx4 v137, %[srdwo], s46 offen offset:3072 lds\n\t"
    "s_add_u32 m0, s47, 0xc000\n\t"
    "s_xor_b32 s46, s43, 0x30000\n\t"
    "s_lshr_b32 s46, s46, 2\n\t"
    "s_add_u32 s46, s46, %[obase]\n\t"
    "buffer_load_dwordx4 v142, %[srdws], s46 offen sc1 lds\n\t"
    "buffer_load_dwordx4 v143, %[srdws], s46 offen offset:1024 sc1 lds\n\t"
    "buffer_load_dwordx4 v142, %[srdws], s46 offen offset:2048 sc1 lds\n\t"
    "buffer_load_dwordx4 v143, %[srdws], s46 offen offset:3072 sc1 lds\n\t"
    "s_add_u32 m0, s47, 0x1c000\n\t"
    "s_bfe_u32 s46, %[htab], 0x30018\n\t"
    "s_lshl_b32 s46, s46, 17\n\t"
    "buffer_load_dwordx4 v134, %[srdwo], s46 offen lds\n\t"
    "buffer_load_dwordx4 v135, %[srdwo], s46 offen offset:1024 lds\n\t"
    "buffer_load_dwordx4 v136, %[srdwo], s46 offen offset:2048 lds\n\t"
    "buffer_load_dwordx4 v137, %[srdwo], s46 offen offset:3072 lds\n\t"
    "s_add_u32 m0, s47, 0x14000\n\t"
    "s_xor_b32 s46, s43, 0x40000\n\t"
    "s_lshr_b32 s46, s46, 2\n\t"
    "s_add_u32 s46, s46, %[obase]\n\t"
    "buffer_load_dwordx4 v142, %[srdws], s46 offen sc1 lds\n\t"
    "buffer_load_dwordx4 v143, %[srdws], s46 offen offset:1024 sc1 lds\n\t"
    "buffer_load_dwordx4 v142, %[srdws], s46 offen offset:2048 sc1 lds\n\t"
    "buffer_load_dwordx4 v143, %[srdws], s46 offen offset:3072 sc1 lds\n\t"
    "s_waitcnt vmcnt(8)\n\t"
    "s_barrier\n\t"
    "s_add_u32 m0, s47, 0x20000\n\t"
    "s_bfe_u32 s46, %[htab], 0x30018\n\t"
    "s_lshl_b32 s46, s46, 17\n\t"
    "s_or_b32 s46, s46, 0x10000\n\t"
    "buffer_load_dwordx4 v134, %[srdwo], s46 offen lds\n\t"
    "buffer_load_dwordx4 v135, %[srdwo], s46 offen offset:1024 lds\n\t"
    "buffer_load_dwordx4 v136, %[srdwo], s46 offen offset:2048 lds\n\t"
    "buffer_load_dwordx4 v137, %[srdwo], s46 offen offset:3072 lds\n\t"
    "s_add_u32 m0, s47, 0x18000\n\t"
    "s_xor_b32 s46, s43, 0x50000\n\t"
    "s_lshr_b32 s46, s46, 2\n\t"
    "s_add_u32 s46, s46, %[obase]\n\t"
    "buffer_load_dwordx4 v142, %[srdws], s46 offen sc1 lds\n\t"
    "buffer_load_dwordx4 v143, %[srdws], s46 offen offset:1024 sc1 lds\n\t"
    "buffer_load_dwordx4 v142, %[srdws], s46 offen offset:2048 sc1 lds\n\t"
    "buffer_load_dwordx4 v143, %[srdws], s46 offen offset:3072 sc1 lds\n\t"
    "s_add_u32 m0, s47, 0x8000\n\t"
    "s_bfe_u32 s46, %[htab], 0x3001c\n\t"
    "s_lshl_b32 s46, s46, 17\n\t"
    "buffer_load_dwordx4 v134, %[srdwo], s46 offen lds\n\t"
    "buffer_load_dwordx4 v135, %[srdwo], s46 offen offset:1024 lds\n\t"
    "buffer_load_dwordx4 v136, %[srdwo], s46 offen offset:2048 lds\n\t"
    "buffer_load_dwordx4 v137, %[srdwo], s46 offen offset:3072 lds\n\t"
    "s_add_u32 m0, s47, 0x4000\n\t"
    "s_xor_b32 s46, s43, 0x60000\n\t"
    "s_lshr_b32 s46, s46, 2\n\t"
    "s_add_u32 s46, s46, %[obase]\n\t"
    "buffer_load_dwordx4 v142, %[srdws], s46 offen sc1 lds\n\t"
    "buffer_load_dwordx4 v143, %[srdws], s46 offen offset:1024 sc1 lds\n\t"
    "buffer_load_dwordx4 v142, %[srdws], s46 offen offset:2048 sc1 lds\n\t"
    "buffer_load_dwordx4 v143, %[srdws], s46 offen offset:3072 sc1 lds\n\t"
    "s_waitcnt vmcnt(8)\n\t"
    "s_barrier\n\t"
    "s_add_u32 m0, s47, 0x10000\n\t"
    "s_bfe_u32 s46, %[htab], 0x3001c\n\t"
    "s_lshl_b32 s46, s46, 17\n\t"
    "s_or_b32 s46, s46, 0x10000\n\t"
    "buffer_load_dwordx4 v134, %[srdwo], s46 offen lds\n\t"
    "buffer_load_dwordx4 v135, %[srdwo], s46 offen offset:1024 lds\n\t"
    "buffer_load_dwordx4 v136, %[srdwo], s46 offen offset:2048 lds\n\t"
    "buffer_load_dwordx4 v137, %[srdwo], s46 offen offset:3072 lds\n\t"
    "s_add_u32 m0, s47, 0x0\n\t"
    "s_xor_b32 s46, s43, 0x70000\n\t"
    "s_lshr_b32 s46, s46, 2\n\t"
    "s_add_u32 s46, s46, %[obase]\n\t"
    "buffer_load_dwordx4 v142, %[srdws], s46 offen sc1 lds\n\t"
    "buffer_load_dwordx4 v143, %[srdws], s46 offen offset:1024 sc1 lds\n\t"
    "buffer_load_dwordx4 v142, %[srdws], s46 offen offset:2048 sc1 lds\n\t"
    "buffer_load_dwordx4 v143, %[srdws], s46 offen offset:3072 sc1 lds\n\t"
    "s_waitcnt vmcnt(0)\n\t"
    "s_branch END_%=\n\t"
    "G1V0_%=:\n\t"
    "s_waitcnt vmcnt(12)\n\t"
    "s_barrier\n\t"
    "s_add_u32 m0, s47, 0x14000\n\t"
    "s_lshr_b32 s46, s48, 9\n\t"
    "s_add_u32 s46, s46, s58\n\t"
    "s_add_u32 s46, s46, 0x100000\n\t"
    "s_and_b32 s46, s46, 0x3fffff\n\t"
    "buffer_load_dwordx4 v134, %[srdwq], s46 offen lds\n\t"
    "buffer_load_dwordx4 v135, %[srdwq], s46 offen offset:1024 lds\n\t"
    "buffer_load_dwordx4 v136, %[srdwq], s46 offen offset:2048 lds\n\t"
    "buffer_load_dwordx4 v137, %[srdwq], s46 offen offset:3072 lds\n\t"
    "ds_read_b128 v[0:3], v250 offset:16384\n\t"
    "ds_read_b128 v[4:7], v250 offset:18432\n\t"
    "ds_read_b128 v[8:11], v250 offset:16896\n\t"
    "ds_read_b128 v[12:15], v250 offset:18944\n\t"
    "s_add_u32 m0, s47, 0x18000\n\t"
    "s_add_u32 s46, s57, 0x100\n\t"
    "s_and_b32 s46, s46, 0x3ff\n\t"
    "buffer_load_dwordx4 v138, %[srdx], s46 offen lds\n\t"
    "buffer_load_dwordx4 v139, %[srdx], s46 offen offset:1024 lds\n\t"
    "buffer_load_dwordx4 v140, %[srdx], s46 offen offset:2048 lds\n\t"
    "buffer_load_dwordx4 v141, %[srdx], s46 offen offset:3072 lds\n\t"
    "s_waitcnt vmcnt(12)\n\t"
    "s_waitcnt lgkmcnt(0)\n\t"
    "s_barrier\n\t"
    "s_add_u32 m0, s47, 0x1c000\n\t"
    "s_lshr_b32 s46, s48, 9\n\t"
    "s_add_u32 s46, s46, s58\n\t"
    "s_add_u32 s46, s46, 0x180000\n\t"
    "s_and_b32 s46, s46, 0x3fffff\n\t"
    "buffer_load_dwordx4 v134, %[srdwq], s46 offen lds\n\t"
    "buffer_load_dwordx4 v135, %[srdwq], s46 offen offset:1024 lds\n\t"
    "buffer_load_dwordx4 v136, %[srdwq], s46 offen offset:2048 lds\n\t"
    "buffer_load_dwordx4 v137, %[srdwq], s46 offen offset:3072 lds\n\t"
    "ds_read_b128 v[16:19], v250 offset:49152\n\t"
    "ds_read_b128 v[20:23], v250 offset:51200\n\t"
    "ds_read_b128 v[24:27], v250 offset:49664\n\t"
    "ds_read_b128 v[28:31], v250 offset:51712\n\t"
    "s_add_u32 m0, s47, 0x20000\n\t"
    "s_add_u32 s46, s57, 0x180\n\t"
    "s_and_b32 s46, s46, 0x3ff\n\t"
    "buffer_load_dwordx4 v138, %[srdx], s46 offen lds\n\t"
    "buffer_load_dwordx4 v139, %[srdx], s46 offen offset:1024 lds\n\t"
    "buffer_load_dwordx4 v140, %[srdx], s46 offen offset:2048 lds\n\t"
    "buffer_load_dwordx4 v141, %[srdx], s46 offen offset:3072 lds\n\t"
    "s_waitcnt vmcnt(8)\n\t"
    "s_waitcnt lgkmcnt(0)\n\t"
    "s_barrier\n\t"
    "s_add_u32 m0, s47, 0x8000\n\t"
    "s_lshr_b32 s46, s48, 9\n\t"
    "s_add_u32 s46, s46, s58\n\t"
    "s_add_u32 s46, s46, 0x200000\n\t"
    "s_and_b32 s46, s46, 0x3fffff\n\t"
    "buffer_load_dwordx4 v134, %[srdwq], s46 offen lds\n\t"
    "buffer_load_dwordx4 v135, %[srdwq], s46 offen offset:1024 lds\n\t"
    "buffer_load_dwordx4 v136, %[srdwq], s46 offen offset:2048 lds\n\t"
    "buffer_load_dwordx4 v137, %[srdwq], s46 offen offset:3072 lds\n\t"
    "v_add_u32 v250, 0x10000, v250\n\t"
    "ds_read_b128 v[32:35], v250 offset:32768\n\t"
    "ds_read_b128 v[36:39], v250 offset:34816\n\t"
    "ds_read_b128 v[40:43], v250 offset:33280\n\t"
    "ds_read_b128 v[44:47], v250 offset:35328\n\t"
    "s_add_u32 m0, s47, 0xc000\n\t"
    "s_add_u32 s46, s57, 0x200\n\t"
    "s_and_b32 s46, s46, 0x3ff\n\t"
    "buffer_load_dwordx4 v138, %[srdx], s46 offen lds\n\t"
    "buffer_load_dwordx4 v139, %[srdx], s46 offen offset:1024 lds\n\t"
    "buffer_load_dwordx4 v140, %[srdx], s46 offen offset:2048 lds\n\t"
    "buffer_load_dwordx4 v141, %[srdx], s46 offen offset:3072 lds\n\t"
    "s_waitcnt vmcnt(8)\n\t"
    "s_waitcnt lgkmcnt(0)\n\t"
    "s_barrier\n\t"
    "s_add_u32 m0, s47, 0x14000\n\t"
    "s_lshr_b32 s46, s48, 9\n\t"
    "s_add_u32 s46, s46, s58\n\t"
    "s_add_u32 s46, s46, 0x280000\n\t"
    "s_and_b32 s46, s46, 0x3fffff\n\t"
    "buffer_load_dwordx4 v134, %[srdwq], s46 offen lds\n\t"
    "buffer_load_dwordx4 v135, %[srdwq], s46 offen offset:1024 lds\n\t"
    "buffer_load_dwordx4 v136, %[srdwq], s46 offen offset:2048 lds\n\t"
    "buffer_load_dwordx4 v137, %[srdwq], s46 offen offset:3072 lds\n\t"
    "v_add_u32 v250, 0x10000, v250\n\t"
    "ds_read_b128 v[48:51], v250 offset:0\n\t"
    "ds_read_b128 v[52:55], v250 offset:2048\n\t"
    "ds_read_b128 v[56:59], v250 offset:512\n\t"
    "ds_read_b128 v[60:63], v250 offset:2560\n\t"
    "s_add_u32 m0, s47, 0x18000\n\t"
    "s_add_u32 s46, s57, 0x280\n\t"
    "s_and_b32 s46, s46, 0x3ff\n\t"
    "buffer_load_dwordx4 v138, %[srdx], s46 offen lds\n\t"
    "buffer_load_dwordx4 v139, %[srdx], s46 offen offset:1024 lds\n\t"
    "buffer_load_dwordx4 v140, %[srdx], s46 offen offset:2048 lds\n\t"
    "buffer_load_dwordx4 v141, %[srdx], s46 offen offset:3072 lds\n\t"
    "s_add_u32 m0, s47, 0x24000\n\t"
    "s_add_u32 s46, s59, 0x0\n\t"
    "s_and_b32 s46, s46, 0x1ffff\n\t"
    "s_or_b32 s46, s46, s48\n\t"
    "s_waitcnt vmcnt(12)\n\t"
    "buffer_load_dwordx4 v128, %[srdk], s46 offen nt lds\n\t"
    "s_waitcnt vmcnt(12)\n\t"
    "buffer_load_dwordx4 v129, %[srdk], s46 offen offset:1024 nt lds\n\t"
    "s_waitcnt vmcnt(12)\n\t"
    "buffer_load_dwordx4 v130, %[srdk], s46 offen offset:2048 nt lds\n\t"
    "s_waitcnt vmcnt(12)\n\t"
    "buffer_load_dwordx4 v131, %[srdk], s46 offen offset:3072 nt lds\n\t"
    "s_add_u32 m0, s47, 0x0\n\t"
    "s_add_u32 s46, s59, 0x4000\n\t"
    "s_and_b32 s46, s46, 0x1ffff\n\t"
    "s_or_b32 s46, s46, s48\n\t"
    "s_waitcnt vmcnt(12)\n\t"
    "buffer_load_dwordx4 v128, %[srdk], s46 offen nt lds\n\t"
    "s_waitcnt vmcnt(12)\n\t"
    "buffer_load_dwordx4 v129, %[srdk], s46 offen offset:1024 nt lds\n\t"
    "s_waitcnt vmcnt(12)\n\t"
    "buffer_load_dwordx4 v130, %[srdk], s46 offen offset:2048 nt lds\n\t"
    "s_waitcnt vmcnt(12)\n\t"
    "buffer_load_dwordx4 v131, %[srdk], s46 offen offset:3072 nt lds\n\t"
    "s_add_u32 m0, s47, 0x4000\n\t"
    "s_add_u32 s46, s59, 0x8000\n\t"
    "s_and_b32 s46, s46, 0x1ffff\n\t"
    "s_or_b32 s46, s46, s48\n\t"
    "s_waitcnt vmcnt(12)\n\t"
    "buffer_load_dwordx4 v128, %[srdk], s46 offen nt lds\n\t"
    "s_waitcnt vmcnt(12)\n\t"
    "buffer_load_dwordx4 v129, %[srdk], s46 offen offset:1024 nt lds\n\t"
    "s_waitcnt vmcnt(12)\n\t"
    "buffer_load_dwordx4 v130, %[srdk], s46 offen offset:2048 nt lds\n\t"
    "s_waitcnt vmcnt(12)\n\t"
    "buffer_load_dwordx4 v131, %[srdk], s46 offen offset:3072 nt lds\n\t"
    "s_waitcnt lgkmcnt(0)\n\t"
    "s_barrier\n\t"
    "s_add_u32 m0, s47, 0x1c000\n\t"
    "s_lshr_b32 s46, s48, 9\n\t"
    "s_add_u32 s46, s46, s58\n\t"
    "s_add_u32 s46, s46, 0x300000\n\t"
    "s_and_b32 s46, s46, 0x3fffff\n\t"
    "buffer_load_dwordx4 v134, %[srdwq], s46 offen lds\n\t"
    "buffer_load_dwordx4 v135, %[srdwq], s46 offen offset:1024 lds\n\t"
    "buffer_load_dwordx4 v136, %[srdwq], s46 offen offset:2048 lds\n\t"
    "buffer_load_dwordx4 v137, %[srdwq], s46 offen offset:3072 lds\n\t"
    "v_add_u32 v250, 0xfffe0000, v250\n\t"
    "ds_read_b128 v[64:67], v250 offset:49152\n\t"
    "ds_read_b128 v[68:71], v250 offset:51200\n\t"
    "ds_read_b128 v[72:75], v250 offset:49664\n\t"
    "ds_read_b128 v[76:79], v250 offset:51712\n\t"
    "s_add_u32 m0, s47, 0x20000\n\t"
    "s_add_u32 s46, s57, 0x300\n\t"
    "s_and_b32 s46, s46, 0x3ff\n\t"
    "buffer_load_dwordx4 v138, %[srdx], s46 offen lds\n\t"
    "buffer_load_dwordx4 v139, %[srdx], s46 offen offset:1024 lds\n\t"
    "buffer_load_dwordx4 v140, %[srdx], s46 offen offset:2048 lds\n\t"
    "buffer_load_dwordx4 v141, %[srdx], s46 offen offset:3072 lds\n\t"
    "s_waitcnt vmcnt(20)\n\t"
    "s_waitcnt lgkmcnt(0)\n\t"
    "s_barrier\n\t"
    "s_add_u32 m0, s47, 0x8000\n\t"
    "s_lshr_b32 s46, s48, 9\n\t"
    "s_add_u32 s46, s46, s58\n\t"
    "s_add_u32 s46, s46, 0x380000\n\t"
    "s_and_b32 s46, s46, 0x3fffff\n\t"
    "buffer_load_dwordx4 v134, %[srdwq], s46 offen lds\n\t"
    "buffer_load_dwordx4 v135, %[srdwq], s46 offen offset:1024 lds\n\t"
    "buffer_load_dwordx4 v136, %[srdwq], s46 offen offset:2048 lds\n\t"
    "buffer_load_dwordx4 v137, %[srdwq], s46 offen offset:3072 lds\n\t"
    "v_add_u32 v250, 0x10000, v250\n\t"
    "ds_read_b128 v[80:83], v250 offset:32768\n\t"
    "ds_read_b128 v[84:87], v250 offset:34816\n\t"
    "ds_read_b128 v[88:91], v250 offset:33280\n\t"
    "ds_read_b128 v[92:95], v250 offset:35328\n\t"
    "s_add_u32 m0, s47, 0xc000\n\t"
    "s_add_u32 s46, s57, 0x380\n\t"
    "s_and_b32 s46, s46, 0x3ff\n\t"
    "buffer_load_dwordx4 v138, %[srdx], s46 offen lds\n\t"
    "buffer_load_dwordx4 v139, %[srdx], s46 offen offset:1024 lds\n\t"
    "buffer_load_dwordx4 v140, %[srdx], s46 offen offset:2048 lds\n\t"
    "buffer_load_dwordx4 v141, %[srdx], s46 offen offset:3072 lds\n\t"
    "s_waitcnt vmcnt(8)\n\t"
    "s_waitcnt lgkmcnt(0)\n\t"
    "s_barrier\n\t"
    "s_add_u32 m0, s47, 0x14000\n\t"
    "s_add_u32 s46, s59, 0xc000\n\t"
    "s_and_b32 s46, s46, 0x1ffff\n\t"
    "s_or_b32 s46, s46, s48\n\t"
    "buffer_load_dwordx4 v128, %[srdk], s46 offen nt lds\n\t"
    "buffer_load_dwordx4 v129, %[srdk], s46 offen offset:1024 nt lds\n\t"
    "buffer_load_dwordx4 v130, %[srdk], s46 offen offset:2048 nt lds\n\t"
    "buffer_load_dwordx4 v131, %[srdk], s46 offen offset:3072 nt lds\n\t"
    "v_add_u32 v250, 0x10000, v250\n\t"
    "ds_read_b128 v[96:99], v250 offset:0\n\t"
    "ds_read_b128 v[100:103], v250 offset:2048\n\t"
    "ds_read_b128 v[104:107], v250 offset:512\n\t"
    "ds_read_b128 v[108:111], v250 offset:2560\n\t"
    "s_add_u32 m0, s47, 0x18000\n\t"
    "s_add_u32 s46, s59, 0x10000\n\t"
    "s_and_b32 s46, s46, 0x1ffff\n\t"
    "s_or_b32 s46, s46, s48\n\t"
    "buffer_load_dwordx4 v128, %[srdk], s46 offen nt lds\n\t"
    "s_waitcnt vmcnt(12)\n\t"
    "buffer_load_dwordx4 v129, %[srdk], s46 offen offset:1024 nt lds\n\t"
    "s_waitcnt vmcnt(12)\n\t"
    "buffer_load_dwordx4 v130, %[srdk], s46 offen offset:2048 nt lds\n\t"
    "s_waitcnt vmcnt(12)\n\t"
    "buffer_load_dwordx4 v131, %[srdk], s46 offen offset:3072 nt lds\n\t"
    "s_branch G1V_%=\n\t"
    "G0_%=:\n\t"
    "s_getpc_b64 s[62:63]\n\t"
    "PF1_%=:\n\t"
    "s_sub_u32 s62, s62, PF1_%=-START_%=\n\t"
    "s_subb_u32 s63, s63, 0\n\t"
    "s_and_b32 s54, %[wj], 15\n\t"
    "s_lshl_b32 s54, s54, 2\n\t"
    "s_add_u32 s54, s54, s50\n\t"
    "s_lshl_b32 s54, s54, 11\n\t"
    "v_lshl_add_u32 v255, v253, 5, s54\n\t"
    "v_min_u32 v255, END_%=-START_%=-4, v255\n\t"
    "global_load_dword v128, v255, s[62:63]\n\t"
    "s_lshl_b32 s42, %[qi], 17\n\t"
    "s_or_b32 s43, s42, 0x80000\n\t"
    "s_cmp_eq_u32 s50, 3\n\t"
    "s_mov_b32 s60, 0xc1c00000\n\t"
    "s_cselect_b32 s60, 0xff800000, s60\n\t"
    "s_cselect_b32 s61, 0x80000000, 0\n\t"
    "v_mov_b32 v248, %[lds]\n\t"
    "v_bfe_u32 v254, v253, 4, 1\n\t"
    "v_lshl_add_u32 v248, v254, 12, v248\n\t"
    "v_bfe_u32 v254, v253, 5, 1\n\t"
    "v_lshl_add_u32 v248, v254, 11, v248\n\t"
    "v_bfe_u32 v254, v253, 2, 2\n\t"
    "v_lshl_add_u32 v248, v254, 6, v248\n\t"
    "v_bfe_u32 v254, v253, 4, 1\n\t"
    "v_lshl_add_u32 v248, v254, 5, v248\n\t"
    "v_bfe_u32 v254, v253, 0, 2\n\t"
    "v_lshl_add_u32 v248, v254, 3, v248\n\t"
    "v_xor_b32 v249, 32, v248\n\t"
    "v_mov_b32 v250, s47\n\t"
    "v_bfe_u32 v254, v253, 3, 1\n\t"
    "v_lshl_add_u32 v250, v254, 10, v250\n\t"
    "v_bfe_u32 v254, v253, 0, 3\n\t"
    "v_lshl_add_u32 v250, v254, 6, v250\n\t"
    "v_bfe_u32 v254, v253, 4, 1\n\t"
    "v_lshl_add_u32 v250, v254, 5, v250\n\t"
    "v_bfe_u32 v254, v253, 5, 1\n\t"
    "v_lshl_add_u32 v250, v254, 4, v250\n\t"
    "v_bfe_u32 v254, v253, 3, 1\n\t"
    "v_lshlrev_b32 v254, 4, v254\n\t"
    "v_xor_b32 v250, v250, v254\n\t"
    "s_waitcnt lgkmcnt(0)\n\t"
    "s_add_u32 s44, s44, 0x7f4a7c15\n\t"
    "s_addc_u32 s45, s45, 0x9e3779b9\n\t"
    "s_cmp_eq_u32 s50, 3\n\t"
    "s_cbranch_scc1 W3S0_%=\n\t"
    "s_barrier\n\t"
    "ds_read_b128 v[0:3], v250 offset:16384\n\t"
    "ds_read_b128 v[4:7], v250 offset:18432\n\t"
    "ds_read_b128 v[8:11], v250 offset:16896\n\t"
    "ds_read_b128 v[12:15], v250 offset:18944\n\t"
    "ds_read_b64_tr_b16 v[216:217], v248 offset:0\n\t"
    "ds_read_b64_tr_b16 v[218:219], v248 offset:256\n\t"
    "ds_read_b64_tr_b16 v[220:221], v248 offset:8192\n\t"
    "ds_read_b64_tr_b16 v[222:223], v248 offset:8448\n\t"
    "ds_read_b64_tr_b16 v[224:225], v249 offset:0\n\t"
    "ds_read_b64_tr_b16 v[226:227], v249 offset:256\n\t"
    "ds_read_b64_tr_b16 v[228:229], v249 offset:8192\n\t"
    "ds_read_b64_tr_b16 v[230:231], v249 offset:8448\n\t"
    "ds_read_b64_tr_b16 v[232:233], v248 offset:512\n\t"
    "ds_read_b64_tr_b16 v[234:235], v248 offset:768\n\t"
    "ds_read_b64_tr_b16 v[236:237], v248 offset:8704\n\t"
    "ds_read_b64_tr_b16 v[238:239], v248 offset:8960\n\t"
    "ds_read_b64_tr_b16 v[240:241], v249 offset:512\n\t"
    "ds_read_b64_tr_b16 v[242:243], v249 offset:768\n\t"
    "ds_read_b64_tr_b16 v[244:245], v249 offset:8704\n\t"
    "ds_read_b64_tr_b16 v[246:247], v249 offset:8960\n\t"
    "s_waitcnt lgkmcnt(12)\n\t"
    "v_mfma_f32_16x16x32_bf16 v[152:155], v[216:219], v[0:3], 0\n\t"
    "v_mfma_f32_16x16x32_bf16 v[152:155], v[220:223], v[8:11], v[152:155]\n\t"
    "v_mfma_f32_16x16x32_bf16 v[160:163], v[220:223], v[12:15], 0\n\t"
    "v_mfma_f32_16x16x32_bf16 v[160:163], v[216:219], v[4:7], v[160:163]\n\t"
    "ds_read_b64_tr_b16 v[216:217], v248 offset:1024\n\t"
    "s_waitcnt lgkmcnt(9)\n\t"
    "v_mfma_f32_16x16x32_bf16 v[156:159], v[224:227], v[0:3], 0\n\t"
    "ds_read_b64_tr_b16 v[218:219], v248 offset:1280\n\t"
    "v_mfma_f32_16x16x32_bf16 v[156:159], v[228:231], v[8:11], v[156:159]\n\t"
    "ds_read_b64_tr_b16 v[220:221], v248 offset:9216\n\t"
    "v_mfma_f32_16x16x32_bf16 v[164:167], v[228:231], v[12:15], 0\n\t"
    "ds_read_b64_tr_b16 v[222:223], v248 offset:9472\n\t"
    "v_mfma_f32_16x16x32_bf16 v[164:167], v[224:227], v[4:7], v[164:167]\n\t"
    "ds_read_b64_tr_b16 v[224:225], v249 offset:1024\n\t"
    "s_waitcnt lgkmcnt(9)\n\t"
    "v_mfma_f32_16x16x32_bf16 v[168:171], v[232:235], v[0:3], 0\n\t"
    "ds_read_b64_tr_b16 v[226:227], v249 offset:1280\n\t"
    "v_mfma_f32_16x16x32_bf16 v[168:171], v[236:239], v[8:11], v[168:171]\n\t"
    "ds_read_b64_tr_b16 v[228:229], v249 offset:9216\n\t"
    "v_mfma_f32_16x16x32_bf16 v[176:179], v[236:239], v[12:15], 0\n\t"
    "ds_read_b64_tr_b16 v[230:231], v249 offset:9472\n\t"
    "v_mfma_f32_16x16x32_bf16 v[176:179], v[232:235], v[4:7], v[176:179]\n\t"
    "ds_read_b64_tr_b16 v[232:233], v248 offset:1536\n\t"
    "s_waitcnt lgkmcnt(9)\n\t"
    "v_mfma_f32_16x16x32_bf16 v[172:175], v[240:243], v[0:3], 0\n\t"
    "ds_read_b64_tr_b16 v[234:235], v248 offset:1792\n\t"
    "v_mfma_f32_16x16x32_bf16 v[172:175], v[244:247], v[8:11], v[172:175]\n\t"
    "ds_read_b64_tr_b16 v[236:237], v248 offset:9728\n\t"
    "v_mfma_f32_16x16x32_bf16 v[180:183], v[244:247], v[12:15], 0\n\t"
    "ds_read_b64_tr_b16 v[238:239], v248 offset:9984\n\t"
    "v_mfma_f32_16x16x32_bf16 v[180:183], v[240:243], v[4:7], v[180:183]\n\t"
    "ds_read_b64_tr_b16 v[240:241], v249 offset:1536\n\t"
    "s_waitcnt lgkmcnt(9)\n\t"
    "v_mfma_f32_16x16x32_bf16 v[184:187], v[216:219], v[0:3], 0\n\t"
    "ds_read_b64_tr_b16 v[242:243], v249 offset:1792\n\t"
    "v_mfma_f32_16x16x32_bf16 v[184:187], v[220:223], v[8:11], v[184:187]\n\t"
    "ds_read_b64_tr_b16 v[244:245], v249 offset:9728\n\t"
    "v_mfma_f32_16x16x32_bf16 v[192:195], v[220:223], v[12:15], 0\n\t"
    "ds_read_b64_tr_b16 v[246:247], v249 offset:9984\n\t"
    "v_mfma_f32_16x16x32_bf16 v[192:195], v[216:219], v[4:7], v[192:195]\n\t"
    "s_waitcnt lgkmcnt(8)\n\t"
    "v_mfma_f32_16x16x32_bf16 v[188:191], v[224:227], v[0:3], 0\n\t"
    "v_mfma_f32_16x16x32_bf16 v[188:191], v[228:231], v[8:11], v[188:191]\n\t"
    "v_mfma_f32_16x16x32_bf16 v[196:199], v[228:231], v[12:15], 0\n\t"
    "v_mfma_f32_16x16x32_bf16 v[196:199], v[224:227], v[4:7], v[196:199]\n\t"
    "s_waitcnt lgkmcnt(0)\n\t"
    "s_barrier\n\t"
    "ds_read_b128 v[16:19], v250 offset:49152\n\t"
    "ds_read_b128 v[20:23], v250 offset:51200\n\t"
    "ds_read_b128 v[24:27], v250 offset:49664\n\t"
    "v_mfma_f32_16x16x32_bf16 v[200:203], v[232:235], v[0:3], 0\n\t"
    "ds_read_b128 v[28:31], v250 offset:51712\n\t"
    "ds_read_b64_tr_b16 v[216:217], v248 offset:32768\n\t"
    "ds_read_b64_tr_b16 v[218:219], v248 offset:33024\n\t"
    "v_mfma_f32_16x16x32_bf16 v[200:203], v[236:239], v[8:11], v[200:203]\n\t"
    "ds_read_b64_tr_b16 v[220:221], v248 offset:40960\n\t"
    "ds_read_b64_tr_b16 v[222:223], v248 offset:41216\n\t"
    "ds_read_b64_tr_b16 v[224:225], v249 offset:32768\n\t"
    "v_mfma_f32_16x16x32_bf16 v[208:211], v[236:239], v[12:15], 0\n\t"
    "ds_read_b64_tr_b16 v[226:227], v249 offset:33024\n\t"
    "ds_read_b64_tr_b16 v[228:229], v249 offset:40960\n\t"
    "ds_read_b64_tr_b16 v[230:231], v249 offset:41216\n\t"
    "v_mfma_f32_16x16x32_bf16 v[208:211], v[232:235], v[4:7], v[208:211]\n\t"
    "ds_read_b64_tr_b16 v[232:233], v248 offset:33280\n\t"
    "v_mfma_f32_16x16x32_bf16 v[204:207], v[240:243], v[0:3], 0\n\t"
    "ds_read_b64_tr_b16 v[234:235], v248 offset:33536\n\t"
    "v_mfma_f32_16x16x32_bf16 v[204:207], v[244:247], v[8:11], v[204:207]\n\t"
    "ds_read_b64_tr_b16 v[236:237], v248 offset:41472\n\t"
    "v_mfma_f32_16x16x32_bf16 v[212:215], v[244:247], v[12:15], 0\n\t"
    "ds_read_b64_tr_b16 v[238:239], v248 offset:41728\n\t"
    "v_mfma_f32_16x16x32_bf16 v[212:215], v[240:243], v[4:7], v[212:215]\n\t"
    "ds_read_b64_tr_b16 v[240:241], v249 offset:33280\n\t"
    "s_waitcnt lgkmcnt(9)\n\t"
    "v_mfma_f32_16x16x32_bf16 v[152:155], v[216:219], v[16:19], v[152:155]\n\t"
    "ds_read_b64_tr_b16 v[242:243], v249 offset:33536\n\t"
    "v_mfma_f32_16x16x32_bf16 v[152:155], v[220:223], v[24:27], v[152:155]\n\t"
    "ds_read_b64_tr_b16 v[244:245], v249 offset:41472\n\t"
    "v_mfma_f32_16x16x32_bf16 v[160:163], v[220:223], v[28:31], v[160:163]\n\t"
    "ds_read_b64_tr_b16 v[246:247], v249 offset:41728\n\t"
    "v_mfma_f32_16x16x32_bf16 v[160:163], v[216:219], v[20:23], v[160:163]\n\t"
    "ds_read_b64_tr_b16 v[216:217], v248 offset:33792\n\t"
    "s_waitcnt lgkmcnt(9)\n\t"
    "v_mfma_f32_16x16x32_bf16 v[156:159], v[224:227], v[16:19], v[156:159]\n\t"
    "ds_read_b64_tr_b16 v[218:219], v248 offset:34048\n\t"
    "v_mfma_f32_16x16x32_bf16 v[156:159], v[228:231], v[24:27], v[156:159]\n\t"
    "ds_read_b64_tr_b16 v[220:221], v248 offset:41984\n\t"
    "v_mfma_f32_16x16x32_bf16 v[164:167], v[228:231], v[28:31], v[164:167]\n\t"
    "ds_read_b64_tr_b16 v[222:223], v248 offset:42240\n\t"
    "v_mfma_f32_16x16x32_bf16 v[164:167], v[224:227], v[20:23], v[164:167]\n\t"
    "ds_read_b64_tr_b16 v[224:225], v249 offset:33792\n\t"
    "s_waitcnt lgkmcnt(9)\n\t"
    "v_mfma_f32_16x16x32_bf16 v[168:171], v[232:235], v[16:19], v[168:171]\n\t"
    "ds_read_b64_tr_b16 v[226:227], v249 offset:34048\n\t"
    "v_mfma_f32_16x16x32_bf16 v[168:171], v[236:239], v[24:27], v[168:171]\n\t"
    "ds_read_b64_tr_b16 v[228:229], v249 offset:41984\n\t"
    "v_mfma_f32_16x16x32_bf16 v[176:179], v[236:239], v[28:31], v[176:179]\n\t"
    "ds_read_b64_tr_b16 v[230:231], v249 offset:42240\n\t"
    "v_mfma_f32_16x16x32_bf16 v[176:179], v[232:235], v[20:23], v[176:179]\n\t"
    "ds_read_b64_tr_b16 v[232:233], v248 offset:34304\n\t"
    "s_waitcnt lgkmcnt(9)\n\t"
    "v_mfma_f32_16x16x32_bf16 v[172:175], v[240:243], v[16:19], v[172:175]\n\t"
    "ds_read_b64_tr_b16 v[234:235], v248 offset:34560\n\t"
    "v_mfma_f32_16x16x32_bf16 v[172:175], v[244:247], v[24:27], v[172:175]\n\t"
    "ds_read_b64_tr_b16 v[236:237], v248 offset:42496\n\t"
    "v_mfma_f32_16x16x32_bf16 v[180:183], v[244:247], v[28:31], v[180:183]\n\t"
    "ds_read_b64_tr_b16 v[238:239], v248 offset:42752\n\t"
    "v_mfma_f32_16x16x32_bf16 v[180:183], v[240:243], v[20:23], v[180:183]\n\t"
    "ds_read_b64_tr_b16 v[240:241], v249 offset:34304\n\t"
    "s_waitcnt lgkmcnt(9)\n\t"
    "v_mfma_f32_16x16x32_bf16 v[184:187], v[216:219], v[16:19], v[184:187]\n\t"
    "ds_read_b64_tr_b16 v[242:243], v249 offset:34560\n\t"
    "v_mfma_f32_16x16x32_bf16 v[184:187], v[220:223], v[24:27], v[184:187]\n\t"
    "ds_read_b64_tr_b16 v[244:245], v249 offset:42496\n\t"
    "v_mfma_f32_16x16x32_bf16 v[192:195], v[220:223], v[28:31], v[192:195]\n\t"
    "ds_read_b64_tr_b16 v[246:247], v249 offset:42752\n\t"
    "v_mfma_f32_16x16x32_bf16 v[192:195], v[216:219], v[20:23], v[192:195]\n\t"
    "s_waitcnt lgkmcnt(8)\n\t"
    "v_mfma_f32_16x16x32_bf16 v[188:191], v[224:227], v[16:19], v[188:191]\n\t"
    "v_mfma_f32_16x16x32_bf16 v[188:191], v[228:231], v[24:27], v[188:191]\n\t"
    "v_mfma_f32_16x16x32_bf16 v[196:199], v[228:231], v[28:31], v[196:199]\n\t"
    "v_mfma_f32_16x16x32_bf16 v[196:199], v[224:227], v[20:23], v[196:199]\n\t"
    "s_waitcnt lgkmcnt(0)\n\t"
    "s_barrier\n\t"
    "v_add_u32 v250, 0x10000, v250\n\t"
    "ds_read_b128 v[32:35], v250 offset:32768\n\t"
    "ds_read_b128 v[36:39], v250 offset:34816\n\t"
    "ds_read_b128 v[40:43], v250 offset:33280\n\t"
    "v_mfma_f32_16x16x32_bf16 v[200:203], v[232:235], v[16:19], v[200:203]\n\t"
    "ds_read_b128 v[44:47], v250 offset:35328\n\t"
    "v_add_u32 v248, 0x10000, v248\n\t"
    "v_add_u32 v249, 0x10000, v249\n\t"
    "ds_read_b64_tr_b16 v[216:217], v248 offset:16384\n\t"
    "v_mfma_f32_16x16x32_bf16 v[200:203], v[236:239], v[24:27], v[200:203]\n\t"
    "ds_read_b64_tr_b16 v[218:219], v248 offset:16640\n\t"
    "ds_read_b64_tr_b16 v[220:221], v248 offset:24576\n\t"
    "ds_read_b64_tr_b16 v[222:223], v248 offset:24832\n\t"
    "ds_read_b64_tr_b16 v[224:225], v249 offset:16384\n\t"
    "v_mfma_f32_16x16x32_bf16 v[208:211], v[236:239], v[28:31], v[208:211]\n\t"
    "ds_read_b64_tr_b16 v[226:227], v249 offset:16640\n\t"
    "ds_read_b64_tr_b16 v[228:229], v249 offset:24576\n\t"
    "ds_read_b64_tr_b16 v[230:231], v249 offset:24832\n\t"
    "v_mfma_f32_16x16x32_bf16 v[208:211], v[232:235], v[20:23], v[208:211]\n\t"
    "ds_read_b64_tr_b16 v[232:233], v248 offset:16896\n\t"
    "v_mfma_f32_16x16x32_bf16 v[204:207], v[240:243], v[16:19], v[204:207]\n\t"
    "ds_read_b64_tr_b16 v[234:235], v248 offset:17152\n\t"
    "v_mfma_f32_16x16x32_bf16 v[204:207], v[244:247], v[24:27], v[204:207]\n\t"
    "ds_read_b64_tr_b16 v[236:237], v248 offset:25088\n\t"
    "v_mfma_f32_16x16x32_bf16 v[212:215], v[244:247], v[28:31], v[212:215]\n\t"
    "ds_read_b64_tr_b16 v[238:239], v248 offset:25344\n\t"
    "v_mfma_f32_16x16x32_bf16 v[212:215], v[240:243], v[20:23], v[212:215]\n\t"
    "ds_read_b64_tr_b16 v[240:241], v249 offset:16896\n\t"
    "s_waitcnt lgkmcnt(9)\n\t"
    "v_mfma_f32_16x16x32_bf16 v[152:155], v[216:219], v[32:35], v[152:155]\n\t"
    "ds_read_b64_tr_b16 v[242:243], v249 offset:17152\n\t"
    "v_mfma_f32_16x16x32_bf16 v[152:155], v[220:223], v[40:43], v[152:155]\n\t"
    "ds_read_b64_tr_b16 v[244:245], v249 offset:25088\n\t"
    "v_mfma_f32_16x16x32_bf16 v[160:163], v[220:223], v[44:47], v[160:163]\n\t"
    "ds_read_b64_tr_b16 v[246:247], v249 offset:25344\n\t"
    "v_mfma_f32_16x16x32_bf16 v[160:163], v[216:219], v[36:39], v[160:163]\n\t"
    "ds_read_b64_tr_b16 v[216:217], v248 offset:17408\n\t"
    "s_waitcnt lgkmcnt(9)\n\t"
    "v_mfma_f32_16x16x32_bf16 v[156:159], v[224:227], v[32:35], v[156:159]\n\t"
    "ds_read_b64_tr_b16 v[218:219], v248 offset:17664\n\t"
    "v_mfma_f32_16x16x32_bf16 v[156:159], v[228:231], v[40:43], v[156:159]\n\t"
    "ds_read_b64_tr_b16 v[220:221], v248 offset:25600\n\t"
    "v_mfma_f32_16x16x32_bf16 v[164:167], v[228:231], v[44:47], v[164:167]\n\t"
    "ds_read_b64_tr_b16 v[222:223], v248 offset:25856\n\t"
    "v_mfma_f32_16x16x32_bf16 v[164:167], v[224:227], v[36:39], v[164:167]\n\t"
    "ds_read_b64_tr_b16 v[224:225], v249 offset:17408\n\t"
    "s_waitcnt lgkmcnt(9)\n\t"
    "v_mfma_f32_16x16x32_bf16 v[168:171], v[232:235], v[32:35], v[168:171]\n\t"
    "ds_read_b64_tr_b16 v[226:227], v249 offset:17664\n\t"
    "v_mfma_f32_16x16x32_bf16 v[168:171], v[236:239], v[40:43], v[168:171]\n\t"
    "ds_read_b64_tr_b16 v[228:229], v249 offset:25600\n\t"
    "v_mfma_f32_16x16x32_bf16 v[176:179], v[236:239], v[44:47], v[176:179]\n\t"
    "ds_read_b64_tr_b16 v[230:231], v249 offset:25856\n\t"
    "v_mfma_f32_16x16x32_bf16 v[176:179], v[232:235], v[36:39], v[176:179]\n\t"
    "ds_read_b64_tr_b16 v[232:233], v248 offset:17920\n\t"
    "s_waitcnt lgkmcnt(9)\n\t"
    "v_mfma_f32_16x16x32_bf16 v[172:175], v[240:243], v[32:35], v[172:175]\n\t"
    "ds_read_b64_tr_b16 v[234:235], v248 offset:18176\n\t"
    "v_mfma_f32_16x16x32_bf16 v[172:175], v[244:247], v[40:43], v[172:175]\n\t"
    "ds_read_b64_tr_b16 v[236:237], v248 offset:26112\n\t"
    "v_mfma_f32_16x16x32_bf16 v[180:183], v[244:247], v[44:47], v[180:183]\n\t"
    "ds_read_b64_tr_b16 v[238:239], v248 offset:26368\n\t"
    "v_mfma_f32_16x16x32_bf16 v[180:183], v[240:243], v[36:39], v[180:183]\n\t"
    "ds_read_b64_tr_b16 v[240:241], v249 offset:17920\n\t"
    "s_waitcnt lgkmcnt(9)\n\t"
    "v_mfma_f32_16x16x32_bf16 v[184:187], v[216:219], v[32:35], v[184:187]\n\t"
    "ds_read_b64_tr_b16 v[242:243], v249 offset:18176\n\t"
    "v_mfma_f32_16x16x32_bf16 v[184:187], v[220:223], v[40:43], v[184:187]\n\t"
    "ds_read_b64_tr_b16 v[244:245], v249 offset:26112\n\t"
    "v_mfma_f32_16x16x32_bf16 v[192:195], v[220:223], v[44:47], v[192:195]\n\t"
    "ds_read_b64_tr_b16 v[246:247], v249 offset:26368\n\t"
    "v_mfma_f32_16x16x32_bf16 v[192:195], v[216:219], v[36:39], v[192:195]\n\t"
    "s_waitcnt lgkmcnt(8)\n\t"
    "v_mfma_f32_16x16x32_bf16 v[188:191], v[224:227], v[32:35], v[188:191]\n\t"
    "v_mfma_f32_16x16x32_bf16 v[188:191], v[228:231], v[40:43], v[188:191]\n\t"
    "v_mfma_f32_16x16x32_bf16 v[196:199], v[228:231], v[44:47], v[196:199]\n\t"
    "v_mfma_f32_16x16x32_bf16 v[196:199], v[224:227], v[36:39], v[196:199]\n\t"
    "s_waitcnt lgkmcnt(0)\n\t"
    "s_barrier\n\t"
    "v_add_u32 v250, 0x10000, v250\n\t"
    "ds_read_b128 v[48:51], v250 offset:0\n\t"
    "ds_read_b128 v[52:55], v250 offset:2048\n\t"
    "ds_read_b128 v[56:59], v250 offset:512\n\t"
    "v_mfma_f32_16x16x32_bf16 v[200:203], v[232:235], v[32:35], v[200:203]\n\t"
    "ds_read_b128 v[60:63], v250 offset:2560\n\t"
    "ds_read_b64_tr_b16 v[216:217], v248 offset:49152\n\t"
    "ds_read_b64_tr_b16 v[218:219], v248 offset:49408\n\t"
    "v_mfma_f32_16x16x32_bf16 v[200:203], v[236:239], v[40:43], v[200:203]\n\t"
    "ds_read_b64_tr_b16 v[220:221], v248 offset:57344\n\t"
    "ds_read_b64_tr_b16 v[222:223], v248 offset:57600\n\t"
    "ds_read_b64_tr_b16 v[224:225], v249 offset:49152\n\t"
    "v_mfma_f32_16x16x32_bf16 v[208:211], v[236:239], v[44:47], v[208:211]\n\t"
    "ds_read_b64_tr_b16 v[226:227], v249 offset:49408\n\t"
    "ds_read_b64_tr_b16 v[228:229], v249 offset:57344\n\t"
    "ds_read_b64_tr_b16 v[230:231], v249 offset:57600\n\t"
    "v_mfma_f32_16x16x32_bf16 v[208:211], v[232:235], v[36:39], v[208:211]\n\t"
    "ds_read_b64_tr_b16 v[232:233], v248 offset:49664\n\t"
    "v_mfma_f32_16x16x32_bf16 v[204:207], v[240:243], v[32:35], v[204:207]\n\t"
    "ds_read_b64_tr_b16 v[234:235], v248 offset:49920\n\t"
    "v_mfma_f32_16x16x32_bf16 v[204:207], v[244:247], v[40:43], v[204:207]\n\t"
    "ds_read_b64_tr_b16 v[236:237], v248 offset:57856\n\t"
    "v_mfma_f32_16x16x32_bf16 v[212:215], v[244:247], v[44:47], v[212:215]\n\t"
    "ds_read_b64_tr_b16 v[238:239], v248 offset:58112\n\t"
    "v_mfma_f32_16x16x32_bf16 v[212:215], v[240:243], v[36:39], v[212:215]\n\t"
    "ds_read_b64_tr_b16 v[240:241], v249 offset:49664\n\t"
    "s_waitcnt lgkmcnt(9)\n\t"
    "v_mfma_f32_16x16x32_bf16 v[152:155], v[216:219], v[48:51], v[152:155]\n\t"
    "ds_read_b64_tr_b16 v[242:243], v249 offset:49920\n\t"
    "v_mfma_f32_16x16x32_bf16 v[152:155], v[220:223], v[56:59], v[152:155]\n\t"
    "ds_read_b64_tr_b16 v[244:245], v249 offset:57856\n\t"
    "v_mfma_f32_16x16x32_bf16 v[160:163], v[220:223], v[60:63], v[160:163]\n\t"
    "ds_read_b64_tr_b16 v[246:247], v249 offset:58112\n\t"
    "v_mfma_f32_16x16x32_bf16 v[160:163], v[216:219], v[52:55], v[160:163]\n\t"
    "ds_read_b64_tr_b16 v[216:217], v248 offset:50176\n\t"
    "s_waitcnt lgkmcnt(9)\n\t"
    "v_mfma_f32_16x16x32_bf16 v[156:159], v[224:227], v[48:51], v[156:159]\n\t"
    "ds_read_b64_tr_b16 v[218:219], v248 offset:50432\n\t"
    "v_mfma_f32_16x16x32_bf16 v[156:159], v[228:231], v[56:59], v[156:159]\n\t"
    "ds_read_b64_tr_b16 v[220:221], v248 offset:58368\n\t"
    "v_mfma_f32_16x16x32_bf16 v[164:167], v[228:231], v[60:63], v[164:167]\n\t"
    "ds_read_b64_tr_b16 v[222:223], v248 offset:58624\n\t"
    "v_mfma_f32_16x16x32_bf16 v[164:167], v[224:227], v[52:55], v[164:167]\n\t"
    "ds_read_b64_tr_b16 v[224:225], v249 offset:50176\n\t"
    "s_waitcnt lgkmcnt(9)\n\t"
    "v_mfma_f32_16x16x32_bf16 v[168:171], v[232:235], v[48:51], v[168:171]\n\t"
    "ds_read_b64_tr_b16 v[226:227], v249 offset:50432\n\t"
    "v_mfma_f32_16x16x32_bf16 v[168:171], v[236:239], v[56:59], v[168:171]\n\t"
    "ds_read_b64_tr_b16 v[228:229], v249 offset:58368\n\t"
    "v_mfma_f32_16x16x32_bf16 v[176:179], v[236:239], v[60:63], v[176:179]\n\t"
    "ds_read_b64_tr_b16 v[230:231], v249 offset:58624\n\t"
    "v_mfma_f32_16x16x32_bf16 v[176:179], v[232:235], v[52:55], v[176:179]\n\t"
    "ds_read_b64_tr_b16 v[232:233], v248 offset:50688\n\t"
    "s_waitcnt lgkmcnt(9)\n\t"
    "v_mfma_f32_16x16x32_bf16 v[172:175], v[240:243], v[48:51], v[172:175]\n\t"
    "ds_read_b64_tr_b16 v[234:235], v248 offset:50944\n\t"
    "v_mfma_f32_16x16x32_bf16 v[172:175], v[244:247], v[56:59], v[172:175]\n\t"
    "ds_read_b64_tr_b16 v[236:237], v248 offset:58880\n\t"
    "v_mfma_f32_16x16x32_bf16 v[180:183], v[244:247], v[60:63], v[180:183]\n\t"
    "ds_read_b64_tr_b16 v[238:239], v248 offset:59136\n\t"
    "v_mfma_f32_16x16x32_bf16 v[180:183], v[240:243], v[52:55], v[180:183]\n\t"
    "ds_read_b64_tr_b16 v[240:241], v249 offset:50688\n\t"
    "s_waitcnt lgkmcnt(9)\n\t"
    "v_mfma_f32_16x16x32_bf16 v[184:187], v[216:219], v[48:51], v[184:187]\n\t"
    "ds_read_b64_tr_b16 v[242:243], v249 offset:50944\n\t"
    "v_mfma_f32_16x16x32_bf16 v[184:187], v[220:223], v[56:59], v[184:187]\n\t"
    "ds_read_b64_tr_b16 v[244:245], v249 offset:58880\n\t"
    "v_mfma_f32_16x16x32_bf16 v[192:195], v[220:223], v[60:63], v[192:195]\n\t"
    "ds_read_b64_tr_b16 v[246:247], v249 offset:59136\n\t"
    "v_mfma_f32_16x16x32_bf16 v[192:195], v[216:219], v[52:55], v[192:195]\n\t"
    "s_waitcnt lgkmcnt(8)\n\t"
    "v_mfma_f32_16x16x32_bf16 v[188:191], v[224:227], v[48:51], v[188:191]\n\t"
    "v_mfma_f32_16x16x32_bf16 v[188:191], v[228:231], v[56:59], v[188:191]\n\t"
    "v_mfma_f32_16x16x32_bf16 v[196:199], v[228:231], v[60:63], v[196:199]\n\t"
    "v_mfma_f32_16x16x32_bf16 v[196:199], v[224:227], v[52:55], v[196:199]\n\t"
    "s_waitcnt lgkmcnt(0)\n\t"
    "s_barrier\n\t"
    "v_add_u32 v250, 0xfffe0000, v250\n\t"
    "ds_read_b128 v[64:67], v250 offset:49152\n\t"
    "ds_read_b128 v[68:71], v250 offset:51200\n\t"
    "ds_read_b128 v[72:75], v250 offset:49664\n\t"
    "v_mfma_f32_16x16x32_bf16 v[200:203], v[232:235], v[48:51], v[200:203]\n\t"
    "ds_read_b128 v[76:79], v250 offset:51712\n\t"
    "v_add_u32 v248, 0xffff0000, v248\n\t"
    "v_add_u32 v249, 0xffff0000, v249\n\t"
    "ds_read_b64_tr_b16 v[216:217], v248 offset:32768\n\t"
    "v_mfma_f32_16x16x32_bf16 v[200:203], v[236:239], v[56:59], v[200:203]\n\t"
    "ds_read_b64_tr_b16 v[218:219], v248 offset:33024\n\t"
    "ds_read_b64_tr_b16 v[220:221], v248 offset:40960\n\t"
    "ds_read_b64_tr_b16 v[222:223], v248 offset:41216\n\t"
    "ds_read_b64_tr_b16 v[224:225], v249 offset:32768\n\t"
    "v_mfma_f32_16x16x32_bf16 v[208:211], v[236:239], v[60:63], v[208:211]\n\t"
    "ds_read_b64_tr_b16 v[226:227], v249 offset:33024\n\t"
    "ds_read_b64_tr_b16 v[228:229], v249 offset:40960\n\t"
    "ds_read_b64_tr_b16 v[230:231], v249 offset:41216\n\t"
    "v_mfma_f32_16x16x32_bf16 v[208:211], v[232:235], v[52:55], v[208:211]\n\t"
    "ds_read_b64_tr_b16 v[232:233], v248 offset:33280\n\t"
    "v_mfma_f32_16x16x32_bf16 v[204:207], v[240:243], v[48:51], v[204:207]\n\t"
    "ds_read_b64_tr_b16 v[234:235], v248 offset:33536\n\t"
    "v_mfma_f32_16x16x32_bf16 v[204:207], v[244:247], v[56:59], v[204:207]\n\t"
    "ds_read_b64_tr_b16 v[236:237], v248 offset:41472\n\t"
    "v_mfma_f32_16x16x32_bf16 v[212:215], v[244:247], v[60:63], v[212:215]\n\t"
    "ds_read_b64_tr_b16 v[238:239], v248 offset:41728\n\t"
    "v_mfma_f32_16x16x32_bf16 v[212:215], v[240:243], v[52:55], v[212:215]\n\t"
    "ds_read_b64_tr_b16 v[240:241], v249 offset:33280\n\t"
    "s_waitcnt lgkmcnt(9)\n\t"
    "v_mfma_f32_16x16x32_bf16 v[152:155], v[216:219], v[64:67], v[152:155]\n\t"
    "ds_read_b64_tr_b16 v[242:243], v249 offset:33536\n\t"
    "v_mfma_f32_16x16x32_bf16 v[152:155], v[220:223], v[72:75], v[152:155]\n\t"
    "ds_read_b64_tr_b16 v[244:245], v249 offset:41472\n\t"
    "v_mfma_f32_16x16x32_bf16 v[160:163], v[220:223], v[76:79], v[160:163]\n\t"
    "ds_read_b64_tr_b16 v[246:247], v249 offset:41728\n\t"
    "v_mfma_f32_16x16x32_bf16 v[160:163], v[216:219], v[68:71], v[160:163]\n\t"
    "ds_read_b64_tr_b16 v[216:217], v248 offset:33792\n\t"
    "s_waitcnt lgkmcnt(9)\n\t"
    "v_mfma_f32_16x16x32_bf16 v[156:159], v[224:227], v[64:67], v[156:159]\n\t"
    "ds_read_b64_tr_b16 v[218:219], v248 offset:34048\n\t"
    "v_mfma_f32_16x16x32_bf16 v[156:159], v[228:231], v[72:75], v[156:159]\n\t"
    "ds_read_b64_tr_b16 v[220:221], v248 offset:41984\n\t"
    "v_mfma_f32_16x16x32_bf16 v[164:167], v[228:231], v[76:79], v[164:167]\n\t"
    "ds_read_b64_tr_b16 v[222:223], v248 offset:42240\n\t"
    "v_mfma_f32_16x16x32_bf16 v[164:167], v[224:227], v[68:71], v[164:167]\n\t"
    "ds_read_b64_tr_b16 v[224:225], v249 offset:33792\n\t"
    "s_waitcnt lgkmcnt(9)\n\t"
    "v_mfma_f32_16x16x32_bf16 v[168:171], v[232:235], v[64:67], v[168:171]\n\t"
    "ds_read_b64_tr_b16 v[226:227], v249 offset:34048\n\t"
    "v_mfma_f32_16x16x32_bf16 v[168:171], v[236:239], v[72:75], v[168:171]\n\t"
    "ds_read_b64_tr_b16 v[228:229], v249 offset:41984\n\t"
    "v_mfma_f32_16x16x32_bf16 v[176:179], v[236:239], v[76:79], v[176:179]\n\t"
    "ds_read_b64_tr_b16 v[230:231], v249 offset:42240\n\t"
    "v_mfma_f32_16x16x32_bf16 v[176:179], v[232:235], v[68:71], v[176:179]\n\t"
    "ds_read_b64_tr_b16 v[232:233], v248 offset:34304\n\t"
    "s_waitcnt lgkmcnt(9)\n\t"
    "v_mfma_f32_16x16x32_bf16 v[172:175], v[240:243], v[64:67], v[172:175]\n\t"
    "ds_read_b64_tr_b16 v[234:235], v248 offset:34560\n\t"
    "v_mfma_f32_16x16x32_bf16 v[172:175], v[244:247], v[72:75], v[172:175]\n\t"
    "ds_read_b64_tr_b16 v[236:237], v248 offset:42496\n\t"
    "v_mfma_f32_16x16x32_bf16 v[180:183], v[244:247], v[76:79], v[180:183]\n\t"
    "ds_read_b64_tr_b16 v[238:239], v248 offset:42752\n\t"
    "v_mfma_f32_16x16x32_bf16 v[180:183], v[240:243], v[68:71], v[180:183]\n\t"
    "ds_read_b64_tr_b16 v[240:241], v249 offset:34304\n\t"
    "s_waitcnt lgkmcnt(9)\n\t"
    "v_mfma_f32_16x16x32_bf16 v[184:187], v[216:219], v[64:67], v[184:187]\n\t"
    "ds_read_b64_tr_b16 v[242:243], v249 offset:34560\n\t"
    "v_mfma_f32_16x16x32_bf16 v[184:187], v[220:223], v[72:75], v[184:187]\n\t"
    "ds_read_b64_tr_b16 v[244:245], v249 offset:42496\n\t"
    "v_mfma_f32_16x16x32_bf16 v[192:195], v[220:223], v[76:79], v[192:195]\n\t"
    "ds_read_b64_tr_b16 v[246:247], v249 offset:42752\n\t"
    "v_mfma_f32_16x16x32_bf16 v[192:195], v[216:219], v[68:71], v[192:195]\n\t"
    "s_waitcnt lgkmcnt(8)\n\t"
    "v_mfma_f32_16x16x32_bf16 v[188:191], v[224:227], v[64:67], v[188:191]\n\t"
    "v_mfma_f32_16x16x32_bf16 v[188:191], v[228:231], v[72:75], v[188:191]\n\t"
    "v_mfma_f32_16x16x32_bf16 v[196:199], v[228:231], v[76:79], v[196:199]\n\t"
    "v_mfma_f32_16x16x32_bf16 v[196:199], v[224:227], v[68:71], v[196:199]\n\t"
    "s_waitcnt lgkmcnt(0)\n\t"
    "s_barrier\n\t"
    "v_add_u32 v250, 0x10000, v250\n\t"
    "ds_read_b128 v[80:83], v250 offset:32768\n\t"
    "ds_read_b128 v[84:87], v250 offset:34816\n\t"
    "ds_read_b128 v[88:91], v250 offset:33280\n\t"
    "v_mfma_f32_16x16x32_bf16 v[200:203], v[232:235], v[64:67], v[200:203]\n\t"
    "ds_read_b128 v[92:95], v250 offset:35328\n\t"
    "v_add_u32 v248, 0x10000, v248\n\t"
    "v_add_u32 v249, 0x10000, v249\n\t"
    "ds_read_b64_tr_b16 v[216:217], v248 offset:16384\n\t"
    "v_mfma_f32_16x16x32_bf16 v[200:203], v[236:239], v[72:75], v[200:203]\n\t"
    "ds_read_b64_tr_b16 v[218:219], v248 offset:16640\n\t"
    "ds_read_b64_tr_b16 v[220:221], v248 offset:24576\n\t"
    "ds_read_b64_tr_b16 v[222:223], v248 offset:24832\n\t"
    "ds_read_b64_tr_b16 v[224:225], v249 offset:16384\n\t"
    "v_mfma_f32_16x16x32_bf16 v[208:211], v[236:239], v[76:79], v[208:211]\n\t"
    "ds_read_b64_tr_b16 v[226:227], v249 offset:16640\n\t"
    "ds_read_b64_tr_b16 v[228:229], v249 offset:24576\n\t"
    "ds_read_b64_tr_b16 v[230:231], v249 offset:24832\n\t"
    "v_mfma_f32_16x16x32_bf16 v[208:211], v[232:235], v[68:71], v[208:211]\n\t"
    "ds_read_b64_tr_b16 v[232:233], v248 offset:16896\n\t"
    "v_mfma_f32_16x16x32_bf16 v[204:207], v[240:243], v[64:67], v[204:207]\n\t"
    "ds_read_b64_tr_b16 v[234:235], v248 offset:17152\n\t"
    "v_mfma_f32_16x16x32_bf16 v[204:207], v[244:247], v[72:75], v[204:207]\n\t"
    "ds_read_b64_tr_b16 v[236:237], v248 offset:25088\n\t"
    "v_mfma_f32_16x16x32_bf16 v[212:215], v[244:247], v[76:79], v[212:215]\n\t"
    "ds_read_b64_tr_b16 v[238:239], v248 offset:25344\n\t"
    "v_mfma_f32_16x16x32_bf16 v[212:215], v[240:243], v[68:71], v[212:215]\n\t"
    "ds_read_b64_tr_b16 v[240:241], v249 offset:16896\n\t"
    "s_waitcnt lgkmcnt(9)\n\t"
    "v_mfma_f32_16x16x32_bf16 v[152:155], v[216:219], v[80:83], v[152:155]\n\t"
    "ds_read_b64_tr_b16 v[242:243], v249 offset:17152\n\t"
    "v_mfma_f32_16x16x32_bf16 v[152:155], v[220:223], v[88:91], v[152:155]\n\t"
    "ds_read_b64_tr_b16 v[244:245], v249 offset:25088\n\t"
    "v_mfma_f32_16x16x32_bf16 v[160:163], v[220:223], v[92:95], v[160:163]\n\t"
    "ds_read_b64_tr_b16 v[246:247], v249 offset:25344\n\t"
    "v_mfma_f32_16x16x32_bf16 v[160:163], v[216:219], v[84:87], v[160:163]\n\t"
    "ds_read_b64_tr_b16 v[216:217], v248 offset:17408\n\t"
    "s_waitcnt lgkmcnt(9)\n\t"
    "v_mfma_f32_16x16x32_bf16 v[156:159], v[224:227], v[80:83], v[156:159]\n\t"
    "ds_read_b64_tr_b16 v[218:219], v248 offset:17664\n\t"
    "v_mfma_f32_16x16x32_bf16 v[156:159], v[228:231], v[88:91], v[156:159]\n\t"
    "ds_read_b64_tr_b16 v[220:221], v248 offset:25600\n\t"
    "v_mfma_f32_16x16x32_bf16 v[164:167], v[228:231], v[92:95], v[164:167]\n\t"
    "ds_read_b64_tr_b16 v[222:223], v248 offset:25856\n\t"
    "v_mfma_f32_16x16x32_bf16 v[164:167], v[224:227], v[84:87], v[164:167]\n\t"
    "ds_read_b64_tr_b16 v[224:225], v249 offset:17408\n\t"
    "s_waitcnt lgkmcnt(9)\n\t"
    "v_mfma_f32_16x16x32_bf16 v[168:171], v[232:235], v[80:83], v[168:171]\n\t"
    "ds_read_b64_tr_b16 v[226:227], v249 offset:17664\n\t"
    "v_mfma_f32_16x16x32_bf16 v[168:171], v[236:239], v[88:91], v[168:171]\n\t"
    "ds_read_b64_tr_b16 v[228:229], v249 offset:25600\n\t"
    "v_mfma_f32_16x16x32_bf16 v[176:179], v[236:239], v[92:95], v[176:179]\n\t"
    "ds_read_b64_tr_b16 v[230:231], v249 offset:25856\n\t"
    "v_mfma_f32_16x16x32_bf16 v[176:179], v[232:235], v[84:87], v[176:179]\n\t"
    "ds_read_b64_tr_b16 v[232:233], v248 offset:17920\n\t"
    "s_waitcnt lgkmcnt(9)\n\t"
    "v_mfma_f32_16x16x32_bf16 v[172:175], v[240:243], v[80:83], v[172:175]\n\t"
    "ds_read_b64_tr_b16 v[234:235], v248 offset:18176\n\t"
    "v_mfma_f32_16x16x32_bf16 v[172:175], v[244:247], v[88:91], v[172:175]\n\t"
    "ds_read_b64_tr_b16 v[236:237], v248 offset:26112\n\t"
    "v_mfma_f32_16x16x32_bf16 v[180:183], v[244:247], v[92:95], v[180:183]\n\t"
    "ds_read_b64_tr_b16 v[238:239], v248 offset:26368\n\t"
    "v_mfma_f32_16x16x32_bf16 v[180:183], v[240:243], v[84:87], v[180:183]\n\t"
    "ds_read_b64_tr_b16 v[240:241], v249 offset:17920\n\t"
    "s_waitcnt lgkmcnt(9)\n\t"
    "v_mfma_f32_16x16x32_bf16 v[184:187], v[216:219], v[80:83], v[184:187]\n\t"
    "ds_read_b64_tr_b16 v[242:243], v249 offset:18176\n\t"
    "v_mfma_f32_16x16x32_bf16 v[184:187], v[220:223], v[88:91], v[184:187]\n\t"
    "ds_read_b64_tr_b16 v[244:245], v249 offset:26112\n\t"
    "v_mfma_f32_16x16x32_bf16 v[192:195], v[220:223], v[92:95], v[192:195]\n\t"
    "ds_read_b64_tr_b16 v[246:247], v249 offset:26368\n\t"
    "v_mfma_f32_16x16x32_bf16 v[192:195], v[216:219], v[84:87], v[192:195]\n\t"
    "s_waitcnt lgkmcnt(8)\n\t"
    "v_mfma_f32_16x16x32_bf16 v[188:191], v[224:227], v[80:83], v[188:191]\n\t"
    "v_mfma_f32_16x16x32_bf16 v[188:191], v[228:231], v[88:91], v[188:191]\n\t"
    "v_mfma_f32_16x16x32_bf16 v[196:199], v[228:231], v[92:95], v[196:199]\n\t"
    "v_mfma_f32_16x16x32_bf16 v[196:199], v[224:227], v[84:87], v[196:199]\n\t"
    "s_waitcnt lgkmcnt(0)\n\t"
    "s_barrier\n\t"
    "v_add_u32 v250, 0x10000, v250\n\t"
    "ds_read_b128 v[96:99], v250 offset:0\n\t"
    "ds_read_b128 v[100:103], v250 offset:2048\n\t"
    "ds_read_b128 v[104:107], v250 offset:512\n\t"
    "v_mfma_f32_16x16x32_bf16 v[200:203], v[232:235], v[80:83], v[200:203]\n\t"
    "ds_read_b128 v[108:111], v250 offset:2560\n\t"
    "ds_read_b64_tr_b16 v[216:217], v248 offset:49152\n\t"
    "ds_read_b64_tr_b16 v[218:219], v248 offset:49408\n\t"
    "v_mfma_f32_16x16x32_bf16 v[200:203], v[236:239], v[88:91], v[200:203]\n\t"
    "ds_read_b64_tr_b16 v[220:221], v248 offset:57344\n\t"
    "ds_read_b64_tr_b16 v[222:223], v248 offset:57600\n\t"
    "ds_read_b64_tr_b16 v[224:225], v249 offset:49152\n\t"
    "v_mfma_f32_16x16x32_bf16 v[208:211], v[236:239], v[92:95], v[208:211]\n\t"
    "ds_read_b64_tr_b16 v[226:227], v249 offset:49408\n\t"
    "ds_read_b64_tr_b16 v[228:229], v249 offset:57344\n\t"
    "ds_read_b64_tr_b16 v[230:231], v249 offset:57600\n\t"
    "v_mfma_f32_16x16x32_bf16 v[208:211], v[232:235], v[84:87], v[208:211]\n\t"
    "ds_read_b64_tr_b16 v[232:233], v248 offset:49664\n\t"
    "v_mfma_f32_16x16x32_bf16 v[204:207], v[240:243], v[80:83], v[204:207]\n\t"
    "ds_read_b64_tr_b16 v[234:235], v248 offset:49920\n\t"
    "v_mfma_f32_16x16x32_bf16 v[204:207], v[244:247], v[88:91], v[204:207]\n\t"
    "ds_read_b64_tr_b16 v[236:237], v248 offset:57856\n\t"
    "v_mfma_f32_16x16x32_bf16 v[212:215], v[244:247], v[92:95], v[212:215]\n\t"
    "ds_read_b64_tr_b16 v[238:239], v248 offset:58112\n\t"
    "v_mfma_f32_16x16x32_bf16 v[212:215], v[240:243], v[84:87], v[212:215]\n\t"
    "ds_read_b64_tr_b16 v[240:241], v249 offset:49664\n\t"
    "s_waitcnt lgkmcnt(9)\n\t"
    "v_mfma_f32_16x16x32_bf16 v[152:155], v[216:219], v[96:99], v[152:155]\n\t"
    "ds_read_b64_tr_b16 v[242:243], v249 offset:49920\n\t"
    "v_mfma_f32_16x16x32_bf16 v[152:155], v[220:223], v[104:107], v[152:155]\n\t"
    "ds_read_b64_tr_b16 v[244:245], v249 offset:57856\n\t"
    "v_mfma_f32_16x16x32_bf16 v[160:163], v[220:223], v[108:111], v[160:163]\n\t"
    "ds_read_b64_tr_b16 v[246:247], v249 offset:58112\n\t"
    "v_mfma_f32_16x16x32_bf16 v[160:163], v[216:219], v[100:103], v[160:163]\n\t"
    "ds_read_b64_tr_b16 v[216:217], v248 offset:50176\n\t"
    "s_waitcnt lgkmcnt(9)\n\t"
    "v_mfma_f32_16x16x32_bf16 v[156:159], v[224:227], v[96:99], v[156:159]\n\t"
    "ds_read_b64_tr_b16 v[218:219], v248 offset:50432\n\t"
    "v_mfma_f32_16x16x32_bf16 v[156:159], v[228:231], v[104:107], v[156:159]\n\t"
    "ds_read_b64_tr_b16 v[220:221], v248 offset:58368\n\t"
    "v_mfma_f32_16x16x32_bf16 v[164:167], v[228:231], v[108:111], v[164:167]\n\t"
    "ds_read_b64_tr_b16 v[222:223], v248 offset:58624\n\t"
    "v_mfma_f32_16x16x32_bf16 v[164:167], v[224:227], v[100:103], v[164:167]\n\t"
    "ds_read_b64_tr_b16 v[224:225], v249 offset:50176\n\t"
    "s_waitcnt lgkmcnt(9)\n\t"
    "v_mfma_f32_16x16x32_bf16 v[168:171], v[232:235], v[96:99], v[168:171]\n\t"
    "ds_read_b64_tr_b16 v[226:227], v249 offset:50432\n\t"
    "v_mfma_f32_16x16x32_bf16 v[168:171], v[236:239], v[104:107], v[168:171]\n\t"
    "ds_read_b64_tr_b16 v[228:229], v249 offset:58368\n\t"
    "v_mfma_f32_16x16x32_bf16 v[176:179], v[236:239], v[108:111], v[176:179]\n\t"
    "ds_read_b64_tr_b16 v[230:231], v249 offset:58624\n\t"
    "v_mfma_f32_16x16x32_bf16 v[176:179], v[232:235], v[100:103], v[176:179]\n\t"
    "ds_read_b64_tr_b16 v[232:233], v248 offset:50688\n\t"
    "s_waitcnt lgkmcnt(9)\n\t"
    "v_mfma_f32_16x16x32_bf16 v[172:175], v[240:243], v[96:99], v[172:175]\n\t"
    "ds_read_b64_tr_b16 v[234:235], v248 offset:50944\n\t"
    "v_mfma_f32_16x16x32_bf16 v[172:175], v[244:247], v[104:107], v[172:175]\n\t"
    "ds_read_b64_tr_b16 v[236:237], v248 offset:58880\n\t"
    "v_mfma_f32_16x16x32_bf16 v[180:183], v[244:247], v[108:111], v[180:183]\n\t"
    "ds_read_b64_tr_b16 v[238:239], v248 offset:59136\n\t"
    "v_mfma_f32_16x16x32_bf16 v[180:183], v[240:243], v[100:103], v[180:183]\n\t"
    "ds_read_b64_tr_b16 v[240:241], v249 offset:50688\n\t"
    "s_waitcnt lgkmcnt(9)\n\t"
    "v_mfma_f32_16x16x32_bf16 v[184:187], v[216:219], v[96:99], v[184:187]\n\t"
    "ds_read_b64_tr_b16 v[242:243], v249 offset:50944\n\t"
    "v_mfma_f32_16x16x32_bf16 v[184:187], v[220:223], v[104:107], v[184:187]\n\t"
    "ds_read_b64_tr_b16 v[244:245], v249 offset:58880\n\t"
    "v_mfma_f32_16x16x32_bf16 v[192:195], v[220:223], v[108:111], v[192:195]\n\t"
    "ds_read_b64_tr_b16 v[246:247], v249 offset:59136\n\t"
    "v_mfma_f32_16x16x32_bf16 v[192:195], v[216:219], v[100:103], v[192:195]\n\t"
    "s_waitcnt lgkmcnt(8)\n\t"
    "v_mfma_f32_16x16x32_bf16 v[188:191], v[224:227], v[96:99], v[188:191]\n\t"
    "v_mfma_f32_16x16x32_bf16 v[188:191], v[228:231], v[104:107], v[188:191]\n\t"
    "v_mfma_f32_16x16x32_bf16 v[196:199], v[228:231], v[108:111], v[196:199]\n\t"
    "v_mfma_f32_16x16x32_bf16 v[196:199], v[224:227], v[100:103], v[196:199]\n\t"
    "s_waitcnt lgkmcnt(0)\n\t"
    "s_barrier\n\t"
    "v_add_u32 v250, 0xfffe0000, v250\n\t"
    "ds_read_b128 v[112:115], v250 offset:49152\n\t"
    "ds_read_b128 v[116:119], v250 offset:51200\n\t"
    "ds_read_b128 v[120:123], v250 offset:49664\n\t"
    "v_mfma_f32_16x16x32_bf16 v[200:203], v[232:235], v[96:99], v[200:203]\n\t"
    "ds_read_b128 v[124:127], v250 offset:51712\n\t"
    "v_add_u32 v248, 0xffff0000, v248\n\t"
    "v_add_u32 v249, 0xffff0000, v249\n\t"
    "ds_read_b64_tr_b16 v[216:217], v248 offset:32768\n\t"
    "v_mfma_f32_16x16x32_bf16 v[200:203], v[236:239], v[104:107], v[200:203]\n\t"
    "ds_read_b64_tr_b16 v[218:219], v248 offset:33024\n\t"
    "ds_read_b64_tr_b16 v[220:221], v248 offset:40960\n\t"
    "ds_read_b64_tr_b16 v[222:223], v248 offset:41216\n\t"
    "ds_read_b64_tr_b16 v[224:225], v249 offset:32768\n\t"
    "v_mfma_f32_16x16x32_bf16 v[208:211], v[236:239], v[108:111], v[208:211]\n\t"
    "ds_read_b64_tr_b16 v[226:227], v249 offset:33024\n\t"
    "ds_read_b64_tr_b16 v[228:229], v249 offset:40960\n\t"
    "ds_read_b64_tr_b16 v[230:231], v249 offset:41216\n\t"
    "v_mfma_f32_16x16x32_bf16 v[208:211], v[232:235], v[100:103], v[208:211]\n\t"
    "ds_read_b64_tr_b16 v[232:233], v248 offset:33280\n\t"
    "v_mfma_f32_16x16x32_bf16 v[204:207], v[240:243], v[96:99], v[204:207]\n\t"
    "ds_read_b64_tr_b16 v[234:235], v248 offset:33536\n\t"
    "v_mfma_f32_16x16x32_bf16 v[204:207], v[244:247], v[104:107], v[204:207]\n\t"
    "ds_read_b64_tr_b16 v[236:237], v248 offset:41472\n\t"
    "v_mfma_f32_16x16x32_bf16 v[212:215], v[244:247], v[108:111], v[212:215]\n\t"
    "ds_read_b64_tr_b16 v[238:239], v248 offset:41728\n\t"
    "v_mfma_f32_16x16x32_bf16 v[212:215], v[240:243], v[100:103], v[212:215]\n\t"
    "ds_read_b64_tr_b16 v[240:241], v249 offset:33280\n\t"
    "s_waitcnt lgkmcnt(9)\n\t"
    "v_mfma_f32_16x16x32_bf16 v[152:155], v[216:219], v[112:115], v[152:155]\n\t"
    "ds_read_b64_tr_b16 v[242:243], v249 offset:33536\n\t"
    "v_mfma_f32_16x16x32_bf16 v[152:155], v[220:223], v[120:123], v[152:155]\n\t"
    "ds_read_b64_tr_b16 v[244:245], v249 offset:41472\n\t"
    "v_mfma_f32_16x16x32_bf16 v[160:163], v[220:223], v[124:127], v[160:163]\n\t"
    "ds_read_b64_tr_b16 v[246:247], v249 offset:41728\n\t"
    "v_mfma_f32_16x16x32_bf16 v[160:163], v[216:219], v[116:119], v[160:163]\n\t"
    "ds_read_b64_tr_b16 v[216:217], v248 offset:33792\n\t"
    "s_waitcnt lgkmcnt(9)\n\t"
    "v_mfma_f32_16x16x32_bf16 v[156:159], v[224:227], v[112:115], v[156:159]\n\t"
    "ds_read_b64_tr_b16 v[218:219], v248 offset:34048\n\t"
    "v_mfma_f32_16x16x32_bf16 v[156:159], v[228:231], v[120:123], v[156:159]\n\t"
    "ds_read_b64_tr_b16 v[220:221], v248 offset:41984\n\t"
    "v_mfma_f32_16x16x32_bf16 v[164:167], v[228:231], v[124:127], v[164:167]\n\t"
    "ds_read_b64_tr_b16 v[222:223], v248 offset:42240\n\t"
    "v_mfma_f32_16x16x32_bf16 v[164:167], v[224:227], v[116:119], v[164:167]\n\t"
    "ds_read_b64_tr_b16 v[224:225], v249 offset:33792\n\t"
    "s_waitcnt lgkmcnt(9)\n\t"
    "v_mfma_f32_16x16x32_bf16 v[168:171], v[232:235], v[112:115], v[168:171]\n\t"
    "ds_read_b64_tr_b16 v[226:227], v249 offset:34048\n\t"
    "v_mfma_f32_16x16x32_bf16 v[168:171], v[236:239], v[120:123], v[168:171]\n\t"
    "ds_read_b64_tr_b16 v[228:229], v249 offset:41984\n\t"
    "v_mul_f32 v152, 0x3e0293ee, v152\n\t"
    "v_mul_f32 v153, 0x3e0293ee, v153\n\t"
    "v_cvt_pk_bf16_f32 v152, v152, v153\n\t"
    "v_mfma_f32_16x16x32_bf16 v[176:179], v[236:239], v[124:127], v[176:179]\n\t"
    "ds_read_b64_tr_b16 v[230:231], v249 offset:42240\n\t"
    "v_mul_f32 v154, 0x3e0293ee, v154\n\t"
    "v_mul_f32 v155, 0x3e0293ee, v155\n\t"
    "v_cvt_pk_bf16_f32 v153, v154, v155\n\t"
    "v_mfma_f32_16x16x32_bf16 v[176:179], v[232:235], v[116:119], v[176:179]\n\t"
    "ds_read_b64_tr_b16 v[232:233], v248 offset:34304\n\t"
    "v_mul_f32 v156, 0x3e0293ee, v156\n\t"
    "v_mul_f32 v157, 0x3e0293ee, v157\n\t"
    "v_cvt_pk_bf16_f32 v154, v156, v157\n\t"
    "s_waitcnt lgkmcnt(9)\n\t"
    "v_mfma_f32_16x16x32_bf16 v[172:175], v[240:243], v[112:115], v[172:175]\n\t"
    "ds_read_b64_tr_b16 v[234:235], v248 offset:34560\n\t"
    "v_mul_f32 v158, 0x3e0293ee, v158\n\t"
    "v_mul_f32 v159, 0x3e0293ee, v159\n\t"
    "v_cvt_pk_bf16_f32 v155, v158, v159\n\t"
    "v_mfma_f32_16x16x32_bf16 v[172:175], v[244:247], v[120:123], v[172:175]\n\t"
    "ds_read_b64_tr_b16 v[236:237], v248 offset:42496\n\t"
    "v_mul_f32 v160, 0x3e0293ee, v160\n\t"
    "v_mul_f32 v161, 0x3e0293ee, v161\n\t"
    "v_cvt_pk_bf16_f32 v156, v160, v161\n\t"
    "v_mfma_f32_16x16x32_bf16 v[180:183], v[244:247], v[124:127], v[180:183]\n\t"
    "ds_read_b64_tr_b16 v[238:239], v248 offset:42752\n\t"
    "v_mul_f32 v162, 0x3e0293ee, v162\n\t"
    "v_mul_f32 v163, 0x3e0293ee, v163\n\t"
    "v_cvt_pk_bf16_f32 v157, v162, v163\n\t"
    "v_mfma_f32_16x16x32_bf16 v[180:183], v[240:243], v[116:119], v[180:183]\n\t"
    "ds_read_b64_tr_b16 v[240:241], v249 offset:34304\n\t"
    "v_mul_f32 v164, 0x3e0293ee, v164\n\t"
    "v_mul_f32 v165, 0x3e0293ee, v165\n\t"
    "v_cvt_pk_bf16_f32 v158, v164, v165\n\t"
    "s_waitcnt lgkmcnt(9)\n\t"
    "v_mfma_f32_16x16x32_bf16 v[184:187], v[216:219], v[112:115], v[184:187]\n\t"
    "ds_read_b64_tr_b16 v[242:243], v249 offset:34560\n\t"
    "v_mul_f32 v166, 0x3e0293ee, v166\n\t"
    "v_mul_f32 v167, 0x3e0293ee, v167\n\t"
    "v_cvt_pk_bf16_f32 v159, v166, v167\n\t"
    "v_mfma_f32_16x16x32_bf16 v[184:187], v[220:223], v[120:123], v[184:187]\n\t"
    "ds_read_b64_tr_b16 v[244:245], v249 offset:42496\n\t"
    "v_permlane16_swap_b32 v152, v154\n\t"
    "v_permlane16_swap_b32 v153, v155\n\t"
    "v_permlane16_swap_b32 v156, v158\n\t"
    "v_mfma_f32_16x16x32_bf16 v[192:195], v[220:223], v[124:127], v[192:195]\n\t"
    "ds_read_b64_tr_b16 v[246:247], v249 offset:42752\n\t"
    "v_permlane16_swap_b32 v157, v159\n\t"
    "v_mul_f32 v168, 0x3e0293ee, v168\n\t"
    "v_mul_f32 v169, 0x3e0293ee, v169\n\t"
    "v_mfma_f32_16x16x32_bf16 v[192:195], v[216:219], v[116:119], v[192:195]\n\t"
    "v_cvt_pk_bf16_f32 v160, v168, v169\n\t"
    "v_mul_f32 v170, 0x3e0293ee, v170\n\t"
    "v_mul_f32 v171, 0x3e0293ee, v171\n\t"
    "v_cvt_pk_bf16_f32 v161, v170, v171\n\t"
    "s_waitcnt lgkmcnt(8)\n\t"
    "v_mfma_f32_16x16x32_bf16 v[188:191], v[224:227], v[112:115], v[188:191]\n\t"
    "v_mul_f32 v172, 0x3e0293ee, v172\n\t"
    "v_mul_f32 v173, 0x3e0293ee, v173\n\t"
    "v_cvt_pk_bf16_f32 v162, v172, v173\n\t"
    "v_mul_f32 v174, 0x3e0293ee, v174\n\t"
    "v_mfma_f32_16x16x32_bf16 v[188:191], v[228:231], v[120:123], v[188:191]\n\t"
    "v_mul_f32 v175, 0x3e0293ee, v175\n\t"
    "v_cvt_pk_bf16_f32 v163, v174, v175\n\t"
    "v_mul_f32 v176, 0x3e0293ee, v176\n\t"
    "v_mul_f32 v177, 0x3e0293ee, v177\n\t"
    "v_mfma_f32_16x16x32_bf16 v[196:199], v[228:231], v[124:127], v[196:199]\n\t"
    "v_cvt_pk_bf16_f32 v164, v176, v177\n\t"
    "v_mul_f32 v178, 0x3e0293ee, v178\n\t"
    "v_mul_f32 v179, 0x3e0293ee, v179\n\t"
    "v_cvt_pk_bf16_f32 v165, v178, v179\n\t"
    "v_mfma_f32_16x16x32_bf16 v[196:199], v[224:227], v[116:119], v[196:199]\n\t"
    "s_waitcnt lgkmcnt(0)\n\t"
    "s_barrier\n\t"
    "v_mov_b32 v250, %[lds]\n\t"
    "v_bfe_u32 v254, v253, 3, 1\n\t"
    "v_lshl_add_u32 v250, v254, 11, v250\n\t"
    "v_bfe_u32 v254, v253, 0, 3\n\t"
    "v_lshl_add_u32 v250, v254, 6, v250\n\t"
    "v_mfma_f32_16x16x32_bf16 v[200:203], v[232:235], v[112:115], v[200:203]\n\t"
    "v_bfe_u32 v254, v253, 4, 1\n\t"
    "v_lshl_add_u32 v250, v254, 5, v250\n\t"
    "v_bfe_u32 v254, v253, 5, 1\n\t"
    "v_lshl_add_u32 v250, v254, 4, v250\n\t"
    "v_mfma_f32_16x16x32_bf16 v[200:203], v[236:239], v[120:123], v[200:203]\n\t"
    "v_bfe_u32 v254, v253, 3, 1\n\t"
    "v_lshlrev_b32 v254, 4, v254\n\t"
    "v_xor_b32 v250, v250, v254\n\t"
    "v_add_u32 v250, 0x20000, v250\n\t"
    "v_mfma_f32_16x16x32_bf16 v[208:211], v[236:239], v[124:127], v[208:211]\n\t"
    "ds_read_b128 v[216:219], v250 offset:16384\n\t"
    "ds_read_b128 v[220:223], v250 offset:16896\n\t"
    "ds_read_b128 v[224:227], v250 offset:17408\n\t"
    "ds_read_b128 v[228:231], v250 offset:17920\n\t"
    "v_mfma_f32_16x16x32_bf16 v[208:211], v[232:235], v[116:119], v[208:211]\n\t"
    "ds_read_b128 v[232:235], v250 offset:20480\n\t"
    "v_mul_f32 v180, 0x3e0293ee, v180\n\t"
    "v_mul_f32 v181, 0x3e0293ee, v181\n\t"
    "v_cvt_pk_bf16_f32 v166, v180, v181\n\t"
    "v_mfma_f32_16x16x32_bf16 v[204:207], v[240:243], v[112:115], v[204:207]\n\t"
    "ds_read_b128 v[236:239], v250 offset:20992\n\t"
    "v_mul_f32 v182, 0x3e0293ee, v182\n\t"
    "v_mul_f32 v183, 0x3e0293ee, v183\n\t"
    "v_cvt_pk_bf16_f32 v167, v182, v183\n\t"
    "v_mfma_f32_16x16x32_bf16 v[204:207], v[244:247], v[120:123], v[204:207]\n\t"
    "v_permlane16_swap_b32 v160, v162\n\t"
    "v_permlane16_swap_b32 v161, v163\n\t"
    "v_permlane16_swap_b32 v164, v166\n\t"
    "v_permlane16_swap_b32 v165, v167\n\t"
    "v_mfma_f32_16x16x32_bf16 v[212:215], v[244:247], v[124:127], v[212:215]\n\t"
    "v_mul_f32 v184, 0x3e0293ee, v184\n\t"
    "v_mul_f32 v185, 0x3e0293ee, v185\n\t"
    "v_cvt_pk_bf16_f32 v168, v184, v185\n\t"
    "v_mul_f32 v186, 0x3e0293ee, v186\n\t"
    "v_mfma_f32_16x16x32_bf16 v[212:215], v[240:243], v[116:119], v[212:215]\n\t"
    "ds_read_b128 v[240:243], v250 offset:21504\n\t"
    "v_mul_f32 v187, 0x3e0293ee, v187\n\t"
    "v_cvt_pk_bf16_f32 v169, v186, v187\n\t"
    "v_mul_f32 v188, 0x3e0293ee, v188\n\t"
    "s_waitcnt lgkmcnt(5)\n\t"
    "v_mfma_f32_16x16x32_bf16 v[120:123], v[216:219], v[152:155], 0\n\t"
    "ds_read_b128 v[244:247], v250 offset:22016\n\t"
    "v_mul_f32 v189, 0x3e0293ee, v189\n\t"
    "v_cvt_pk_bf16_f32 v170, v188, v189\n\t"
    "v_mul_f32 v190, 0x3e0293ee, v190\n\t"
    "v_mfma_f32_16x16x32_bf16 v[120:123], v[220:223], v[160:163], v[120:123]\n\t"
    "v_mul_f32 v191, 0x3e0293ee, v191\n\t"
    "v_cvt_pk_bf16_f32 v171, v190, v191\n\t"
    "v_mul_f32 v192, 0x3e0293ee, v192\n\t"
    "v_mul_f32 v193, 0x3e0293ee, v193\n\t"
    "v_cvt_pk_bf16_f32 v172, v192, v193\n\t"
    "v_mul_f32 v194, 0x3e0293ee, v194\n\t"
    "v_mul_f32 v195, 0x3e0293ee, v195\n\t"
    "v_cvt_pk_bf16_f32 v173, v194, v195\n\t"
    "v_mul_f32 v196, 0x3e0293ee, v196\n\t"
    "v_mul_f32 v197, 0x3e0293ee, v197\n\t"
    "v_cvt_pk_bf16_f32 v174, v196, v197\n\t"
    "v_mul_f32 v198, 0x3e0293ee, v198\n\t"
    "v_mul_f32 v199, 0x3e0293ee, v199\n\t"
    "v_cvt_pk_bf16_f32 v175, v198, v199\n\t"
    "v_permlane16_swap_b32 v168, v170\n\t"
    "v_permlane16_swap_b32 v169, v171\n\t"
    "s_waitcnt lgkmcnt(4)\n\t"
    "s_nop 0\n\t"
    "v_mfma_f32_16x16x32_bf16 v[120:123], v[224:227], v[168:171], v[120:123]\n\t"
    "v_permlane16_swap_b32 v172, v174\n\t"
    "v_permlane16_swap_b32 v173, v175\n\t"
    "v_mul_f32 v200, 0x3e0293ee, v200\n\t"
    "v_mul_f32 v201, 0x3e0293ee, v201\n\t"
    "v_cvt_pk_bf16_f32 v176, v200, v201\n\t"
    "v_mul_f32 v202, 0x3e0293ee, v202\n\t"
    "v_mul_f32 v203, 0x3e0293ee, v203\n\t"
    "v_cvt_pk_bf16_f32 v177, v202, v203\n\t"
    "v_mul_f32 v204, 0x3e0293ee, v204\n\t"
    "v_mul_f32 v205, 0x3e0293ee, v205\n\t"
    "v_cvt_pk_bf16_f32 v178, v204, v205\n\t"
    "v_mul_f32 v206, 0x3e0293ee, v206\n\t"
    "v_mul_f32 v207, 0x3e0293ee, v207\n\t"
    "v_cvt_pk_bf16_f32 v179, v206, v207\n\t"
    "v_mul_f32 v208, 0x3e0293ee, v208\n\t"
    "v_mul_f32 v209, 0x3e0293ee, v209\n\t"
    "v_cvt_pk_bf16_f32 v180, v208, v209\n\t"
    "v_mul_f32 v210, 0x3e0293ee, v210\n\t"
    "v_mul_f32 v211, 0x3e0293ee, v211\n\t"
    "v_cvt_pk_bf16_f32 v181, v210, v211\n\t"
    "v_mul_f32 v212, 0x3e0293ee, v212\n\t"
    "v_mul_f32 v213, 0x3e0293ee, v213\n\t"
    "v_cvt_pk_bf16_f32 v182, v212, v213\n\t"
    "v_mul_f32 v214, 0x3e0293ee, v214\n\t"
    "v_mul_f32 v215, 0x3e0293ee, v215\n\t"
    "v_cvt_pk_bf16_f32 v183, v214, v215\n\t"
    "v_permlane16_swap_b32 v176, v178\n\t"
    "v_permlane16_swap_b32 v177, v179\n\t"
    "s_nop 1\n\t"
    "v_mfma_f32_16x16x32_bf16 v[120:123], v[228:231], v[176:179], v[120:123]\n\t"
    "v_permlane16_swap_b32 v180, v182\n\t"
    "v_permlane16_swap_b32 v181, v183\n\t"
    "s_waitcnt vmcnt(0)\n\t"
    "s_nop 0\n\t"
    "v_mfma_f32_16x16x32_bf16 v[128:131], v[228:231], v[180:183], 0\n\t"
    "v_mfma_f32_16x16x32_bf16 v[128:131], v[224:227], v[172:175], v[128:131]\n\t"
    "v_mfma_f32_16x16x32_bf16 v[128:131], v[220:223], v[164:167], v[128:131]\n\t"
    "v_mfma_f32_16x16x32_bf16 v[128:131], v[216:219], v[156:159], v[128:131]\n\t"
    "ds_read_b128 v[216:219], v250 offset:24576\n\t"
    "s_waitcnt lgkmcnt(3)\n\t"
    "v_mfma_f32_16x16x32_bf16 v[124:127], v[232:235], v[152:155], 0\n\t"
    "ds_read_b128 v[220:223], v250 offset:25088\n\t"
    "v_mfma_f32_16x16x32_bf16 v[124:127], v[236:239], v[160:163], v[124:127]\n\t"
    "ds_read_b128 v[224:227], v250 offset:25600\n\t"
    "s_waitcnt lgkmcnt(3)\n\t"
    "v_mfma_f32_16x16x32_bf16 v[124:127], v[240:243], v[168:171], v[124:127]\n\t"
    "ds_read_b128 v[228:231], v250 offset:26112\n\t"
    "v_mfma_f32_16x16x32_bf16 v[124:127], v[244:247], v[176:179], v[124:127]\n\t"
    "v_mfma_f32_16x16x32_bf16 v[132:135], v[244:247], v[180:183], 0\n\t"
    "v_mfma_f32_16x16x32_bf16 v[132:135], v[240:243], v[172:175], v[132:135]\n\t"
    "v_mfma_f32_16x16x32_bf16 v[132:135], v[236:239], v[164:167], v[132:135]\n\t"
    "v_mfma_f32_16x16x32_bf16 v[132:135], v[232:235], v[156:159], v[132:135]\n\t"
    "ds_read_b128 v[232:235], v250 offset:28672\n\t"
    "s_waitcnt lgkmcnt(3)\n\t"
    "v_mfma_f32_16x16x32_bf16 v[104:107], v[216:219], v[152:155], 0\n\t"
    "ds_read_b128 v[236:239], v250 offset:29184\n\t"
    "v_mfma_f32_16x16x32_bf16 v[104:107], v[220:223], v[160:163], v[104:107]\n\t"
    "ds_read_b128 v[240:243], v250 offset:29696\n\t"
    "v_max3_f32 v192, v120, v121, v122\n\t"
    "v_max3_f32 v193, v128, v129, v130\n\t"
    "v_max3_f32 v192, v192, v123, v124\n\t"
    "s_waitcnt lgkmcnt(3)\n\t"
    "v_mfma_f32_16x16x32_bf16 v[104:107], v[224:227], v[168:171], v[104:107]\n\t"
    "ds_read_b128 v[244:247], v250 offset:30208\n\t"
    "v_max3_f32 v193, v193, v131, v132\n\t"
    "v_max3_f32 v192, v192, v125, v126\n\t"
    "v_max3_f32 v193, v193, v133, v134\n\t"
    "v_mfma_f32_16x16x32_bf16 v[104:107], v[228:231], v[176:179], v[104:107]\n\t"
    "v_max_f32 v192, v192, v127\n\t"
    "v_max_f32 v193, v193, v135\n\t"
    "v_mov_b32 v194, v192\n\t"
    "v_mov_b32 v195, v193\n\t"
    "v_mfma_f32_16x16x32_bf16 v[112:115], v[228:231], v[180:183], 0\n\t"
    "v_permlane32_swap_b32 v192, v194\n\t"
    "v_permlane32_swap_b32 v193, v195\n\t"
    "v_max_f32 v192, v192, v194\n\t"
    "v_max_f32 v193, v193, v195\n\t"
    "v_mfma_f32_16x16x32_bf16 v[112:115], v[224:227], v[172:175], v[112:115]\n\t"
    "v_mov_b32 v194, v192\n\t"
    "v_mov_b32 v195, v193\n\t"
    "s_nop 0\n\t"
    "v_permlane16_swap_b32 v192, v194\n\t"
    "v_permlane16_swap_b32 v193, v195\n\t"
    "v_mfma_f32_16x16x32_bf16 v[112:115], v[220:223], v[164:167], v[112:115]\n\t"
    "v_max_f32 v192, v192, v194\n\t"
    "v_max_f32 v193, v193, v195\n\t"
    "v_mov_b32 v251, 0\n\t"
    "v_mov_b32 v252, 0\n\t"
    "v_mfma_f32_16x16x32_bf16 v[112:115], v[216:219], v[156:159], v[112:115]\n\t"
    "s_waitcnt lgkmcnt(0)\n\t"
    "s_barrier\n\t"
    "v_mov_b32 v248, %[lds]\n\t"
    "v_bfe_u32 v254, v253, 5, 1\n\t"
    "v_sub_f32 v184, 0xc1c00000, v192\n\t"
    "v_sub_f32 v188, s60, v193\n\t"
    "v_mfma_f32_16x16x32_bf16 v[108:111], v[232:235], v[152:155], 0\n\t"
    "v_lshl_add_u32 v248, v254, 11, v248\n\t"
    "v_bfe_u32 v254, v253, 2, 3\n\t"
    "v_mov_b32 v185, v184\n\t"
    "v_mov_b32 v186, v184\n\t"
    "v_mfma_f32_16x16x32_bf16 v[108:111], v[236:239], v[160:163], v[108:111]\n\t"
    "v_lshl_add_u32 v248, v254, 6, v248\n\t"
    "v_bfe_u32 v254, v253, 4, 1\n\t"
    "v_mov_b32 v187, v184\n\t"
    "v_mov_b32 v189, v188\n\t"
    "v_mfma_f32_16x16x32_bf16 v[108:111], v[240:243], v[168:171], v[108:111]\n\t"
    "v_lshl_add_u32 v248, v254, 5, v248\n\t"
    "v_bfe_u32 v254, v253, 0, 2\n\t"
    "v_mov_b32 v190, v188\n\t"
    "v_mov_b32 v191, v188\n\t"
    "v_mfma_f32_16x16x32_bf16 v[108:111], v[244:247], v[176:179], v[108:111]\n\t"
    "v_lshl_add_u32 v248, v254, 3, v248\n\t"
    "v_xor_b32 v249, 32, v248\n\t"
    "v_add_f32 v120, v184, v120\n\t"
    "v_add_f32 v121, v184, v121\n\t"
    "v_mfma_f32_16x16x32_bf16 v[116:119], v[244:247], v[180:183], 0\n\t"
    "v_add_u32 v250, 0xfffe0000, v250\n\t"
    "ds_read_b128 v[216:219], v250 offset:0\n\t"
    "v_add_f32 v122, v184, v122\n\t"
    "v_add_f32 v123, v184, v123\n\t"
    "v_mfma_f32_16x16x32_bf16 v[116:119], v[240:243], v[172:175], v[116:119]\n\t"
    "ds_read_b128 v[220:223], v250 offset:512\n\t"
    "ds_read_b128 v[224:227], v250 offset:1024\n\t"
    "v_add_f32 v124, v184, v124\n\t"
    "v_add_f32 v125, v184, v125\n\t"
    "v_mfma_f32_16x16x32_bf16 v[116:119], v[236:239], v[164:167], v[116:119]\n\t"
    "ds_read_b128 v[228:231], v250 offset:1536\n\t"
    "v_add_f32 v126, v184, v126\n\t"
    "v_add_f32 v127, v184, v127\n\t"
    "v_add_f32 v128, v188, v128\n\t"
    "v_mfma_f32_16x16x32_bf16 v[116:119], v[232:235], v[156:159], v[116:119]\n\t"
    "ds_read_b128 v[232:235], v250 offset:4096\n\t"
    "v_add_f32 v129, v188, v129\n\t"
    "v_add_f32 v130, v188, v130\n\t"
    "v_add_f32 v131, v188, v131\n\t"
    "s_waitcnt lgkmcnt(3)\n\t"
    "v_mfma_f32_16x16x32_bf16 v[136:139], v[216:219], v[152:155], v[184:187]\n\t"
    "ds_read_b128 v[236:239], v250 offset:4608\n\t"
    "v_add_f32 v132, v188, v132\n\t"
    "v_add_f32 v133, v188, v133\n\t"
    "v_add_f32 v134, v188, v134\n\t"
    "v_mfma_f32_16x16x32_bf16 v[136:139], v[220:223], v[160:163], v[136:139]\n\t"
    "ds_read_b128 v[240:243], v250 offset:5120\n\t"
    "v_add_f32 v135, v188, v135\n\t"
    "v_exp_f32 v120, v120\n\t"
    "s_waitcnt lgkmcnt(3)\n\t"
    "v_mfma_f32_16x16x32_bf16 v[136:139], v[224:227], v[168:171], v[136:139]\n\t"
    "ds_read_b128 v[244:247], v250 offset:5632\n\t"
    "v_exp_f32 v121, v121\n\t"
    "v_exp_f32 v122, v122\n\t"
    "v_mfma_f32_16x16x32_bf16 v[136:139], v[228:231], v[176:179], v[136:139]\n\t"
    "v_exp_f32 v123, v123\n\t"
    "v_cvt_pk_bf16_f32 v0, v120, v121\n\t"
    "v_exp_f32 v124, v124\n\t"
    "v_mfma_f32_16x16x32_bf16 v[144:147], v[228:231], v[180:183], v[188:191]\n\t"
    "v_exp_f32 v125, v125\n\t"
    "v_cvt_pk_bf16_f32 v1, v122, v123\n\t"
    "v_exp_f32 v126, v126\n\t"
    "v_mfma_f32_16x16x32_bf16 v[144:147], v[224:227], v[172:175], v[144:147]\n\t"
    "v_exp_f32 v127, v127\n\t"
    "v_cvt_pk_bf16_f32 v2, v124, v125\n\t"
    "v_or3_b32 v251, v251, v0, v1\n\t"
    "v_mfma_f32_16x16x32_bf16 v[144:147], v[220:223], v[164:167], v[144:147]\n\t"
    "v_exp_f32 v128, v128\n\t"
    "v_exp_f32 v129, v129\n\t"
    "v_mfma_f32_16x16x32_bf16 v[144:147], v[216:219], v[156:159], v[144:147]\n\t"
    "ds_read_b128 v[216:219], v250 offset:8192\n\t"
    "v_cvt_pk_bf16_f32 v3, v126, v127\n\t"
    "v_exp_f32 v130, v130\n\t"
    "s_waitcnt lgkmcnt(3)\n\t"
    "v_mfma_f32_16x16x32_bf16 v[140:143], v[232:235], v[152:155], v[184:187]\n\t"
    "ds_read_b128 v[220:223], v250 offset:8704\n\t"
    "v_exp_f32 v131, v131\n\t"
    "v_cvt_pk_bf16_f32 v4, v128, v129\n\t"
    "v_mfma_f32_16x16x32_bf16 v[140:143], v[236:239], v[160:163], v[140:143]\n\t"
    "ds_read_b128 v[224:227], v250 offset:9216\n\t"
    "v_or3_b32 v251, v251, v2, v3\n\t"
    "v_exp_f32 v132, v132\n\t"
    "s_waitcnt lgkmcnt(3)\n\t"
    "v_mfma_f32_16x16x32_bf16 v[140:143], v[240:243], v[168:171], v[140:143]\n\t"
    "ds_read_b128 v[228:231], v250 offset:9728\n\t"
    "v_exp_f32 v133, v133\n\t"
    "v_cvt_pk_bf16_f32 v5, v130, v131\n\t"
    "v_mfma_f32_16x16x32_bf16 v[140:143], v[244:247], v[176:179], v[140:143]\n\t"
    "v_exp_f32 v134, v134\n\t"
    "v_exp_f32 v135, v135\n\t"
    "v_mfma_f32_16x16x32_bf16 v[148:151], v[244:247], v[180:183], v[188:191]\n\t"
    "v_cvt_pk_bf16_f32 v6, v132, v133\n\t"
    "v_or3_b32 v251, v251, v4, v5\n\t"
    "v_cvt_pk_bf16_f32 v7, v134, v135\n\t"
    "v_or3_b32 v251, v251, v6, v7\n\t"
    "v_mfma_f32_16x16x32_bf16 v[148:151], v[240:243], v[172:175], v[148:151]\n\t"
    "v_add_f32 v104, v184, v104\n\t"
    "v_add_f32 v105, v184, v105\n\t"
    "v_add_f32 v106, v184, v106\n\t"
    "v_add_f32 v107, v184, v107\n\t"
    "v_mfma_f32_16x16x32_bf16 v[148:151], v[236:239], v[164:167], v[148:151]\n\t"
    "v_add_f32 v108, v184, v108\n\t"
    "v_add_f32 v109, v184, v109\n\t"
    "v_add_f32 v110, v184, v110\n\t"
    "v_add_f32 v111, v184, v111\n\t"
    "v_mfma_f32_16x16x32_bf16 v[148:151], v[232:235], v[156:159], v[148:151]\n\t"
    "ds_read_b128 v[232:235], v250 offset:12288\n\t"
    "v_add_f32 v112, v188, v112\n\t"
    "v_add_f32 v113, v188, v113\n\t"
    "v_add_f32 v114, v188, v114\n\t"
    "s_waitcnt lgkmcnt(3)\n\t"
    "v_mfma_f32_16x16x32_bf16 v[120:123], v[216:219], v[152:155], v[184:187]\n\t"
    "ds_read_b128 v[236:239], v250 offset:12800\n\t"
    "v_add_f32 v115, v188, v115\n\t"
    "v_add_f32 v116, v188, v116\n\t"
    "v_add_f32 v117, v188, v117\n\t"
    "v_mfma_f32_16x16x32_bf16 v[120:123], v[220:223], v[160:163], v[120:123]\n\t"
    "ds_read_b128 v[240:243], v250 offset:13312\n\t"
    "v_add_f32 v118, v188, v118\n\t"
    "v_add_f32 v119, v188, v119\n\t"
    "v_exp_f32 v104, v104\n\t"
    "s_waitcnt lgkmcnt(3)\n\t"
    "v_mfma_f32_16x16x32_bf16 v[120:123], v[224:227], v[168:171], v[120:123]\n\t"
    "ds_read_b128 v[244:247], v250 offset:13824\n\t"
    "v_exp_f32 v105, v105\n\t"
    "v_exp_f32 v106, v106\n\t"
    "v_mfma_f32_16x16x32_bf16 v[120:123], v[228:231], v[176:179], v[120:123]\n\t"
    "v_exp_f32 v107, v107\n\t"
    "v_cvt_pk_bf16_f32 v8, v104, v105\n\t"
    "v_exp_f32 v108, v108\n\t"
    "v_mfma_f32_16x16x32_bf16 v[128:131], v[228:231], v[180:183], v[188:191]\n\t"
    "v_exp_f32 v109, v109\n\t"
    "v_cvt_pk_bf16_f32 v9, v106, v107\n\t"
    "v_exp_f32 v110, v110\n\t"
    "v_mfma_f32_16x16x32_bf16 v[128:131], v[224:227], v[172:175], v[128:131]\n\t"
    "v_exp_f32 v111, v111\n\t"
    "v_cvt_pk_bf16_f32 v10, v108, v109\n\t"
    "v_or3_b32 v251, v251, v8, v9\n\t"
    "v_mfma_f32_16x16x32_bf16 v[128:131], v[220:223], v[164:167], v[128:131]\n\t"
    "v_exp_f32 v112, v112\n\t"
    "v_exp_f32 v113, v113\n\t"
    "v_mfma_f32_16x16x32_bf16 v[128:131], v[216:219], v[156:159], v[128:131]\n\t"
    "s_waitcnt lgkmcnt(0)\n\t"
    "s_barrier\n\t"
    "ds_read_b128 v[216:219], v250 offset:16384\n\t"
    "v_cvt_pk_bf16_f32 v11, v110, v111\n\t"
    "v_exp_f32 v114, v114\n\t"
    "v_mfma_f32_16x16x32_bf16 v[124:127], v[232:235], v[152:155], v[184:187]\n\t"
    "ds_read_b128 v[220:223], v250 offset:16896\n\t"
    "v_exp_f32 v115, v115\n\t"
    "v_cvt_pk_bf16_f32 v12, v112, v113\n\t"
    "v_mfma_f32_16x16x32_bf16 v[124:127], v[236:239], v[160:163], v[124:127]\n\t"
    "ds_read_b128 v[224:227], v250 offset:17408\n\t"
    "v_or3_b32 v251, v251, v10, v11\n\t"
    "v_exp_f32 v116, v116\n\t"
    "v_mfma_f32_16x16x32_bf16 v[124:127], v[240:243], v[168:171], v[124:127]\n\t"
    "ds_read_b128 v[228:231], v250 offset:17920\n\t"
    "v_exp_f32 v117, v117\n\t"
    "v_cvt_pk_bf16_f32 v13, v114, v115\n\t"
    "v_mfma_f32_16x16x32_bf16 v[124:127], v[244:247], v[176:179], v[124:127]\n\t"
    "v_exp_f32 v118, v118\n\t"
    "v_exp_f32 v119, v119\n\t"
    "v_mfma_f32_16x16x32_bf16 v[132:135], v[244:247], v[180:183], v[188:191]\n\t"
    "v_cvt_pk_bf16_f32 v14, v116, v117\n\t"
    "v_or3_b32 v251, v251, v12, v13\n\t"
    "v_cvt_pk_bf16_f32 v15, v118, v119\n\t"
    "v_or3_b32 v251, v251, v14, v15\n\t"
    "v_mfma_f32_16x16x32_bf16 v[132:135], v[240:243], v[172:175], v[132:135]\n\t"
    "v_exp_f32 v136, v136\n\t"
    "v_exp_f32 v137, v137\n\t"
    "v_mfma_f32_16x16x32_bf16 v[132:135], v[236:239], v[164:167], v[132:135]\n\t"
    "v_exp_f32 v138, v138\n\t"
    "v_exp_f32 v139, v139\n\t"
    "v_mfma_f32_16x16x32_bf16 v[132:135], v[232:235], v[156:159], v[132:135]\n\t"
    "ds_read_b128 v[232:235], v250 offset:20480\n\t"
    "v_cvt_pk_bf16_f32 v16, v136, v137\n\t"
    "v_exp_f32 v140, v140\n\t"
    "s_waitcnt lgkmcnt(3)\n\t"
    "v_mfma_f32_16x16x32_bf16 v[104:107], v[216:219], v[152:155], v[184:187]\n\t"
    "ds_read_b128 v[236:239], v250 offset:20992\n\t"
    "v_exp_f32 v141, v141\n\t"
    "v_cvt_pk_bf16_f32 v17, v138, v139\n\t"
    "v_mfma_f32_16x16x32_bf16 v[104:107], v[220:223], v[160:163], v[104:107]\n\t"
    "ds_read_b128 v[240:243], v250 offset:21504\n\t"
    "v_exp_f32 v142, v142\n\t"
    "v_exp_f32 v143, v143\n\t"
    "s_waitcnt lgkmcnt(3)\n\t"
    "v_mfma_f32_16x16x32_bf16 v[104:107], v[224:227], v[168:171], v[104:107]\n\t"
    "ds_read_b128 v[244:247], v250 offset:22016\n\t"
    "v_cvt_pk_bf16_f32 v18, v140, v141\n\t"
    "v_or3_b32 v251, v251, v16, v17\n\t"
    "v_exp_f32 v144, v144\n\t"
    "v_mfma_f32_16x16x32_bf16 v[104:107], v[228:231], v[176:179], v[104:107]\n\t"
    "v_exp_f32 v145, v145\n\t"
    "v_cvt_pk_bf16_f32 v19, v142, v143\n\t"
    "v_exp_f32 v146, v146\n\t"
    "v_mfma_f32_16x16x32_bf16 v[112:115], v[228:231], v[180:183], v[188:191]\n\t"
    "v_exp_f32 v147, v147\n\t"
    "v_cvt_pk_bf16_f32 v20, v144, v145\n\t"
    "v_or3_b32 v251, v251, v18, v19\n\t"
    "v_mfma_f32_16x16x32_bf16 v[112:115], v[224:227], v[172:175], v[112:115]\n\t"
    "v_exp_f32 v148, v148\n\t"
    "v_exp_f32 v149, v149\n\t"
    "v_mfma_f32_16x16x32_bf16 v[112:115], v[220:223], v[164:167], v[112:115]\n\t"
    "v_cvt_pk_bf16_f32 v21, v146, v147\n\t"
    "v_exp_f32 v150, v150\n\t"
    "v_exp_f32 v151, v151\n\t"
    "v_mfma_f32_16x16x32_bf16 v[112:115], v[216:219], v[156:159], v[112:115]\n\t"
    "ds_read_b128 v[216:219], v250 offset:24576\n\t"
    "v_cvt_pk_bf16_f32 v22, v148, v149\n\t"
    "v_or3_b32 v251, v251, v20, v21\n\t"
    "v_cvt_pk_bf16_f32 v23, v150, v151\n\t"
    "s_waitcnt lgkmcnt(3)\n\t"
    "v_mfma_f32_16x16x32_bf16 v[108:111], v[232:235], v[152:155], v[184:187]\n\t"
    "ds_read_b128 v[220:223], v250 offset:25088\n\t"
    "v_or3_b32 v251, v251, v22, v23\n\t"
    "v_exp_f32 v120, v120\n\t"
    "v_mfma_f32_16x16x32_bf16 v[108:111], v[236:239], v[160:163], v[108:111]\n\t"
    "ds_read_b128 v[224:227], v250 offset:25600\n\t"
    "v_exp_f32 v121, v121\n\t"
    "v_exp_f32 v122, v122\n\t"
    "s_waitcnt lgkmcnt(3)\n\t"
    "v_mfma_f32_16x16x32_bf16 v[108:111], v[240:243], v[168:171], v[108:111]\n\t"
    "ds_read_b128 v[228:231], v250 offset:26112\n\t"
    "v_exp_f32 v123, v123\n\t"
    "v_cvt_pk_bf16_f32 v24, v120, v121\n\t"
    "v_mfma_f32_16x16x32_bf16 v[108:111], v[244:247], v[176:179], v[108:111]\n\t"
    "v_exp_f32 v124, v124\n\t"
    "v_exp_f32 v125, v125\n\t"
    "v_mfma_f32_16x16x32_bf16 v[116:119], v[244:247], v[180:183], v[188:191]\n\t"
    "v_cvt_pk_bf16_f32 v25, v122, v123\n\t"
    "v_exp_f32 v126, v126\n\t"
    "v_exp_f32 v127, v127\n\t"
    "v_mfma_f32_16x16x32_bf16 v[116:119], v[240:243], v[172:175], v[116:119]\n\t"
    "v_cvt_pk_bf16_f32 v26, v124, v125\n\t"
    "v_or3_b32 v251, v251, v24, v25\n\t"
    "v_exp_f32 v128, v128\n\t"
    "v_mfma_f32_16x16x32_bf16 v[116:119], v[236:239], v[164:167], v[116:119]\n\t"
    "v_exp_f32 v129, v129\n\t"
    "v_cvt_pk_bf16_f32 v27, v126, v127\n\t"
    "v_exp_f32 v130, v130\n\t"
    "v_mfma_f32_16x16x32_bf16 v[116:119], v[232:235], v[156:159], v[116:119]\n\t"
    "ds_read_b128 v[232:235], v250 offset:28672\n\t"
    "v_exp_f32 v131, v131\n\t"
    "v_cvt_pk_bf16_f32 v28, v128, v129\n\t"
    "s_waitcnt lgkmcnt(3)\n\t"
    "v_mfma_f32_16x16x32_bf16 v[136:139], v[216:219], v[152:155], v[184:187]\n\t"
    "ds_read_b128 v[236:239], v250 offset:29184\n\t"
    "v_or3_b32 v251, v251, v26, v27\n\t"
    "v_exp_f32 v132, v132\n\t"
    "v_mfma_f32_16x16x32_bf16 v[136:139], v[220:223], v[160:163], v[136:139]\n\t"
    "ds_read_b128 v[240:243], v250 offset:29696\n\t"
    "v_exp_f32 v133, v133\n\t"
    "v_cvt_pk_bf16_f32 v29, v130, v131\n\t"
    "s_waitcnt lgkmcnt(3)\n\t"
    "v_mfma_f32_16x16x32_bf16 v[136:139], v[224:227], v[168:171], v[136:139]\n\t"
    "ds_read_b128 v[244:247], v250 offset:30208\n\t"
    "v_exp_f32 v134, v134\n\t"
    "v_exp_f32 v135, v135\n\t"
    "v_mfma_f32_16x16x32_bf16 v[136:139], v[228:231], v[176:179], v[136:139]\n\t"
    "v_cvt_pk_bf16_f32 v30, v132, v133\n\t"
    "v_or3_b32 v251, v251, v28, v29\n\t"
    "v_cvt_pk_bf16_f32 v31, v134, v135\n\t"
    "v_or3_b32 v251, v251, v30, v31\n\t"
    "v_mfma_f32_16x16x32_bf16 v[144:147], v[228:231], v[180:183], v[188:191]\n\t"
    "v_exp_f32 v104, v104\n\t"
    "v_exp_f32 v105, v105\n\t"
    "v_mfma_f32_16x16x32_bf16 v[144:147], v[224:227], v[172:175], v[144:147]\n\t"
    "v_exp_f32 v106, v106\n\t"
    "v_exp_f32 v107, v107\n\t"
    "v_mfma_f32_16x16x32_bf16 v[144:147], v[220:223], v[164:167], v[144:147]\n\t"
    "v_cvt_pk_bf16_f32 v32, v104, v105\n\t"
    "v_exp_f32 v108, v108\n\t"
    "v_exp_f32 v109, v109\n\t"
    "v_mfma_f32_16x16x32_bf16 v[144:147], v[216:219], v[156:159], v[144:147]\n\t"
    "s_waitcnt lgkmcnt(0)\n\t"
    "s_barrier\n\t"
    "v_add_u32 v250, 0x10000, v250\n\t"
    "v_cvt_pk_bf16_f32 v33, v106, v107\n\t"
    "v_exp_f32 v110, v110\n\t"
    "v_mfma_f32_16x16x32_bf16 v[140:143], v[232:235], v[152:155], v[184:187]\n\t"
    "ds_read_b128 v[216:219], v250 offset:16384\n\t"
    "v_exp_f32 v111, v111\n\t"
    "v_cvt_pk_bf16_f32 v34, v108, v109\n\t"
    "v_mfma_f32_16x16x32_bf16 v[140:143], v[236:239], v[160:163], v[140:143]\n\t"
    "ds_read_b128 v[220:223], v250 offset:16896\n\t"
    "v_or3_b32 v251, v251, v32, v33\n\t"
    "v_exp_f32 v112, v112\n\t"
    "v_mfma_f32_16x16x32_bf16 v[140:143], v[240:243], v[168:171], v[140:143]\n\t"
    "ds_read_b128 v[224:227], v250 offset:17408\n\t"
    "v_exp_f32 v113, v113\n\t"
    "v_cvt_pk_bf16_f32 v35, v110, v111\n\t"
    "v_mfma_f32_16x16x32_bf16 v[140:143], v[244:247], v[176:179], v[140:143]\n\t"
    "ds_read_b128 v[228:231], v250 offset:17920\n\t"
    "v_exp_f32 v114, v114\n\t"
    "v_exp_f32 v115, v115\n\t"
    "v_mfma_f32_16x16x32_bf16 v[148:151], v[244:247], v[180:183], v[188:191]\n\t"
    "v_cvt_pk_bf16_f32 v36, v112, v113\n\t"
    "v_or3_b32 v251, v251, v34, v35\n\t"
    "v_exp_f32 v116, v116\n\t"
    "v_mfma_f32_16x16x32_bf16 v[148:151], v[240:243], v[172:175], v[148:151]\n\t"
    "v_exp_f32 v117, v117\n\t"
    "v_cvt_pk_bf16_f32 v37, v114, v115\n\t"
    "v_exp_f32 v118, v118\n\t"
    "v_mfma_f32_16x16x32_bf16 v[148:151], v[236:239], v[164:167], v[148:151]\n\t"
    "v_exp_f32 v119, v119\n\t"
    "v_cvt_pk_bf16_f32 v38, v116, v117\n\t"
    "v_or3_b32 v251, v251, v36, v37\n\t"
    "v_mfma_f32_16x16x32_bf16 v[148:151], v[232:235], v[156:159], v[148:151]\n\t"
    "ds_read_b128 v[232:235], v250 offset:20480\n\t"
    "v_cvt_pk_bf16_f32 v39, v118, v119\n\t"
    "v_or3_b32 v251, v251, v38, v39\n\t"
    "s_waitcnt lgkmcnt(3)\n\t"
    "v_mfma_f32_16x16x32_bf16 v[120:123], v[216:219], v[152:155], v[184:187]\n\t"
    "ds_read_b128 v[236:239], v250 offset:20992\n\t"
    "v_mfma_f32_16x16x32_bf16 v[120:123], v[220:223], v[160:163], v[120:123]\n\t"
    "ds_read_b128 v[240:243], v250 offset:21504\n\t"
    "v_exp_f32 v136, v136\n\t"
    "v_exp_f32 v137, v137\n\t"
    "s_waitcnt lgkmcnt(3)\n\t"
    "v_mfma_f32_16x16x32_bf16 v[120:123], v[224:227], v[168:171], v[120:123]\n\t"
    "ds_read_b128 v[244:247], v250 offset:22016\n\t"
    "v_exp_f32 v138, v138\n\t"
    "v_exp_f32 v139, v139\n\t"
    "v_mfma_f32_16x16x32_bf16 v[120:123], v[228:231], v[176:179], v[120:123]\n\t"
    "v_cvt_pk_bf16_f32 v40, v136, v137\n\t"
    "v_exp_f32 v140, v140\n\t"
    "v_exp_f32 v141, v141\n\t"
    "v_mfma_f32_16x16x32_bf16 v[128:131], v[228:231], v[180:183], v[188:191]\n\t"
    "v_cvt_pk_bf16_f32 v41, v138, v139\n\t"
    "v_exp_f32 v142, v142\n\t"
    "v_exp_f32 v143, v143\n\t"
    "v_mfma_f32_16x16x32_bf16 v[128:131], v[224:227], v[172:175], v[128:131]\n\t"
    "v_cvt_pk_bf16_f32 v42, v140, v141\n\t"
    "v_or3_b32 v251, v251, v40, v41\n\t"
    "v_exp_f32 v144, v144\n\t"
    "v_mfma_f32_16x16x32_bf16 v[128:131], v[220:223], v[164:167], v[128:131]\n\t"
    "v_exp_f32 v145, v145\n\t"
    "v_cvt_pk_bf16_f32 v43, v142, v143\n\t"
    "v_exp_f32 v146, v146\n\t"
    "v_mfma_f32_16x16x32_bf16 v[128:131], v[216:219], v[156:159], v[128:131]\n\t"
    "ds_read_b128 v[216:219], v250 offset:24576\n\t"
    "v_exp_f32 v147, v147\n\t"
    "v_cvt_pk_bf16_f32 v44, v144, v145\n\t"
    "s_waitcnt lgkmcnt(3)\n\t"
    "v_mfma_f32_16x16x32_bf16 v[124:127], v[232:235], v[152:155], v[184:187]\n\t"
    "ds_read_b128 v[220:223], v250 offset:25088\n\t"
    "v_or3_b32 v251, v251, v42, v43\n\t"
    "v_exp_f32 v148, v148\n\t"
    "v_mfma_f32_16x16x32_bf16 v[124:127], v[236:239], v[160:163], v[124:127]\n\t"
    "ds_read_b128 v[224:227], v250 offset:25600\n\t"
    "v_exp_f32 v149, v149\n\t"
    "v_cvt_pk_bf16_f32 v45, v146, v147\n\t"
    "s_waitcnt lgkmcnt(3)\n\t"
    "v_mfma_f32_16x16x32_bf16 v[124:127], v[240:243], v[168:171], v[124:127]\n\t"
    "ds_read_b128 v[228:231], v250 offset:26112\n\t"
    "v_exp_f32 v150, v150\n\t"
    "v_exp_f32 v151, v151\n\t"
    "v_mfma_f32_16x16x32_bf16 v[124:127], v[244:247], v[176:179], v[124:127]\n\t"
    "v_cvt_pk_bf16_f32 v46, v148, v149\n\t"
    "v_or3_b32 v251, v251, v44, v45\n\t"
    "v_cvt_pk_bf16_f32 v47, v150, v151\n\t"
    "v_or3_b32 v251, v251, v46, v47\n\t"
    "v_mfma_f32_16x16x32_bf16 v[132:135], v[244:247], v[180:183], v[188:191]\n\t"
    "v_mfma_f32_16x16x32_bf16 v[132:135], v[240:243], v[172:175], v[132:135]\n\t"
    "v_mfma_f32_16x16x32_bf16 v[132:135], v[236:239], v[164:167], v[132:135]\n\t"
    "v_mfma_f32_16x16x32_bf16 v[132:135], v[232:235], v[156:159], v[132:135]\n\t"
    "ds_read_b128 v[232:235], v250 offset:28672\n\t"
    "s_waitcnt lgkmcnt(3)\n\t"
    "v_mfma_f32_16x16x32_bf16 v[104:107], v[216:219], v[152:155], v[184:187]\n\t"
    "ds_read_b128 v[236:239], v250 offset:29184\n\t"
    "v_mfma_f32_16x16x32_bf16 v[104:107], v[220:223], v[160:163], v[104:107]\n\t"
    "ds_read_b128 v[240:243], v250 offset:29696\n\t"
    "v_exp_f32 v120, v120\n\t"
    "v_exp_f32 v121, v121\n\t"
    "s_waitcnt lgkmcnt(3)\n\t"
    "v_mfma_f32_16x16x32_bf16 v[104:107], v[224:227], v[168:171], v[104:107]\n\t"
    "ds_read_b128 v[244:247], v250 offset:30208\n\t"
    "v_exp_f32 v122, v122\n\t"
    "v_exp_f32 v123, v123\n\t"
    "v_mfma_f32_16x16x32_bf16 v[104:107], v[228:231], v[176:179], v[104:107]\n\t"
    "v_cvt_pk_bf16_f32 v48, v120, v121\n\t"
    "v_exp_f32 v124, v124\n\t"
    "v_exp_f32 v125, v125\n\t"
    "v_mfma_f32_16x16x32_bf16 v[112:115], v[228:231], v[180:183], v[188:191]\n\t"
    "v_cvt_pk_bf16_f32 v49, v122, v123\n\t"
    "v_exp_f32 v126, v126\n\t"
    "v_exp_f32 v127, v127\n\t"
    "v_mfma_f32_16x16x32_bf16 v[112:115], v[224:227], v[172:175], v[112:115]\n\t"
    "v_cvt_pk_bf16_f32 v50, v124, v125\n\t"
    "v_or3_b32 v251, v251, v48, v49\n\t"
    "v_exp_f32 v128, v128\n\t"
    "v_mfma_f32_16x16x32_bf16 v[112:115], v[220:223], v[164:167], v[112:115]\n\t"
    "v_exp_f32 v129, v129\n\t"
    "v_cvt_pk_bf16_f32 v51, v126, v127\n\t"
    "v_exp_f32 v130, v130\n\t"
    "v_mfma_f32_16x16x32_bf16 v[112:115], v[216:219], v[156:159], v[112:115]\n\t"
    "s_waitcnt lgkmcnt(0)\n\t"
    "s_barrier\n\t"
    "ds_read_b128 v[216:219], v250 offset:32768\n\t"
    "v_exp_f32 v131, v131\n\t"
    "v_cvt_pk_bf16_f32 v52, v128, v129\n\t"
    "v_mfma_f32_16x16x32_bf16 v[108:111], v[232:235], v[152:155], v[184:187]\n\t"
    "ds_read_b128 v[220:223], v250 offset:33280\n\t"
    "v_or3_b32 v251, v251, v50, v51\n\t"
    "v_exp_f32 v132, v132\n\t"
    "v_mfma_f32_16x16x32_bf16 v[108:111], v[236:239], v[160:163], v[108:111]\n\t"
    "ds_read_b128 v[224:227], v250 offset:33792\n\t"
    "v_exp_f32 v133, v133\n\t"
    "v_cvt_pk_bf16_f32 v53, v130, v131\n\t"
    "v_mfma_f32_16x16x32_bf16 v[108:111], v[240:243], v[168:171], v[108:111]\n\t"
    "ds_read_b128 v[228:231], v250 offset:34304\n\t"
    "v_exp_f32 v134, v134\n\t"
    "v_exp_f32 v135, v135\n\t"
    "v_mfma_f32_16x16x32_bf16 v[108:111], v[244:247], v[176:179], v[108:111]\n\t"
    "v_cvt_pk_bf16_f32 v54, v132, v133\n\t"
    "v_or3_b32 v251, v251, v52, v53\n\t"
    "v_cvt_pk_bf16_f32 v55, v134, v135\n\t"
    "v_or3_b32 v251, v251, v54, v55\n\t"
    "v_mfma_f32_16x16x32_bf16 v[116:119], v[244:247], v[180:183], v[188:191]\n\t"
    "v_mfma_f32_16x16x32_bf16 v[116:119], v[240:243], v[172:175], v[116:119]\n\t"
    "v_mfma_f32_16x16x32_bf16 v[116:119], v[236:239], v[164:167], v[116:119]\n\t"
    "v_mfma_f32_16x16x32_bf16 v[116:119], v[232:235], v[156:159], v[116:119]\n\t"
    "ds_read_b128 v[232:235], v250 offset:36864\n\t"
    "s_waitcnt lgkmcnt(3)\n\t"
    "v_mfma_f32_16x16x32_bf16 v[136:139], v[216:219], v[152:155], v[184:187]\n\t"
    "ds_read_b128 v[236:239], v250 offset:37376\n\t"
    "v_mfma_f32_16x16x32_bf16 v[136:139], v[220:223], v[160:163], v[136:139]\n\t"
    "ds_read_b128 v[240:243], v250 offset:37888\n\t"
    "v_exp_f32 v104, v104\n\t"
    "v_exp_f32 v105, v105\n\t"
    "s_waitcnt lgkmcnt(3)\n\t"
    "v_mfma_f32_16x16x32_bf16 v[136:139], v[224:227], v[168:171], v[136:139]\n\t"
    "ds_read_b128 v[244:247], v250 offset:38400\n\t"
    "v_exp_f32 v106, v106\n\t"
    "v_exp_f32 v107, v107\n\t"
    "v_mfma_f32_16x16x32_bf16 v[136:139], v[228:231], v[176:179], v[136:139]\n\t"
    "v_cvt_pk_bf16_f32 v56, v104, v105\n\t"
    "v_exp_f32 v108, v108\n\t"
    "v_exp_f32 v109, v109\n\t"
    "v_mfma_f32_16x16x32_bf16 v[144:147], v[228:231], v[180:183], v[188:191]\n\t"
    "v_cvt_pk_bf16_f32 v57, v106, v107\n\t"
    "v_exp_f32 v110, v110\n\t"
    "v_exp_f32 v111, v111\n\t"
    "v_mfma_f32_16x16x32_bf16 v[144:147], v[224:227], v[172:175], v[144:147]\n\t"
    "v_cvt_pk_bf16_f32 v58, v108, v109\n\t"
    "v_or3_b32 v251, v251, v56, v57\n\t"
    "v_exp_f32 v112, v112\n\t"
    "v_mfma_f32_16x16x32_bf16 v[144:147], v[220:223], v[164:167], v[144:147]\n\t"
    "v_exp_f32 v113, v113\n\t"
    "v_cvt_pk_bf16_f32 v59, v110, v111\n\t"
    "v_exp_f32 v114, v114\n\t"
    "v_mfma_f32_16x16x32_bf16 v[144:147], v[216:219], v[156:159], v[144:147]\n\t"
    "ds_read_b128 v[216:219], v250 offset:40960\n\t"
    "v_exp_f32 v115, v115\n\t"
    "v_cvt_pk_bf16_f32 v60, v112, v113\n\t"
    "s_waitcnt lgkmcnt(3)\n\t"
    "v_mfma_f32_16x16x32_bf16 v[140:143], v[232:235], v[152:155], v[184:187]\n\t"
    "ds_read_b128 v[220:223], v250 offset:41472\n\t"
    "v_or3_b32 v251, v251, v58, v59\n\t"
    "v_exp_f32 v116, v116\n\t"
    "v_mfma_f32_16x16x32_bf16 v[140:143], v[236:239], v[160:163], v[140:143]\n\t"
    "ds_read_b128 v[224:227], v250 offset:41984\n\t"
    "v_exp_f32 v117, v117\n\t"
    "v_cvt_pk_bf16_f32 v61, v114, v115\n\t"
    "s_waitcnt lgkmcnt(3)\n\t"
    "v_mfma_f32_16x16x32_bf16 v[140:143], v[240:243], v[168:171], v[140:143]\n\t"
    "ds_read_b128 v[228:231], v250 offset:42496\n\t"
    "v_exp_f32 v118, v118\n\t"
    "v_exp_f32 v119, v119\n\t"
    "v_mfma_f32_16x16x32_bf16 v[140:143], v[244:247], v[176:179], v[140:143]\n\t"
    "v_cvt_pk_bf16_f32 v62, v116, v117\n\t"
    "v_or3_b32 v251, v251, v60, v61\n\t"
    "v_cvt_pk_bf16_f32 v63, v118, v119\n\t"
    "v_or3_b32 v251, v251, v62, v63\n\t"
    "v_mfma_f32_16x16x32_bf16 v[148:151], v[244:247], v[180:183], v[188:191]\n\t"
    "v_mfma_f32_16x16x32_bf16 v[148:151], v[240:243], v[172:175], v[148:151]\n\t"
    "v_mfma_f32_16x16x32_bf16 v[148:151], v[236:239], v[164:167], v[148:151]\n\t"
    "v_mfma_f32_16x16x32_bf16 v[148:151], v[232:235], v[156:159], v[148:151]\n\t"
    "ds_read_b128 v[232:235], v250 offset:45056\n\t"
    "s_waitcnt lgkmcnt(3)\n\t"
    "v_mfma_f32_16x16x32_bf16 v[120:123], v[216:219], v[152:155], v[184:187]\n\t"
    "ds_read_b128 v[236:239], v250 offset:45568\n\t"
    "v_mfma_f32_16x16x32_bf16 v[120:123], v[220:223], v[160:163], v[120:123]\n\t"
    "ds_read_b128 v[240:243], v250 offset:46080\n\t"
    "v_exp_f32 v136, v136\n\t"
    "v_exp_f32 v137, v137\n\t"
    "s_waitcnt lgkmcnt(3)\n\t"
    "v_mfma_f32_16x16x32_bf16 v[120:123], v[224:227], v[168:171], v[120:123]\n\t"
    "ds_read_b128 v[244:247], v250 offset:46592\n\t"
    "v_exp_f32 v138, v138\n\t"
    "v_exp_f32 v139, v139\n\t"
    "v_mfma_f32_16x16x32_bf16 v[120:123], v[228:231], v[176:179], v[120:123]\n\t"
    "v_cvt_pk_bf16_f32 v64, v136, v137\n\t"
    "v_exp_f32 v140, v140\n\t"
    "v_exp_f32 v141, v141\n\t"
    "v_mfma_f32_16x16x32_bf16 v[128:131], v[228:231], v[180:183], v[188:191]\n\t"
    "v_cvt_pk_bf16_f32 v65, v138, v139\n\t"
    "v_exp_f32 v142, v142\n\t"
    "v_exp_f32 v143, v143\n\t"
    "v_mfma_f32_16x16x32_bf16 v[128:131], v[224:227], v[172:175], v[128:131]\n\t"
    "v_cvt_pk_bf16_f32 v66, v140, v141\n\t"
    "v_or3_b32 v251, v251, v64, v65\n\t"
    "v_exp_f32 v144, v144\n\t"
    "v_mfma_f32_16x16x32_bf16 v[128:131], v[220:223], v[164:167], v[128:131]\n\t"
    "v_exp_f32 v145, v145\n\t"
    "v_cvt_pk_bf16_f32 v67, v142, v143\n\t"
    "v_exp_f32 v146, v146\n\t"
    "v_mfma_f32_16x16x32_bf16 v[128:131], v[216:219], v[156:159], v[128:131]\n\t"
    "s_waitcnt lgkmcnt(0)\n\t"
    "s_barrier\n\t"
    "ds_read_b128 v[216:219], v250 offset:49152\n\t"
    "v_exp_f32 v147, v147\n\t"
    "v_cvt_pk_bf16_f32 v68, v144, v145\n\t"
    "v_mfma_f32_16x16x32_bf16 v[124:127], v[232:235], v[152:155], v[184:187]\n\t"
    "ds_read_b128 v[220:223], v250 offset:49664\n\t"
    "v_or3_b32 v251, v251, v66, v67\n\t"
    "v_exp_f32 v148, v148\n\t"
    "v_mfma_f32_16x16x32_bf16 v[124:127], v[236:239], v[160:163], v[124:127]\n\t"
    "ds_read_b128 v[224:227], v250 offset:50176\n\t"
    "v_exp_f32 v149, v149\n\t"
    "v_cvt_pk_bf16_f32 v69, v146, v147\n\t"
    "v_mfma_f32_16x16x32_bf16 v[124:127], v[240:243], v[168:171], v[124:127]\n\t"
    "ds_read_b128 v[228:231], v250 offset:50688\n\t"
    "v_exp_f32 v150, v150\n\t"
    "v_exp_f32 v151, v151\n\t"
    "v_mfma_f32_16x16x32_bf16 v[124:127], v[244:247], v[176:179], v[124:127]\n\t"
    "v_cvt_pk_bf16_f32 v70, v148, v149\n\t"
    "v_or3_b32 v251, v251, v68, v69\n\t"
    "v_cvt_pk_bf16_f32 v71, v150, v151\n\t"
    "v_or3_b32 v251, v251, v70, v71\n\t"
    "v_mfma_f32_16x16x32_bf16 v[132:135], v[244:247], v[180:183], v[188:191]\n\t"
    "v_mfma_f32_16x16x32_bf16 v[132:135], v[240:243], v[172:175], v[132:135]\n\t"
    "v_mfma_f32_16x16x32_bf16 v[132:135], v[236:239], v[164:167], v[132:135]\n\t"
    "v_mfma_f32_16x16x32_bf16 v[132:135], v[232:235], v[156:159], v[132:135]\n\t"
    "ds_read_b128 v[232:235], v250 offset:53248\n\t"
    "s_waitcnt lgkmcnt(3)\n\t"
    "v_mfma_f32_16x16x32_bf16 v[104:107], v[216:219], v[152:155], v[184:187]\n\t"
    "ds_read_b128 v[236:239], v250 offset:53760\n\t"
    "v_mfma_f32_16x16x32_bf16 v[104:107], v[220:223], v[160:163], v[104:107]\n\t"
    "ds_read_b128 v[240:243], v250 offset:54272\n\t"
    "v_exp_f32 v120, v120\n\t"
    "v_exp_f32 v121, v121\n\t"
    "s_waitcnt lgkmcnt(3)\n\t"
    "v_mfma_f32_16x16x32_bf16 v[104:107], v[224:227], v[168:171], v[104:107]\n\t"
    "ds_read_b128 v[244:247], v250 offset:54784\n\t"
    "v_exp_f32 v122, v122\n\t"
    "v_exp_f32 v123, v123\n\t"
    "v_mfma_f32_16x16x32_bf16 v[104:107], v[228:231], v[176:179], v[104:107]\n\t"
    "v_cvt_pk_bf16_f32 v72, v120, v121\n\t"
    "v_exp_f32 v124, v124\n\t"
    "v_exp_f32 v125, v125\n\t"
    "v_mfma_f32_16x16x32_bf16 v[112:115], v[228:231], v[180:183], v[188:191]\n\t"
    "v_cvt_pk_bf16_f32 v73, v122, v123\n\t"
    "v_exp_f32 v126, v126\n\t"
    "v_exp_f32 v127, v127\n\t"
    "v_mfma_f32_16x16x32_bf16 v[112:115], v[224:227], v[172:175], v[112:115]\n\t"
    "v_cvt_pk_bf16_f32 v74, v124, v125\n\t"
    "v_or3_b32 v251, v251, v72, v73\n\t"
    "v_exp_f32 v128, v128\n\t"
    "v_mfma_f32_16x16x32_bf16 v[112:115], v[220:223], v[164:167], v[112:115]\n\t"
    "v_exp_f32 v129, v129\n\t"
    "v_cvt_pk_bf16_f32 v75, v126, v127\n\t"
    "v_exp_f32 v130, v130\n\t"
    "v_mfma_f32_16x16x32_bf16 v[112:115], v[216:219], v[156:159], v[112:115]\n\t"
    "ds_read_b128 v[216:219], v250 offset:57344\n\t"
    "v_exp_f32 v131, v131\n\t"
    "v_cvt_pk_bf16_f32 v76, v128, v129\n\t"
    "s_waitcnt lgkmcnt(3)\n\t"
    "v_mfma_f32_16x16x32_bf16 v[108:111], v[232:235], v[152:155], v[184:187]\n\t"
    "ds_read_b128 v[220:223], v250 offset:57856\n\t"
    "v_or3_b32 v251, v251, v74, v75\n\t"
    "v_exp_f32 v132, v132\n\t"
    "v_mfma_f32_16x16x32_bf16 v[108:111], v[236:239], v[160:163], v[108:111]\n\t"
    "ds_read_b128 v[224:227], v250 offset:58368\n\t"
    "v_exp_f32 v133, v133\n\t"
    "v_cvt_pk_bf16_f32 v77, v130, v131\n\t"
    "s_waitcnt lgkmcnt(3)\n\t"
    "v_mfma_f32_16x16x32_bf16 v[108:111], v[240:243], v[168:171], v[108:111]\n\t"
    "ds_read_b128 v[228:231], v250 offset:58880\n\t"
    "v_exp_f32 v134, v134\n\t"
    "v_exp_f32 v135, v135\n\t"
    "v_mfma_f32_16x16x32_bf16 v[108:111], v[244:247], v[176:179], v[108:111]\n\t"
    "v_cvt_pk_bf16_f32 v78, v132, v133\n\t"
    "v_or3_b32 v251, v251, v76, v77\n\t"
    "v_cvt_pk_bf16_f32 v79, v134, v135\n\t"
    "v_or3_b32 v251, v251, v78, v79\n\t"
    "v_mfma_f32_16x16x32_bf16 v[116:119], v[244:247], v[180:183], v[188:191]\n\t"
    "v_mfma_f32_16x16x32_bf16 v[116:119], v[240:243], v[172:175], v[116:119]\n\t"
    "v_mfma_f32_16x16x32_bf16 v[116:119], v[236:239], v[164:167], v[116:119]\n\t"
    "v_mfma_f32_16x16x32_bf16 v[116:119], v[232:235], v[156:159], v[116:119]\n\t"
    "ds_read_b128 v[232:235], v250 offset:61440\n\t"
    "s_waitcnt lgkmcnt(3)\n\t"
    "v_mfma_f32_16x16x32_bf16 v[136:139], v[216:219], v[152:155], v[184:187]\n\t"
    "ds_read_b128 v[236:239], v250 offset:61952\n\t"
    "v_mfma_f32_16x16x32_bf16 v[136:139], v[220:223], v[160:163], v[136:139]\n\t"
    "ds_read_b128 v[240:243], v250 offset:62464\n\t"
    "v_exp_f32 v104, v104\n\t"
    "v_exp_f32 v105, v105\n\t"
    "s_waitcnt lgkmcnt(3)\n\t"
    "v_mfma_f32_16x16x32_bf16 v[136:139], v[224:227], v[168:171], v[136:139]\n\t"
    "ds_read_b128 v[244:247], v250 offset:62976\n\t"
    "v_exp_f32 v106, v106\n\t"
    "v_exp_f32 v107, v107\n\t"
    "v_mfma_f32_16x16x32_bf16 v[136:139], v[228:231], v[176:179], v[136:139]\n\t"
    "v_cvt_pk_bf16_f32 v80, v104, v105\n\t"
    "v_exp_f32 v108, v108\n\t"
    "v_exp_f32 v109, v109\n\t"
    "v_mfma_f32_16x16x32_bf16 v[144:147], v[228:231], v[180:183], v[188:191]\n\t"
    "v_cvt_pk_bf16_f32 v81, v106, v107\n\t"
    "v_exp_f32 v110, v110\n\t"
    "v_exp_f32 v111, v111\n\t"
    "v_mfma_f32_16x16x32_bf16 v[144:147], v[224:227], v[172:175], v[144:147]\n\t"
    "v_cvt_pk_bf16_f32 v82, v108, v109\n\t"
    "v_or3_b32 v251, v251, v80, v81\n\t"
    "v_exp_f32 v112, v112\n\t"
    "v_mfma_f32_16x16x32_bf16 v[144:147], v[220:223], v[164:167], v[144:147]\n\t"
    "v_exp_f32 v113, v113\n\t"
    "v_cvt_pk_bf16_f32 v83, v110, v111\n\t"
    "v_exp_f32 v114, v114\n\t"
    "v_mfma_f32_16x16x32_bf16 v[144:147], v[216:219], v[156:159], v[144:147]\n\t"
    "s_waitcnt lgkmcnt(0)\n\t"
    "s_barrier\n\t"
    "v_add_u32 v250, 0xffff0000, v250\n\t"
    "v_exp_f32 v115, v115\n\t"
    "v_cvt_pk_bf16_f32 v84, v112, v113\n\t"
    "v_mfma_f32_16x16x32_bf16 v[140:143], v[232:235], v[152:155], v[184:187]\n\t"
    "ds_read_b128 v[216:219], v250 offset:32768\n\t"
    "v_or3_b32 v251, v251, v82, v83\n\t"
    "v_exp_f32 v116, v116\n\t"
    "v_mfma_f32_16x16x32_bf16 v[140:143], v[236:239], v[160:163], v[140:143]\n\t"
    "ds_read_b128 v[220:223], v250 offset:33280\n\t"
    "v_exp_f32 v117, v117\n\t"
    "v_cvt_pk_bf16_f32 v85, v114, v115\n\t"
    "v_mfma_f32_16x16x32_bf16 v[140:143], v[240:243], v[168:171], v[140:143]\n\t"
    "ds_read_b128 v[224:227], v250 offset:33792\n\t"
    "v_exp_f32 v118, v118\n\t"
    "v_exp_f32 v119, v119\n\t"
    "v_mfma_f32_16x16x32_bf16 v[140:143], v[244:247], v[176:179], v[140:143]\n\t"
    "ds_read_b128 v[228:231], v250 offset:34304\n\t"
    "v_cvt_pk_bf16_f32 v86, v116, v117\n\t"
    "v_or3_b32 v251, v251, v84, v85\n\t"
    "v_cvt_pk_bf16_f32 v87, v118, v119\n\t"
    "v_mfma_f32_16x16x32_bf16 v[148:151], v[244:247], v[180:183], v[188:191]\n\t"
    "v_or3_b32 v251, v251, v86, v87\n\t"
    "v_mfma_f32_16x16x32_bf16 v[148:151], v[240:243], v[172:175], v[148:151]\n\t"
    "v_mfma_f32_16x16x32_bf16 v[148:151], v[236:239], v[164:167], v[148:151]\n\t"
    "v_mfma_f32_16x16x32_bf16 v[148:151], v[232:235], v[156:159], v[148:151]\n\t"
    "ds_read_b128 v[232:235], v250 offset:36864\n\t"
    "s_waitcnt lgkmcnt(3)\n\t"
    "v_mfma_f32_16x16x32_bf16 v[120:123], v[216:219], v[152:155], v[184:187]\n\t"
    "ds_read_b128 v[236:239], v250 offset:37376\n\t"
    "v_mfma_f32_16x16x32_bf16 v[120:123], v[220:223], v[160:163], v[120:123]\n\t"
    "ds_read_b128 v[240:243], v250 offset:37888\n\t"
    "v_exp_f32 v136, v136\n\t"
    "v_exp_f32 v137, v137\n\t"
    "s_waitcnt lgkmcnt(3)\n\t"
    "v_mfma_f32_16x16x32_bf16 v[120:123], v[224:227], v[168:171], v[120:123]\n\t"
    "ds_read_b128 v[244:247], v250 offset:38400\n\t"
    "v_exp_f32 v138, v138\n\t"
    "v_exp_f32 v139, v139\n\t"
    "v_mfma_f32_16x16x32_bf16 v[120:123], v[228:231], v[176:179], v[120:123]\n\t"
    "v_cvt_pk_bf16_f32 v88, v136, v137\n\t"
    "v_exp_f32 v140, v140\n\t"
    "v_exp_f32 v141, v141\n\t"
    "v_mfma_f32_16x16x32_bf16 v[128:131], v[228:231], v[180:183], v[188:191]\n\t"
    "v_cvt_pk_bf16_f32 v89, v138, v139\n\t"
    "v_exp_f32 v142, v142\n\t"
    "v_exp_f32 v143, v143\n\t"
    "v_mfma_f32_16x16x32_bf16 v[128:131], v[224:227], v[172:175], v[128:131]\n\t"
    "v_cvt_pk_bf16_f32 v90, v140, v141\n\t"
    "v_or3_b32 v251, v251, v88, v89\n\t"
    "v_exp_f32 v144, v144\n\t"
    "v_mfma_f32_16x16x32_bf16 v[128:131], v[220:223], v[164:167], v[128:131]\n\t"
    "v_exp_f32 v145, v145\n\t"
    "v_cvt_pk_bf16_f32 v91, v142, v143\n\t"
    "v_exp_f32 v146, v146\n\t"
    "v_mfma_f32_16x16x32_bf16 v[128:131], v[216:219], v[156:159], v[128:131]\n\t"
    "ds_read_b128 v[216:219], v250 offset:40960\n\t"
    "v_exp_f32 v147, v147\n\t"
    "v_cvt_pk_bf16_f32 v92, v144, v145\n\t"
    "s_waitcnt lgkmcnt(3)\n\t"
    "v_mfma_f32_16x16x32_bf16 v[124:127], v[232:235], v[152:155], v[184:187]\n\t"
    "ds_read_b128 v[220:223], v250 offset:41472\n\t"
    "v_or3_b32 v251, v251, v90, v91\n\t"
    "v_exp_f32 v148, v148\n\t"
    "v_mfma_f32_16x16x32_bf16 v[124:127], v[236:239], v[160:163], v[124:127]\n\t"
    "ds_read_b128 v[224:227], v250 offset:41984\n\t"
    "v_exp_f32 v149, v149\n\t"
    "v_cvt_pk_bf16_f32 v93, v146, v147\n\t"
    "s_waitcnt lgkmcnt(3)\n\t"
    "v_mfma_f32_16x16x32_bf16 v[124:127], v[240:243], v[168:171], v[124:127]\n\t"
    "ds_read_b128 v[228:231], v250 offset:42496\n\t"
    "v_exp_f32 v150, v150\n\t"
    "v_exp_f32 v151, v151\n\t"
    "v_mfma_f32_16x16x32_bf16 v[124:127], v[244:247], v[176:179], v[124:127]\n\t"
    "v_cvt_pk_bf16_f32 v94, v148, v149\n\t"
    "v_or3_b32 v251, v251, v92, v93\n\t"
    "v_cvt_pk_bf16_f32 v95, v150, v151\n\t"
    "v_or3_b32 v251, v251, v94, v95\n\t"
    "v_mfma_f32_16x16x32_bf16 v[132:135], v[244:247], v[180:183], v[188:191]\n\t"
    "v_mfma_f32_16x16x32_bf16 v[132:135], v[240:243], v[172:175], v[132:135]\n\t"
    "v_mfma_f32_16x16x32_bf16 v[132:135], v[236:239], v[164:167], v[132:135]\n\t"
    "v_mfma_f32_16x16x32_bf16 v[132:135], v[232:235], v[156:159], v[132:135]\n\t"
    "ds_read_b128 v[232:235], v250 offset:45056\n\t"
    "s_waitcnt lgkmcnt(3)\n\t"
    "v_mfma_f32_16x16x32_bf16 v[104:107], v[216:219], v[152:155], v[184:187]\n\t"
    "ds_read_b128 v[236:239], v250 offset:45568\n\t"
    "v_mfma_f32_16x16x32_bf16 v[104:107], v[220:223], v[160:163], v[104:107]\n\t"
    "ds_read_b128 v[240:243], v250 offset:46080\n\t"
    "v_exp_f32 v120, v120\n\t"
    "v_exp_f32 v121, v121\n\t"
    "s_waitcnt lgkmcnt(3)\n\t"
    "v_mfma_f32_16x16x32_bf16 v[104:107], v[224:227], v[168:171], v[104:107]\n\t"
    "ds_read_b128 v[244:247], v250 offset:46592\n\t"
    "v_exp_f32 v122, v122\n\t"
    "v_exp_f32 v123, v123\n\t"
    "v_mfma_f32_16x16x32_bf16 v[104:107], v[228:231], v[176:179], v[104:107]\n\t"
    "v_cvt_pk_bf16_f32 v96, v120, v121\n\t"
    "v_exp_f32 v124, v124\n\t"
    "v_exp_f32 v125, v125\n\t"
    "v_mfma_f32_16x16x32_bf16 v[112:115], v[228:231], v[180:183], v[188:191]\n\t"
    "v_cvt_pk_bf16_f32 v97, v122, v123\n\t"
    "v_exp_f32 v126, v126\n\t"
    "v_exp_f32 v127, v127\n\t"
    "v_mfma_f32_16x16x32_bf16 v[112:115], v[224:227], v[172:175], v[112:115]\n\t"
    "v_cvt_pk_bf16_f32 v98, v124, v125\n\t"
    "v_or3_b32 v251, v251, v96, v97\n\t"
    "v_exp_f32 v128, v128\n\t"
    "v_mfma_f32_16x16x32_bf16 v[112:115], v[220:223], v[164:167], v[112:115]\n\t"
    "v_exp_f32 v129, v129\n\t"
    "v_cvt_pk_bf16_f32 v99, v126, v127\n\t"
    "v_exp_f32 v130, v130\n\t"
    "v_mfma_f32_16x16x32_bf16 v[112:115], v[216:219], v[156:159], v[112:115]\n\t"
    "s_waitcnt lgkmcnt(0)\n\t"
    "s_barrier\n\t"
    "ds_read_b128 v[216:219], v250 offset:49152\n\t"
    "v_exp_f32 v131, v131\n\t"
    "v_cvt_pk_bf16_f32 v100, v128, v129\n\t"
    "v_mfma_f32_16x16x32_bf16 v[108:111], v[232:235], v[152:155], v[184:187]\n\t"
    "ds_read_b128 v[220:223], v250 offset:49664\n\t"
    "v_or3_b32 v251, v251, v98, v99\n\t"
    "v_exp_f32 v132, v132\n\t"
    "v_mfma_f32_16x16x32_bf16 v[108:111], v[236:239], v[160:163], v[108:111]\n\t"
    "ds_read_b128 v[224:227], v250 offset:50176\n\t"
    "v_exp_f32 v133, v133\n\t"
    "v_cvt_pk_bf16_f32 v101, v130, v131\n\t"
    "v_mfma_f32_16x16x32_bf16 v[108:111], v[240:243], v[168:171], v[108:111]\n\t"
    "ds_read_b128 v[228:231], v250 offset:50688\n\t"
    "v_exp_f32 v134, v134\n\t"
    "v_exp_f32 v135, v135\n\t"
    "v_mfma_f32_16x16x32_bf16 v[108:111], v[244:247], v[176:179], v[108:111]\n\t"
    "v_cvt_pk_bf16_f32 v102, v132, v133\n\t"
    "v_or3_b32 v251, v251, v100, v101\n\t"
    "v_cvt_pk_bf16_f32 v103, v134, v135\n\t"
    "v_or3_b32 v251, v251, v102, v103\n\t"
    "v_mfma_f32_16x16x32_bf16 v[116:119], v[244:247], v[180:183], v[188:191]\n\t"
    "v_mfma_f32_16x16x32_bf16 v[116:119], v[240:243], v[172:175], v[116:119]\n\t"
    "v_mfma_f32_16x16x32_bf16 v[116:119], v[236:239], v[164:167], v[116:119]\n\t"
    "v_mfma_f32_16x16x32_bf16 v[116:119], v[232:235], v[156:159], v[116:119]\n\t"
    "ds_read_b128 v[232:235], v250 offset:53248\n\t"
    "s_waitcnt lgkmcnt(3)\n\t"
    "v_mfma_f32_16x16x32_bf16 v[136:139], v[216:219], v[152:155], v[184:187]\n\t"
    "ds_read_b128 v[236:239], v250 offset:53760\n\t"
    "v_mfma_f32_16x16x32_bf16 v[136:139], v[220:223], v[160:163], v[136:139]\n\t"
    "ds_read_b128 v[240:243], v250 offset:54272\n\t"
    "v_exp_f32 v104, v104\n\t"
    "v_exp_f32 v105, v105\n\t"
    "s_waitcnt lgkmcnt(3)\n\t"
    "v_mfma_f32_16x16x32_bf16 v[136:139], v[224:227], v[168:171], v[136:139]\n\t"
    "ds_read_b128 v[244:247], v250 offset:54784\n\t"
    "v_exp_f32 v106, v106\n\t"
    "v_exp_f32 v107, v107\n\t"
    "v_mfma_f32_16x16x32_bf16 v[136:139], v[228:231], v[176:179], v[136:139]\n\t"
    "v_cvt_pk_bf16_f32 v104, v104, v105\n\t"
    "v_exp_f32 v108, v108\n\t"
    "v_exp_f32 v109, v109\n\t"
    "v_mfma_f32_16x16x32_bf16 v[144:147], v[228:231], v[180:183], v[188:191]\n\t"
    "v_cvt_pk_bf16_f32 v105, v106, v107\n\t"
    "v_exp_f32 v110, v110\n\t"
    "v_exp_f32 v111, v111\n\t"
    "v_mfma_f32_16x16x32_bf16 v[144:147], v[224:227], v[172:175], v[144:147]\n\t"
    "v_cvt_pk_bf16_f32 v106, v108, v109\n\t"
    "v_or3_b32 v251, v251, v104, v105\n\t"
    "v_exp_f32 v112, v112\n\t"
    "v_mfma_f32_16x16x32_bf16 v[144:147], v[220:223], v[164:167], v[144:147]\n\t"
    "v_exp_f32 v113, v113\n\t"
    "v_cvt_pk_bf16_f32 v107, v110, v111\n\t"
    "v_exp_f32 v114, v114\n\t"
    "v_mfma_f32_16x16x32_bf16 v[144:147], v[216:219], v[156:159], v[144:147]\n\t"
    "ds_read_b128 v[216:219], v250 offset:57344\n\t"
    "v_exp_f32 v115, v115\n\t"
    "v_cvt_pk_bf16_f32 v108, v112, v113\n\t"
    "s_waitcnt lgkmcnt(3)\n\t"
    "v_mfma_f32_16x16x32_bf16 v[140:143], v[232:235], v[152:155], v[184:187]\n\t"
    "ds_read_b128 v[220:223], v250 offset:57856\n\t"
    "v_or3_b32 v251, v251, v106, v107\n\t"
    "v_exp_f32 v116, v116\n\t"
    "v_mfma_f32_16x16x32_bf16 v[140:143], v[236:239], v[160:163], v[140:143]\n\t"
    "ds_read_b128 v[224:227], v250 offset:58368\n\t"
    "v_exp_f32 v117, v117\n\t"
    "v_cvt_pk_bf16_f32 v109, v114, v115\n\t"
    "s_waitcnt lgkmcnt(3)\n\t"
    "v_mfma_f32_16x16x32_bf16 v[140:143], v[240:243], v[168:171], v[140:143]\n\t"
    "ds_read_b128 v[228:231], v250 offset:58880\n\t"
    "v_exp_f32 v118, v118\n\t"
    "v_exp_f32 v119, v119\n\t"
    "v_mfma_f32_16x16x32_bf16 v[140:143], v[244:247], v[176:179], v[140:143]\n\t"
    "v_cvt_pk_bf16_f32 v110, v116, v117\n\t"
    "v_or3_b32 v251, v251, v108, v109\n\t"
    "v_cvt_pk_bf16_f32 v111, v118, v119\n\t"
    "v_or3_b32 v251, v251, v110, v111\n\t"
    "v_mfma_f32_16x16x32_bf16 v[148:151], v[244:247], v[180:183], v[188:191]\n\t"
    "v_mfma_f32_16x16x32_bf16 v[148:151], v[240:243], v[172:175], v[148:151]\n\t"
    "v_mfma_f32_16x16x32_bf16 v[148:151], v[236:239], v[164:167], v[148:151]\n\t"
    "v_mfma_f32_16x16x32_bf16 v[148:151], v[232:235], v[156:159], v[148:151]\n\t"
    "ds_read_b128 v[232:235], v250 offset:61440\n\t"
    "s_waitcnt lgkmcnt(3)\n\t"
    "v_mfma_f32_16x16x32_bf16 v[120:123], v[216:219], v[152:155], v[184:187]\n\t"
    "ds_read_b128 v[236:239], v250 offset:61952\n\t"
    "v_mfma_f32_16x16x32_bf16 v[120:123], v[220:223], v[160:163], v[120:123]\n\t"
    "ds_read_b128 v[240:243], v250 offset:62464\n\t"
    "v_exp_f32 v136, v136\n\t"
    "v_exp_f32 v137, v137\n\t"
    "s_waitcnt lgkmcnt(3)\n\t"
    "v_mfma_f32_16x16x32_bf16 v[120:123], v[224:227], v[168:171], v[120:123]\n\t"
    "ds_read_b128 v[244:247], v250 offset:62976\n\t"
    "v_exp_f32 v138, v138\n\t"
    "v_exp_f32 v139, v139\n\t"
    "v_mfma_f32_16x16x32_bf16 v[120:123], v[228:231], v[176:179], v[120:123]\n\t"
    "v_cvt_pk_bf16_f32 v112, v136, v137\n\t"
    "v_exp_f32 v140, v140\n\t"
    "v_exp_f32 v141, v141\n\t"
    "v_mfma_f32_16x16x32_bf16 v[128:131], v[228:231], v[180:183], v[188:191]\n\t"
    "v_cvt_pk_bf16_f32 v113, v138, v139\n\t"
    "v_exp_f32 v142, v142\n\t"
    "v_exp_f32 v143, v143\n\t"
    "v_mfma_f32_16x16x32_bf16 v[128:131], v[224:227], v[172:175], v[128:131]\n\t"
    "v_cvt_pk_bf16_f32 v114, v140, v141\n\t"
    "v_or3_b32 v251, v251, v112, v113\n\t"
    "v_exp_f32 v144, v144\n\t"
    "v_mfma_f32_16x16x32_bf16 v[128:131], v[220:223], v[164:167], v[128:131]\n\t"
    "v_exp_f32 v145, v145\n\t"
    "v_cvt_pk_bf16_f32 v115, v142, v143\n\t"
    "v_exp_f32 v146, v146\n\t"
    "v_mfma_f32_16x16x32_bf16 v[128:131], v[216:219], v[156:159], v[128:131]\n\t"
    "s_waitcnt lgkmcnt(0)\n\t"
    "s_barrier\n\t"
    "v_add_u32 v248, 0x10000, v248\n\t"
    "v_add_u32 v249, 0x10000, v249\n\t"
    "v_exp_f32 v147, v147\n\t"
    "v_mfma_f32_16x16x32_bf16 v[124:127], v[232:235], v[152:155], v[184:187]\n\t"
    "ds_read_b64_tr_b16 v[216:217], v248 offset:0\n\t"
    "ds_read_b64_tr_b16 v[218:219], v248 offset:4096\n\t"
    "v_cvt_pk_bf16_f32 v116, v144, v145\n\t"
    "v_or3_b32 v251, v251, v114, v115\n\t"
    "v_mfma_f32_16x16x32_bf16 v[124:127], v[236:239], v[160:163], v[124:127]\n\t"
    "ds_read_b64_tr_b16 v[220:221], v248 offset:8192\n\t"
    "v_exp_f32 v148, v148\n\t"
    "v_exp_f32 v149, v149\n\t"
    "v_mfma_f32_16x16x32_bf16 v[124:127], v[240:243], v[168:171], v[124:127]\n\t"
    "ds_read_b64_tr_b16 v[222:223], v248 offset:12288\n\t"
    "v_cvt_pk_bf16_f32 v117, v146, v147\n\t"
    "v_exp_f32 v150, v150\n\t"
    "v_mfma_f32_16x16x32_bf16 v[124:127], v[244:247], v[176:179], v[124:127]\n\t"
    "ds_read_b64_tr_b16 v[224:225], v249 offset:0\n\t"
    "v_exp_f32 v151, v151\n\t"
    "v_cvt_pk_bf16_f32 v118, v148, v149\n\t"
    "v_mfma_f32_16x16x32_bf16 v[132:135], v[244:247], v[180:183], v[188:191]\n\t"
    "ds_read_b64_tr_b16 v[226:227], v249 offset:4096\n\t"
    "v_or3_b32 v251, v251, v116, v117\n\t"
    "v_cvt_pk_bf16_f32 v119, v150, v151\n\t"
    "v_or3_b32 v251, v251, v118, v119\n\t"
    "v_mfma_f32_16x16x32_bf16 v[132:135], v[240:243], v[172:175], v[132:135]\n\t"
    "ds_read_b64_tr_b16 v[228:229], v249 offset:8192\n\t"
    "v_mfma_f32_16x16x32_bf16 v[132:135], v[236:239], v[164:167], v[132:135]\n\t"
    "ds_read_b64_tr_b16 v[230:231], v249 offset:12288\n\t"
    "v_mfma_f32_16x16x32_bf16 v[132:135], v[232:235], v[156:159], v[132:135]\n\t"
    "ds_read_b64_tr_b16 v[232:233], v248 offset:512\n\t"
    "v_exp_f32 v120, v120\n\t"
    "v_exp_f32 v121, v121\n\t"
    "v_exp_f32 v122, v122\n\t"
    "v_exp_f32 v123, v123\n\t"
    "v_cvt_pk_bf16_f32 v120, v120, v121\n\t"
    "v_exp_f32 v124, v124\n\t"
    "v_exp_f32 v125, v125\n\t"
    "v_cvt_pk_bf16_f32 v121, v122, v123\n\t"
    "v_exp_f32 v126, v126\n\t"
    "v_exp_f32 v127, v127\n\t"
    "v_cvt_pk_bf16_f32 v122, v124, v125\n\t"
    "v_or3_b32 v251, v251, v120, v121\n\t"
    "v_exp_f32 v128, v128\n\t"
    "v_exp_f32 v129, v129\n\t"
    "v_cvt_pk_bf16_f32 v123, v126, v127\n\t"
    "v_exp_f32 v130, v130\n\t"
    "v_exp_f32 v131, v131\n\t"
    "v_cvt_pk_bf16_f32 v124, v128, v129\n\t"
    "v_or3_b32 v251, v251, v122, v123\n\t"
    "v_exp_f32 v132, v132\n\t"
    "v_exp_f32 v133, v133\n\t"
    "v_cvt_pk_bf16_f32 v125, v130, v131\n\t"
    "v_exp_f32 v134, v134\n\t"
    "v_exp_f32 v135, v135\n\t"
    "v_cvt_pk_bf16_f32 v126, v132, v133\n\t"
    "v_or3_b32 v251, v251, v124, v125\n\t"
    "v_cvt_pk_bf16_f32 v127, v134, v135\n\t"
    "v_or3_b32 v251, v251, v126, v127\n\t"
    "v_and_b32 v251, 0x40004000, v251\n\t"
    "v_cmp_ne_u32 vcc, 0, v251\n\t"
    "s_nop 3\n\t"
    "s_cmp_lg_u64 vcc, 0\n\t"
    "s_cbranch_scc1 FB%=\n\t"
    "FBRET0_%=:\n\t"
    "v_and_b32 v138, 3, v253\n\t"
    "v_cmp_eq_u32 vcc, 0, v138\n\t"
    "v_mov_b32 v139, 0x3f803f80\n\t"
    "v_mov_b32 v140, 0\n\t"
    "v_cndmask_b32 v138, v140, v139, vcc\n\t"
    "v_mov_b32 v139, v138\n\t"
    "v_mov_b32 v140, v138\n\t"
    "v_mov_b32 v141, v138\n\t"
    "s_waitcnt lgkmcnt(5)\n\t"
    "v_mfma_f32_16x16x32_bf16 v[152:155], v[216:219], v[0:3], 0\n\t"
    "ds_read_b64_tr_b16 v[234:235], v248 offset:4608\n\t"
    "ds_read_b64_tr_b16 v[236:237], v248 offset:8704\n\t"
    "ds_read_b64_tr_b16 v[238:239], v248 offset:12800\n\t"
    "v_mfma_f32_16x16x32_bf16 v[152:155], v[220:223], v[8:11], v[152:155]\n\t"
    "ds_read_b64_tr_b16 v[240:241], v249 offset:512\n\t"
    "ds_read_b64_tr_b16 v[242:243], v249 offset:4608\n\t"
    "v_mfma_f32_16x16x32_bf16 v[160:163], v[220:223], v[12:15], 0\n\t"
    "ds_read_b64_tr_b16 v[244:245], v249 offset:8704\n\t"
    "ds_read_b64_tr_b16 v[246:247], v249 offset:12800\n\t"
    "v_mfma_f32_16x16x32_bf16 v[160:163], v[216:219], v[4:7], v[160:163]\n\t"
    "v_mfma_f32_16x16x32_bf16 v[144:147], v[138:141], v[0:3], 0\n\t"
    "ds_read_b64_tr_b16 v[216:217], v248 offset:1024\n\t"
    "s_waitcnt lgkmcnt(9)\n\t"
    "v_mfma_f32_16x16x32_bf16 v[156:159], v[224:227], v[0:3], 0\n\t"
    "ds_read_b64_tr_b16 v[218:219], v248 offset:5120\n\t"
    "v_mfma_f32_16x16x32_bf16 v[156:159], v[228:231], v[8:11], v[156:159]\n\t"
    "ds_read_b64_tr_b16 v[220:221], v248 offset:9216\n\t"
    "v_mfma_f32_16x16x32_bf16 v[164:167], v[228:231], v[12:15], 0\n\t"
    "ds_read_b64_tr_b16 v[222:223], v248 offset:13312\n\t"
    "v_mfma_f32_16x16x32_bf16 v[164:167], v[224:227], v[4:7], v[164:167]\n\t"
    "v_lshlrev_b32 v142, 4, v253\n\t"
    "s_lshl_b32 s52, s50, 12\n\t"
    "v_mfma_f32_16x16x32_bf16 v[148:151], v[138:141], v[4:7], 0\n\t"
    "ds_read_b64_tr_b16 v[224:225], v249 offset:1024\n\t"
    "v_add_u32 v142, s52, v142\n\t"
    "s_waitcnt lgkmcnt(9)\n\t"
    "v_mfma_f32_16x16x32_bf16 v[168:171], v[232:235], v[0:3], 0\n\t"
    "ds_read_b64_tr_b16 v[226:227], v249 offset:5120\n\t"
    "v_or_b32 v143, s61, v142\n\t"
    "v_mfma_f32_16x16x32_bf16 v[168:171], v[236:239], v[8:11], v[168:171]\n\t"
    "ds_read_b64_tr_b16 v[228:229], v249 offset:9216\n\t"
    "v_bfe_u32 v254, v253, 2, 3\n\t"
    "v_mfma_f32_16x16x32_bf16 v[176:179], v[236:239], v[12:15], 0\n\t"
    "ds_read_b64_tr_b16 v[230:231], v249 offset:13312\n\t"
    "v_lshlrev_b32 v128, 8, v254\n\t"
    "v_mfma_f32_16x16x32_bf16 v[176:179], v[232:235], v[4:7], v[176:179]\n\t"
    "ds_read_b64_tr_b16 v[232:233], v248 offset:1536\n\t"
    "v_bfe_u32 v254, v253, 5, 1\n\t"
    "s_waitcnt lgkmcnt(9)\n\t"
    "v_mfma_f32_16x16x32_bf16 v[172:175], v[240:243], v[0:3], 0\n\t"
    "ds_read_b64_tr_b16 v[234:235], v248 offset:5632\n\t"
    "v_lshl_add_u32 v128, v254, 6, v128\n\t"
    "v_mfma_f32_16x16x32_bf16 v[172:175], v[244:247], v[8:11], v[172:175]\n\t"
    "ds_read_b64_tr_b16 v[236:237], v248 offset:9728\n\t"
    "v_bfe_u32 v254, v253, 0, 2\n\t"
    "v_mfma_f32_16x16x32_bf16 v[180:183], v[244:247], v[12:15], 0\n\t"
    "ds_read_b64_tr_b16 v[238:239], v248 offset:13824\n\t"
    "v_lshl_add_u32 v128, v254, 4, v128\n\t"
    "v_mfma_f32_16x16x32_bf16 v[180:183], v[240:243], v[4:7], v[180:183]\n\t"
    "s_lshl_b32 s52, s50, 12\n\t"
    "s_add_u32 s52, s52, 0x400\n\t"
    "v_mfma_f32_16x16x32_bf16 v[144:147], v[138:141], v[8:11], v[144:147]\n\t"
    "ds_read_b64_tr_b16 v[240:241], v249 offset:1536\n\t"
    "v_add_u32 v128, s52, v128\n\t"
    "s_waitcnt lgkmcnt(9)\n\t"
    "v_mfma_f32_16x16x32_bf16 v[184:187], v[216:219], v[0:3], 0\n\t"
    "ds_read_b64_tr_b16 v[242:243], v249 offset:5632\n\t"
    "v_add_u32 v129, 0xfffffc80, v128\n\t"
    "v_mfma_f32_16x16x32_bf16 v[184:187], v[220:223], v[8:11], v[184:187]\n\t"
    "ds_read_b64_tr_b16 v[244:245], v249 offset:9728\n\t"
    "v_xor_b32 v130, 16, v128\n\t"
    "v_mfma_f32_16x16x32_bf16 v[192:195], v[220:223], v[12:15], 0\n\t"
    "ds_read_b64_tr_b16 v[246:247], v249 offset:13824\n\t"
    "v_add_u32 v131, 0xfffffc80, v130\n\t"
    "v_mfma_f32_16x16x32_bf16 v[192:195], v[216:219], v[4:7], v[192:195]\n\t"
    "v_mov_b32 v132, v128\n\t"
    "v_bfe_u32 v254, v253, 4, 1\n\t"
    "s_waitcnt lgkmcnt(8)\n\t"
    "v_mfma_f32_16x16x32_bf16 v[188:191], v[224:227], v[0:3], 0\n\t"
    "v_lshlrev_b32 v254, 5, v254\n\t"
    "v_xor_b32 v132, v132, v254\n\t"
    "v_mfma_f32_16x16x32_bf16 v[188:191], v[228:231], v[8:11], v[188:191]\n\t"
    "v_add_u32 v133, 0xfffffc80, v132\n\t"
    "v_bfe_u32 v254, v253, 2, 3\n\t"
    "v_mfma_f32_16x16x32_bf16 v[196:199], v[228:231], v[12:15], 0\n\t"
    "v_lshlrev_b32 v134, 10, v254\n\t"
    "v_bfe_u32 v254, v253, 5, 1\n\t"
    "v_mfma_f32_16x16x32_bf16 v[196:199], v[224:227], v[4:7], v[196:199]\n\t"
    "v_lshl_add_u32 v134, v254, 6, v134\n\t"
    "v_bfe_u32 v254, v253, 0, 2\n\t"
    "v_mfma_f32_16x16x32_bf16 v[148:151], v[138:141], v[12:15], v[148:151]\n\t"
    "s_waitcnt lgkmcnt(0)\n\t"
    "s_barrier\n\t"
    "v_add_u32 v248, 0xffff0000, v248\n\t"
    "v_add_u32 v249, 0xffff0000, v249\n\t"
    "ds_read_b64_tr_b16 v[216:217], v248 offset:0\n\t"
    "v_mfma_f32_16x16x32_bf16 v[200:203], v[232:235], v[0:3], 0\n\t"
    "ds_read_b64_tr_b16 v[218:219], v248 offset:4096\n\t"
    "ds_read_b64_tr_b16 v[220:221], v248 offset:8192\n\t"
    "ds_read_b64_tr_b16 v[222:223], v248 offset:12288\n\t"
    "v_mfma_f32_16x16x32_bf16 v[200:203], v[236:239], v[8:11], v[200:203]\n\t"
    "ds_read_b64_tr_b16 v[224:225], v249 offset:0\n\t"
    "ds_read_b64_tr_b16 v[226:227], v249 offset:4096\n\t"
    "v_mfma_f32_16x16x32_bf16 v[208:211], v[236:239], v[12:15], 0\n\t"
    "ds_read_b64_tr_b16 v[228:229], v249 offset:8192\n\t"
    "ds_read_b64_tr_b16 v[230:231], v249 offset:12288\n\t"
    "v_mfma_f32_16x16x32_bf16 v[208:211], v[232:235], v[4:7], v[208:211]\n\t"
    "v_lshl_add_u32 v134, v254, 4, v134\n\t"
    "v_bfe_u32 v254, v253, 4, 1\n\t"
    "v_mfma_f32_16x16x32_bf16 v[144:147], v[138:141], v[16:19], v[144:147]\n\t"
    "ds_read_b64_tr_b16 v[232:233], v248 offset:512\n\t"
    "v_lshlrev_b32 v254, 5, v254\n\t"
    "v_mfma_f32_16x16x32_bf16 v[204:207], v[240:243], v[0:3], 0\n\t"
    "ds_read_b64_tr_b16 v[234:235], v248 offset:4608\n\t"
    "v_xor_b32 v134, v134, v254\n\t"
    "v_mfma_f32_16x16x32_bf16 v[204:207], v[244:247], v[8:11], v[204:207]\n\t"
    "ds_read_b64_tr_b16 v[236:237], v248 offset:8704\n\t"
    "v_xor_b32 v134, %[octo], v134\n\t"
    "v_mfma_f32_16x16x32_bf16 v[212:215], v[244:247], v[12:15], 0\n\t"
    "ds_read_b64_tr_b16 v[238:239], v248 offset:12800\n\t"
    "s_lshl_b32 s52, s50, 14\n\t"
    "v_mfma_f32_16x16x32_bf16 v[212:215], v[240:243], v[4:7], v[212:215]\n\t"
    "ds_read_b64_tr_b16 v[240:241], v249 offset:512\n\t"
    "v_add_u32 v134, s52, v134\n\t"
    "s_waitcnt lgkmcnt(9)\n\t"
    "v_mfma_f32_16x16x32_bf16 v[152:155], v[216:219], v[16:19], v[152:155]\n\t"
    "ds_read_b64_tr_b16 v[242:243], v249 offset:4608\n\t"
    "v_xor_b32 v135, 0x80, v134\n\t"
    "v_mfma_f32_16x16x32_bf16 v[152:155], v[220:223], v[24:27], v[152:155]\n\t"
    "ds_read_b64_tr_b16 v[244:245], v249 offset:8704\n\t"
    "v_add_u32 v136, 0x1c00, v134\n\t"
    "v_mfma_f32_16x16x32_bf16 v[160:163], v[220:223], v[28:31], v[160:163]\n\t"
    "ds_read_b64_tr_b16 v[246:247], v249 offset:12800\n\t"
    "v_add_u32 v137, 0x1800, v135\n\t"
    "v_mfma_f32_16x16x32_bf16 v[160:163], v[216:219], v[20:23], v[160:163]\n\t"
    "v_add_u32 v134, 0x400, v134\n\t"
    "v_mfma_f32_16x16x32_bf16 v[148:151], v[138:141], v[20:23], v[148:151]\n\t"
    "ds_read_b64_tr_b16 v[216:217], v248 offset:1024\n\t"
    "s_waitcnt lgkmcnt(9)\n\t"
    "v_mfma_f32_16x16x32_bf16 v[156:159], v[224:227], v[16:19], v[156:159]\n\t"
    "ds_read_b64_tr_b16 v[218:219], v248 offset:5120\n\t"
    "v_mfma_f32_16x16x32_bf16 v[156:159], v[228:231], v[24:27], v[156:159]\n\t"
    "ds_read_b64_tr_b16 v[220:221], v248 offset:9216\n\t"
    "v_mfma_f32_16x16x32_bf16 v[164:167], v[228:231], v[28:31], v[164:167]\n\t"
    "ds_read_b64_tr_b16 v[222:223], v248 offset:13312\n\t"
    "v_mfma_f32_16x16x32_bf16 v[164:167], v[224:227], v[20:23], v[164:167]\n\t"
    "ds_read_b64_tr_b16 v[224:225], v249 offset:1024\n\t"
    "s_waitcnt lgkmcnt(9)\n\t"
    "v_mfma_f32_16x16x32_bf16 v[168:171], v[232:235], v[16:19], v[168:171]\n\t"
    "ds_read_b64_tr_b16 v[226:227], v249 offset:5120\n\t"
    "v_mfma_f32_16x16x32_bf16 v[168:171], v[236:239], v[24:27], v[168:171]\n\t"
    "ds_read_b64_tr_b16 v[228:229], v249 offset:9216\n\t"
    "v_mfma_f32_16x16x32_bf16 v[176:179], v[236:239], v[28:31], v[176:179]\n\t"
    "ds_read_b64_tr_b16 v[230:231], v249 offset:13312\n\t"
    "v_mfma_f32_16x16x32_bf16 v[176:179], v[232:235], v[20:23], v[176:179]\n\t"
    "v_mfma_f32_16x16x32_bf16 v[144:147], v[138:141], v[24:27], v[144:147]\n\t"
    "ds_read_b64_tr_b16 v[232:233], v248 offset:1536\n\t"
    "s_waitcnt lgkmcnt(9)\n\t"
    "v_mfma_f32_16x16x32_bf16 v[172:175], v[240:243], v[16:19], v[172:175]\n\t"
    "ds_read_b64_tr_b16 v[234:235], v248 offset:5632\n\t"
    "v_mfma_f32_16x16x32_bf16 v[172:175], v[244:247], v[24:27], v[172:175]\n\t"
    "ds_read_b64_tr_b16 v[236:237], v248 offset:9728\n\t"
    "v_mfma_f32_16x16x32_bf16 v[180:183], v[244:247], v[28:31], v[180:183]\n\t"
    "ds_read_b64_tr_b16 v[238:239], v248 offset:13824\n\t"
    "v_mfma_f32_16x16x32_bf16 v[180:183], v[240:243], v[20:23], v[180:183]\n\t"
    "v_mfma_f32_16x16x32_bf16 v[148:151], v[138:141], v[28:31], v[148:151]\n\t"
    "ds_read_b64_tr_b16 v[240:241], v249 offset:1536\n\t"
    "s_waitcnt lgkmcnt(9)\n\t"
    "v_mfma_f32_16x16x32_bf16 v[184:187], v[216:219], v[16:19], v[184:187]\n\t"
    "ds_read_b64_tr_b16 v[242:243], v249 offset:5632\n\t"
    "v_mfma_f32_16x16x32_bf16 v[184:187], v[220:223], v[24:27], v[184:187]\n\t"
    "ds_read_b64_tr_b16 v[244:245], v249 offset:9728\n\t"
    "v_mfma_f32_16x16x32_bf16 v[192:195], v[220:223], v[28:31], v[192:195]\n\t"
    "ds_read_b64_tr_b16 v[246:247], v249 offset:13824\n\t"
    "v_mfma_f32_16x16x32_bf16 v[192:195], v[216:219], v[20:23], v[192:195]\n\t"
    "s_waitcnt lgkmcnt(8)\n\t"
    "v_mfma_f32_16x16x32_bf16 v[188:191], v[224:227], v[16:19], v[188:191]\n\t"
    "v_mfma_f32_16x16x32_bf16 v[188:191], v[228:231], v[24:27], v[188:191]\n\t"
    "v_mfma_f32_16x16x32_bf16 v[196:199], v[228:231], v[28:31], v[196:199]\n\t"
    "v_mfma_f32_16x16x32_bf16 v[196:199], v[224:227], v[20:23], v[196:199]\n\t"
    "v_mfma_f32_16x16x32_bf16 v[144:147], v[138:141], v[32:35], v[144:147]\n\t"
    "s_waitcnt lgkmcnt(0)\n\t"
    "s_barrier\n\t"
    "ds_read_b64_tr_b16 v[216:217], v248 offset:16384\n\t"
    "ds_read_b64_tr_b16 v[218:219], v248 offset:20480\n\t"
    "v_mfma_f32_16x16x32_bf16 v[200:203], v[232:235], v[16:19], v[200:203]\n\t"
    "ds_read_b64_tr_b16 v[220:221], v248 offset:24576\n\t"
    "ds_read_b64_tr_b16 v[222:223], v248 offset:28672\n\t"
    "v_mfma_f32_16x16x32_bf16 v[200:203], v[236:239], v[24:27], v[200:203]\n\t"
    "ds_read_b64_tr_b16 v[224:225], v249 offset:16384\n\t"
    "ds_read_b64_tr_b16 v[226:227], v249 offset:20480\n\t"
    "v_mfma_f32_16x16x32_bf16 v[208:211], v[236:239], v[28:31], v[208:211]\n\t"
    "ds_read_b64_tr_b16 v[228:229], v249 offset:24576\n\t"
    "ds_read_b64_tr_b16 v[230:231], v249 offset:28672\n\t"
    "v_mfma_f32_16x16x32_bf16 v[208:211], v[232:235], v[20:23], v[208:211]\n\t"
    "ds_read_b64_tr_b16 v[232:233], v248 offset:16896\n\t"
    "v_mfma_f32_16x16x32_bf16 v[204:207], v[240:243], v[16:19], v[204:207]\n\t"
    "ds_read_b64_tr_b16 v[234:235], v248 offset:20992\n\t"
    "v_mfma_f32_16x16x32_bf16 v[204:207], v[244:247], v[24:27], v[204:207]\n\t"
    "ds_read_b64_tr_b16 v[236:237], v248 offset:25088\n\t"
    "v_mfma_f32_16x16x32_bf16 v[212:215], v[244:247], v[28:31], v[212:215]\n\t"
    "ds_read_b64_tr_b16 v[238:239], v248 offset:29184\n\t"
    "v_mfma_f32_16x16x32_bf16 v[212:215], v[240:243], v[20:23], v[212:215]\n\t"
    "v_mfma_f32_16x16x32_bf16 v[148:151], v[138:141], v[36:39], v[148:151]\n\t"
    "ds_read_b64_tr_b16 v[240:241], v249 offset:16896\n\t"
    "s_waitcnt lgkmcnt(9)\n\t"
    "v_mfma_f32_16x16x32_bf16 v[152:155], v[216:219], v[32:35], v[152:155]\n\t"
    "ds_read_b64_tr_b16 v[242:243], v249 offset:20992\n\t"
    "v_mfma_f32_16x16x32_bf16 v[152:155], v[220:223], v[40:43], v[152:155]\n\t"
    "ds_read_b64_tr_b16 v[244:245], v249 offset:25088\n\t"
    "v_mfma_f32_16x16x32_bf16 v[160:163], v[220:223], v[44:47], v[160:163]\n\t"
    "ds_read_b64_tr_b16 v[246:247], v249 offset:29184\n\t"
    "v_mfma_f32_16x16x32_bf16 v[160:163], v[216:219], v[36:39], v[160:163]\n\t"
    "v_mfma_f32_16x16x32_bf16 v[144:147], v[138:141], v[40:43], v[144:147]\n\t"
    "ds_read_b64_tr_b16 v[216:217], v248 offset:17408\n\t"
    "s_waitcnt lgkmcnt(9)\n\t"
    "v_mfma_f32_16x16x32_bf16 v[156:159], v[224:227], v[32:35], v[156:159]\n\t"
    "ds_read_b64_tr_b16 v[218:219], v248 offset:21504\n\t"
    "v_mfma_f32_16x16x32_bf16 v[156:159], v[228:231], v[40:43], v[156:159]\n\t"
    "ds_read_b64_tr_b16 v[220:221], v248 offset:25600\n\t"
    "v_mfma_f32_16x16x32_bf16 v[164:167], v[228:231], v[44:47], v[164:167]\n\t"
    "ds_read_b64_tr_b16 v[222:223], v248 offset:29696\n\t"
    "v_mfma_f32_16x16x32_bf16 v[164:167], v[224:227], v[36:39], v[164:167]\n\t"
    "ds_read_b64_tr_b16 v[224:225], v249 offset:17408\n\t"
    "s_waitcnt lgkmcnt(9)\n\t"
    "v_mfma_f32_16x16x32_bf16 v[168:171], v[232:235], v[32:35], v[168:171]\n\t"
    "ds_read_b64_tr_b16 v[226:227], v249 offset:21504\n\t"
    "v_mfma_f32_16x16x32_bf16 v[168:171], v[236:239], v[40:43], v[168:171]\n\t"
    "ds_read_b64_tr_b16 v[228:229], v249 offset:25600\n\t"
    "v_mfma_f32_16x16x32_bf16 v[176:179], v[236:239], v[44:47], v[176:179]\n\t"
    "ds_read_b64_tr_b16 v[230:231], v249 offset:29696\n\t"
    "v_mfma_f32_16x16x32_bf16 v[176:179], v[232:235], v[36:39], v[176:179]\n\t"
    "v_mfma_f32_16x16x32_bf16 v[148:151], v[138:141], v[44:47], v[148:151]\n\t"
    "ds_read_b64_tr_b16 v[232:233], v248 offset:17920\n\t"
    "s_waitcnt lgkmcnt(9)\n\t"
    "v_mfma_f32_16x16x32_bf16 v[172:175], v[240:243], v[32:35], v[172:175]\n\t"
    "ds_read_b64_tr_b16 v[234:235], v248 offset:22016\n\t"
    "v_mfma_f32_16x16x32_bf16 v[172:175], v[244:247], v[40:43], v[172:175]\n\t"
    "ds_read_b64_tr_b16 v[236:237], v248 offset:26112\n\t"
    "v_mfma_f32_16x16x32_bf16 v[180:183], v[244:247], v[44:47], v[180:183]\n\t"
    "ds_read_b64_tr_b16 v[238:239], v248 offset:30208\n\t"
    "v_mfma_f32_16x16x32_bf16 v[180:183], v[240:243], v[36:39], v[180:183]\n\t"
    "ds_read_b64_tr_b16 v[240:241], v249 offset:17920\n\t"
    "s_waitcnt lgkmcnt(9)\n\t"
    "v_mfma_f32_16x16x32_bf16 v[184:187], v[216:219], v[32:35], v[184:187]\n\t"
    "ds_read_b64_tr_b16 v[242:243], v249 offset:22016\n\t"
    "v_mfma_f32_16x16x32_bf16 v[184:187], v[220:223], v[40:43], v[184:187]\n\t"
    "ds_read_b64_tr_b16 v[244:245], v249 offset:26112\n\t"
    "v_mfma_f32_16x16x32_bf16 v[192:195], v[220:223], v[44:47], v[192:195]\n\t"
    "ds_read_b64_tr_b16 v[246:247], v249 offset:30208\n\t"
    "v_mfma_f32_16x16x32_bf16 v[192:195], v[216:219], v[36:39], v[192:195]\n\t"
    "v_mfma_f32_16x16x32_bf16 v[144:147], v[138:141], v[48:51], v[144:147]\n\t"
    "s_waitcnt lgkmcnt(8)\n\t"
    "v_mfma_f32_16x16x32_bf16 v[188:191], v[224:227], v[32:35], v[188:191]\n\t"
    "v_mfma_f32_16x16x32_bf16 v[188:191], v[228:231], v[40:43], v[188:191]\n\t"
    "v_mfma_f32_16x16x32_bf16 v[196:199], v[228:231], v[44:47], v[196:199]\n\t"
    "v_mfma_f32_16x16x32_bf16 v[196:199], v[224:227], v[36:39], v[196:199]\n\t"
    "v_mfma_f32_16x16x32_bf16 v[148:151], v[138:141], v[52:55], v[148:151]\n\t"
    "s_waitcnt lgkmcnt(0)\n\t"
    "s_barrier\n\t"
    "v_add_u32 v248, 0x10000, v248\n\t"
    "v_add_u32 v249, 0x10000, v249\n\t"
    "ds_read_b64_tr_b16 v[216:217], v248 offset:16384\n\t"
    "v_mfma_f32_16x16x32_bf16 v[200:203], v[232:235], v[32:35], v[200:203]\n\t"
    "ds_read_b64_tr_b16 v[218:219], v248 offset:20480\n\t"
    "ds_read_b64_tr_b16 v[220:221], v248 offset:24576\n\t"
    "ds_read_b64_tr_b16 v[222:223], v248 offset:28672\n\t"
    "v_mfma_f32_16x16x32_bf16 v[200:203], v[236:239], v[40:43], v[200:203]\n\t"
    "ds_read_b64_tr_b16 v[224:225], v249 offset:16384\n\t"
    "ds_read_b64_tr_b16 v[226:227], v249 offset:20480\n\t"
    "v_mfma_f32_16x16x32_bf16 v[208:211], v[236:239], v[44:47], v[208:211]\n\t"
    "ds_read_b64_tr_b16 v[228:229], v249 offset:24576\n\t"
    "ds_read_b64_tr_b16 v[230:231], v249 offset:28672\n\t"
    "v_mfma_f32_16x16x32_bf16 v[208:211], v[232:235], v[36:39], v[208:211]\n\t"
    "ds_read_b64_tr_b16 v[232:233], v248 offset:16896\n\t"
    "v_mfma_f32_16x16x32_bf16 v[204:207], v[240:243], v[32:35], v[204:207]\n\t"
    "ds_read_b64_tr_b16 v[234:235], v248 offset:20992\n\t"
    "v_mfma_f32_16x16x32_bf16 v[204:207], v[244:247], v[40:43], v[204:207]\n\t"
    "ds_read_b64_tr_b16 v[236:237], v248 offset:25088\n\t"
    "v_mfma_f32_16x16x32_bf16 v[212:215], v[244:247], v[44:47], v[212:215]\n\t"
    "ds_read_b64_tr_b16 v[238:239], v248 offset:29184\n\t"
    "v_mfma_f32_16x16x32_bf16 v[212:215], v[240:243], v[36:39], v[212:215]\n\t"
    "v_mfma_f32_16x16x32_bf16 v[144:147], v[138:141], v[56:59], v[144:147]\n\t"
    "ds_read_b64_tr_b16 v[240:241], v249 offset:16896\n\t"
    "s_waitcnt lgkmcnt(9)\n\t"
    "v_mfma_f32_16x16x32_bf16 v[152:155], v[216:219], v[48:51], v[152:155]\n\t"
    "ds_read_b64_tr_b16 v[242:243], v249 offset:20992\n\t"
    "v_mfma_f32_16x16x32_bf16 v[152:155], v[220:223], v[56:59], v[152:155]\n\t"
    "ds_read_b64_tr_b16 v[244:245], v249 offset:25088\n\t"
    "v_mfma_f32_16x16x32_bf16 v[160:163], v[220:223], v[60:63], v[160:163]\n\t"
    "ds_read_b64_tr_b16 v[246:247], v249 offset:29184\n\t"
    "v_mfma_f32_16x16x32_bf16 v[160:163], v[216:219], v[52:55], v[160:163]\n\t"
    "ds_read_b64_tr_b16 v[216:217], v248 offset:17408\n\t"
    "s_waitcnt lgkmcnt(9)\n\t"
    "v_mfma_f32_16x16x32_bf16 v[156:159], v[224:227], v[48:51], v[156:159]\n\t"
    "ds_read_b64_tr_b16 v[218:219], v248 offset:21504\n\t"
    "v_mfma_f32_16x16x32_bf16 v[156:159], v[228:231], v[56:59], v[156:159]\n\t"
    "ds_read_b64_tr_b16 v[220:221], v248 offset:25600\n\t"
    "v_mfma_f32_16x16x32_bf16 v[164:167], v[228:231], v[60:63], v[164:167]\n\t"
    "ds_read_b64_tr_b16 v[222:223], v248 offset:29696\n\t"
    "v_mfma_f32_16x16x32_bf16 v[164:167], v[224:227], v[52:55], v[164:167]\n\t"
    "v_mfma_f32_16x16x32_bf16 v[148:151], v[138:141], v[60:63], v[148:151]\n\t"
    "ds_read_b64_tr_b16 v[224:225], v249 offset:17408\n\t"
    "s_waitcnt lgkmcnt(9)\n\t"
    "v_mfma_f32_16x16x32_bf16 v[168:171], v[232:235], v[48:51], v[168:171]\n\t"
    "ds_read_b64_tr_b16 v[226:227], v249 offset:21504\n\t"
    "v_mfma_f32_16x16x32_bf16 v[168:171], v[236:239], v[56:59], v[168:171]\n\t"
    "ds_read_b64_tr_b16 v[228:229], v249 offset:25600\n\t"
    "v_mfma_f32_16x16x32_bf16 v[176:179], v[236:239], v[60:63], v[176:179]\n\t"
    "ds_read_b64_tr_b16 v[230:231], v249 offset:29696\n\t"
    "v_mfma_f32_16x16x32_bf16 v[176:179], v[232:235], v[52:55], v[176:179]\n\t"
    "ds_read_b64_tr_b16 v[232:233], v248 offset:17920\n\t"
    "s_waitcnt lgkmcnt(9)\n\t"
    "v_mfma_f32_16x16x32_bf16 v[172:175], v[240:243], v[48:51], v[172:175]\n\t"
    "ds_read_b64_tr_b16 v[234:235], v248 offset:22016\n\t"
    "v_mfma_f32_16x16x32_bf16 v[172:175], v[244:247], v[56:59], v[172:175]\n\t"
    "ds_read_b64_tr_b16 v[236:237], v248 offset:26112\n\t"
    "v_mfma_f32_16x16x32_bf16 v[180:183], v[244:247], v[60:63], v[180:183]\n\t"
    "ds_read_b64_tr_b16 v[238:239], v248 offset:30208\n\t"
    "v_mfma_f32_16x16x32_bf16 v[180:183], v[240:243], v[52:55], v[180:183]\n\t"
    "v_mfma_f32_16x16x32_bf16 v[144:147], v[138:141], v[64:67], v[144:147]\n\t"
    "ds_read_b64_tr_b16 v[240:241], v249 offset:17920\n\t"
    "s_waitcnt lgkmcnt(9)\n\t"
    "v_mfma_f32_16x16x32_bf16 v[184:187], v[216:219], v[48:51], v[184:187]\n\t"
    "ds_read_b64_tr_b16 v[242:243], v249 offset:22016\n\t"
    "v_mfma_f32_16x16x32_bf16 v[184:187], v[220:223], v[56:59], v[184:187]\n\t"
    "ds_read_b64_tr_b16 v[244:245], v249 offset:26112\n\t"
    "v_mfma_f32_16x16x32_bf16 v[192:195], v[220:223], v[60:63], v[192:195]\n\t"
    "ds_read_b64_tr_b16 v[246:247], v249 offset:30208\n\t"
    "v_mfma_f32_16x16x32_bf16 v[192:195], v[216:219], v[52:55], v[192:195]\n\t"
    "v_mfma_f32_16x16x32_bf16 v[148:151], v[138:141], v[68:71], v[148:151]\n\t"
    "s_waitcnt lgkmcnt(8)\n\t"
    "v_mfma_f32_16x16x32_bf16 v[188:191], v[224:227], v[48:51], v[188:191]\n\t"
    "v_mfma_f32_16x16x32_bf16 v[188:191], v[228:231], v[56:59], v[188:191]\n\t"
    "v_mfma_f32_16x16x32_bf16 v[196:199], v[228:231], v[60:63], v[196:199]\n\t"
    "v_mfma_f32_16x16x32_bf16 v[196:199], v[224:227], v[52:55], v[196:199]\n\t"
    "s_waitcnt lgkmcnt(0)\n\t"
    "s_barrier\n\t"
    "ds_read_b64_tr_b16 v[216:217], v248 offset:32768\n\t"
    "ds_read_b64_tr_b16 v[218:219], v248 offset:36864\n\t"
    "v_mfma_f32_16x16x32_bf16 v[200:203], v[232:235], v[48:51], v[200:203]\n\t"
    "ds_read_b64_tr_b16 v[220:221], v248 offset:40960\n\t"
    "ds_read_b64_tr_b16 v[222:223], v248 offset:45056\n\t"
    "v_mfma_f32_16x16x32_bf16 v[200:203], v[236:239], v[56:59], v[200:203]\n\t"
    "ds_read_b64_tr_b16 v[224:225], v249 offset:32768\n\t"
    "ds_read_b64_tr_b16 v[226:227], v249 offset:36864\n\t"
    "v_mfma_f32_16x16x32_bf16 v[208:211], v[236:239], v[60:63], v[208:211]\n\t"
    "ds_read_b64_tr_b16 v[228:229], v249 offset:40960\n\t"
    "ds_read_b64_tr_b16 v[230:231], v249 offset:45056\n\t"
    "v_mfma_f32_16x16x32_bf16 v[208:211], v[232:235], v[52:55], v[208:211]\n\t"
    "v_mfma_f32_16x16x32_bf16 v[144:147], v[138:141], v[72:75], v[144:147]\n\t"
    "ds_read_b64_tr_b16 v[232:233], v248 offset:33280\n\t"
    "v_mfma_f32_16x16x32_bf16 v[204:207], v[240:243], v[48:51], v[204:207]\n\t"
    "ds_read_b64_tr_b16 v[234:235], v248 offset:37376\n\t"
    "v_mfma_f32_16x16x32_bf16 v[204:207], v[244:247], v[56:59], v[204:207]\n\t"
    "ds_read_b64_tr_b16 v[236:237], v248 offset:41472\n\t"
    "v_mfma_f32_16x16x32_bf16 v[212:215], v[244:247], v[60:63], v[212:215]\n\t"
    "ds_read_b64_tr_b16 v[238:239], v248 offset:45568\n\t"
    "v_mfma_f32_16x16x32_bf16 v[212:215], v[240:243], v[52:55], v[212:215]\n\t"
    "ds_read_b64_tr_b16 v[240:241], v249 offset:33280\n\t"
    "s_waitcnt lgkmcnt(9)\n\t"
    "v_mfma_f32_16x16x32_bf16 v[152:155], v[216:219], v[64:67], v[152:155]\n\t"
    "ds_read_b64_tr_b16 v[242:243], v249 offset:37376\n\t"
    "v_mfma_f32_16x16x32_bf16 v[152:155], v[220:223], v[72:75], v[152:155]\n\t"
    "ds_read_b64_tr_b16 v[244:245], v249 offset:41472\n\t"
    "v_mfma_f32_16x16x32_bf16 v[160:163], v[220:223], v[76:79], v[160:163]\n\t"
    "ds_read_b64_tr_b16 v[246:247], v249 offset:45568\n\t"
    "v_mfma_f32_16x16x32_bf16 v[160:163], v[216:219], v[68:71], v[160:163]\n\t"
    "v_mfma_f32_16x16x32_bf16 v[148:151], v[138:141], v[76:79], v[148:151]\n\t"
    "ds_read_b64_tr_b16 v[216:217], v248 offset:33792\n\t"
    "s_waitcnt lgkmcnt(9)\n\t"
    "v_mfma_f32_16x16x32_bf16 v[156:159], v[224:227], v[64:67], v[156:159]\n\t"
    "ds_read_b64_tr_b16 v[218:219], v248 offset:37888\n\t"
    "v_mfma_f32_16x16x32_bf16 v[156:159], v[228:231], v[72:75], v[156:159]\n\t"
    "ds_read_b64_tr_b16 v[220:221], v248 offset:41984\n\t"
    "v_mfma_f32_16x16x32_bf16 v[164:167], v[228:231], v[76:79], v[164:167]\n\t"
    "ds_read_b64_tr_b16 v[222:223], v248 offset:46080\n\t"
    "v_mfma_f32_16x16x32_bf16 v[164:167], v[224:227], v[68:71], v[164:167]\n\t"
    "v_mfma_f32_16x16x32_bf16 v[144:147], v[138:141], v[80:83], v[144:147]\n\t"
    "ds_read_b64_tr_b16 v[224:225], v249 offset:33792\n\t"
    "s_waitcnt lgkmcnt(9)\n\t"
    "v_mfma_f32_16x16x32_bf16 v[168:171], v[232:235], v[64:67], v[168:171]\n\t"
    "ds_read_b64_tr_b16 v[226:227], v249 offset:37888\n\t"
    "v_mfma_f32_16x16x32_bf16 v[168:171], v[236:239], v[72:75], v[168:171]\n\t"
    "ds_read_b64_tr_b16 v[228:229], v249 offset:41984\n\t"
    "v_mfma_f32_16x16x32_bf16 v[176:179], v[236:239], v[76:79], v[176:179]\n\t"
    "ds_read_b64_tr_b16 v[230:231], v249 offset:46080\n\t"
    "v_mfma_f32_16x16x32_bf16 v[176:179], v[232:235], v[68:71], v[176:179]\n\t"
    "ds_read_b64_tr_b16 v[232:233], v248 offset:34304\n\t"
    "s_waitcnt lgkmcnt(9)\n\t"
    "v_mfma_f32_16x16x32_bf16 v[172:175], v[240:243], v[64:67], v[172:175]\n\t"
    "ds_read_b64_tr_b16 v[234:235], v248 offset:38400\n\t"
    "v_mfma_f32_16x16x32_bf16 v[172:175], v[244:247], v[72:75], v[172:175]\n\t"
    "ds_read_b64_tr_b16 v[236:237], v248 offset:42496\n\t"
    "v_mfma_f32_16x16x32_bf16 v[180:183], v[244:247], v[76:79], v[180:183]\n\t"
    "ds_read_b64_tr_b16 v[238:239], v248 offset:46592\n\t"
    "v_mfma_f32_16x16x32_bf16 v[180:183], v[240:243], v[68:71], v[180:183]\n\t"
    "v_mfma_f32_16x16x32_bf16 v[148:151], v[138:141], v[84:87], v[148:151]\n\t"
    "ds_read_b64_tr_b16 v[240:241], v249 offset:34304\n\t"
    "s_waitcnt lgkmcnt(9)\n\t"
    "v_mfma_f32_16x16x32_bf16 v[184:187], v[216:219], v[64:67], v[184:187]\n\t"
    "ds_read_b64_tr_b16 v[242:243], v249 offset:38400\n\t"
    "v_mfma_f32_16x16x32_bf16 v[184:187], v[220:223], v[72:75], v[184:187]\n\t"
    "ds_read_b64_tr_b16 v[244:245], v249 offset:42496\n\t"
    "v_mfma_f32_16x16x32_bf16 v[192:195], v[220:223], v[76:79], v[192:195]\n\t"
    "ds_read_b64_tr_b16 v[246:247], v249 offset:46592\n\t"
    "v_mfma_f32_16x16x32_bf16 v[192:195], v[216:219], v[68:71], v[192:195]\n\t"
    "s_waitcnt lgkmcnt(8)\n\t"
    "v_mfma_f32_16x16x32_bf16 v[188:191], v[224:227], v[64:67], v[188:191]\n\t"
    "v_mfma_f32_16x16x32_bf16 v[188:191], v[228:231], v[72:75], v[188:191]\n\t"
    "v_mfma_f32_16x16x32_bf16 v[196:199], v[228:231], v[76:79], v[196:199]\n\t"
    "v_mfma_f32_16x16x32_bf16 v[196:199], v[224:227], v[68:71], v[196:199]\n\t"
    "v_mfma_f32_16x16x32_bf16 v[144:147], v[138:141], v[88:91], v[144:147]\n\t"
    "s_waitcnt lgkmcnt(0)\n\t"
    "s_barrier\n\t"
    "ds_read_b64_tr_b16 v[216:217], v248 offset:49152\n\t"
    "ds_read_b64_tr_b16 v[218:219], v248 offset:53248\n\t"
    "v_mfma_f32_16x16x32_bf16 v[200:203], v[232:235], v[64:67], v[200:203]\n\t"
    "ds_read_b64_tr_b16 v[220:221], v248 offset:57344\n\t"
    "ds_read_b64_tr_b16 v[222:223], v248 offset:61440\n\t"
    "v_mfma_f32_16x16x32_bf16 v[200:203], v[236:239], v[72:75], v[200:203]\n\t"
    "ds_read_b64_tr_b16 v[224:225], v249 offset:49152\n\t"
    "ds_read_b64_tr_b16 v[226:227], v249 offset:53248\n\t"
    "v_mfma_f32_16x16x32_bf16 v[208:211], v[236:239], v[76:79], v[208:211]\n\t"
    "ds_read_b64_tr_b16 v[228:229], v249 offset:57344\n\t"
    "ds_read_b64_tr_b16 v[230:231], v249 offset:61440\n\t"
    "v_mfma_f32_16x16x32_bf16 v[208:211], v[232:235], v[68:71], v[208:211]\n\t"
    "v_mfma_f32_16x16x32_bf16 v[148:151], v[138:141], v[92:95], v[148:151]\n\t"
    "ds_read_b64_tr_b16 v[232:233], v248 offset:49664\n\t"
    "v_mfma_f32_16x16x32_bf16 v[204:207], v[240:243], v[64:67], v[204:207]\n\t"
    "ds_read_b64_tr_b16 v[234:235], v248 offset:53760\n\t"
    "v_mfma_f32_16x16x32_bf16 v[204:207], v[244:247], v[72:75], v[204:207]\n\t"
    "ds_read_b64_tr_b16 v[236:237], v248 offset:57856\n\t"
    "v_mfma_f32_16x16x32_bf16 v[212:215], v[244:247], v[76:79], v[212:215]\n\t"
    "ds_read_b64_tr_b16 v[238:239], v248 offset:61952\n\t"
    "v_mfma_f32_16x16x32_bf16 v[212:215], v[240:243], v[68:71], v[212:215]\n\t"
    "ds_read_b64_tr_b16 v[240:241], v249 offset:49664\n\t"
    "s_waitcnt lgkmcnt(9)\n\t"
    "v_mfma_f32_16x16x32_bf16 v[152:155], v[216:219], v[80:83], v[152:155]\n\t"
    "ds_read_b64_tr_b16 v[242:243], v249 offset:53760\n\t"
    "v_mfma_f32_16x16x32_bf16 v[152:155], v[220:223], v[88:91], v[152:155]\n\t"
    "ds_read_b64_tr_b16 v[244:245], v249 offset:57856\n\t"
    "v_mfma_f32_16x16x32_bf16 v[160:163], v[220:223], v[92:95], v[160:163]\n\t"
    "ds_read_b64_tr_b16 v[246:247], v249 offset:61952\n\t"
    "v_mfma_f32_16x16x32_bf16 v[160:163], v[216:219], v[84:87], v[160:163]\n\t"
    "v_mfma_f32_16x16x32_bf16 v[144:147], v[138:141], v[96:99], v[144:147]\n\t"
    "ds_read_b64_tr_b16 v[216:217], v248 offset:50176\n\t"
    "s_waitcnt lgkmcnt(9)\n\t"
    "v_mfma_f32_16x16x32_bf16 v[156:159], v[224:227], v[80:83], v[156:159]\n\t"
    "ds_read_b64_tr_b16 v[218:219], v248 offset:54272\n\t"
    "v_mfma_f32_16x16x32_bf16 v[156:159], v[228:231], v[88:91], v[156:159]\n\t"
    "ds_read_b64_tr_b16 v[220:221], v248 offset:58368\n\t"
    "v_mfma_f32_16x16x32_bf16 v[164:167], v[228:231], v[92:95], v[164:167]\n\t"
    "ds_read_b64_tr_b16 v[222:223], v248 offset:62464\n\t"
    "v_mfma_f32_16x16x32_bf16 v[164:167], v[224:227], v[84:87], v[164:167]\n\t"
    "ds_read_b64_tr_b16 v[224:225], v249 offset:50176\n\t"
    "s_waitcnt lgkmcnt(9)\n\t"
    "v_mfma_f32_16x16x32_bf16 v[168:171], v[232:235], v[80:83], v[168:171]\n\t"
    "ds_read_b64_tr_b16 v[226:227], v249 offset:54272\n\t"
    "v_mfma_f32_16x16x32_bf16 v[168:171], v[236:239], v[88:91], v[168:171]\n\t"
    "ds_read_b64_tr_b16 v[228:229], v249 offset:58368\n\t"
    "v_mfma_f32_16x16x32_bf16 v[176:179], v[236:239], v[92:95], v[176:179]\n\t"
    "ds_read_b64_tr_b16 v[230:231], v249 offset:62464\n\t"
    "v_mfma_f32_16x16x32_bf16 v[176:179], v[232:235], v[84:87], v[176:179]\n\t"
    "v_mfma_f32_16x16x32_bf16 v[148:151], v[138:141], v[100:103], v[148:151]\n\t"
    "ds_read_b64_tr_b16 v[232:233], v248 offset:50688\n\t"
    "s_waitcnt lgkmcnt(9)\n\t"
    "v_mfma_f32_16x16x32_bf16 v[172:175], v[240:243], v[80:83], v[172:175]\n\t"
    "ds_read_b64_tr_b16 v[234:235], v248 offset:54784\n\t"
    "v_mfma_f32_16x16x32_bf16 v[172:175], v[244:247], v[88:91], v[172:175]\n\t"
    "ds_read_b64_tr_b16 v[236:237], v248 offset:58880\n\t"
    "v_mfma_f32_16x16x32_bf16 v[180:183], v[244:247], v[92:95], v[180:183]\n\t"
    "ds_read_b64_tr_b16 v[238:239], v248 offset:62976\n\t"
    "v_mfma_f32_16x16x32_bf16 v[180:183], v[240:243], v[84:87], v[180:183]\n\t"
    "v_mfma_f32_16x16x32_bf16 v[144:147], v[138:141], v[104:107], v[144:147]\n\t"
    "ds_read_b64_tr_b16 v[240:241], v249 offset:50688\n\t"
    "s_waitcnt lgkmcnt(9)\n\t"
    "v_mfma_f32_16x16x32_bf16 v[184:187], v[216:219], v[80:83], v[184:187]\n\t"
    "ds_read_b64_tr_b16 v[242:243], v249 offset:54784\n\t"
    "v_mfma_f32_16x16x32_bf16 v[184:187], v[220:223], v[88:91], v[184:187]\n\t"
    "ds_read_b64_tr_b16 v[244:245], v249 offset:58880\n\t"
    "v_mfma_f32_16x16x32_bf16 v[192:195], v[220:223], v[92:95], v[192:195]\n\t"
    "ds_read_b64_tr_b16 v[246:247], v249 offset:62976\n\t"
    "v_mfma_f32_16x16x32_bf16 v[192:195], v[216:219], v[84:87], v[192:195]\n\t"
    "s_waitcnt lgkmcnt(8)\n\t"
    "v_mfma_f32_16x16x32_bf16 v[188:191], v[224:227], v[80:83], v[188:191]\n\t"
    "v_mfma_f32_16x16x32_bf16 v[188:191], v[228:231], v[88:91], v[188:191]\n\t"
    "v_mfma_f32_16x16x32_bf16 v[196:199], v[228:231], v[92:95], v[196:199]\n\t"
    "v_mfma_f32_16x16x32_bf16 v[196:199], v[224:227], v[84:87], v[196:199]\n\t"
    "v_mfma_f32_16x16x32_bf16 v[148:151], v[138:141], v[108:111], v[148:151]\n\t"
    "s_waitcnt lgkmcnt(0)\n\t"
    "s_barrier\n\t"
    "v_add_u32 v248, 0xffff0000, v248\n\t"
    "v_add_u32 v249, 0xffff0000, v249\n\t"
    "ds_read_b64_tr_b16 v[216:217], v248 offset:32768\n\t"
    "v_mfma_f32_16x16x32_bf16 v[200:203], v[232:235], v[80:83], v[200:203]\n\t"
    "ds_read_b64_tr_b16 v[218:219], v248 offset:36864\n\t"
    "ds_read_b64_tr_b16 v[220:221], v248 offset:40960\n\t"
    "ds_read_b64_tr_b16 v[222:223], v248 offset:45056\n\t"
    "v_mfma_f32_16x16x32_bf16 v[200:203], v[236:239], v[88:91], v[200:203]\n\t"
    "ds_read_b64_tr_b16 v[224:225], v249 offset:32768\n\t"
    "ds_read_b64_tr_b16 v[226:227], v249 offset:36864\n\t"
    "v_mfma_f32_16x16x32_bf16 v[208:211], v[236:239], v[92:95], v[208:211]\n\t"
    "ds_read_b64_tr_b16 v[228:229], v249 offset:40960\n\t"
    "ds_read_b64_tr_b16 v[230:231], v249 offset:45056\n\t"
    "v_mfma_f32_16x16x32_bf16 v[208:211], v[232:235], v[84:87], v[208:211]\n\t"
    "ds_read_b64_tr_b16 v[232:233], v248 offset:33280\n\t"
    "v_mfma_f32_16x16x32_bf16 v[204:207], v[240:243], v[80:83], v[204:207]\n\t"
    "ds_read_b64_tr_b16 v[234:235], v248 offset:37376\n\t"
    "v_mfma_f32_16x16x32_bf16 v[204:207], v[244:247], v[88:91], v[204:207]\n\t"
    "ds_read_b64_tr_b16 v[236:237], v248 offset:41472\n\t"
    "v_mfma_f32_16x16x32_bf16 v[212:215], v[244:247], v[92:95], v[212:215]\n\t"
    "ds_read_b64_tr_b16 v[238:239], v248 offset:45568\n\t"
    "v_mfma_f32_16x16x32_bf16 v[212:215], v[240:243], v[84:87], v[212:215]\n\t"
    "v_mfma_f32_16x16x32_bf16 v[144:147], v[138:141], v[112:115], v[144:147]\n\t"
    "ds_read_b64_tr_b16 v[240:241], v249 offset:33280\n\t"
    "s_waitcnt lgkmcnt(9)\n\t"
    "v_mfma_f32_16x16x32_bf16 v[152:155], v[216:219], v[96:99], v[152:155]\n\t"
    "ds_read_b64_tr_b16 v[242:243], v249 offset:37376\n\t"
    "v_mfma_f32_16x16x32_bf16 v[152:155], v[220:223], v[104:107], v[152:155]\n\t"
    "ds_read_b64_tr_b16 v[244:245], v249 offset:41472\n\t"
    "v_mfma_f32_16x16x32_bf16 v[160:163], v[220:223], v[108:111], v[160:163]\n\t"
    "ds_read_b64_tr_b16 v[246:247], v249 offset:45568\n\t"
    "v_mfma_f32_16x16x32_bf16 v[160:163], v[216:219], v[100:103], v[160:163]\n\t"
    "v_mfma_f32_16x16x32_bf16 v[148:151], v[138:141], v[116:119], v[148:151]\n\t"
    "ds_read_b64_tr_b16 v[216:217], v248 offset:33792\n\t"
    "s_waitcnt lgkmcnt(9)\n\t"
    "v_mfma_f32_16x16x32_bf16 v[156:159], v[224:227], v[96:99], v[156:159]\n\t"
    "ds_read_b64_tr_b16 v[218:219], v248 offset:37888\n\t"
    "v_mfma_f32_16x16x32_bf16 v[156:159], v[228:231], v[104:107], v[156:159]\n\t"
    "ds_read_b64_tr_b16 v[220:221], v248 offset:41984\n\t"
    "v_mfma_f32_16x16x32_bf16 v[164:167], v[228:231], v[108:111], v[164:167]\n\t"
    "ds_read_b64_tr_b16 v[222:223], v248 offset:46080\n\t"
    "v_mfma_f32_16x16x32_bf16 v[164:167], v[224:227], v[100:103], v[164:167]\n\t"
    "ds_read_b64_tr_b16 v[224:225], v249 offset:33792\n\t"
    "s_waitcnt lgkmcnt(9)\n\t"
    "v_mfma_f32_16x16x32_bf16 v[168:171], v[232:235], v[96:99], v[168:171]\n\t"
    "ds_read_b64_tr_b16 v[226:227], v249 offset:37888\n\t"
    "v_mfma_f32_16x16x32_bf16 v[168:171], v[236:239], v[104:107], v[168:171]\n\t"
    "ds_read_b64_tr_b16 v[228:229], v249 offset:41984\n\t"
    "v_mfma_f32_16x16x32_bf16 v[176:179], v[236:239], v[108:111], v[176:179]\n\t"
    "ds_read_b64_tr_b16 v[230:231], v249 offset:46080\n\t"
    "v_mfma_f32_16x16x32_bf16 v[176:179], v[232:235], v[100:103], v[176:179]\n\t"
    "v_mfma_f32_16x16x32_bf16 v[144:147], v[138:141], v[120:123], v[144:147]\n\t"
    "ds_read_b64_tr_b16 v[232:233], v248 offset:34304\n\t"
    "s_waitcnt lgkmcnt(9)\n\t"
    "v_mfma_f32_16x16x32_bf16 v[172:175], v[240:243], v[96:99], v[172:175]\n\t"
    "ds_read_b64_tr_b16 v[234:235], v248 offset:38400\n\t"
    "v_mfma_f32_16x16x32_bf16 v[172:175], v[244:247], v[104:107], v[172:175]\n\t"
    "ds_read_b64_tr_b16 v[236:237], v248 offset:42496\n\t"
    "v_mfma_f32_16x16x32_bf16 v[180:183], v[244:247], v[108:111], v[180:183]\n\t"
    "ds_read_b64_tr_b16 v[238:239], v248 offset:46592\n\t"
    "v_mfma_f32_16x16x32_bf16 v[180:183], v[240:243], v[100:103], v[180:183]\n\t"
    "ds_read_b64_tr_b16 v[240:241], v249 offset:34304\n\t"
    "s_waitcnt lgkmcnt(9)\n\t"
    "v_mfma_f32_16x16x32_bf16 v[184:187], v[216:219], v[96:99], v[184:187]\n\t"
    "ds_read_b64_tr_b16 v[242:243], v249 offset:38400\n\t"
    "v_mfma_f32_16x16x32_bf16 v[184:187], v[220:223], v[104:107], v[184:187]\n\t"
    "ds_read_b64_tr_b16 v[244:245], v249 offset:42496\n\t"
    "v_mfma_f32_16x16x32_bf16 v[192:195], v[220:223], v[108:111], v[192:195]\n\t"
    "ds_read_b64_tr_b16 v[246:247], v249 offset:46592\n\t"
    "v_mfma_f32_16x16x32_bf16 v[192:195], v[216:219], v[100:103], v[192:195]\n\t"
    "v_mfma_f32_16x16x32_bf16 v[148:151], v[138:141], v[124:127], v[148:151]\n\t"
    "s_waitcnt lgkmcnt(8)\n\t"
    "v_mfma_f32_16x16x32_bf16 v[188:191], v[224:227], v[96:99], v[188:191]\n\t"
    "v_mfma_f32_16x16x32_bf16 v[188:191], v[228:231], v[104:107], v[188:191]\n\t"
    "v_mfma_f32_16x16x32_bf16 v[196:199], v[228:231], v[108:111], v[196:199]\n\t"
    "v_mfma_f32_16x16x32_bf16 v[196:199], v[224:227], v[100:103], v[196:199]\n\t"
    "s_waitcnt lgkmcnt(0)\n\t"
    "s_barrier\n\t"
    "ds_read_b64_tr_b16 v[216:217], v248 offset:49152\n\t"
    "ds_read_b64_tr_b16 v[218:219], v248 offset:53248\n\t"
    "v_mfma_f32_16x16x32_bf16 v[200:203], v[232:235], v[96:99], v[200:203]\n\t"
    "ds_read_b64_tr_b16 v[220:221], v248 offset:57344\n\t"
    "ds_read_b64_tr_b16 v[222:223], v248 offset:61440\n\t"
    "v_mfma_f32_16x16x32_bf16 v[200:203], v[236:239], v[104:107], v[200:203]\n\t"
    "ds_read_b64_tr_b16 v[224:225], v249 offset:49152\n\t"
    "ds_read_b64_tr_b16 v[226:227], v249 offset:53248\n\t"
    "v_mfma_f32_16x16x32_bf16 v[208:211], v[236:239], v[108:111], v[208:211]\n\t"
    "ds_read_b64_tr_b16 v[228:229], v249 offset:57344\n\t"
    "ds_read_b64_tr_b16 v[230:231], v249 offset:61440\n\t"
    "v_mfma_f32_16x16x32_bf16 v[208:211], v[232:235], v[100:103], v[208:211]\n\t"
    "ds_read_b64_tr_b16 v[232:233], v248 offset:49664\n\t"
    "v_mfma_f32_16x16x32_bf16 v[204:207], v[240:243], v[96:99], v[204:207]\n\t"
    "ds_read_b64_tr_b16 v[234:235], v248 offset:53760\n\t"
    "v_mfma_f32_16x16x32_bf16 v[204:207], v[244:247], v[104:107], v[204:207]\n\t"
    "ds_read_b64_tr_b16 v[236:237], v248 offset:57856\n\t"
    "v_mfma_f32_16x16x32_bf16 v[212:215], v[244:247], v[108:111], v[212:215]\n\t"
    "ds_read_b64_tr_b16 v[238:239], v248 offset:61952\n\t"
    "v_mfma_f32_16x16x32_bf16 v[212:215], v[240:243], v[100:103], v[212:215]\n\t"
    "ds_read_b64_tr_b16 v[240:241], v249 offset:49664\n\t"
    "v_max_f32 v252, 0xda24260, v148\n\t"
    "s_waitcnt lgkmcnt(9)\n\t"
    "v_mfma_f32_16x16x32_bf16 v[152:155], v[216:219], v[112:115], v[152:155]\n\t"
    "ds_read_b64_tr_b16 v[242:243], v249 offset:53760\n\t"
    "v_rcp_f32 v251, v144\n\t"
    "v_rcp_f32 v252, v252\n\t"
    "v_mfma_f32_16x16x32_bf16 v[152:155], v[220:223], v[120:123], v[152:155]\n\t"
    "ds_read_b64_tr_b16 v[244:245], v249 offset:57856\n\t"
    "v_mfma_f32_16x16x32_bf16 v[160:163], v[220:223], v[124:127], v[160:163]\n\t"
    "ds_read_b64_tr_b16 v[246:247], v249 offset:61952\n\t"
    "v_mfma_f32_16x16x32_bf16 v[160:163], v[216:219], v[116:119], v[160:163]\n\t"
    "ds_read_b64_tr_b16 v[216:217], v248 offset:50176\n\t"
    "v_lshlrev_b32 v145, 5, v253\n\t"
    "v_cmp_gt_u32 vcc, 4, v253\n\t"
    "v_mov_b32 v254, 0x80000000\n\t"
    "s_waitcnt lgkmcnt(9)\n\t"
    "v_mfma_f32_16x16x32_bf16 v[156:159], v[224:227], v[112:115], v[156:159]\n\t"
    "ds_read_b64_tr_b16 v[218:219], v248 offset:54272\n\t"
    "v_cndmask_b32 v145, v254, v145, vcc\n\t"
    "v_mov_b32 v144, 0\n\t"
    "v_cmp_eq_u32 vcc, 0, v253\n\t"
    "v_mfma_f32_16x16x32_bf16 v[156:159], v[228:231], v[120:123], v[156:159]\n\t"
    "ds_read_b64_tr_b16 v[220:221], v248 offset:58368\n\t"
    "v_mov_b32 v254, 0x80000000\n\t"
    "v_cndmask_b32 v144, v254, v144, vcc\n\t"
    "v_mov_b32 v148, s44\n\t"
    "v_mfma_f32_16x16x32_bf16 v[164:167], v[228:231], v[124:127], v[164:167]\n\t"
    "ds_read_b64_tr_b16 v[222:223], v248 offset:62464\n\t"
    "v_mov_b32 v149, s45\n\t"
    "v_mfma_f32_16x16x32_bf16 v[164:167], v[224:227], v[116:119], v[164:167]\n\t"
    "ds_read_b64_tr_b16 v[224:225], v249 offset:50176\n\t"
    "s_waitcnt lgkmcnt(9)\n\t"
    "v_mfma_f32_16x16x32_bf16 v[168:171], v[232:235], v[112:115], v[168:171]\n\t"
    "ds_read_b64_tr_b16 v[226:227], v249 offset:54272\n\t"
    "v_mfma_f32_16x16x32_bf16 v[168:171], v[236:239], v[120:123], v[168:171]\n\t"
    "ds_read_b64_tr_b16 v[228:229], v249 offset:58368\n\t"
    "v_mul_f32 v152, v251, v152\n\t"
    "v_mul_f32 v153, v251, v153\n\t"
    "v_cvt_pk_bf16_f32 v0, v152, v153\n\t"
    "v_mfma_f32_16x16x32_bf16 v[176:179], v[236:239], v[124:127], v[176:179]\n\t"
    "ds_read_b64_tr_b16 v[230:231], v249 offset:62464\n\t"
    "v_mul_f32 v154, v251, v154\n\t"
    "v_mul_f32 v155, v251, v155\n\t"
    "v_cvt_pk_bf16_f32 v1, v154, v155\n\t"
    "v_mfma_f32_16x16x32_bf16 v[176:179], v[232:235], v[116:119], v[176:179]\n\t"
    "ds_read_b64_tr_b16 v[232:233], v248 offset:50688\n\t"
    "v_mul_f32 v156, v251, v156\n\t"
    "v_mul_f32 v157, v251, v157\n\t"
    "v_cvt_pk_bf16_f32 v2, v156, v157\n\t"
    "s_waitcnt lgkmcnt(9)\n\t"
    "v_mfma_f32_16x16x32_bf16 v[172:175], v[240:243], v[112:115], v[172:175]\n\t"
    "ds_read_b64_tr_b16 v[234:235], v248 offset:54784\n\t"
    "v_mul_f32 v158, v251, v158\n\t"
    "v_mul_f32 v159, v251, v159\n\t"
    "v_cvt_pk_bf16_f32 v3, v158, v159\n\t"
    "v_mfma_f32_16x16x32_bf16 v[172:175], v[244:247], v[120:123], v[172:175]\n\t"
    "ds_read_b64_tr_b16 v[236:237], v248 offset:58880\n\t"
    "v_mul_f32 v160, v252, v160\n\t"
    "v_mul_f32 v161, v252, v161\n\t"
    "v_cvt_pk_bf16_f32 v4, v160, v161\n\t"
    "v_mfma_f32_16x16x32_bf16 v[180:183], v[244:247], v[124:127], v[180:183]\n\t"
    "ds_read_b64_tr_b16 v[238:239], v248 offset:62976\n\t"
    "v_mul_f32 v162, v252, v162\n\t"
    "v_mul_f32 v163, v252, v163\n\t"
    "v_cvt_pk_bf16_f32 v5, v162, v163\n\t"
    "v_mfma_f32_16x16x32_bf16 v[180:183], v[240:243], v[116:119], v[180:183]\n\t"
    "ds_read_b64_tr_b16 v[240:241], v249 offset:50688\n\t"
    "v_mul_f32 v164, v252, v164\n\t"
    "v_mul_f32 v165, v252, v165\n\t"
    "v_cvt_pk_bf16_f32 v6, v164, v165\n\t"
    "s_waitcnt lgkmcnt(9)\n\t"
    "v_mfma_f32_16x16x32_bf16 v[184:187], v[216:219], v[112:115], v[184:187]\n\t"
    "ds_read_b64_tr_b16 v[242:243], v249 offset:54784\n\t"
    "v_mul_f32 v166, v252, v166\n\t"
    "v_mul_f32 v167, v252, v167\n\t"
    "v_cvt_pk_bf16_f32 v7, v166, v167\n\t"
    "v_mfma_f32_16x16x32_bf16 v[184:187], v[220:223], v[120:123], v[184:187]\n\t"
    "ds_read_b64_tr_b16 v[244:245], v249 offset:58880\n\t"
    "v_mul_f32 v168, v251, v168\n\t"
    "v_mul_f32 v169, v251, v169\n\t"
    "v_cvt_pk_bf16_f32 v8, v168, v169\n\t"
    "v_mfma_f32_16x16x32_bf16 v[192:195], v[220:223], v[124:127], v[192:195]\n\t"
    "ds_read_b64_tr_b16 v[246:247], v249 offset:62976\n\t"
    "v_mul_f32 v170, v251, v170\n\t"
    "v_mul_f32 v171, v251, v171\n\t"
    "v_cvt_pk_bf16_f32 v9, v170, v171\n\t"
    "v_mfma_f32_16x16x32_bf16 v[192:195], v[216:219], v[116:119], v[192:195]\n\t"
    "v_mul_f32 v172, v251, v172\n\t"
    "v_mul_f32 v173, v251, v173\n\t"
    "v_cvt_pk_bf16_f32 v10, v172, v173\n\t"
    "v_mul_f32 v174, v251, v174\n\t"
    "s_waitcnt lgkmcnt(8)\n\t"
    "v_mfma_f32_16x16x32_bf16 v[188:191], v[224:227], v[112:115], v[188:191]\n\t"
    "v_mul_f32 v175, v251, v175\n\t"
    "v_cvt_pk_bf16_f32 v11, v174, v175\n\t"
    "v_mul_f32 v176, v252, v176\n\t"
    "v_mul_f32 v177, v252, v177\n\t"
    "v_mfma_f32_16x16x32_bf16 v[188:191], v[228:231], v[120:123], v[188:191]\n\t"
    "v_cvt_pk_bf16_f32 v12, v176, v177\n\t"
    "v_mul_f32 v178, v252, v178\n\t"
    "v_mul_f32 v179, v252, v179\n\t"
    "v_cvt_pk_bf16_f32 v13, v178, v179\n\t"
    "v_mfma_f32_16x16x32_bf16 v[196:199], v[228:231], v[124:127], v[196:199]\n\t"
    "v_mul_f32 v180, v252, v180\n\t"
    "v_mul_f32 v181, v252, v181\n\t"
    "v_cvt_pk_bf16_f32 v14, v180, v181\n\t"
    "v_mul_f32 v182, v252, v182\n\t"
    "v_mfma_f32_16x16x32_bf16 v[196:199], v[224:227], v[116:119], v[196:199]\n\t"
    "s_waitcnt lgkmcnt(0)\n\t"
    "s_barrier\n\t"
    "v_mul_f32 v183, v252, v183\n\t"
    "v_cvt_pk_bf16_f32 v15, v182, v183\n\t"
    "v_mfma_f32_16x16x32_bf16 v[200:203], v[232:235], v[112:115], v[200:203]\n\t"
    "v_mfma_f32_16x16x32_bf16 v[200:203], v[236:239], v[120:123], v[200:203]\n\t"
    "v_mul_f32 v184, v251, v184\n\t"
    "v_mul_f32 v185, v251, v185\n\t"
    "v_cvt_pk_bf16_f32 v16, v184, v185\n\t"
    "v_mul_f32 v186, v251, v186\n\t"
    "v_mfma_f32_16x16x32_bf16 v[208:211], v[236:239], v[124:127], v[208:211]\n\t"
    "v_mul_f32 v187, v251, v187\n\t"
    "v_cvt_pk_bf16_f32 v17, v186, v187\n\t"
    "v_mul_f32 v188, v251, v188\n\t"
    "v_mul_f32 v189, v251, v189\n\t"
    "v_mfma_f32_16x16x32_bf16 v[208:211], v[232:235], v[116:119], v[208:211]\n\t"
    "v_cvt_pk_bf16_f32 v18, v188, v189\n\t"
    "v_mul_f32 v190, v251, v190\n\t"
    "v_mul_f32 v191, v251, v191\n\t"
    "v_cvt_pk_bf16_f32 v19, v190, v191\n\t"
    "v_mfma_f32_16x16x32_bf16 v[204:207], v[240:243], v[112:115], v[204:207]\n\t"
    "v_mul_f32 v192, v252, v192\n\t"
    "v_mul_f32 v193, v252, v193\n\t"
    "v_cvt_pk_bf16_f32 v20, v192, v193\n\t"
    "v_mul_f32 v194, v252, v194\n\t"
    "v_mfma_f32_16x16x32_bf16 v[204:207], v[244:247], v[120:123], v[204:207]\n\t"
    "v_mul_f32 v195, v252, v195\n\t"
    "v_cvt_pk_bf16_f32 v21, v194, v195\n\t"
    "v_mul_f32 v196, v252, v196\n\t"
    "v_mul_f32 v197, v252, v197\n\t"
    "v_mfma_f32_16x16x32_bf16 v[212:215], v[244:247], v[124:127], v[212:215]\n\t"
    "v_cvt_pk_bf16_f32 v22, v196, v197\n\t"
    "v_mul_f32 v198, v252, v198\n\t"
    "v_mul_f32 v199, v252, v199\n\t"
    "v_cvt_pk_bf16_f32 v23, v198, v199\n\t"
    "v_mfma_f32_16x16x32_bf16 v[212:215], v[240:243], v[116:119], v[212:215]\n\t"
    "v_mul_f32 v200, v251, v200\n\t"
    "v_mul_f32 v201, v251, v201\n\t"
    "v_cvt_pk_bf16_f32 v24, v200, v201\n\t"
    "v_mul_f32 v202, v251, v202\n\t"
    "v_mul_f32 v203, v251, v203\n\t"
    "v_cvt_pk_bf16_f32 v25, v202, v203\n\t"
    "v_mul_f32 v204, v251, v204\n\t"
    "v_mul_f32 v205, v251, v205\n\t"
    "v_cvt_pk_bf16_f32 v26, v204, v205\n\t"
    "v_mul_f32 v206, v251, v206\n\t"
    "v_mul_f32 v207, v251, v207\n\t"
    "v_cvt_pk_bf16_f32 v27, v206, v207\n\t"
    "v_mul_f32 v208, v252, v208\n\t"
    "v_mul_f32 v209, v252, v209\n\t"
    "v_cvt_pk_bf16_f32 v28, v208, v209\n\t"
    "v_mul_f32 v210, v252, v210\n\t"
    "v_mul_f32 v211, v252, v211\n\t"
    "v_cvt_pk_bf16_f32 v29, v210, v211\n\t"
    "v_mul_f32 v212, v252, v212\n\t"
    "v_mul_f32 v213, v252, v213\n\t"
    "v_cvt_pk_bf16_f32 v30, v212, v213\n\t"
    "v_mul_f32 v214, v252, v214\n\t"
    "v_mul_f32 v215, v252, v215\n\t"
    "v_cvt_pk_bf16_f32 v31, v214, v215\n\t"
    "W3J0_%=:\n\t"
    "s_setprio 1\n\t"
    "s_add_u32 m0, s47, 0x8000\n\t"
    "s_add_u32 s46, s59, 0x1c000\n\t"
    "s_and_b32 s46, s46, 0x1ffff\n\t"
    "s_or_b32 s46, s46, s49\n\t"
    "buffer_load_dwordx4 v128, %[srdk], s46 offen nt lds\n\t"
    "buffer_load_dwordx4 v129, %[srdk], s46 offen offset:1024 nt lds\n\t"
    "buffer_load_dwordx4 v130, %[srdk], s46 offen offset:2048 nt lds\n\t"
    "buffer_load_dwordx4 v131, %[srdk], s46 offen offset:3072 nt lds\n\t"
    "v_lshlrev_b32 v138, 4, v253\n\t"
    "s_lshl_b32 s52, s50, 11\n\t"
    "v_add_u32 v138, s52, v138\n\t"
    "v_or_b32 v139, s61, v138\n\t"
    "v_bfe_u32 v254, v253, 0, 4\n\t"
    "v_lshlrev_b32 v140, 11, v254\n\t"
    "v_bfe_u32 v254, v253, 4, 1\n\t"
    "v_lshl_add_u32 v140, v254, 5, v140\n\t"
    "v_bfe_u32 v254, v253, 5, 1\n\t"
    "v_lshl_add_u32 v140, v254, 4, v140\n\t"
    "s_lshl_b32 s52, s50, 16\n\t"
    "v_add_u32 v140, s52, v140\n\t"
    "v_mov_b32 v248, %[lds]\n\t"
    "v_bfe_u32 v254, v253, 5, 1\n\t"
    "v_lshl_add_u32 v248, v254, 11, v248\n\t"
    "v_bfe_u32 v254, v253, 2, 3\n\t"
    "v_lshl_add_u32 v248, v254, 6, v248\n\t"
    "v_bfe_u32 v254, v253, 4, 1\n\t"
    "v_lshl_add_u32 v248, v254, 5, v248\n\t"
    "v_bfe_u32 v254, v253, 0, 2\n\t"
    "v_lshl_add_u32 v248, v254, 3, v248\n\t"
    "v_xor_b32 v249, 32, v248\n\t"
    "s_lshl_b32 s52, s50, 9\n\t"
    "v_add_u32 v248, s52, v248\n\t"
    "v_xor_b32 v249, 32, v248\n\t"
    "v_lshl_add_u32 v250, v253, 4, %[lds]\n\t"
    "s_barrier\n\t"
    "s_add_u32 m0, s47, 0x20000\n\t"
    "s_add_u32 s46, s59, 0x0\n\t"
    "s_and_b32 s46, s46, 0x1ffff\n\t"
    "s_or_b32 s46, s46, s49\n\t"
    "buffer_load_dwordx4 v132, %[srdv], s46 offen nt lds\n\t"
    "buffer_load_dwordx4 v133, %[srdv], s46 offen offset:1024 nt lds\n\t"
    "buffer_load_dwordx4 v132, %[srdv], s46 offen offset:2048 nt lds\n\t"
    "s_barrier\n\t"
    "s_add_u32 m0, s47, 0x20000\n\t"
    "s_add_u32 s46, s59, 0x0\n\t"
    "s_and_b32 s46, s46, 0x1ffff\n\t"
    "s_or_b32 s46, s46, s49\n\t"
    "buffer_load_dwordx4 v133, %[srdv], s46 offen offset:3072 nt lds\n\t"
    "s_add_u32 m0, s47, 0xc000\n\t"
    "s_add_u32 s46, s59, 0x4000\n\t"
    "s_and_b32 s46, s46, 0x1ffff\n\t"
    "s_or_b32 s46, s46, s49\n\t"
    "buffer_load_dwordx4 v132, %[srdv], s46 offen nt lds\n\t"
    "buffer_load_dwordx4 v133, %[srdv], s46 offen offset:1024 nt lds\n\t"
    "s_barrier\n\t"
    "s_add_u32 m0, s47, 0xc000\n\t"
    "s_add_u32 s46, s59, 0x4000\n\t"
    "s_and_b32 s46, s46, 0x1ffff\n\t"
    "s_or_b32 s46, s46, s49\n\t"
    "buffer_load_dwordx4 v132, %[srdv], s46 offen offset:2048 nt lds\n\t"
    "buffer_load_dwordx4 v133, %[srdv], s46 offen offset:3072 nt lds\n\t"
    "s_add_u32 m0, s47, 0x10000\n\t"
    "s_add_u32 s46, s59, 0x8000\n\t"
    "s_and_b32 s46, s46, 0x1ffff\n\t"
    "s_or_b32 s46, s46, s49\n\t"
    "buffer_load_dwordx4 v132, %[srdv], s46 offen nt lds\n\t"
    "s_barrier\n\t"
    "s_add_u32 m0, s47, 0x10000\n\t"
    "s_add_u32 s46, s59, 0x8000\n\t"
    "s_and_b32 s46, s46, 0x1ffff\n\t"
    "s_or_b32 s46, s46, s49\n\t"
    "s_waitcnt vmcnt(12)\n\t"
    "buffer_load_dwordx4 v133, %[srdv], s46 offen offset:1024 nt lds\n\t"
    "s_waitcnt vmcnt(12)\n\t"
    "buffer_load_dwordx4 v132, %[srdv], s46 offen offset:2048 nt lds\n\t"
    "s_waitcnt vmcnt(12)\n\t"
    "buffer_load_dwordx4 v133, %[srdv], s46 offen offset:3072 nt lds\n\t"
    "s_barrier\n\t"
    "s_add_u32 m0, s47, 0x0\n\t"
    "s_add_u32 s46, s59, 0xc000\n\t"
    "s_and_b32 s46, s46, 0x1ffff\n\t"
    "s_or_b32 s46, s46, s49\n\t"
    "s_waitcnt vmcnt(12)\n\t"
    "buffer_load_dwordx4 v132, %[srdv], s46 offen nt lds\n\t"
    "s_waitcnt vmcnt(12)\n\t"
    "buffer_load_dwordx4 v133, %[srdv], s46 offen offset:1024 nt lds\n\t"
    "s_waitcnt vmcnt(12)\n\t"
    "buffer_load_dwordx4 v132, %[srdv], s46 offen offset:2048 nt lds\n\t"
    "s_add_u32 m0, s47, 0x4000\n\t"
    "s_bfe_u32 s46, %[htab], 0x30000\n\t"
    "s_lshl_b32 s46, s46, 17\n\t"
    "buffer_load_dwordx4 v134, %[srdwo], s46 offen lds\n\t"
    "buffer_load_dwordx4 v135, %[srdwo], s46 offen offset:1024 lds\n\t"
    "buffer_load_dwordx4 v136, %[srdwo], s46 offen offset:2048 lds\n\t"
    "buffer_load_dwordx4 v137, %[srdwo], s46 offen offset:3072 lds\n\t"
    "s_barrier\n\t"
    "s_add_u32 m0, s47, 0x0\n\t"
    "s_add_u32 s46, s59, 0xc000\n\t"
    "s_and_b32 s46, s46, 0x1ffff\n\t"
    "s_or_b32 s46, s46, s49\n\t"
    "s_waitcnt vmcnt(12)\n\t"
    "buffer_load_dwordx4 v133, %[srdv], s46 offen offset:3072 nt lds\n\t"
    "s_add_u32 m0, s47, 0x14000\n\t"
    "s_add_u32 s46, s59, 0x10000\n\t"
    "s_and_b32 s46, s46, 0x1ffff\n\t"
    "s_or_b32 s46, s46, s49\n\t"
    "s_waitcnt vmcnt(12)\n\t"
    "buffer_load_dwordx4 v132, %[srdv], s46 offen lds\n\t"
    "s_waitcnt vmcnt(12)\n\t"
    "buffer_load_dwordx4 v133, %[srdv], s46 offen offset:1024 lds\n\t"
    "s_barrier\n\t"
    "s_add_u32 m0, s47, 0x14000\n\t"
    "s_add_u32 s46, s59, 0x10000\n\t"
    "s_and_b32 s46, s46, 0x1ffff\n\t"
    "s_or_b32 s46, s46, s49\n\t"
    "s_waitcnt vmcnt(12)\n\t"
    "buffer_load_dwordx4 v132, %[srdv], s46 offen offset:2048 lds\n\t"
    "s_waitcnt vmcnt(12)\n\t"
    "buffer_load_dwordx4 v133, %[srdv], s46 offen offset:3072 lds\n\t"
    "s_add_u32 m0, s47, 0x18000\n\t"
    "s_add_u32 s46, s59, 0x14000\n\t"
    "s_and_b32 s46, s46, 0x1ffff\n\t"
    "s_or_b32 s46, s46, s49\n\t"
    "s_waitcnt vmcnt(12)\n\t"
    "buffer_load_dwordx4 v132, %[srdv], s46 offen lds\n\t"
    "s_add_u32 m0, s47, 0x24000\n\t"
    "s_bfe_u32 s46, %[htab], 0x30000\n\t"
    "s_lshl_b32 s46, s46, 17\n\t"
    "s_or_b32 s46, s46, 0x10000\n\t"
    "buffer_load_dwordx4 v134, %[srdwo], s46 offen lds\n\t"
    "buffer_load_dwordx4 v135, %[srdwo], s46 offen offset:1024 lds\n\t"
    "buffer_load_dwordx4 v136, %[srdwo], s46 offen offset:2048 lds\n\t"
    "buffer_load_dwordx4 v137, %[srdwo], s46 offen offset:3072 lds\n\t"
    "s_barrier\n\t"
    "s_add_u32 m0, s47, 0x18000\n\t"
    "s_add_u32 s46, s59, 0x14000\n\t"
    "s_and_b32 s46, s46, 0x1ffff\n\t"
    "s_or_b32 s46, s46, s49\n\t"
    "s_waitcnt vmcnt(12)\n\t"
    "buffer_load_dwordx4 v133, %[srdv], s46 offen offset:1024 lds\n\t"
    "s_waitcnt vmcnt(12)\n\t"
    "buffer_load_dwordx4 v132, %[srdv], s46 offen offset:2048 lds\n\t"
    "s_add_u32 m0, s47, 0x1c000\n\t"
    "s_bfe_u32 s46, %[htab], 0x30010\n\t"
    "s_lshl_b32 s46, s46, 17\n\t"
    "buffer_load_dwordx4 v134, %[srdwo], s46 offen lds\n\t"
    "buffer_load_dwordx4 v135, %[srdwo], s46 offen offset:1024 lds\n\t"
    "buffer_load_dwordx4 v136, %[srdwo], s46 offen offset:2048 lds\n\t"
    "buffer_load_dwordx4 v137, %[srdwo], s46 offen offset:3072 lds\n\t"
    "s_barrier\n\t"
    "s_add_u32 m0, s47, 0x18000\n\t"
    "s_add_u32 s46, s59, 0x14000\n\t"
    "s_and_b32 s46, s46, 0x1ffff\n\t"
    "s_or_b32 s46, s46, s49\n\t"
    "s_waitcnt vmcnt(12)\n\t"
    "buffer_load_dwordx4 v133, %[srdv], s46 offen offset:3072 lds\n\t"
    "s_add_u32 m0, s47, 0x8000\n\t"
    "s_add_u32 s46, s59, 0x18000\n\t"
    "s_and_b32 s46, s46, 0x1ffff\n\t"
    "s_or_b32 s46, s46, s49\n\t"
    "s_waitcnt vmcnt(12)\n\t"
    "buffer_load_dwordx4 v132, %[srdv], s46 offen lds\n\t"
    "s_barrier\n\t"
    "s_add_u32 m0, s47, 0x8000\n\t"
    "s_add_u32 s46, s59, 0x18000\n\t"
    "s_and_b32 s46, s46, 0x1ffff\n\t"
    "s_or_b32 s46, s46, s49\n\t"
    "s_waitcnt vmcnt(12)\n\t"
    "buffer_load_dwordx4 v133, %[srdv], s46 offen offset:1024 lds\n\t"
    "s_waitcnt vmcnt(12)\n\t"
    "buffer_load_dwordx4 v132, %[srdv], s46 offen offset:2048 lds\n\t"
    "s_waitcnt vmcnt(12)\n\t"
    "buffer_load_dwordx4 v133, %[srdv], s46 offen offset:3072 lds\n\t"
    "s_add_u32 m0, s47, 0x20000\n\t"
    "s_bfe_u32 s46, %[htab], 0x30010\n\t"
    "s_lshl_b32 s46, s46, 17\n\t"
    "s_or_b32 s46, s46, 0x10000\n\t"
    "buffer_load_dwordx4 v134, %[srdwo], s46 offen lds\n\t"
    "buffer_load_dwordx4 v135, %[srdwo], s46 offen offset:1024 lds\n\t"
    "buffer_load_dwordx4 v136, %[srdwo], s46 offen offset:2048 lds\n\t"
    "buffer_load_dwordx4 v137, %[srdwo], s46 offen offset:3072 lds\n\t"
    "s_barrier\n\t"
    "s_add_u32 m0, s47, 0xc000\n\t"
    "s_add_u32 s46, s59, 0x1c000\n\t"
    "s_and_b32 s46, s46, 0x1ffff\n\t"
    "s_or_b32 s46, s46, s49\n\t"
    "s_waitcnt vmcnt(12)\n\t"
    "buffer_load_dwordx4 v132, %[srdv], s46 offen lds\n\t"
    "s_waitcnt vmcnt(12)\n\t"
    "buffer_load_dwordx4 v133, %[srdv], s46 offen offset:1024 lds\n\t"
    "s_barrier\n\t"
    "s_add_u32 m0, s47, 0xc000\n\t"
    "s_add_u32 s46, s59, 0x1c000\n\t"
    "s_and_b32 s46, s46, 0x1ffff\n\t"
    "s_or_b32 s46, s46, s49\n\t"
    "s_waitcnt vmcnt(12)\n\t"
    "buffer_load_dwordx4 v132, %[srdv], s46 offen offset:2048 lds\n\t"
    "s_waitcnt vmcnt(12)\n\t"
    "buffer_load_dwordx4 v133, %[srdv], s46 offen offset:3072 lds\n\t"
    "s_waitcnt vmcnt(12)\n\t"
    "s_barrier\n\t"
    "s_xor_b32 s53, s42, 0x0\n\t"
    "s_lshr_b32 s53, s53, 2\n\t"
    "s_add_u32 s53, s53, %[obase]\n\t"
    "buffer_store_dwordx4 v[0:3], v142, %[srdws], s53 offen sc1\n\t"
    "buffer_store_dwordx4 v[4:7], v143, %[srdws], s53 offen offset:1024 sc1\n\t"
    "s_xor_b32 s53, s42, 0x0\n\t"
    "s_lshr_b32 s53, s53, 2\n\t"
    "s_add_u32 s53, s53, %[obase]\n\t"
    "buffer_store_dwordx4 v[8:11], v142, %[srdws], s53 offen offset:2048 sc1\n\t"
    "buffer_store_dwordx4 v[12:15], v143, %[srdws], s53 offen offset:3072 sc1\n\t"
    "s_add_u32 s54, s47, 0x10000\n\t"
    "v_lshl_add_u32 v146, v253, 4, s54\n\t"
    "ds_write_b128 v146, v[0:3] offset:0\n\t"
    "ds_write_b128 v146, v[4:7] offset:1024\n\t"
    "ds_write_b128 v146, v[8:11] offset:2048\n\t"
    "ds_write_b128 v146, v[12:15] offset:3072\n\t"
    "s_add_u32 s54, s47, 0x0\n\t"
    "v_lshl_add_u32 v146, v253, 4, s54\n\t"
    "ds_write_b128 v146, v[16:19] offset:0\n\t"
    "ds_write_b128 v146, v[20:23] offset:1024\n\t"
    "ds_write_b128 v146, v[24:27] offset:2048\n\t"
    "ds_write_b128 v146, v[28:31] offset:3072\n\t"
    "s_waitcnt vmcnt(12)\n\t"
    "s_waitcnt lgkmcnt(0)\n\t"
    "s_barrier\n\t"
    "s_xor_b32 s53, s42, 0x10000\n\t"
    "s_lshr_b32 s53, s53, 2\n\t"
    "s_add_u32 s53, s53, %[obase]\n\t"
    "buffer_store_dwordx4 v[16:19], v142, %[srdws], s53 offen sc1\n\t"
    "buffer_store_dwordx4 v[20:23], v143, %[srdws], s53 offen offset:1024 sc1\n\t"
    "s_xor_b32 s53, s42, 0x10000\n\t"
    "s_lshr_b32 s53, s53, 2\n\t"
    "s_add_u32 s53, s53, %[obase]\n\t"
    "buffer_store_dwordx4 v[24:27], v142, %[srdws], s53 offen offset:2048 sc1\n\t"
    "buffer_store_dwordx4 v[28:31], v143, %[srdws], s53 offen offset:3072 sc1\n\t"
    "s_lshr_b32 s56, %[wj], 3\n\t"
    "s_xor_b32 s56, s56, %[qi]\n\t"
    "s_lshl_b32 s56, s56, 2\n\t"
    "s_add_u32 s56, s56, 0\n\t"
    "s_lshr_b32 s56, %[htab], s56\n\t"
    "s_and_b32 s56, s56, 7\n\t"
    "s_lshl_b32 s56, s56, 3\n\t"
    "s_and_b32 s54, %[wj], 7\n\t"
    "s_or_b32 s56, s56, s54\n\t"
    "s_lshl_b32 s56, s56, 2\n\t"
    "s_add_u32 s56, s56, s50\n\t"
    "s_lshl_b32 s56, s56, 12\n\t"
    "s_add_u32 s56, s56, 0x400\n\t"
    "s_sub_u32 s56, s56, %[cbo]\n\t"
    "v_lshlrev_b32 v141, 6, v253\n\t"
    "buffer_load_dword v254, v141, %[srdwo], s56 offen\n\t"
    "s_waitcnt vmcnt(9)\n\t"
    "s_barrier\n\t"
    "s_waitcnt vmcnt(1)\n\t"
    "s_lshl_b32 s55, s50, 3\n\t"
    "s_add_u32 s55, s55, %[f1base]\n\t"
    "s_lshr_b32 s52, s42, 12\n\t"
    "s_add_u32 s55, s55, s52\n\t"
    "buffer_store_dwordx2 v[148:149], v144, %[srdws], s55 offen sc1\n\t"
    "ds_read_b64_tr_b16 v[208:209], v248 offset:16384\n\t"
    "ds_read_b64_tr_b16 v[210:211], v248 offset:20480\n\t"
    "ds_read_b64_tr_b16 v[212:213], v249 offset:16384\n\t"
    "ds_read_b64_tr_b16 v[214:215], v249 offset:20480\n\t"
    "v_add_u32 v250, 0x10000, v250\n\t"
    "ds_read_b128 v[216:219], v250 offset:0\n\t"
    "ds_read_b128 v[220:223], v250 offset:1024\n\t"
    "ds_read_b128 v[224:227], v250 offset:4096\n\t"
    "ds_read_b128 v[228:231], v250 offset:5120\n\t"
    "ds_read_b128 v[232:235], v250 offset:8192\n\t"
    "ds_read_b128 v[236:239], v250 offset:9216\n\t"
    "ds_read_b128 v[240:243], v250 offset:12288\n\t"
    "s_waitcnt lgkmcnt(5)\n\t"
    "v_mfma_f32_16x16x32_bf16 v[152:155], v[208:211], v[216:219], 0\n\t"
    "ds_read_b64_tr_b16 v[28:29], v248 offset:24576\n\t"
    "v_mfma_f32_16x16x32_bf16 v[156:159], v[212:215], v[216:219], 0\n\t"
    "ds_read_b64_tr_b16 v[30:31], v248 offset:28672\n\t"
    "s_add_u32 m0, s47, 0x8000\n\t"
    "s_bfe_u32 s46, %[htab], 0x30004\n\t"
    "s_lshl_b32 s46, s46, 17\n\t"
    "buffer_load_dwordx4 v134, %[srdwo], s46 offen lds\n\t"
    "v_mfma_f32_16x16x32_bf16 v[160:163], v[208:211], v[220:223], 0\n\t"
    "ds_read_b64_tr_b16 v[244:245], v249 offset:24576\n\t"
    "v_mfma_f32_16x16x32_bf16 v[164:167], v[212:215], v[220:223], 0\n\t"
    "ds_read_b64_tr_b16 v[246:247], v249 offset:28672\n\t"
    "s_waitcnt lgkmcnt(7)\n\t"
    "v_mfma_f32_16x16x32_bf16 v[168:171], v[208:211], v[224:227], 0\n\t"
    "ds_read_b128 v[0:3], v250 offset:2048\n\t"
    "v_mfma_f32_16x16x32_bf16 v[172:175], v[212:215], v[224:227], 0\n\t"
    "ds_read_b128 v[4:7], v250 offset:3072\n\t"
    "buffer_load_dwordx4 v135, %[srdwo], s46 offen offset:1024 lds\n\t"
    "v_mfma_f32_16x16x32_bf16 v[176:179], v[208:211], v[228:231], 0\n\t"
    "ds_read_b128 v[8:11], v250 offset:6144\n\t"
    "v_mfma_f32_16x16x32_bf16 v[180:183], v[212:215], v[228:231], 0\n\t"
    "ds_read_b128 v[12:15], v250 offset:7168\n\t"
    "s_waitcnt lgkmcnt(9)\n\t"
    "v_mfma_f32_16x16x32_bf16 v[184:187], v[208:211], v[232:235], 0\n\t"
    "ds_read_b128 v[16:19], v250 offset:10240\n\t"
    "v_mfma_f32_16x16x32_bf16 v[188:191], v[212:215], v[232:235], 0\n\t"
    "ds_read_b128 v[20:23], v250 offset:11264\n\t"
    "buffer_load_dwordx4 v136, %[srdwo], s46 offen offset:2048 lds\n\t"
    "v_mfma_f32_16x16x32_bf16 v[192:195], v[208:211], v[236:239], 0\n\t"
    "ds_read_b128 v[24:27], v250 offset:14336\n\t"
    "v_mfma_f32_16x16x32_bf16 v[196:199], v[212:215], v[236:239], 0\n\t"
    "s_waitcnt lgkmcnt(11)\n\t"
    "v_mfma_f32_16x16x32_bf16 v[200:203], v[208:211], v[240:243], 0\n\t"
    "v_mfma_f32_16x16x32_bf16 v[204:207], v[212:215], v[240:243], 0\n\t"
    "buffer_load_dwordx4 v137, %[srdwo], s46 offen offset:3072 lds\n\t"
    "s_waitcnt lgkmcnt(5)\n\t"
    "v_mfma_f32_16x16x32_bf16 v[152:155], v[28:31], v[0:3], v[152:155]\n\t"
    "v_add_u32 v248, 0x20000, v248\n\t"
    "v_mfma_f32_16x16x32_bf16 v[156:159], v[244:247], v[0:3], v[156:159]\n\t"
    "v_add_u32 v249, 0x20000, v249\n\t"
    "s_lshr_b32 s56, %[wj], 3\n\t"
    "s_xor_b32 s56, s56, %[qi]\n\t"
    "s_lshl_b32 s56, s56, 2\n\t"
    "s_add_u32 s56, s56, 16\n\t"
    "s_lshr_b32 s56, %[htab], s56\n\t"
    "s_and_b32 s56, s56, 7\n\t"
    "s_lshl_b32 s56, s56, 3\n\t"
    "s_and_b32 s54, %[wj], 7\n\t"
    "s_or_b32 s56, s56, s54\n\t"
    "s_lshl_b32 s56, s56, 2\n\t"
    "s_add_u32 s56, s56, s50\n\t"
    "s_lshl_b32 s56, s56, 12\n\t"
    "s_add_u32 s56, s56, 0x400\n\t"
    "s_sub_u32 s56, s56, %[cbo]\n\t"
    "v_lshlrev_b32 v141, 6, v253\n\t"
    "buffer_load_dword v255, v141, %[srdwo], s56 offen\n\t"
    "v_mfma_f32_16x16x32_bf16 v[160:163], v[28:31], v[4:7], v[160:163]\n\t"
    "ds_read_b64_tr_b16 v[208:209], v248 offset:16384\n\t"
    "v_mfma_f32_16x16x32_bf16 v[164:167], v[244:247], v[4:7], v[164:167]\n\t"
    "ds_read_b64_tr_b16 v[210:211], v248 offset:20480\n\t"
    "s_waitcnt lgkmcnt(5)\n\t"
    "v_mfma_f32_16x16x32_bf16 v[168:171], v[28:31], v[8:11], v[168:171]\n\t"
    "ds_read_b64_tr_b16 v[212:213], v249 offset:16384\n\t"
    "v_mfma_f32_16x16x32_bf16 v[172:175], v[244:247], v[8:11], v[172:175]\n\t"
    "ds_read_b64_tr_b16 v[214:215], v249 offset:20480\n\t"
    "v_mfma_f32_16x16x32_bf16 v[176:179], v[28:31], v[12:15], v[176:179]\n\t"
    "v_add_u32 v250, 0xffff0000, v250\n\t"
    "v_mfma_f32_16x16x32_bf16 v[180:183], v[244:247], v[12:15], v[180:183]\n\t"
    "ds_read_b128 v[216:219], v250 offset:0\n\t"
    "s_waitcnt lgkmcnt(6)\n\t"
    "v_mfma_f32_16x16x32_bf16 v[184:187], v[28:31], v[16:19], v[184:187]\n\t"
    "ds_read_b128 v[220:223], v250 offset:1024\n\t"
    "v_mfma_f32_16x16x32_bf16 v[188:191], v[244:247], v[16:19], v[188:191]\n\t"
    "ds_read_b128 v[224:227], v250 offset:4096\n\t"
    "v_mfma_f32_16x16x32_bf16 v[192:195], v[28:31], v[20:23], v[192:195]\n\t"
    "ds_read_b128 v[228:231], v250 offset:5120\n\t"
    "v_mfma_f32_16x16x32_bf16 v[196:199], v[244:247], v[20:23], v[196:199]\n\t"
    "ds_read_b128 v[232:235], v250 offset:8192\n\t"
    "s_waitcnt lgkmcnt(9)\n\t"
    "v_mfma_f32_16x16x32_bf16 v[200:203], v[28:31], v[24:27], v[200:203]\n\t"
    "ds_read_b128 v[236:239], v250 offset:9216\n\t"
    "v_mfma_f32_16x16x32_bf16 v[204:207], v[244:247], v[24:27], v[204:207]\n\t"
    "ds_read_b128 v[240:243], v250 offset:12288\n\t"
    "s_waitcnt lgkmcnt(5)\n\t"
    "v_mfma_f32_16x16x32_bf16 v[152:155], v[208:211], v[216:219], v[152:155]\n\t"
    "ds_read_b64_tr_b16 v[28:29], v248 offset:24576\n\t"
    "v_mfma_f32_16x16x32_bf16 v[156:159], v[212:215], v[216:219], v[156:159]\n\t"
    "ds_read_b64_tr_b16 v[30:31], v248 offset:28672\n\t"
    "v_mfma_f32_16x16x32_bf16 v[160:163], v[208:211], v[220:223], v[160:163]\n\t"
    "ds_read_b64_tr_b16 v[244:245], v249 offset:24576\n\t"
    "v_mfma_f32_16x16x32_bf16 v[164:167], v[212:215], v[220:223], v[164:167]\n\t"
    "ds_read_b64_tr_b16 v[246:247], v249 offset:28672\n\t"
    "s_waitcnt lgkmcnt(7)\n\t"
    "v_mfma_f32_16x16x32_bf16 v[168:171], v[208:211], v[224:227], v[168:171]\n\t"
    "ds_read_b128 v[0:3], v250 offset:2048\n\t"
    "v_mfma_f32_16x16x32_bf16 v[172:175], v[212:215], v[224:227], v[172:175]\n\t"
    "ds_read_b128 v[4:7], v250 offset:3072\n\t"
    "v_mfma_f32_16x16x32_bf16 v[176:179], v[208:211], v[228:231], v[176:179]\n\t"
    "ds_read_b128 v[8:11], v250 offset:6144\n\t"
    "v_mfma_f32_16x16x32_bf16 v[180:183], v[212:215], v[228:231], v[180:183]\n\t"
    "ds_read_b128 v[12:15], v250 offset:7168\n\t"
    "s_waitcnt lgkmcnt(9)\n\t"
    "v_mfma_f32_16x16x32_bf16 v[184:187], v[208:211], v[232:235], v[184:187]\n\t"
    "ds_read_b128 v[16:19], v250 offset:10240\n\t"
    "v_mfma_f32_16x16x32_bf16 v[188:191], v[212:215], v[232:235], v[188:191]\n\t"
    "ds_read_b128 v[20:23], v250 offset:11264\n\t"
    "v_mfma_f32_16x16x32_bf16 v[192:195], v[208:211], v[236:239], v[192:195]\n\t"
    "ds_read_b128 v[24:27], v250 offset:14336\n\t"
    "v_mfma_f32_16x16x32_bf16 v[196:199], v[212:215], v[236:239], v[196:199]\n\t"
    "s_waitcnt lgkmcnt(11)\n\t"
    "v_mfma_f32_16x16x32_bf16 v[200:203], v[208:211], v[240:243], v[200:203]\n\t"
    "v_mfma_f32_16x16x32_bf16 v[204:207], v[212:215], v[240:243], v[204:207]\n\t"
    "s_waitcnt lgkmcnt(5)\n\t"
    "v_mfma_f32_16x16x32_bf16 v[152:155], v[28:31], v[0:3], v[152:155]\n\t"
    "v_mfma_f32_16x16x32_bf16 v[156:159], v[244:247], v[0:3], v[156:159]\n\t"
    "v_mfma_f32_16x16x32_bf16 v[160:163], v[28:31], v[4:7], v[160:163]\n\t"
    "v_mfma_f32_16x16x32_bf16 v[164:167], v[244:247], v[4:7], v[164:167]\n\t"
    "s_waitcnt lgkmcnt(3)\n\t"
    "v_mfma_f32_16x16x32_bf16 v[168:171], v[28:31], v[8:11], v[168:171]\n\t"
    "v_mfma_f32_16x16x32_bf16 v[172:175], v[244:247], v[8:11], v[172:175]\n\t"
    "v_mfma_f32_16x16x32_bf16 v[176:179], v[28:31], v[12:15], v[176:179]\n\t"
    "s_waitcnt lgkmcnt(0)\n\t"
    "s_barrier\n\t"
    "v_add_u32 v248, 0xffff0000, v248\n\t"
    "v_add_u32 v249, 0xffff0000, v249\n\t"
    "ds_read_b64_tr_b16 v[208:209], v248 offset:49152\n\t"
    "ds_read_b64_tr_b16 v[210:211], v248 offset:53248\n\t"
    "ds_read_b64_tr_b16 v[212:213], v249 offset:49152\n\t"
    "ds_read_b64_tr_b16 v[214:215], v249 offset:53248\n\t"
    "v_add_u32 v250, 0x10000, v250\n\t"
    "v_mfma_f32_16x16x32_bf16 v[180:183], v[244:247], v[12:15], v[180:183]\n\t"
    "ds_read_b128 v[216:219], v250 offset:16384\n\t"
    "v_mfma_f32_16x16x32_bf16 v[184:187], v[28:31], v[16:19], v[184:187]\n\t"
    "ds_read_b128 v[220:223], v250 offset:17408\n\t"
    "v_mfma_f32_16x16x32_bf16 v[188:191], v[244:247], v[16:19], v[188:191]\n\t"
    "ds_read_b128 v[224:227], v250 offset:20480\n\t"
    "v_mfma_f32_16x16x32_bf16 v[192:195], v[28:31], v[20:23], v[192:195]\n\t"
    "ds_read_b128 v[228:231], v250 offset:21504\n\t"
    "v_mfma_f32_16x16x32_bf16 v[196:199], v[244:247], v[20:23], v[196:199]\n\t"
    "ds_read_b128 v[232:235], v250 offset:24576\n\t"
    "v_mfma_f32_16x16x32_bf16 v[200:203], v[28:31], v[24:27], v[200:203]\n\t"
    "ds_read_b128 v[236:239], v250 offset:25600\n\t"
    "v_mfma_f32_16x16x32_bf16 v[204:207], v[244:247], v[24:27], v[204:207]\n\t"
    "ds_read_b128 v[240:243], v250 offset:28672\n\t"
    "s_waitcnt lgkmcnt(5)\n\t"
    "v_mfma_f32_16x16x32_bf16 v[152:155], v[208:211], v[216:219], v[152:155]\n\t"
    "ds_read_b64_tr_b16 v[28:29], v248 offset:57344\n\t"
    "v_mfma_f32_16x16x32_bf16 v[156:159], v[212:215], v[216:219], v[156:159]\n\t"
    "ds_read_b64_tr_b16 v[30:31], v248 offset:61440\n\t"
    "v_mfma_f32_16x16x32_bf16 v[160:163], v[208:211], v[220:223], v[160:163]\n\t"
    "ds_read_b64_tr_b16 v[244:245], v249 offset:57344\n\t"
    "v_mfma_f32_16x16x32_bf16 v[164:167], v[212:215], v[220:223], v[164:167]\n\t"
    "ds_read_b64_tr_b16 v[246:247], v249 offset:61440\n\t"
    "s_waitcnt lgkmcnt(7)\n\t"
    "v_mfma_f32_16x16x32_bf16 v[168:171], v[208:211], v[224:227], v[168:171]\n\t"
    "ds_read_b128 v[0:3], v250 offset:18432\n\t"
    "v_mfma_f32_16x16x32_bf16 v[172:175], v[212:215], v[224:227], v[172:175]\n\t"
    "ds_read_b128 v[4:7], v250 offset:19456\n\t"
    "v_mfma_f32_16x16x32_bf16 v[176:179], v[208:211], v[228:231], v[176:179]\n\t"
    "ds_read_b128 v[8:11], v250 offset:22528\n\t"
    "v_mfma_f32_16x16x32_bf16 v[180:183], v[212:215], v[228:231], v[180:183]\n\t"
    "ds_read_b128 v[12:15], v250 offset:23552\n\t"
    "s_waitcnt lgkmcnt(9)\n\t"
    "v_mfma_f32_16x16x32_bf16 v[184:187], v[208:211], v[232:235], v[184:187]\n\t"
    "ds_read_b128 v[16:19], v250 offset:26624\n\t"
    "v_mfma_f32_16x16x32_bf16 v[188:191], v[212:215], v[232:235], v[188:191]\n\t"
    "ds_read_b128 v[20:23], v250 offset:27648\n\t"
    "v_mfma_f32_16x16x32_bf16 v[192:195], v[208:211], v[236:239], v[192:195]\n\t"
    "ds_read_b128 v[24:27], v250 offset:30720\n\t"
    "v_mfma_f32_16x16x32_bf16 v[196:199], v[212:215], v[236:239], v[196:199]\n\t"
    "s_waitcnt lgkmcnt(11)\n\t"
    "v_mfma_f32_16x16x32_bf16 v[200:203], v[208:211], v[240:243], v[200:203]\n\t"
    "v_mfma_f32_16x16x32_bf16 v[204:207], v[212:215], v[240:243], v[204:207]\n\t"
    "s_waitcnt lgkmcnt(5)\n\t"
    "v_mfma_f32_16x16x32_bf16 v[152:155], v[28:31], v[0:3], v[152:155]\n\t"
    "v_add_u32 v248, 0x10000, v248\n\t"
    "v_mfma_f32_16x16x32_bf16 v[156:159], v[244:247], v[0:3], v[156:159]\n\t"
    "v_add_u32 v249, 0x10000, v249\n\t"
    "v_mfma_f32_16x16x32_bf16 v[160:163], v[28:31], v[4:7], v[160:163]\n\t"
    "ds_read_b64_tr_b16 v[208:209], v248 offset:0\n\t"
    "v_mfma_f32_16x16x32_bf16 v[164:167], v[244:247], v[4:7], v[164:167]\n\t"
    "ds_read_b64_tr_b16 v[210:211], v248 offset:4096\n\t"
    "s_waitcnt lgkmcnt(5)\n\t"
    "v_mfma_f32_16x16x32_bf16 v[168:171], v[28:31], v[8:11], v[168:171]\n\t"
    "ds_read_b64_tr_b16 v[212:213], v249 offset:0\n\t"
    "v_mfma_f32_16x16x32_bf16 v[172:175], v[244:247], v[8:11], v[172:175]\n\t"
    "ds_read_b64_tr_b16 v[214:215], v249 offset:4096\n\t"
    "v_mfma_f32_16x16x32_bf16 v[176:179], v[28:31], v[12:15], v[176:179]\n\t"
    "ds_read_b128 v[216:219], v250 offset:32768\n\t"
    "v_mfma_f32_16x16x32_bf16 v[180:183], v[244:247], v[12:15], v[180:183]\n\t"
    "ds_read_b128 v[220:223], v250 offset:33792\n\t"
    "s_waitcnt lgkmcnt(7)\n\t"
    "v_mfma_f32_16x16x32_bf16 v[184:187], v[28:31], v[16:19], v[184:187]\n\t"
    "ds_read_b128 v[224:227], v250 offset:36864\n\t"
    "v_mfma_f32_16x16x32_bf16 v[188:191], v[244:247], v[16:19], v[188:191]\n\t"
    "ds_read_b128 v[228:231], v250 offset:37888\n\t"
    "v_mfma_f32_16x16x32_bf16 v[192:195], v[28:31], v[20:23], v[192:195]\n\t"
    "ds_read_b128 v[232:235], v250 offset:40960\n\t"
    "v_mfma_f32_16x16x32_bf16 v[196:199], v[244:247], v[20:23], v[196:199]\n\t"
    "ds_read_b128 v[236:239], v250 offset:41984\n\t"
    "s_waitcnt lgkmcnt(10)\n\t"
    "v_mfma_f32_16x16x32_bf16 v[200:203], v[28:31], v[24:27], v[200:203]\n\t"
    "ds_read_b128 v[240:243], v250 offset:45056\n\t"
    "v_mfma_f32_16x16x32_bf16 v[204:207], v[244:247], v[24:27], v[204:207]\n\t"
    "s_waitcnt lgkmcnt(5)\n\t"
    "v_mfma_f32_16x16x32_bf16 v[152:155], v[208:211], v[216:219], v[152:155]\n\t"
    "ds_read_b64_tr_b16 v[28:29], v248 offset:8192\n\t"
    "v_mfma_f32_16x16x32_bf16 v[156:159], v[212:215], v[216:219], v[156:159]\n\t"
    "ds_read_b64_tr_b16 v[30:31], v248 offset:12288\n\t"
    "v_mfma_f32_16x16x32_bf16 v[160:163], v[208:211], v[220:223], v[160:163]\n\t"
    "ds_read_b64_tr_b16 v[244:245], v249 offset:8192\n\t"
    "v_mfma_f32_16x16x32_bf16 v[164:167], v[212:215], v[220:223], v[164:167]\n\t"
    "ds_read_b64_tr_b16 v[246:247], v249 offset:12288\n\t"
    "s_waitcnt lgkmcnt(7)\n\t"
    "v_mfma_f32_16x16x32_bf16 v[168:171], v[208:211], v[224:227], v[168:171]\n\t"
    "ds_read_b128 v[0:3], v250 offset:34816\n\t"
    "v_mfma_f32_16x16x32_bf16 v[172:175], v[212:215], v[224:227], v[172:175]\n\t"
    "ds_read_b128 v[4:7], v250 offset:35840\n\t"
    "v_mfma_f32_16x16x32_bf16 v[176:179], v[208:211], v[228:231], v[176:179]\n\t"
    "ds_read_b128 v[8:11], v250 offset:38912\n\t"
    "v_mfma_f32_16x16x32_bf16 v[180:183], v[212:215], v[228:231], v[180:183]\n\t"
    "ds_read_b128 v[12:15], v250 offset:39936\n\t"
    "s_waitcnt lgkmcnt(9)\n\t"
    "v_mfma_f32_16x16x32_bf16 v[184:187], v[208:211], v[232:235], v[184:187]\n\t"
    "ds_read_b128 v[16:19], v250 offset:43008\n\t"
    "v_mfma_f32_16x16x32_bf16 v[188:191], v[212:215], v[232:235], v[188:191]\n\t"
    "ds_read_b128 v[20:23], v250 offset:44032\n\t"
    "v_mfma_f32_16x16x32_bf16 v[192:195], v[208:211], v[236:239], v[192:195]\n\t"
    "ds_read_b128 v[24:27], v250 offset:47104\n\t"
    "v_mfma_f32_16x16x32_bf16 v[196:199], v[212:215], v[236:239], v[196:199]\n\t"
    "s_waitcnt lgkmcnt(11)\n\t"
    "v_mfma_f32_16x16x32_bf16 v[200:203], v[208:211], v[240:243], v[200:203]\n\t"
    "v_mfma_f32_16x16x32_bf16 v[204:207], v[212:215], v[240:243], v[204:207]\n\t"
    "s_lshl_b32 s55, s50, 3\n\t"
    "s_add_u32 s55, s55, %[f1base]\n\t"
    "buffer_load_dwordx2 v[150:151], v145, %[srdws], s55 offen sc1\n\t"
    "s_waitcnt lgkmcnt(5)\n\t"
    "v_mfma_f32_16x16x32_bf16 v[152:155], v[28:31], v[0:3], v[152:155]\n\t"
    "v_mfma_f32_16x16x32_bf16 v[156:159], v[244:247], v[0:3], v[156:159]\n\t"
    "v_mfma_f32_16x16x32_bf16 v[160:163], v[28:31], v[4:7], v[160:163]\n\t"
    "v_mfma_f32_16x16x32_bf16 v[164:167], v[244:247], v[4:7], v[164:167]\n\t"
    "s_waitcnt lgkmcnt(3)\n\t"
    "v_mfma_f32_16x16x32_bf16 v[168:171], v[28:31], v[8:11], v[168:171]\n\t"
    "v_mfma_f32_16x16x32_bf16 v[172:175], v[244:247], v[8:11], v[172:175]\n\t"
    "v_mfma_f32_16x16x32_bf16 v[176:179], v[28:31], v[12:15], v[176:179]\n\t"
    "s_waitcnt lgkmcnt(0)\n\t"
    "s_barrier\n\t"
    "v_mfma_f32_16x16x32_bf16 v[180:183], v[244:247], v[12:15], v[180:183]\n\t"
    "v_mfma_f32_16x16x32_bf16 v[184:187], v[28:31], v[16:19], v[184:187]\n\t"
    "v_mfma_f32_16x16x32_bf16 v[188:191], v[244:247], v[16:19], v[188:191]\n\t"
    "v_mfma_f32_16x16x32_bf16 v[192:195], v[28:31], v[20:23], v[192:195]\n\t"
    "v_mfma_f32_16x16x32_bf16 v[196:199], v[244:247], v[20:23], v[196:199]\n\t"
    "v_mfma_f32_16x16x32_bf16 v[200:203], v[28:31], v[24:27], v[200:203]\n\t"
    "v_mfma_f32_16x16x32_bf16 v[204:207], v[244:247], v[24:27], v[204:207]\n\t"
    "s_lshl_b32 s55, s50, 3\n\t"
    "s_add_u32 s55, s55, %[f1base]\n\t"
    "s_mov_b32 s54, 2000000\n\t"
    "s_waitcnt vmcnt(0)\n\t"
    "v_cmp_ne_u64 vcc, s[44:45], v[150:151]\n\t"
    "s_nop 3\n\t"
    "s_and_b64 vcc, vcc, 15\n\t"
    "s_cbranch_scc0 PA_done%=\n\t"
    "PA_loop%=:\n\t"
    "buffer_load_dwordx2 v[150:151], v145, %[srdws], s55 offen sc1\n\t"
    "s_sub_u32 s54, s54, 1\n\t"
    "s_cbranch_scc1 PA_tmo%=\n\t"
    "s_waitcnt vmcnt(0)\n\t"
    "v_cmp_ne_u64 vcc, s[44:45], v[150:151]\n\t"
    "s_nop 3\n\t"
    "s_and_b64 vcc, vcc, 15\n\t"
    "s_cbranch_scc1 PA_loop%=\n\t"
    "PA_tmo%=:\n\t"
    "s_waitcnt vmcnt(0)\n\t"
    "PA_done%=:\n\t"
    "s_add_u32 m0, s47, 0x10000\n\t"
    "s_xor_b32 s46, s42, 0x20000\n\t"
    "s_lshr_b32 s46, s46, 2\n\t"
    "s_add_u32 s46, s46, %[obase]\n\t"
    "buffer_load_dwordx4 v142, %[srdws], s46 offen sc1 lds\n\t"
    "buffer_load_dwordx4 v143, %[srdws], s46 offen offset:1024 sc1 lds\n\t"
    "buffer_load_dwordx4 v142, %[srdws], s46 offen offset:2048 sc1 lds\n\t"
    "buffer_load_dwordx4 v143, %[srdws], s46 offen offset:3072 sc1 lds\n\t"
    "s_add_u32 m0, s47, 0x0\n\t"
    "s_xor_b32 s46, s42, 0x30000\n\t"
    "s_lshr_b32 s46, s46, 2\n\t"
    "s_add_u32 s46, s46, %[obase]\n\t"
    "buffer_load_dwordx4 v142, %[srdws], s46 offen sc1 lds\n\t"
    "buffer_load_dwordx4 v143, %[srdws], s46 offen offset:1024 sc1 lds\n\t"
    "buffer_load_dwordx4 v142, %[srdws], s46 offen offset:2048 sc1 lds\n\t"
    "buffer_load_dwordx4 v143, %[srdws], s46 offen offset:3072 sc1 lds\n\t"
    "s_add_u32 m0, s47, 0x1c000\n\t"
    "s_xor_b32 s46, s42, 0x40000\n\t"
    "s_lshr_b32 s46, s46, 2\n\t"
    "s_add_u32 s46, s46, %[obase]\n\t"
    "buffer_load_dwordx4 v142, %[srdws], s46 offen sc1 lds\n\t"
    "buffer_load_dwordx4 v143, %[srdws], s46 offen offset:1024 sc1 lds\n\t"
    "buffer_load_dwordx4 v142, %[srdws], s46 offen offset:2048 sc1 lds\n\t"
    "buffer_load_dwordx4 v143, %[srdws], s46 offen offset:3072 sc1 lds\n\t"
    "s_waitcnt vmcnt(4)\n\t"
    "s_barrier\n\t"
    "v_add_u32 v248, 0xfffe0000, v248\n\t"
    "v_add_u32 v249, 0xfffe0000, v249\n\t"
    "ds_read_b64_tr_b16 v[208:209], v248 offset:32768\n\t"
    "ds_read_b64_tr_b16 v[210:211], v248 offset:36864\n\t"
    "ds_read_b64_tr_b16 v[212:213], v249 offset:32768\n\t"
    "ds_read_b64_tr_b16 v[214:215], v249 offset:36864\n\t"
    "ds_read_b128 v[216:219], v250 offset:0\n\t"
    "ds_read_b128 v[220:223], v250 offset:1024\n\t"
    "ds_read_b128 v[224:227], v250 offset:4096\n\t"
    "ds_read_b128 v[228:231], v250 offset:5120\n\t"
    "ds_read_b128 v[232:235], v250 offset:8192\n\t"
    "ds_read_b128 v[236:239], v250 offset:9216\n\t"
    "ds_read_b128 v[240:243], v250 offset:12288\n\t"
    "s_waitcnt lgkmcnt(5)\n\t"
    "v_mfma_f32_16x16x32_bf16 v[152:155], v[208:211], v[216:219], v[152:155]\n\t"
    "ds_read_b64_tr_b16 v[28:29], v248 offset:40960\n\t"
    "v_mfma_f32_16x16x32_bf16 v[156:159], v[212:215], v[216:219], v[156:159]\n\t"
    "ds_read_b64_tr_b16 v[30:31], v248 offset:45056\n\t"
    "v_mfma_f32_16x16x32_bf16 v[160:163], v[208:211], v[220:223], v[160:163]\n\t"
    "ds_read_b64_tr_b16 v[244:245], v249 offset:40960\n\t"
    "v_mfma_f32_16x16x32_bf16 v[164:167], v[212:215], v[220:223], v[164:167]\n\t"
    "ds_read_b64_tr_b16 v[246:247], v249 offset:45056\n\t"
    "s_waitcnt lgkmcnt(7)\n\t"
    "v_mfma_f32_16x16x32_bf16 v[168:171], v[208:211], v[224:227], v[168:171]\n\t"
    "ds_read_b128 v[0:3], v250 offset:2048\n\t"
    "v_mfma_f32_16x16x32_bf16 v[172:175], v[212:215], v[224:227], v[172:175]\n\t"
    "ds_read_b128 v[4:7], v250 offset:3072\n\t"
    "v_mfma_f32_16x16x32_bf16 v[176:179], v[208:211], v[228:231], v[176:179]\n\t"
    "ds_read_b128 v[8:11], v250 offset:6144\n\t"
    "v_mfma_f32_16x16x32_bf16 v[180:183], v[212:215], v[228:231], v[180:183]\n\t"
    "ds_read_b128 v[12:15], v250 offset:7168\n\t"
    "s_waitcnt lgkmcnt(9)\n\t"
    "v_mfma_f32_16x16x32_bf16 v[184:187], v[208:211], v[232:235], v[184:187]\n\t"
    "ds_read_b128 v[16:19], v250 offset:10240\n\t"
    "v_mfma_f32_16x16x32_bf16 v[188:191], v[212:215], v[232:235], v[188:191]\n\t"
    "ds_read_b128 v[20:23], v250 offset:11264\n\t"
    "v_mfma_f32_16x16x32_bf16 v[192:195], v[208:211], v[236:239], v[192:195]\n\t"
    "ds_read_b128 v[24:27], v250 offset:14336\n\t"
    "v_mfma_f32_16x16x32_bf16 v[196:199], v[212:215], v[236:239], v[196:199]\n\t"
    "s_waitcnt lgkmcnt(11)\n\t"
    "v_mfma_f32_16x16x32_bf16 v[200:203], v[208:211], v[240:243], v[200:203]\n\t"
    "v_mfma_f32_16x16x32_bf16 v[204:207], v[212:215], v[240:243], v[204:207]\n\t"
    "s_waitcnt lgkmcnt(5)\n\t"
    "v_mfma_f32_16x16x32_bf16 v[152:155], v[28:31], v[0:3], v[152:155]\n\t"
    "ds_read_b64_tr_b16 v[208:209], v248 offset:16384\n\t"
    "v_mfma_f32_16x16x32_bf16 v[156:159], v[244:247], v[0:3], v[156:159]\n\t"
    "ds_read_b64_tr_b16 v[210:211], v248 offset:20480\n\t"
    "v_mfma_f32_16x16x32_bf16 v[160:163], v[28:31], v[4:7], v[160:163]\n\t"
    "ds_read_b64_tr_b16 v[212:213], v249 offset:16384\n\t"
    "v_mfma_f32_16x16x32_bf16 v[164:167], v[244:247], v[4:7], v[164:167]\n\t"
    "ds_read_b64_tr_b16 v[214:215], v249 offset:20480\n\t"
    "s_waitcnt lgkmcnt(7)\n\t"
    "v_mfma_f32_16x16x32_bf16 v[168:171], v[28:31], v[8:11], v[168:171]\n\t"
    "v_add_u32 v250, 0xffff0000, v250\n\t"
    "v_mfma_f32_16x16x32_bf16 v[172:175], v[244:247], v[8:11], v[172:175]\n\t"
    "ds_read_b128 v[216:219], v250 offset:0\n\t"
    "v_mfma_f32_16x16x32_bf16 v[176:179], v[28:31], v[12:15], v[176:179]\n\t"
    "ds_read_b128 v[220:223], v250 offset:1024\n\t"
    "v_mfma_f32_16x16x32_bf16 v[180:183], v[244:247], v[12:15], v[180:183]\n\t"
    "ds_read_b128 v[224:227], v250 offset:4096\n\t"
    "s_waitcnt lgkmcnt(8)\n\t"
    "v_mfma_f32_16x16x32_bf16 v[184:187], v[28:31], v[16:19], v[184:187]\n\t"
    "ds_read_b128 v[228:231], v250 offset:5120\n\t"
    "v_mfma_f32_16x16x32_bf16 v[188:191], v[244:247], v[16:19], v[188:191]\n\t"
    "ds_read_b128 v[232:235], v250 offset:8192\n\t"
    "v_mfma_f32_16x16x32_bf16 v[192:195], v[28:31], v[20:23], v[192:195]\n\t"
    "ds_read_b128 v[236:239], v250 offset:9216\n\t"
    "v_mfma_f32_16x16x32_bf16 v[196:199], v[244:247], v[20:23], v[196:199]\n\t"
    "ds_read_b128 v[240:243], v250 offset:12288\n\t"
    "s_waitcnt lgkmcnt(11)\n\t"
    "v_mfma_f32_16x16x32_bf16 v[200:203], v[28:31], v[24:27], v[200:203]\n\t"
    "v_mfma_f32_16x16x32_bf16 v[204:207], v[244:247], v[24:27], v[204:207]\n\t"
    "s_waitcnt lgkmcnt(5)\n\t"
    "v_mfma_f32_16x16x32_bf16 v[152:155], v[208:211], v[216:219], v[152:155]\n\t"
    "ds_read_b64_tr_b16 v[28:29], v248 offset:24576\n\t"
    "v_mfma_f32_16x16x32_bf16 v[156:159], v[212:215], v[216:219], v[156:159]\n\t"
    "ds_read_b64_tr_b16 v[30:31], v248 offset:28672\n\t"
    "v_mfma_f32_16x16x32_bf16 v[160:163], v[208:211], v[220:223], v[160:163]\n\t"
    "ds_read_b64_tr_b16 v[244:245], v249 offset:24576\n\t"
    "v_mfma_f32_16x16x32_bf16 v[164:167], v[212:215], v[220:223], v[164:167]\n\t"
    "ds_read_b64_tr_b16 v[246:247], v249 offset:28672\n\t"
    "s_waitcnt lgkmcnt(7)\n\t"
    "v_mfma_f32_16x16x32_bf16 v[168:171], v[208:211], v[224:227], v[168:171]\n\t"
    "ds_read_b128 v[0:3], v250 offset:2048\n\t"
    "v_mfma_f32_16x16x32_bf16 v[172:175], v[212:215], v[224:227], v[172:175]\n\t"
    "ds_read_b128 v[4:7], v250 offset:3072\n\t"
    "v_mfma_f32_16x16x32_bf16 v[176:179], v[208:211], v[228:231], v[176:179]\n\t"
    "ds_read_b128 v[8:11], v250 offset:6144\n\t"
    "v_mfma_f32_16x16x32_bf16 v[180:183], v[212:215], v[228:231], v[180:183]\n\t"
    "ds_read_b128 v[12:15], v250 offset:7168\n\t"
    "s_waitcnt lgkmcnt(9)\n\t"
    "v_mfma_f32_16x16x32_bf16 v[184:187], v[208:211], v[232:235], v[184:187]\n\t"
    "ds_read_b128 v[16:19], v250 offset:10240\n\t"
    "v_mfma_f32_16x16x32_bf16 v[188:191], v[212:215], v[232:235], v[188:191]\n\t"
    "ds_read_b128 v[20:23], v250 offset:11264\n\t"
    "v_mfma_f32_16x16x32_bf16 v[192:195], v[208:211], v[236:239], v[192:195]\n\t"
    "ds_read_b128 v[24:27], v250 offset:14336\n\t"
    "v_mfma_f32_16x16x32_bf16 v[196:199], v[212:215], v[236:239], v[196:199]\n\t"
    "s_waitcnt lgkmcnt(11)\n\t"
    "v_mfma_f32_16x16x32_bf16 v[200:203], v[208:211], v[240:243], v[200:203]\n\t"
    "v_mfma_f32_16x16x32_bf16 v[204:207], v[212:215], v[240:243], v[204:207]\n\t"
    "s_waitcnt lgkmcnt(5)\n\t"
    "v_mfma_f32_16x16x32_bf16 v[152:155], v[28:31], v[0:3], v[152:155]\n\t"
    "v_mfma_f32_16x16x32_bf16 v[156:159], v[244:247], v[0:3], v[156:159]\n\t"
    "v_mfma_f32_16x16x32_bf16 v[160:163], v[28:31], v[4:7], v[160:163]\n\t"
    "v_mfma_f32_16x16x32_bf16 v[164:167], v[244:247], v[4:7], v[164:167]\n\t"
    "s_waitcnt lgkmcnt(3)\n\t"
    "v_mfma_f32_16x16x32_bf16 v[168:171], v[28:31], v[8:11], v[168:171]\n\t"
    "v_mfma_f32_16x16x32_bf16 v[172:175], v[244:247], v[8:11], v[172:175]\n\t"
    "v_mfma_f32_16x16x32_bf16 v[176:179], v[28:31], v[12:15], v[176:179]\n\t"
    "s_waitcnt vmcnt(0)\n\t"
    "s_waitcnt lgkmcnt(0)\n\t"
    "s_barrier\n\t"
    "v_add_u32 v248, 0x20000, v248\n\t"
    "v_add_u32 v249, 0x20000, v249\n\t"
    "ds_read_b64_tr_b16 v[208:209], v248 offset:16384\n\t"
    "ds_read_b64_tr_b16 v[210:211], v248 offset:20480\n\t"
    "ds_read_b64_tr_b16 v[212:213], v249 offset:16384\n\t"
    "ds_read_b64_tr_b16 v[214:215], v249 offset:20480\n\t"
    "v_add_u32 v250, 0x10000, v250\n\t"
    "v_mfma_f32_16x16x32_bf16 v[180:183], v[244:247], v[12:15], v[180:183]\n\t"
    "ds_read_b128 v[216:219], v250 offset:49152\n\t"
    "v_mfma_f32_16x16x32_bf16 v[184:187], v[28:31], v[16:19], v[184:187]\n\t"
    "ds_read_b128 v[220:223], v250 offset:50176\n\t"
    "v_mfma_f32_16x16x32_bf16 v[188:191], v[244:247], v[16:19], v[188:191]\n\t"
    "ds_read_b128 v[224:227], v250 offset:53248\n\t"
    "v_mfma_f32_16x16x32_bf16 v[192:195], v[28:31], v[20:23], v[192:195]\n\t"
    "ds_read_b128 v[228:231], v250 offset:54272\n\t"
    "v_mfma_f32_16x16x32_bf16 v[196:199], v[244:247], v[20:23], v[196:199]\n\t"
    "ds_read_b128 v[232:235], v250 offset:57344\n\t"
    "v_mfma_f32_16x16x32_bf16 v[200:203], v[28:31], v[24:27], v[200:203]\n\t"
    "ds_read_b128 v[236:239], v250 offset:58368\n\t"
    "v_mfma_f32_16x16x32_bf16 v[204:207], v[244:247], v[24:27], v[204:207]\n\t"
    "ds_read_b128 v[240:243], v250 offset:61440\n\t"
    "s_waitcnt lgkmcnt(5)\n\t"
    "v_mfma_f32_16x16x32_bf16 v[152:155], v[208:211], v[216:219], v[152:155]\n\t"
    "ds_read_b64_tr_b16 v[28:29], v248 offset:24576\n\t"
    "v_mfma_f32_16x16x32_bf16 v[156:159], v[212:215], v[216:219], v[156:159]\n\t"
    "ds_read_b64_tr_b16 v[30:31], v248 offset:28672\n\t"
    "v_mfma_f32_16x16x32_bf16 v[160:163], v[208:211], v[220:223], v[160:163]\n\t"
    "ds_read_b64_tr_b16 v[244:245], v249 offset:24576\n\t"
    "v_mfma_f32_16x16x32_bf16 v[164:167], v[212:215], v[220:223], v[164:167]\n\t"
    "ds_read_b64_tr_b16 v[246:247], v249 offset:28672\n\t"
    "s_waitcnt lgkmcnt(7)\n\t"
    "v_mfma_f32_16x16x32_bf16 v[168:171], v[208:211], v[224:227], v[168:171]\n\t"
    "ds_read_b128 v[0:3], v250 offset:51200\n\t"
    "v_mfma_f32_16x16x32_bf16 v[172:175], v[212:215], v[224:227], v[172:175]\n\t"
    "ds_read_b128 v[4:7], v250 offset:52224\n\t"
    "v_mfma_f32_16x16x32_bf16 v[176:179], v[208:211], v[228:231], v[176:179]\n\t"
    "ds_read_b128 v[8:11], v250 offset:55296\n\t"
    "v_mfma_f32_16x16x32_bf16 v[180:183], v[212:215], v[228:231], v[180:183]\n\t"
    "ds_read_b128 v[12:15], v250 offset:56320\n\t"
    "s_waitcnt lgkmcnt(9)\n\t"
    "v_mfma_f32_16x16x32_bf16 v[184:187], v[208:211], v[232:235], v[184:187]\n\t"
    "ds_read_b128 v[16:19], v250 offset:59392\n\t"
    "v_mfma_f32_16x16x32_bf16 v[188:191], v[212:215], v[232:235], v[188:191]\n\t"
    "ds_read_b128 v[20:23], v250 offset:60416\n\t"
    "v_mfma_f32_16x16x32_bf16 v[192:195], v[208:211], v[236:239], v[192:195]\n\t"
    "ds_read_b128 v[24:27], v250 offset:63488\n\t"
    "v_mfma_f32_16x16x32_bf16 v[196:199], v[212:215], v[236:239], v[196:199]\n\t"
    "s_waitcnt lgkmcnt(11)\n\t"
    "v_mfma_f32_16x16x32_bf16 v[200:203], v[208:211], v[240:243], v[200:203]\n\t"
    "v_mfma_f32_16x16x32_bf16 v[204:207], v[212:215], v[240:243], v[204:207]\n\t"
    "s_waitcnt lgkmcnt(5)\n\t"
    "v_mfma_f32_16x16x32_bf16 v[152:155], v[28:31], v[0:3], v[152:155]\n\t"
    "v_add_u32 v248, 0xfffe0000, v248\n\t"
    "v_mfma_f32_16x16x32_bf16 v[156:159], v[244:247], v[0:3], v[156:159]\n\t"
    "v_add_u32 v249, 0xfffe0000, v249\n\t"
    "v_mfma_f32_16x16x32_bf16 v[160:163], v[28:31], v[4:7], v[160:163]\n\t"
    "ds_read_b64_tr_b16 v[208:209], v248 offset:49152\n\t"
    "v_mfma_f32_16x16x32_bf16 v[164:167], v[244:247], v[4:7], v[164:167]\n\t"
    "ds_read_b64_tr_b16 v[210:211], v248 offset:53248\n\t"
    "s_waitcnt lgkmcnt(5)\n\t"
    "v_mfma_f32_16x16x32_bf16 v[168:171], v[28:31], v[8:11], v[168:171]\n\t"
    "ds_read_b64_tr_b16 v[212:213], v249 offset:49152\n\t"
    "v_mfma_f32_16x16x32_bf16 v[172:175], v[244:247], v[8:11], v[172:175]\n\t"
    "ds_read_b64_tr_b16 v[214:215], v249 offset:53248\n\t"
    "v_mfma_f32_16x16x32_bf16 v[176:179], v[28:31], v[12:15], v[176:179]\n\t"
    "ds_read_b128 v[216:219], v250 offset:16384\n\t"
    "v_mfma_f32_16x16x32_bf16 v[180:183], v[244:247], v[12:15], v[180:183]\n\t"
    "ds_read_b128 v[220:223], v250 offset:17408\n\t"
    "s_waitcnt lgkmcnt(7)\n\t"
    "v_mfma_f32_16x16x32_bf16 v[184:187], v[28:31], v[16:19], v[184:187]\n\t"
    "ds_read_b128 v[224:227], v250 offset:20480\n\t"
    "v_mfma_f32_16x16x32_bf16 v[188:191], v[244:247], v[16:19], v[188:191]\n\t"
    "ds_read_b128 v[228:231], v250 offset:21504\n\t"
    "v_mfma_f32_16x16x32_bf16 v[192:195], v[28:31], v[20:23], v[192:195]\n\t"
    "ds_read_b128 v[232:235], v250 offset:24576\n\t"
    "v_mfma_f32_16x16x32_bf16 v[196:199], v[244:247], v[20:23], v[196:199]\n\t"
    "ds_read_b128 v[236:239], v250 offset:25600\n\t"
    "s_waitcnt lgkmcnt(10)\n\t"
    "v_mfma_f32_16x16x32_bf16 v[200:203], v[28:31], v[24:27], v[200:203]\n\t"
    "ds_read_b128 v[240:243], v250 offset:28672\n\t"
    "v_mfma_f32_16x16x32_bf16 v[204:207], v[244:247], v[24:27], v[204:207]\n\t"
    "s_waitcnt lgkmcnt(5)\n\t"
    "v_mfma_f32_16x16x32_bf16 v[152:155], v[208:211], v[216:219], v[152:155]\n\t"
    "ds_read_b64_tr_b16 v[28:29], v248 offset:57344\n\t"
    "v_mfma_f32_16x16x32_bf16 v[156:159], v[212:215], v[216:219], v[156:159]\n\t"
    "ds_read_b64_tr_b16 v[30:31], v248 offset:61440\n\t"
    "v_mfma_f32_16x16x32_bf16 v[160:163], v[208:211], v[220:223], v[160:163]\n\t"
    "ds_read_b64_tr_b16 v[244:245], v249 offset:57344\n\t"
    "v_mfma_f32_16x16x32_bf16 v[164:167], v[212:215], v[220:223], v[164:167]\n\t"
    "ds_read_b64_tr_b16 v[246:247], v249 offset:61440\n\t"
    "s_waitcnt lgkmcnt(7)\n\t"
    "v_mfma_f32_16x16x32_bf16 v[168:171], v[208:211], v[224:227], v[168:171]\n\t"
    "ds_read_b128 v[0:3], v250 offset:18432\n\t"
    "v_mfma_f32_16x16x32_bf16 v[172:175], v[212:215], v[224:227], v[172:175]\n\t"
    "ds_read_b128 v[4:7], v250 offset:19456\n\t"
    "v_mfma_f32_16x16x32_bf16 v[176:179], v[208:211], v[228:231], v[176:179]\n\t"
    "ds_read_b128 v[8:11], v250 offset:22528\n\t"
    "v_mfma_f32_16x16x32_bf16 v[180:183], v[212:215], v[228:231], v[180:183]\n\t"
    "ds_read_b128 v[12:15], v250 offset:23552\n\t"
    "s_waitcnt lgkmcnt(9)\n\t"
    "v_mfma_f32_16x16x32_bf16 v[184:187], v[208:211], v[232:235], v[184:187]\n\t"
    "ds_read_b128 v[16:19], v250 offset:26624\n\t"
    "v_mfma_f32_16x16x32_bf16 v[188:191], v[212:215], v[232:235], v[188:191]\n\t"
    "ds_read_b128 v[20:23], v250 offset:27648\n\t"
    "v_mfma_f32_16x16x32_bf16 v[192:195], v[208:211], v[236:239], v[192:195]\n\t"
    "ds_read_b128 v[24:27], v250 offset:30720\n\t"
    "v_mfma_f32_16x16x32_bf16 v[196:199], v[212:215], v[236:239], v[196:199]\n\t"
    "s_waitcnt lgkmcnt(11)\n\t"
    "v_mfma_f32_16x16x32_bf16 v[200:203], v[208:211], v[240:243], v[200:203]\n\t"
    "v_mfma_f32_16x16x32_bf16 v[204:207], v[212:215], v[240:243], v[204:207]\n\t"
    "s_waitcnt lgkmcnt(5)\n\t"
    "v_mfma_f32_16x16x32_bf16 v[152:155], v[28:31], v[0:3], v[152:155]\n\t"
    "v_mfma_f32_16x16x32_bf16 v[156:159], v[244:247], v[0:3], v[156:159]\n\t"
    "v_mfma_f32_16x16x32_bf16 v[160:163], v[28:31], v[4:7], v[160:163]\n\t"
    "v_mfma_f32_16x16x32_bf16 v[164:167], v[244:247], v[4:7], v[164:167]\n\t"
    "s_waitcnt lgkmcnt(3)\n\t"
    "v_mfma_f32_16x16x32_bf16 v[168:171], v[28:31], v[8:11], v[168:171]\n\t"
    "v_mfma_f32_16x16x32_bf16 v[172:175], v[244:247], v[8:11], v[172:175]\n\t"
    "v_mfma_f32_16x16x32_bf16 v[176:179], v[28:31], v[12:15], v[176:179]\n\t"
    "s_waitcnt lgkmcnt(0)\n\t"
    "s_barrier\n\t"
    "v_add_u32 v248, 0x20000, v248\n\t"
    "v_add_u32 v249, 0x20000, v249\n\t"
    "ds_read_b64_tr_b16 v[208:209], v248 offset:0\n\t"
    "ds_read_b64_tr_b16 v[210:211], v248 offset:4096\n\t"
    "ds_read_b64_tr_b16 v[212:213], v249 offset:0\n\t"
    "ds_read_b64_tr_b16 v[214:215], v249 offset:4096\n\t"
    "v_mfma_f32_16x16x32_bf16 v[180:183], v[244:247], v[12:15], v[180:183]\n\t"
    "ds_read_b128 v[216:219], v250 offset:32768\n\t"
    "v_mfma_f32_16x16x32_bf16 v[184:187], v[28:31], v[16:19], v[184:187]\n\t"
    "ds_read_b128 v[220:223], v250 offset:33792\n\t"
    "v_mfma_f32_16x16x32_bf16 v[188:191], v[244:247], v[16:19], v[188:191]\n\t"
    "ds_read_b128 v[224:227], v250 offset:36864\n\t"
    "v_mfma_f32_16x16x32_bf16 v[192:195], v[28:31], v[20:23], v[192:195]\n\t"
    "ds_read_b128 v[228:231], v250 offset:37888\n\t"
    "v_mfma_f32_16x16x32_bf16 v[196:199], v[244:247], v[20:23], v[196:199]\n\t"
    "ds_read_b128 v[232:235], v250 offset:40960\n\t"
    "v_mfma_f32_16x16x32_bf16 v[200:203], v[28:31], v[24:27], v[200:203]\n\t"
    "ds_read_b128 v[236:239], v250 offset:41984\n\t"
    "v_mfma_f32_16x16x32_bf16 v[204:207], v[244:247], v[24:27], v[204:207]\n\t"
    "ds_read_b128 v[240:243], v250 offset:45056\n\t"
    "s_waitcnt lgkmcnt(5)\n\t"
    "v_mfma_f32_16x16x32_bf16 v[152:155], v[208:211], v[216:219], v[152:155]\n\t"
    "ds_read_b64_tr_b16 v[28:29], v248 offset:8192\n\t"
    "v_mfma_f32_16x16x32_bf16 v[156:159], v[212:215], v[216:219], v[156:159]\n\t"
    "ds_read_b64_tr_b16 v[30:31], v248 offset:12288\n\t"
    "v_mfma_f32_16x16x32_bf16 v[160:163], v[208:211], v[220:223], v[160:163]\n\t"
    "ds_read_b64_tr_b16 v[244:245], v249 offset:8192\n\t"
    "v_mfma_f32_16x16x32_bf16 v[164:167], v[212:215], v[220:223], v[164:167]\n\t"
    "ds_read_b64_tr_b16 v[246:247], v249 offset:12288\n\t"
    "s_waitcnt lgkmcnt(7)\n\t"
    "v_mfma_f32_16x16x32_bf16 v[168:171], v[208:211], v[224:227], v[168:171]\n\t"
    "ds_read_b128 v[0:3], v250 offset:34816\n\t"
    "v_mfma_f32_16x16x32_bf16 v[172:175], v[212:215], v[224:227], v[172:175]\n\t"
    "ds_read_b128 v[4:7], v250 offset:35840\n\t"
    "v_mfma_f32_16x16x32_bf16 v[176:179], v[208:211], v[228:231], v[176:179]\n\t"
    "ds_read_b128 v[8:11], v250 offset:38912\n\t"
    "v_mfma_f32_16x16x32_bf16 v[180:183], v[212:215], v[228:231], v[180:183]\n\t"
    "ds_read_b128 v[12:15], v250 offset:39936\n\t"
    "s_waitcnt lgkmcnt(9)\n\t"
    "v_mfma_f32_16x16x32_bf16 v[184:187], v[208:211], v[232:235], v[184:187]\n\t"
    "ds_read_b128 v[16:19], v250 offset:43008\n\t"
    "v_mfma_f32_16x16x32_bf16 v[188:191], v[212:215], v[232:235], v[188:191]\n\t"
    "ds_read_b128 v[20:23], v250 offset:44032\n\t"
    "v_mfma_f32_16x16x32_bf16 v[192:195], v[208:211], v[236:239], v[192:195]\n\t"
    "ds_read_b128 v[24:27], v250 offset:47104\n\t"
    "v_mfma_f32_16x16x32_bf16 v[196:199], v[212:215], v[236:239], v[196:199]\n\t"
    "s_waitcnt lgkmcnt(11)\n\t"
    "v_mfma_f32_16x16x32_bf16 v[200:203], v[208:211], v[240:243], v[200:203]\n\t"
    "v_mfma_f32_16x16x32_bf16 v[204:207], v[212:215], v[240:243], v[204:207]\n\t"
    "s_waitcnt lgkmcnt(5)\n\t"
    "v_mfma_f32_16x16x32_bf16 v[152:155], v[28:31], v[0:3], v[152:155]\n\t"
    "v_add_u32 v248, 0xfffe0000, v248\n\t"
    "v_mfma_f32_16x16x32_bf16 v[156:159], v[244:247], v[0:3], v[156:159]\n\t"
    "v_add_u32 v249, 0xfffe0000, v249\n\t"
    "v_mfma_f32_16x16x32_bf16 v[160:163], v[28:31], v[4:7], v[160:163]\n\t"
    "ds_read_b64_tr_b16 v[208:209], v248 offset:16384\n\t"
    "v_mfma_f32_16x16x32_bf16 v[164:167], v[244:247], v[4:7], v[164:167]\n\t"
    "ds_read_b64_tr_b16 v[210:211], v248 offset:20480\n\t"
    "s_waitcnt lgkmcnt(5)\n\t"
    "v_mfma_f32_16x16x32_bf16 v[168:171], v[28:31], v[8:11], v[168:171]\n\t"
    "ds_read_b64_tr_b16 v[212:213], v249 offset:16384\n\t"
    "v_mfma_f32_16x16x32_bf16 v[172:175], v[244:247], v[8:11], v[172:175]\n\t"
    "ds_read_b64_tr_b16 v[214:215], v249 offset:20480\n\t"
    "v_mfma_f32_16x16x32_bf16 v[176:179], v[28:31], v[12:15], v[176:179]\n\t"
    "v_add_u32 v250, 0xffff0000, v250\n\t"
    "v_mfma_f32_16x16x32_bf16 v[180:183], v[244:247], v[12:15], v[180:183]\n\t"
    "ds_read_b128 v[216:219], v250 offset:32768\n\t"
    "s_waitcnt lgkmcnt(6)\n\t"
    "v_mfma_f32_16x16x32_bf16 v[184:187], v[28:31], v[16:19], v[184:187]\n\t"
    "ds_read_b128 v[220:223], v250 offset:33792\n\t"
    "v_mfma_f32_16x16x32_bf16 v[188:191], v[244:247], v[16:19], v[188:191]\n\t"
    "ds_read_b128 v[224:227], v250 offset:36864\n\t"
    "v_mfma_f32_16x16x32_bf16 v[192:195], v[28:31], v[20:23], v[192:195]\n\t"
    "ds_read_b128 v[228:231], v250 offset:37888\n\t"
    "v_mfma_f32_16x16x32_bf16 v[196:199], v[244:247], v[20:23], v[196:199]\n\t"
    "ds_read_b128 v[232:235], v250 offset:40960\n\t"
    "s_waitcnt lgkmcnt(9)\n\t"
    "v_mfma_f32_16x16x32_bf16 v[200:203], v[28:31], v[24:27], v[200:203]\n\t"
    "ds_read_b128 v[236:239], v250 offset:41984\n\t"
    "v_mfma_f32_16x16x32_bf16 v[204:207], v[244:247], v[24:27], v[204:207]\n\t"
    "ds_read_b128 v[240:243], v250 offset:45056\n\t"
    "s_waitcnt lgkmcnt(5)\n\t"
    "v_mfma_f32_16x16x32_bf16 v[152:155], v[208:211], v[216:219], v[152:155]\n\t"
    "ds_read_b64_tr_b16 v[28:29], v248 offset:24576\n\t"
    "v_mfma_f32_16x16x32_bf16 v[156:159], v[212:215], v[216:219], v[156:159]\n\t"
    "ds_read_b64_tr_b16 v[30:31], v248 offset:28672\n\t"
    "v_mfma_f32_16x16x32_bf16 v[160:163], v[208:211], v[220:223], v[160:163]\n\t"
    "ds_read_b64_tr_b16 v[244:245], v249 offset:24576\n\t"
    "v_mfma_f32_16x16x32_bf16 v[164:167], v[212:215], v[220:223], v[164:167]\n\t"
    "ds_read_b64_tr_b16 v[246:247], v249 offset:28672\n\t"
    "s_waitcnt lgkmcnt(7)\n\t"
    "v_mfma_f32_16x16x32_bf16 v[168:171], v[208:211], v[224:227], v[168:171]\n\t"
    "ds_read_b128 v[0:3], v250 offset:34816\n\t"
    "v_mfma_f32_16x16x32_bf16 v[172:175], v[212:215], v[224:227], v[172:175]\n\t"
    "ds_read_b128 v[4:7], v250 offset:35840\n\t"
    "v_mfma_f32_16x16x32_bf16 v[176:179], v[208:211], v[228:231], v[176:179]\n\t"
    "ds_read_b128 v[8:11], v250 offset:38912\n\t"
    "v_mfma_f32_16x16x32_bf16 v[180:183], v[212:215], v[228:231], v[180:183]\n\t"
    "ds_read_b128 v[12:15], v250 offset:39936\n\t"
    "s_waitcnt lgkmcnt(9)\n\t"
    "v_mfma_f32_16x16x32_bf16 v[184:187], v[208:211], v[232:235], v[184:187]\n\t"
    "ds_read_b128 v[16:19], v250 offset:43008\n\t"
    "v_mfma_f32_16x16x32_bf16 v[188:191], v[212:215], v[232:235], v[188:191]\n\t"
    "ds_read_b128 v[20:23], v250 offset:44032\n\t"
    "v_mfma_f32_16x16x32_bf16 v[192:195], v[208:211], v[236:239], v[192:195]\n\t"
    "ds_read_b128 v[24:27], v250 offset:47104\n\t"
    "v_mfma_f32_16x16x32_bf16 v[196:199], v[212:215], v[236:239], v[196:199]\n\t"
    "s_waitcnt lgkmcnt(11)\n\t"
    "v_mfma_f32_16x16x32_bf16 v[200:203], v[208:211], v[240:243], v[200:203]\n\t"
    "v_mfma_f32_16x16x32_bf16 v[204:207], v[212:215], v[240:243], v[204:207]\n\t"
    "s_waitcnt lgkmcnt(5)\n\t"
    "v_mfma_f32_16x16x32_bf16 v[152:155], v[28:31], v[0:3], v[152:155]\n\t"
    "v_mfma_f32_16x16x32_bf16 v[156:159], v[244:247], v[0:3], v[156:159]\n\t"
    "v_mfma_f32_16x16x32_bf16 v[160:163], v[28:31], v[4:7], v[160:163]\n\t"
    "v_mfma_f32_16x16x32_bf16 v[164:167], v[244:247], v[4:7], v[164:167]\n\t"
    "s_waitcnt lgkmcnt(3)\n\t"
    "v_mfma_f32_16x16x32_bf16 v[168:171], v[28:31], v[8:11], v[168:171]\n\t"
    "v_mfma_f32_16x16x32_bf16 v[172:175], v[244:247], v[8:11], v[172:175]\n\t"
    "v_mfma_f32_16x16x32_bf16 v[176:179], v[28:31], v[12:15], v[176:179]\n\t"
    "s_waitcnt lgkmcnt(0)\n\t"
    "s_barrier\n\t"
    "v_add_u32 v248, 0x10000, v248\n\t"
    "v_add_u32 v249, 0x10000, v249\n\t"
    "ds_read_b64_tr_b16 v[208:209], v248 offset:0\n\t"
    "ds_read_b64_tr_b16 v[210:211], v248 offset:4096\n\t"
    "ds_read_b64_tr_b16 v[212:213], v249 offset:0\n\t"
    "ds_read_b64_tr_b16 v[214:215], v249 offset:4096\n\t"
    "v_mfma_f32_16x16x32_bf16 v[180:183], v[244:247], v[12:15], v[180:183]\n\t"
    "ds_read_b128 v[216:219], v250 offset:0\n\t"
    "v_mfma_f32_16x16x32_bf16 v[184:187], v[28:31], v[16:19], v[184:187]\n\t"
    "ds_read_b128 v[220:223], v250 offset:1024\n\t"
    "v_mfma_f32_16x16x32_bf16 v[188:191], v[244:247], v[16:19], v[188:191]\n\t"
    "ds_read_b128 v[224:227], v250 offset:4096\n\t"
    "v_mfma_f32_16x16x32_bf16 v[192:195], v[28:31], v[20:23], v[192:195]\n\t"
    "ds_read_b128 v[228:231], v250 offset:5120\n\t"
    "v_mfma_f32_16x16x32_bf16 v[196:199], v[244:247], v[20:23], v[196:199]\n\t"
    "ds_read_b128 v[232:235], v250 offset:8192\n\t"
    "v_mfma_f32_16x16x32_bf16 v[200:203], v[28:31], v[24:27], v[200:203]\n\t"
    "ds_read_b128 v[236:239], v250 offset:9216\n\t"
    "v_mfma_f32_16x16x32_bf16 v[204:207], v[244:247], v[24:27], v[204:207]\n\t"
    "ds_read_b128 v[240:243], v250 offset:12288\n\t"
    "s_waitcnt lgkmcnt(5)\n\t"
    "v_mfma_f32_16x16x32_bf16 v[152:155], v[208:211], v[216:219], v[152:155]\n\t"
    "ds_read_b64_tr_b16 v[28:29], v248 offset:8192\n\t"
    "v_mfma_f32_16x16x32_bf16 v[156:159], v[212:215], v[216:219], v[156:159]\n\t"
    "ds_read_b64_tr_b16 v[30:31], v248 offset:12288\n\t"
    "v_mfma_f32_16x16x32_bf16 v[160:163], v[208:211], v[220:223], v[160:163]\n\t"
    "ds_read_b64_tr_b16 v[244:245], v249 offset:8192\n\t"
    "v_mfma_f32_16x16x32_bf16 v[164:167], v[212:215], v[220:223], v[164:167]\n\t"
    "ds_read_b64_tr_b16 v[246:247], v249 offset:12288\n\t"
    "s_waitcnt lgkmcnt(7)\n\t"
    "v_mfma_f32_16x16x32_bf16 v[168:171], v[208:211], v[224:227], v[168:171]\n\t"
    "ds_read_b128 v[0:3], v250 offset:2048\n\t"
    "v_mfma_f32_16x16x32_bf16 v[172:175], v[212:215], v[224:227], v[172:175]\n\t"
    "ds_read_b128 v[4:7], v250 offset:3072\n\t"
    "v_mfma_f32_16x16x32_bf16 v[176:179], v[208:211], v[228:231], v[176:179]\n\t"
    "ds_read_b128 v[8:11], v250 offset:6144\n\t"
    "v_mfma_f32_16x16x32_bf16 v[180:183], v[212:215], v[228:231], v[180:183]\n\t"
    "ds_read_b128 v[12:15], v250 offset:7168\n\t"
    "s_waitcnt lgkmcnt(9)\n\t"
    "v_mfma_f32_16x16x32_bf16 v[184:187], v[208:211], v[232:235], v[184:187]\n\t"
    "ds_read_b128 v[16:19], v250 offset:10240\n\t"
    "v_mfma_f32_16x16x32_bf16 v[188:191], v[212:215], v[232:235], v[188:191]\n\t"
    "ds_read_b128 v[20:23], v250 offset:11264\n\t"
    "v_mfma_f32_16x16x32_bf16 v[192:195], v[208:211], v[236:239], v[192:195]\n\t"
    "ds_read_b128 v[24:27], v250 offset:14336\n\t"
    "v_mfma_f32_16x16x32_bf16 v[196:199], v[212:215], v[236:239], v[196:199]\n\t"
    "s_waitcnt lgkmcnt(11)\n\t"
    "v_mfma_f32_16x16x32_bf16 v[200:203], v[208:211], v[240:243], v[200:203]\n\t"
    "v_mfma_f32_16x16x32_bf16 v[204:207], v[212:215], v[240:243], v[204:207]\n\t"
    "s_waitcnt lgkmcnt(5)\n\t"
    "v_mfma_f32_16x16x32_bf16 v[152:155], v[28:31], v[0:3], v[152:155]\n\t"
    "v_add_u32 v248, 0x10000, v248\n\t"
    "v_mfma_f32_16x16x32_bf16 v[156:159], v[244:247], v[0:3], v[156:159]\n\t"
    "v_add_u32 v249, 0x10000, v249\n\t"
    "v_mfma_f32_16x16x32_bf16 v[160:163], v[28:31], v[4:7], v[160:163]\n\t"
    "ds_read_b64_tr_b16 v[208:209], v248 offset:16384\n\t"
    "v_mfma_f32_16x16x32_bf16 v[164:167], v[244:247], v[4:7], v[164:167]\n\t"
    "ds_read_b64_tr_b16 v[210:211], v248 offset:20480\n\t"
    "s_waitcnt lgkmcnt(5)\n\t"
    "v_mfma_f32_16x16x32_bf16 v[168:171], v[28:31], v[8:11], v[168:171]\n\t"
    "ds_read_b64_tr_b16 v[212:213], v249 offset:16384\n\t"
    "v_mfma_f32_16x16x32_bf16 v[172:175], v[244:247], v[8:11], v[172:175]\n\t"
    "ds_read_b64_tr_b16 v[214:215], v249 offset:20480\n\t"
    "v_mfma_f32_16x16x32_bf16 v[176:179], v[28:31], v[12:15], v[176:179]\n\t"
    "ds_read_b128 v[216:219], v250 offset:49152\n\t"
    "v_mfma_f32_16x16x32_bf16 v[180:183], v[244:247], v[12:15], v[180:183]\n\t"
    "ds_read_b128 v[220:223], v250 offset:50176\n\t"
    "s_waitcnt lgkmcnt(7)\n\t"
    "v_mfma_f32_16x16x32_bf16 v[184:187], v[28:31], v[16:19], v[184:187]\n\t"
    "ds_read_b128 v[224:227], v250 offset:53248\n\t"
    "v_mfma_f32_16x16x32_bf16 v[188:191], v[244:247], v[16:19], v[188:191]\n\t"
    "ds_read_b128 v[228:231], v250 offset:54272\n\t"
    "v_mfma_f32_16x16x32_bf16 v[192:195], v[28:31], v[20:23], v[192:195]\n\t"
    "ds_read_b128 v[232:235], v250 offset:57344\n\t"
    "v_mfma_f32_16x16x32_bf16 v[196:199], v[244:247], v[20:23], v[196:199]\n\t"
    "ds_read_b128 v[236:239], v250 offset:58368\n\t"
    "s_waitcnt lgkmcnt(10)\n\t"
    "v_mfma_f32_16x16x32_bf16 v[200:203], v[28:31], v[24:27], v[200:203]\n\t"
    "ds_read_b128 v[240:243], v250 offset:61440\n\t"
    "v_mfma_f32_16x16x32_bf16 v[204:207], v[244:247], v[24:27], v[204:207]\n\t"
    "s_waitcnt lgkmcnt(5)\n\t"
    "v_mfma_f32_16x16x32_bf16 v[152:155], v[208:211], v[216:219], v[152:155]\n\t"
    "ds_read_b64_tr_b16 v[28:29], v248 offset:24576\n\t"
    "v_mfma_f32_16x16x32_bf16 v[156:159], v[212:215], v[216:219], v[156:159]\n\t"
    "ds_read_b64_tr_b16 v[30:31], v248 offset:28672\n\t"
    "v_mfma_f32_16x16x32_bf16 v[160:163], v[208:211], v[220:223], v[160:163]\n\t"
    "ds_read_b64_tr_b16 v[244:245], v249 offset:24576\n\t"
    "v_mfma_f32_16x16x32_bf16 v[164:167], v[212:215], v[220:223], v[164:167]\n\t"
    "ds_read_b64_tr_b16 v[246:247], v249 offset:28672\n\t"
    "s_waitcnt lgkmcnt(7)\n\t"
    "v_mfma_f32_16x16x32_bf16 v[168:171], v[208:211], v[224:227], v[168:171]\n\t"
    "ds_read_b128 v[0:3], v250 offset:51200\n\t"
    "v_mfma_f32_16x16x32_bf16 v[172:175], v[212:215], v[224:227], v[172:175]\n\t"
    "ds_read_b128 v[4:7], v250 offset:52224\n\t"
    "v_mfma_f32_16x16x32_bf16 v[176:179], v[208:211], v[228:231], v[176:179]\n\t"
    "ds_read_b128 v[8:11], v250 offset:55296\n\t"
    "v_mfma_f32_16x16x32_bf16 v[180:183], v[212:215], v[228:231], v[180:183]\n\t"
    "ds_read_b128 v[12:15], v250 offset:56320\n\t"
    "s_waitcnt lgkmcnt(9)\n\t"
    "v_mfma_f32_16x16x32_bf16 v[184:187], v[208:211], v[232:235], v[184:187]\n\t"
    "ds_read_b128 v[16:19], v250 offset:59392\n\t"
    "v_mfma_f32_16x16x32_bf16 v[188:191], v[212:215], v[232:235], v[188:191]\n\t"
    "ds_read_b128 v[20:23], v250 offset:60416\n\t"
    "v_mfma_f32_16x16x32_bf16 v[192:195], v[208:211], v[236:239], v[192:195]\n\t"
    "ds_read_b128 v[24:27], v250 offset:63488\n\t"
    "v_mfma_f32_16x16x32_bf16 v[196:199], v[212:215], v[236:239], v[196:199]\n\t"
    "s_waitcnt lgkmcnt(11)\n\t"
    "v_mfma_f32_16x16x32_bf16 v[200:203], v[208:211], v[240:243], v[200:203]\n\t"
    "v_mfma_f32_16x16x32_bf16 v[204:207], v[212:215], v[240:243], v[204:207]\n\t"
    "s_waitcnt lgkmcnt(5)\n\t"
    "v_mfma_f32_16x16x32_bf16 v[152:155], v[28:31], v[0:3], v[152:155]\n\t"
    "v_mfma_f32_16x16x32_bf16 v[156:159], v[244:247], v[0:3], v[156:159]\n\t"
    "v_mfma_f32_16x16x32_bf16 v[160:163], v[28:31], v[4:7], v[160:163]\n\t"
    "v_mfma_f32_16x16x32_bf16 v[164:167], v[244:247], v[4:7], v[164:167]\n\t"
    "s_waitcnt lgkmcnt(3)\n\t"
    "v_mfma_f32_16x16x32_bf16 v[168:171], v[28:31], v[8:11], v[168:171]\n\t"
    "v_mfma_f32_16x16x32_bf16 v[172:175], v[244:247], v[8:11], v[172:175]\n\t"
    "v_mfma_f32_16x16x32_bf16 v[176:179], v[28:31], v[12:15], v[176:179]\n\t"
    "s_waitcnt lgkmcnt(0)\n\t"
    "s_barrier\n\t"
    "v_add_u32 v248, 0xffff0000, v248\n\t"
    "v_add_u32 v249, 0xffff0000, v249\n\t"
    "ds_read_b64_tr_b16 v[208:209], v248 offset:49152\n\t"
    "ds_read_b64_tr_b16 v[210:211], v248 offset:53248\n\t"
    "ds_read_b64_tr_b16 v[212:213], v249 offset:49152\n\t"
    "ds_read_b64_tr_b16 v[214:215], v249 offset:53248\n\t"
    "v_add_u32 v250, 0x10000, v250\n\t"
    "v_mfma_f32_16x16x32_bf16 v[180:183], v[244:247], v[12:15], v[180:183]\n\t"
    "ds_read_b128 v[216:219], v250 offset:16384\n\t"
    "v_mfma_f32_16x16x32_bf16 v[184:187], v[28:31], v[16:19], v[184:187]\n\t"
    "ds_read_b128 v[220:223], v250 offset:17408\n\t"
    "v_mfma_f32_16x16x32_bf16 v[188:191], v[244:247], v[16:19], v[188:191]\n\t"
    "ds_read_b128 v[224:227], v250 offset:20480\n\t"
    "v_mfma_f32_16x16x32_bf16 v[192:195], v[28:31], v[20:23], v[192:195]\n\t"
    "ds_read_b128 v[228:231], v250 offset:21504\n\t"
    "v_mfma_f32_16x16x32_bf16 v[196:199], v[244:247], v[20:23], v[196:199]\n\t"
    "ds_read_b128 v[232:235], v250 offset:24576\n\t"
    "v_mfma_f32_16x16x32_bf16 v[200:203], v[28:31], v[24:27], v[200:203]\n\t"
    "ds_read_b128 v[236:239], v250 offset:25600\n\t"
    "v_mfma_f32_16x16x32_bf16 v[204:207], v[244:247], v[24:27], v[204:207]\n\t"
    "ds_read_b128 v[240:243], v250 offset:28672\n\t"
    "s_waitcnt lgkmcnt(5)\n\t"
    "v_mfma_f32_16x16x32_bf16 v[152:155], v[208:211], v[216:219], v[152:155]\n\t"
    "ds_read_b64_tr_b16 v[28:29], v248 offset:57344\n\t"
    "v_mfma_f32_16x16x32_bf16 v[156:159], v[212:215], v[216:219], v[156:159]\n\t"
    "ds_read_b64_tr_b16 v[30:31], v248 offset:61440\n\t"
    "v_mfma_f32_16x16x32_bf16 v[160:163], v[208:211], v[220:223], v[160:163]\n\t"
    "ds_read_b64_tr_b16 v[244:245], v249 offset:57344\n\t"
    "v_mfma_f32_16x16x32_bf16 v[164:167], v[212:215], v[220:223], v[164:167]\n\t"
    "ds_read_b64_tr_b16 v[246:247], v249 offset:61440\n\t"
    "s_waitcnt lgkmcnt(7)\n\t"
    "v_mfma_f32_16x16x32_bf16 v[168:171], v[208:211], v[224:227], v[168:171]\n\t"
    "ds_read_b128 v[0:3], v250 offset:18432\n\t"
    "v_mfma_f32_16x16x32_bf16 v[172:175], v[212:215], v[224:227], v[172:175]\n\t"
    "ds_read_b128 v[4:7], v250 offset:19456\n\t"
    "v_mfma_f32_16x16x32_bf16 v[176:179], v[208:211], v[228:231], v[176:179]\n\t"
    "ds_read_b128 v[8:11], v250 offset:22528\n\t"
    "v_mfma_f32_16x16x32_bf16 v[180:183], v[212:215], v[228:231], v[180:183]\n\t"
    "ds_read_b128 v[12:15], v250 offset:23552\n\t"
    "s_waitcnt lgkmcnt(9)\n\t"
    "v_mfma_f32_16x16x32_bf16 v[184:187], v[208:211], v[232:235], v[184:187]\n\t"
    "ds_read_b128 v[16:19], v250 offset:26624\n\t"
    "v_mfma_f32_16x16x32_bf16 v[188:191], v[212:215], v[232:235], v[188:191]\n\t"
    "ds_read_b128 v[20:23], v250 offset:27648\n\t"
    "v_mfma_f32_16x16x32_bf16 v[192:195], v[208:211], v[236:239], v[192:195]\n\t"
    "ds_read_b128 v[24:27], v250 offset:30720\n\t"
    "v_mfma_f32_16x16x32_bf16 v[196:199], v[212:215], v[236:239], v[196:199]\n\t"
    "s_waitcnt lgkmcnt(11)\n\t"
    "v_mfma_f32_16x16x32_bf16 v[200:203], v[208:211], v[240:243], v[200:203]\n\t"
    "v_mfma_f32_16x16x32_bf16 v[204:207], v[212:215], v[240:243], v[204:207]\n\t"
    "s_waitcnt lgkmcnt(5)\n\t"
    "v_mfma_f32_16x16x32_bf16 v[152:155], v[28:31], v[0:3], v[152:155]\n\t"
    "v_add_u32 v248, 0x10000, v248\n\t"
    "v_mfma_f32_16x16x32_bf16 v[156:159], v[244:247], v[0:3], v[156:159]\n\t"
    "v_add_u32 v249, 0x10000, v249\n\t"
    "v_mfma_f32_16x16x32_bf16 v[160:163], v[28:31], v[4:7], v[160:163]\n\t"
    "ds_read_b64_tr_b16 v[208:209], v248 offset:0\n\t"
    "v_mfma_f32_16x16x32_bf16 v[164:167], v[244:247], v[4:7], v[164:167]\n\t"
    "ds_read_b64_tr_b16 v[210:211], v248 offset:4096\n\t"
    "s_waitcnt lgkmcnt(5)\n\t"
    "v_mfma_f32_16x16x32_bf16 v[168:171], v[28:31], v[8:11], v[168:171]\n\t"
    "ds_read_b64_tr_b16 v[212:213], v249 offset:0\n\t"
    "v_mfma_f32_16x16x32_bf16 v[172:175], v[244:247], v[8:11], v[172:175]\n\t"
    "ds_read_b64_tr_b16 v[214:215], v249 offset:4096\n\t"
    "v_mfma_f32_16x16x32_bf16 v[176:179], v[28:31], v[12:15], v[176:179]\n\t"
    "ds_read_b128 v[216:219], v250 offset:32768\n\t"
    "v_mfma_f32_16x16x32_bf16 v[180:183], v[244:247], v[12:15], v[180:183]\n\t"
    "ds_read_b128 v[220:223], v250 offset:33792\n\t"
    "s_waitcnt lgkmcnt(7)\n\t"
    "v_mfma_f32_16x16x32_bf16 v[184:187], v[28:31], v[16:19], v[184:187]\n\t"
    "ds_read_b128 v[224:227], v250 offset:36864\n\t"
    "v_mfma_f32_16x16x32_bf16 v[188:191], v[244:247], v[16:19], v[188:191]\n\t"
    "ds_read_b128 v[228:231], v250 offset:37888\n\t"
    "v_mfma_f32_16x16x32_bf16 v[192:195], v[28:31], v[20:23], v[192:195]\n\t"
    "ds_read_b128 v[232:235], v250 offset:40960\n\t"
    "v_mfma_f32_16x16x32_bf16 v[196:199], v[244:247], v[20:23], v[196:199]\n\t"
    "ds_read_b128 v[236:239], v250 offset:41984\n\t"
    "s_waitcnt lgkmcnt(10)\n\t"
    "v_mfma_f32_16x16x32_bf16 v[200:203], v[28:31], v[24:27], v[200:203]\n\t"
    "ds_read_b128 v[240:243], v250 offset:45056\n\t"
    "v_mfma_f32_16x16x32_bf16 v[204:207], v[244:247], v[24:27], v[204:207]\n\t"
    "s_waitcnt lgkmcnt(5)\n\t"
    "v_mfma_f32_16x16x32_bf16 v[152:155], v[208:211], v[216:219], v[152:155]\n\t"
    "ds_read_b64_tr_b16 v[28:29], v248 offset:8192\n\t"
    "v_mfma_f32_16x16x32_bf16 v[156:159], v[212:215], v[216:219], v[156:159]\n\t"
    "ds_read_b64_tr_b16 v[30:31], v248 offset:12288\n\t"
    "v_mfma_f32_16x16x32_bf16 v[160:163], v[208:211], v[220:223], v[160:163]\n\t"
    "ds_read_b64_tr_b16 v[244:245], v249 offset:8192\n\t"
    "v_mfma_f32_16x16x32_bf16 v[164:167], v[212:215], v[220:223], v[164:167]\n\t"
    "ds_read_b64_tr_b16 v[246:247], v249 offset:12288\n\t"
    "s_waitcnt lgkmcnt(7)\n\t"
    "v_mfma_f32_16x16x32_bf16 v[168:171], v[208:211], v[224:227], v[168:171]\n\t"
    "ds_read_b128 v[0:3], v250 offset:34816\n\t"
    "v_mfma_f32_16x16x32_bf16 v[172:175], v[212:215], v[224:227], v[172:175]\n\t"
    "ds_read_b128 v[4:7], v250 offset:35840\n\t"
    "v_mfma_f32_16x16x32_bf16 v[176:179], v[208:211], v[228:231], v[176:179]\n\t"
    "ds_read_b128 v[8:11], v250 offset:38912\n\t"
    "v_mfma_f32_16x16x32_bf16 v[180:183], v[212:215], v[228:231], v[180:183]\n\t"
    "ds_read_b128 v[12:15], v250 offset:39936\n\t"
    "s_waitcnt lgkmcnt(9)\n\t"
    "v_mfma_f32_16x16x32_bf16 v[184:187], v[208:211], v[232:235], v[184:187]\n\t"
    "ds_read_b128 v[16:19], v250 offset:43008\n\t"
    "v_mfma_f32_16x16x32_bf16 v[188:191], v[212:215], v[232:235], v[188:191]\n\t"
    "ds_read_b128 v[20:23], v250 offset:44032\n\t"
    "v_mfma_f32_16x16x32_bf16 v[192:195], v[208:211], v[236:239], v[192:195]\n\t"
    "ds_read_b128 v[24:27], v250 offset:47104\n\t"
    "v_mfma_f32_16x16x32_bf16 v[196:199], v[212:215], v[236:239], v[196:199]\n\t"
    "s_waitcnt lgkmcnt(11)\n\t"
    "v_mfma_f32_16x16x32_bf16 v[200:203], v[208:211], v[240:243], v[200:203]\n\t"
    "v_mfma_f32_16x16x32_bf16 v[204:207], v[212:215], v[240:243], v[204:207]\n\t"
    "s_waitcnt lgkmcnt(5)\n\t"
    "v_mfma_f32_16x16x32_bf16 v[152:155], v[28:31], v[0:3], v[152:155]\n\t"
    "v_mfma_f32_16x16x32_bf16 v[156:159], v[244:247], v[0:3], v[156:159]\n\t"
    "v_mfma_f32_16x16x32_bf16 v[160:163], v[28:31], v[4:7], v[160:163]\n\t"
    "v_mfma_f32_16x16x32_bf16 v[164:167], v[244:247], v[4:7], v[164:167]\n\t"
    "s_waitcnt lgkmcnt(3)\n\t"
    "v_mfma_f32_16x16x32_bf16 v[168:171], v[28:31], v[8:11], v[168:171]\n\t"
    "v_mfma_f32_16x16x32_bf16 v[172:175], v[244:247], v[8:11], v[172:175]\n\t"
    "v_mfma_f32_16x16x32_bf16 v[176:179], v[28:31], v[12:15], v[176:179]\n\t"
    "s_waitcnt lgkmcnt(0)\n\t"
    "s_barrier\n\t"
    "v_add_u32 v248, 0xfffe0000, v248\n\t"
    "v_add_u32 v249, 0xfffe0000, v249\n\t"
    "ds_read_b64_tr_b16 v[32:33], v248 offset:32768\n\t"
    "ds_read_b64_tr_b16 v[34:35], v248 offset:36864\n\t"
    "ds_read_b64_tr_b16 v[36:37], v249 offset:32768\n\t"
    "ds_read_b64_tr_b16 v[38:39], v249 offset:36864\n\t"
    "ds_read_b64_tr_b16 v[40:41], v248 offset:40960\n\t"
    "ds_read_b64_tr_b16 v[42:43], v248 offset:45056\n\t"
    "ds_read_b64_tr_b16 v[44:45], v249 offset:40960\n\t"
    "ds_read_b64_tr_b16 v[46:47], v249 offset:45056\n\t"
    "v_add_u32 v248, 0x10000, v248\n\t"
    "v_mfma_f32_16x16x32_bf16 v[180:183], v[244:247], v[12:15], v[180:183]\n\t"
    "v_add_u32 v249, 0x10000, v249\n\t"
    "ds_read_b64_tr_b16 v[48:49], v248 offset:0\n\t"
    "v_mfma_f32_16x16x32_bf16 v[184:187], v[28:31], v[16:19], v[184:187]\n\t"
    "ds_read_b64_tr_b16 v[50:51], v248 offset:4096\n\t"
    "ds_read_b64_tr_b16 v[52:53], v249 offset:0\n\t"
    "v_mfma_f32_16x16x32_bf16 v[188:191], v[244:247], v[16:19], v[188:191]\n\t"
    "ds_read_b64_tr_b16 v[54:55], v249 offset:4096\n\t"
    "ds_read_b64_tr_b16 v[56:57], v248 offset:8192\n\t"
    "v_mfma_f32_16x16x32_bf16 v[192:195], v[28:31], v[20:23], v[192:195]\n\t"
    "ds_read_b64_tr_b16 v[58:59], v248 offset:12288\n\t"
    "ds_read_b64_tr_b16 v[60:61], v249 offset:8192\n\t"
    "v_mfma_f32_16x16x32_bf16 v[196:199], v[244:247], v[20:23], v[196:199]\n\t"
    "ds_read_b64_tr_b16 v[62:63], v249 offset:12288\n\t"
    "v_add_u32 v250, 0xffff0000, v250\n\t"
    "v_mfma_f32_16x16x32_bf16 v[200:203], v[28:31], v[24:27], v[200:203]\n\t"
    "ds_read_b128 v[64:67], v250 offset:16384\n\t"
    "ds_read_b128 v[68:71], v250 offset:18432\n\t"
    "v_mfma_f32_16x16x32_bf16 v[204:207], v[244:247], v[24:27], v[204:207]\n\t"
    "ds_read_b128 v[72:75], v250 offset:0\n\t"
    "ds_read_b128 v[76:79], v250 offset:2048\n\t"
    "s_xor_b32 s52, %[oct], s50\n\t"
    "s_lshl_b32 s52, s52, 2\n\t"
    "s_add_u32 s52, s52, s50\n\t"
    "s_lshl_b32 s53, s52, 13\n\t"
    "s_add_u32 s53, s53, %[xbase]\n\t"
    "s_add_u32 s56, s53, 0x1000\n\t"
    "s_lshl_b32 s52, s52, 7\n\t"
    "s_add_u32 s52, s52, %[f2base]\n\t"
    "s_mul_i32 s54, s50, 120\n\t"
    "s_sub_u32 s52, s52, s54\n\t"
    "v_lshlrev_b32 v146, 4, v253\n\t"
    "s_cmp_eq_u32 s50, 0\n\t"
    "s_cselect_b32 s54, 0x80000000, 0\n\t"
    "v_or_b32 v146, s54, v146\n\t"
    "s_waitcnt lgkmcnt(0)\n\t"
    "v_mfma_f32_16x16x32_bf16 v[152:155], v[32:35], v[64:67], v[152:155]\n\t"
    "ds_read_b128 v[80:83], v250 offset:17408\n\t"
    "v_mfma_f32_16x16x32_bf16 v[152:155], v[40:43], v[68:71], v[152:155]\n\t"
    "ds_read_b128 v[84:87], v250 offset:19456\n\t"
    "v_mfma_f32_16x16x32_bf16 v[152:155], v[48:51], v[72:75], v[152:155]\n\t"
    "ds_read_b128 v[88:91], v250 offset:1024\n\t"
    "v_mfma_f32_16x16x32_bf16 v[152:155], v[56:59], v[76:79], v[152:155]\n\t"
    "ds_read_b128 v[92:95], v250 offset:3072\n\t"
    "v_mfma_f32_16x16x32_bf16 v[156:159], v[60:63], v[76:79], v[156:159]\n\t"
    "v_mfma_f32_16x16x32_bf16 v[156:159], v[52:55], v[72:75], v[156:159]\n\t"
    "v_mfma_f32_16x16x32_bf16 v[156:159], v[44:47], v[68:71], v[156:159]\n\t"
    "v_mfma_f32_16x16x32_bf16 v[156:159], v[36:39], v[64:67], v[156:159]\n\t"
    "s_waitcnt lgkmcnt(0)\n\t"
    "v_mfma_f32_16x16x32_bf16 v[160:163], v[32:35], v[80:83], v[160:163]\n\t"
    "ds_read_b128 v[64:67], v250 offset:20480\n\t"
    "v_mfma_f32_16x16x32_bf16 v[160:163], v[40:43], v[84:87], v[160:163]\n\t"
    "ds_read_b128 v[68:71], v250 offset:22528\n\t"
    "v_mfma_f32_16x16x32_bf16 v[160:163], v[48:51], v[88:91], v[160:163]\n\t"
    "ds_read_b128 v[72:75], v250 offset:4096\n\t"
    "v_cvt_pk_bf16_f32 v96, v152, v153\n\t"
    "v_mfma_f32_16x16x32_bf16 v[160:163], v[56:59], v[92:95], v[160:163]\n\t"
    "ds_read_b128 v[76:79], v250 offset:6144\n\t"
    "v_cvt_pk_bf16_f32 v97, v154, v155\n\t"
    "v_mfma_f32_16x16x32_bf16 v[164:167], v[60:63], v[92:95], v[164:167]\n\t"
    "v_cvt_pk_bf16_f32 v98, v156, v157\n\t"
    "v_mfma_f32_16x16x32_bf16 v[164:167], v[52:55], v[88:91], v[164:167]\n\t"
    "v_cvt_pk_bf16_f32 v99, v158, v159\n\t"
    "v_mfma_f32_16x16x32_bf16 v[164:167], v[44:47], v[84:87], v[164:167]\n\t"
    "buffer_store_dwordx4 v[96:99], v146, %[srdws], s53 offen sc1\n\t"
    "v_mfma_f32_16x16x32_bf16 v[164:167], v[36:39], v[80:83], v[164:167]\n\t"
    "s_waitcnt lgkmcnt(0)\n\t"
    "v_mfma_f32_16x16x32_bf16 v[168:171], v[32:35], v[64:67], v[168:171]\n\t"
    "ds_read_b128 v[80:83], v250 offset:21504\n\t"
    "v_mfma_f32_16x16x32_bf16 v[168:171], v[40:43], v[68:71], v[168:171]\n\t"
    "ds_read_b128 v[84:87], v250 offset:23552\n\t"
    "v_mfma_f32_16x16x32_bf16 v[168:171], v[48:51], v[72:75], v[168:171]\n\t"
    "ds_read_b128 v[88:91], v250 offset:5120\n\t"
    "v_cvt_pk_bf16_f32 v100, v160, v161\n\t"
    "v_mfma_f32_16x16x32_bf16 v[168:171], v[56:59], v[76:79], v[168:171]\n\t"
    "ds_read_b128 v[92:95], v250 offset:7168\n\t"
    "v_cvt_pk_bf16_f32 v101, v162, v163\n\t"
    "v_mfma_f32_16x16x32_bf16 v[172:175], v[60:63], v[76:79], v[172:175]\n\t"
    "v_cvt_pk_bf16_f32 v102, v164, v165\n\t"
    "v_mfma_f32_16x16x32_bf16 v[172:175], v[52:55], v[72:75], v[172:175]\n\t"
    "v_cvt_pk_bf16_f32 v103, v166, v167\n\t"
    "v_mfma_f32_16x16x32_bf16 v[172:175], v[44:47], v[68:71], v[172:175]\n\t"
    "buffer_store_dwordx4 v[100:103], v146, %[srdws], s53 offen offset:1024 sc1\n\t"
    "v_mfma_f32_16x16x32_bf16 v[172:175], v[36:39], v[64:67], v[172:175]\n\t"
    "s_waitcnt lgkmcnt(0)\n\t"
    "v_mfma_f32_16x16x32_bf16 v[176:179], v[32:35], v[80:83], v[176:179]\n\t"
    "ds_read_b128 v[64:67], v250 offset:24576\n\t"
    "v_mfma_f32_16x16x32_bf16 v[176:179], v[40:43], v[84:87], v[176:179]\n\t"
    "ds_read_b128 v[68:71], v250 offset:26624\n\t"
    "v_mfma_f32_16x16x32_bf16 v[176:179], v[48:51], v[88:91], v[176:179]\n\t"
    "ds_read_b128 v[72:75], v250 offset:8192\n\t"
    "v_cvt_pk_bf16_f32 v104, v168, v169\n\t"
    "v_mfma_f32_16x16x32_bf16 v[176:179], v[56:59], v[92:95], v[176:179]\n\t"
    "ds_read_b128 v[76:79], v250 offset:10240\n\t"
    "v_cvt_pk_bf16_f32 v105, v170, v171\n\t"
    "v_mfma_f32_16x16x32_bf16 v[180:183], v[60:63], v[92:95], v[180:183]\n\t"
    "v_cvt_pk_bf16_f32 v106, v172, v173\n\t"
    "v_mfma_f32_16x16x32_bf16 v[180:183], v[52:55], v[88:91], v[180:183]\n\t"
    "v_cvt_pk_bf16_f32 v107, v174, v175\n\t"
    "v_mfma_f32_16x16x32_bf16 v[180:183], v[44:47], v[84:87], v[180:183]\n\t"
    "buffer_store_dwordx4 v[104:107], v146, %[srdws], s53 offen offset:2048 sc1\n\t"
    "v_mfma_f32_16x16x32_bf16 v[180:183], v[36:39], v[80:83], v[180:183]\n\t"
    "s_waitcnt lgkmcnt(0)\n\t"
    "v_mfma_f32_16x16x32_bf16 v[184:187], v[32:35], v[64:67], v[184:187]\n\t"
    "ds_read_b128 v[80:83], v250 offset:25600\n\t"
    "v_mfma_f32_16x16x32_bf16 v[184:187], v[40:43], v[68:71], v[184:187]\n\t"
    "ds_read_b128 v[84:87], v250 offset:27648\n\t"
    "v_mfma_f32_16x16x32_bf16 v[184:187], v[48:51], v[72:75], v[184:187]\n\t"
    "ds_read_b128 v[88:91], v250 offset:9216\n\t"
    "v_cvt_pk_bf16_f32 v108, v176, v177\n\t"
    "v_mfma_f32_16x16x32_bf16 v[184:187], v[56:59], v[76:79], v[184:187]\n\t"
    "ds_read_b128 v[92:95], v250 offset:11264\n\t"
    "v_cvt_pk_bf16_f32 v109, v178, v179\n\t"
    "v_mfma_f32_16x16x32_bf16 v[188:191], v[60:63], v[76:79], v[188:191]\n\t"
    "v_cvt_pk_bf16_f32 v110, v180, v181\n\t"
    "v_mfma_f32_16x16x32_bf16 v[188:191], v[52:55], v[72:75], v[188:191]\n\t"
    "v_cvt_pk_bf16_f32 v111, v182, v183\n\t"
    "v_mfma_f32_16x16x32_bf16 v[188:191], v[44:47], v[68:71], v[188:191]\n\t"
    "buffer_store_dwordx4 v[108:111], v146, %[srdws], s53 offen offset:3072 sc1\n\t"
    "v_mfma_f32_16x16x32_bf16 v[188:191], v[36:39], v[64:67], v[188:191]\n\t"
    "s_waitcnt lgkmcnt(0)\n\t"
    "v_mfma_f32_16x16x32_bf16 v[192:195], v[32:35], v[80:83], v[192:195]\n\t"
    "ds_read_b128 v[64:67], v250 offset:28672\n\t"
    "v_mfma_f32_16x16x32_bf16 v[192:195], v[40:43], v[84:87], v[192:195]\n\t"
    "ds_read_b128 v[68:71], v250 offset:30720\n\t"
    "v_mfma_f32_16x16x32_bf16 v[192:195], v[48:51], v[88:91], v[192:195]\n\t"
    "ds_read_b128 v[72:75], v250 offset:12288\n\t"
    "v_cvt_pk_bf16_f32 v112, v184, v185\n\t"
    "v_mfma_f32_16x16x32_bf16 v[192:195], v[56:59], v[92:95], v[192:195]\n\t"
    "ds_read_b128 v[76:79], v250 offset:14336\n\t"
    "v_cvt_pk_bf16_f32 v113, v186, v187\n\t"
    "v_mfma_f32_16x16x32_bf16 v[196:199], v[60:63], v[92:95], v[196:199]\n\t"
    "v_cvt_pk_bf16_f32 v114, v188, v189\n\t"
    "v_mfma_f32_16x16x32_bf16 v[196:199], v[52:55], v[88:91], v[196:199]\n\t"
    "v_cvt_pk_bf16_f32 v115, v190, v191\n\t"
    "v_mfma_f32_16x16x32_bf16 v[196:199], v[44:47], v[84:87], v[196:199]\n\t"
    "buffer_store_dwordx4 v[112:115], v146, %[srdws], s56 offen sc1\n\t"
    "v_mfma_f32_16x16x32_bf16 v[196:199], v[36:39], v[80:83], v[196:199]\n\t"
    "s_waitcnt lgkmcnt(0)\n\t"
    "v_mfma_f32_16x16x32_bf16 v[200:203], v[32:35], v[64:67], v[200:203]\n\t"
    "v_mfma_f32_16x16x32_bf16 v[200:203], v[40:43], v[68:71], v[200:203]\n\t"
    "v_mfma_f32_16x16x32_bf16 v[200:203], v[48:51], v[72:75], v[200:203]\n\t"
    "v_cvt_pk_bf16_f32 v116, v192, v193\n\t"
    "v_mfma_f32_16x16x32_bf16 v[200:203], v[56:59], v[76:79], v[200:203]\n\t"
    "v_cvt_pk_bf16_f32 v117, v194, v195\n\t"
    "v_mfma_f32_16x16x32_bf16 v[204:207], v[60:63], v[76:79], v[204:207]\n\t"
    "v_cvt_pk_bf16_f32 v118, v196, v197\n\t"
    "v_mfma_f32_16x16x32_bf16 v[204:207], v[52:55], v[72:75], v[204:207]\n\t"
    "v_cvt_pk_bf16_f32 v119, v198, v199\n\t"
    "v_mfma_f32_16x16x32_bf16 v[204:207], v[44:47], v[68:71], v[204:207]\n\t"
    "buffer_store_dwordx4 v[116:119], v146, %[srdws], s56 offen offset:1024 sc1\n\t"
    "v_mfma_f32_16x16x32_bf16 v[204:207], v[36:39], v[64:67], v[204:207]\n\t"
    "v_cvt_pk_bf16_f32 v120, v200, v201\n\t"
    "v_cvt_pk_bf16_f32 v121, v202, v203\n\t"
    "s_nop 5\n\t"
    "v_cvt_pk_bf16_f32 v122, v204, v205\n\t"
    "v_cvt_pk_bf16_f32 v123, v206, v207\n\t"
    "buffer_store_dwordx4 v[120:123], v146, %[srdws], s56 offen offset:2048 sc1\n\t"
    "v_add_u32 v250, 0x20000, v250\n\t"
    "s_cmp_eq_u32 s50, 0\n\t"
    "s_cbranch_scc1 XOWN_%=\n\t"
    "s_barrier\n\t"
    "s_waitcnt vmcnt(0)\n\t"
    "buffer_store_dwordx2 v[148:149], v144, %[srdws], s52 offen sc1\n\t"
    "s_branch XJOIN_%=\n\t"
    "XOWN_%=:\n\t"
    "s_nop 7\n\t"
    "ds_write_b128 v250, v[152:155] offset:16384\n\t"
    "ds_write_b128 v250, v[156:159] offset:17408\n\t"
    "ds_write_b128 v250, v[160:163] offset:18432\n\t"
    "ds_write_b128 v250, v[164:167] offset:19456\n\t"
    "ds_write_b128 v250, v[168:171] offset:20480\n\t"
    "ds_write_b128 v250, v[172:175] offset:21504\n\t"
    "ds_write_b128 v250, v[176:179] offset:22528\n\t"
    "ds_write_b128 v250, v[180:183] offset:23552\n\t"
    "ds_write_b128 v250, v[184:187] offset:24576\n\t"
    "ds_write_b128 v250, v[188:191] offset:25600\n\t"
    "ds_write_b128 v250, v[192:195] offset:26624\n\t"
    "ds_write_b128 v250, v[196:199] offset:27648\n\t"
    "ds_write_b128 v250, v[200:203] offset:28672\n\t"
    "ds_write_b128 v250, v[204:207] offset:29696\n\t"
    "s_waitcnt lgkmcnt(0)\n\t"
    "s_barrier\n\t"
    "XJOIN_%=:\n\t"
    "s_lshl_b32 s52, s50, 12\n\t"
    "v_add_u32 v250, s52, v250\n\t"
    "ds_read_b128 v[152:155], v250 offset:16384\n\t"
    "ds_read_b128 v[156:159], v250 offset:17408\n\t"
    "ds_read_b128 v[160:163], v250 offset:18432\n\t"
    "ds_read_b128 v[164:167], v250 offset:19456\n\t"
    "v_lshlrev_b32 v145, 3, v253\n\t"
    "v_cmp_gt_u32 vcc, 4, v253\n\t"
    "v_mov_b32 v254, 0x80000000\n\t"
    "s_nop 0\n\t"
    "v_cndmask_b32 v145, v254, v145, vcc\n\t"
    "s_lshl_b32 s55, %[oct], 9\n\t"
    "s_add_u32 s55, s55, %[f2base]\n\t"
    "buffer_load_dwordx2 v[150:151], v145, %[srdws], s55 offen sc1\n\t"
    "s_mov_b32 s54, 2000000\n\t"
    "s_waitcnt vmcnt(0)\n\t"
    "v_cmp_ne_u64 vcc, s[44:45], v[150:151]\n\t"
    "s_nop 3\n\t"
    "s_and_b64 vcc, vcc, 14\n\t"
    "s_cbranch_scc0 PC_done%=\n\t"
    "PC_loop%=:\n\t"
    "buffer_load_dwordx2 v[150:151], v145, %[srdws], s55 offen sc1\n\t"
    "s_sub_u32 s54, s54, 1\n\t"
    "s_cbranch_scc1 PC_tmo%=\n\t"
    "s_waitcnt vmcnt(0)\n\t"
    "v_cmp_ne_u64 vcc, s[44:45], v[150:151]\n\t"
    "s_nop 3\n\t"
    "s_and_b64 vcc, vcc, 14\n\t"
    "s_cbranch_scc1 PC_loop%=\n\t"
    "PC_tmo%=:\n\t"
    "s_waitcnt vmcnt(0)\n\t"
    "PC_done%=:\n\t"
    "s_lshl_b32 s52, %[oct], 15\n\t"
    "s_add_u32 s52, s52, %[xbase]\n\t"
    "s_add_u32 s53, s52, 0x2000\n\t"
    "buffer_load_dwordx4 v[40:43], v138, %[srdws], s53 offen sc1\n\t"
    "buffer_load_dwordx4 v[44:47], v139, %[srdws], s53 offen offset:1024 sc1\n\t"
    "s_add_u32 s53, s52, 0x4000\n\t"
    "buffer_load_dwordx4 v[48:51], v138, %[srdws], s53 offen sc1\n\t"
    "buffer_load_dwordx4 v[52:55], v139, %[srdws], s53 offen offset:1024 sc1\n\t"
    "s_add_u32 s53, s52, 0x6000\n\t"
    "buffer_load_dwordx4 v[56:59], v138, %[srdws], s53 offen sc1\n\t"
    "buffer_load_dwordx4 v[60:63], v139, %[srdws], s53 offen offset:1024 sc1\n\t"
    "s_or_b32 s52, %[oct], %[qi]\n\t"
    "s_or_b32 s52, s52, s50\n\t"
    "s_cmp_eq_u32 s52, 0\n\t"
    "s_cselect_b32 s52, 0, 0x80000000\n\t"
    "s_add_u32 s53, %[f1base], 0x6000\n\t"
    "s_mov_b32 s55, 0x8000\n\t"
    "v_or_b32 v144, s52, v144\n\t"
    "buffer_store_dwordx2 v[148:149], v144, %[srdws], s53 offen sc1\n\t"
    "s_waitcnt vmcnt(6)\n\t"
    "v_lshlrev_b32 v254, 16, v40\n\t"
    "v_and_b32 v255, 0xffff0000, v40\n\t"
    "s_waitcnt lgkmcnt(3)\n\t"
    "v_add_f32 v152, v152, v254\n\t"
    "v_add_f32 v153, v153, v255\n\t"
    "v_lshlrev_b32 v254, 16, v41\n\t"
    "v_and_b32 v255, 0xffff0000, v41\n\t"
    "v_add_f32 v154, v154, v254\n\t"
    "v_add_f32 v155, v155, v255\n\t"
    "v_lshlrev_b32 v254, 16, v42\n\t"
    "v_and_b32 v255, 0xffff0000, v42\n\t"
    "s_waitcnt lgkmcnt(2)\n\t"
    "v_add_f32 v156, v156, v254\n\t"
    "v_add_f32 v157, v157, v255\n\t"
    "v_lshlrev_b32 v254, 16, v43\n\t"
    "v_and_b32 v255, 0xffff0000, v43\n\t"
    "v_add_f32 v158, v158, v254\n\t"
    "v_add_f32 v159, v159, v255\n\t"
    "s_waitcnt vmcnt(5)\n\t"
    "v_lshlrev_b32 v254, 16, v44\n\t"
    "v_and_b32 v255, 0xffff0000, v44\n\t"
    "s_waitcnt lgkmcnt(1)\n\t"
    "v_add_f32 v160, v160, v254\n\t"
    "v_add_f32 v161, v161, v255\n\t"
    "v_lshlrev_b32 v254, 16, v45\n\t"
    "v_and_b32 v255, 0xffff0000, v45\n\t"
    "v_add_f32 v162, v162, v254\n\t"
    "v_add_f32 v163, v163, v255\n\t"
    "v_lshlrev_b32 v254, 16, v46\n\t"
    "v_and_b32 v255, 0xffff0000, v46\n\t"
    "s_waitcnt lgkmcnt(0)\n\t"
    "v_add_f32 v164, v164, v254\n\t"
    "v_add_f32 v165, v165, v255\n\t"
    "v_lshlrev_b32 v254, 16, v47\n\t"
    "v_and_b32 v255, 0xffff0000, v47\n\t"
    "v_add_f32 v166, v166, v254\n\t"
    "v_add_f32 v167, v167, v255\n\t"
    "s_waitcnt vmcnt(4)\n\t"
    "v_lshlrev_b32 v254, 16, v48\n\t"
    "v_and_b32 v255, 0xffff0000, v48\n\t"
    "v_add_f32 v152, v152, v254\n\t"
    "v_add_f32 v153, v153, v255\n\t"
    "v_lshlrev_b32 v254, 16, v49\n\t"
    "v_and_b32 v255, 0xffff0000, v49\n\t"
    "v_add_f32 v154, v154, v254\n\t"
    "v_add_f32 v155, v155, v255\n\t"
    "v_lshlrev_b32 v254, 16, v50\n\t"
    "v_and_b32 v255, 0xffff0000, v50\n\t"
    "v_add_f32 v156, v156, v254\n\t"
    "v_add_f32 v157, v157, v255\n\t"
    "v_lshlrev_b32 v254, 16, v51\n\t"
    "v_and_b32 v255, 0xffff0000, v51\n\t"
    "v_add_f32 v158, v158, v254\n\t"
    "v_add_f32 v159, v159, v255\n\t"
    "s_waitcnt vmcnt(3)\n\t"
    "v_lshlrev_b32 v254, 16, v52\n\t"
    "v_and_b32 v255, 0xffff0000, v52\n\t"
    "v_add_f32 v160, v160, v254\n\t"
    "v_add_f32 v161, v161, v255\n\t"
    "v_lshlrev_b32 v254, 16, v53\n\t"
    "v_and_b32 v255, 0xffff0000, v53\n\t"
    "v_add_f32 v162, v162, v254\n\t"
    "v_add_f32 v163, v163, v255\n\t"
    "v_lshlrev_b32 v254, 16, v54\n\t"
    "v_and_b32 v255, 0xffff0000, v54\n\t"
    "v_add_f32 v164, v164, v254\n\t"
    "v_add_f32 v165, v165, v255\n\t"
    "v_lshlrev_b32 v254, 16, v55\n\t"
    "v_and_b32 v255, 0xffff0000, v55\n\t"
    "v_add_f32 v166, v166, v254\n\t"
    "v_add_f32 v167, v167, v255\n\t"
    "s_waitcnt vmcnt(2)\n\t"
    "v_lshlrev_b32 v254, 16, v56\n\t"
    "v_and_b32 v255, 0xffff0000, v56\n\t"
    "v_add_f32 v152, v152, v254\n\t"
    "v_add_f32 v153, v153, v255\n\t"
    "v_lshlrev_b32 v254, 16, v57\n\t"
    "v_and_b32 v255, 0xffff0000, v57\n\t"
    "v_add_f32 v154, v154, v254\n\t"
    "v_add_f32 v155, v155, v255\n\t"
    "v_lshlrev_b32 v254, 16, v58\n\t"
    "v_and_b32 v255, 0xffff0000, v58\n\t"
    "v_add_f32 v156, v156, v254\n\t"
    "v_add_f32 v157, v157, v255\n\t"
    "v_lshlrev_b32 v254, 16, v59\n\t"
    "v_and_b32 v255, 0xffff0000, v59\n\t"
    "v_add_f32 v158, v158, v254\n\t"
    "v_add_f32 v159, v159, v255\n\t"
    "s_waitcnt vmcnt(1)\n\t"
    "v_lshlrev_b32 v254, 16, v60\n\t"
    "v_and_b32 v255, 0xffff0000, v60\n\t"
    "v_add_f32 v160, v160, v254\n\t"
    "v_add_f32 v161, v161, v255\n\t"
    "v_lshlrev_b32 v254, 16, v61\n\t"
    "v_and_b32 v255, 0xffff0000, v61\n\t"
    "v_add_f32 v162, v162, v254\n\t"
    "v_add_f32 v163, v163, v255\n\t"
    "v_lshlrev_b32 v254, 16, v62\n\t"
    "v_and_b32 v255, 0xffff0000, v62\n\t"
    "v_add_f32 v164, v164, v254\n\t"
    "v_add_f32 v165, v165, v255\n\t"
    "v_lshlrev_b32 v254, 16, v63\n\t"
    "v_and_b32 v255, 0xffff0000, v63\n\t"
    "v_add_f32 v166, v166, v254\n\t"
    "v_add_f32 v167, v167, v255\n\t"
    "v_cvt_pk_bf16_f32 v152, v152, v153\n\t"
    "v_cvt_pk_bf16_f32 v153, v154, v155\n\t"
    "v_cvt_pk_bf16_f32 v154, v156, v157\n\t"
    "v_cvt_pk_bf16_f32 v155, v158, v159\n\t"
    "v_cvt_pk_bf16_f32 v156, v160, v161\n\t"
    "v_cvt_pk_bf16_f32 v157, v162, v163\n\t"
    "v_cvt_pk_bf16_f32 v158, v164, v165\n\t"
    "v_cvt_pk_bf16_f32 v159, v166, v167\n\t"
    "v_permlane16_swap_b32 v152, v154\n\t"
    "v_permlane16_swap_b32 v153, v155\n\t"
    "v_permlane16_swap_b32 v156, v158\n\t"
    "v_permlane16_swap_b32 v157, v159\n\t"
    "v_add_u32 v141, 0x8000, v140\n\t"
    "buffer_store_dwordx4 v[152:155], v140, %[srdout], 0 offen sc1\n\t"
    "buffer_store_dwordx4 v[156:159], v141, %[srdout], 0 offen sc1\n\t"
    "s_branch END_%=\n\t"
    "W3S0_%=:\n\t"
    "s_barrier\n\t"
    "ds_read_b128 v[0:3], v250 offset:16384\n\t"
    "ds_read_b128 v[4:7], v250 offset:18432\n\t"
    "ds_read_b128 v[8:11], v250 offset:16896\n\t"
    "ds_read_b128 v[12:15], v250 offset:18944\n\t"
    "ds_read_b64_tr_b16 v[216:217], v248 offset:0\n\t"
    "ds_read_b64_tr_b16 v[218:219], v248 offset:256\n\t"
    "ds_read_b64_tr_b16 v[220:221], v248 offset:8192\n\t"
    "ds_read_b64_tr_b16 v[222:223], v248 offset:8448\n\t"
    "ds_read_b64_tr_b16 v[224:225], v249 offset:0\n\t"
    "ds_read_b64_tr_b16 v[226:227], v249 offset:256\n\t"
    "ds_read_b64_tr_b16 v[228:229], v249 offset:8192\n\t"
    "ds_read_b64_tr_b16 v[230:231], v249 offset:8448\n\t"
    "ds_read_b64_tr_b16 v[232:233], v248 offset:512\n\t"
    "ds_read_b64_tr_b16 v[234:235], v248 offset:768\n\t"
    "ds_read_b64_tr_b16 v[236:237], v248 offset:8704\n\t"
    "ds_read_b64_tr_b16 v[238:239], v248 offset:8960\n\t"
    "ds_read_b64_tr_b16 v[240:241], v249 offset:512\n\t"
    "ds_read_b64_tr_b16 v[242:243], v249 offset:768\n\t"
    "ds_read_b64_tr_b16 v[244:245], v249 offset:8704\n\t"
    "ds_read_b64_tr_b16 v[246:247], v249 offset:8960\n\t"
    "s_waitcnt lgkmcnt(12)\n\t"
    "v_mfma_f32_16x16x32_bf16 v[152:155], v[216:219], v[0:3], 0\n\t"
    "v_mfma_f32_16x16x32_bf16 v[152:155], v[220:223], v[8:11], v[152:155]\n\t"
    "ds_read_b64_tr_b16 v[216:217], v248 offset:1024\n\t"
    "s_waitcnt lgkmcnt(9)\n\t"
    "v_mfma_f32_16x16x32_bf16 v[156:159], v[224:227], v[0:3], 0\n\t"
    "ds_read_b64_tr_b16 v[218:219], v248 offset:1280\n\t"
    "v_mfma_f32_16x16x32_bf16 v[156:159], v[228:231], v[8:11], v[156:159]\n\t"
    "ds_read_b64_tr_b16 v[220:221], v248 offset:9216\n\t"
    "ds_read_b64_tr_b16 v[222:223], v248 offset:9472\n\t"
    "ds_read_b64_tr_b16 v[224:225], v249 offset:1024\n\t"
    "s_waitcnt lgkmcnt(9)\n\t"
    "v_mfma_f32_16x16x32_bf16 v[168:171], v[232:235], v[0:3], 0\n\t"
    "ds_read_b64_tr_b16 v[226:227], v249 offset:1280\n\t"
    "v_mfma_f32_16x16x32_bf16 v[168:171], v[236:239], v[8:11], v[168:171]\n\t"
    "ds_read_b64_tr_b16 v[228:229], v249 offset:9216\n\t"
    "ds_read_b64_tr_b16 v[230:231], v249 offset:9472\n\t"
    "ds_read_b64_tr_b16 v[232:233], v248 offset:1536\n\t"
    "s_waitcnt lgkmcnt(9)\n\t"
    "v_mfma_f32_16x16x32_bf16 v[172:175], v[240:243], v[0:3], 0\n\t"
    "ds_read_b64_tr_b16 v[234:235], v248 offset:1792\n\t"
    "v_mfma_f32_16x16x32_bf16 v[172:175], v[244:247], v[8:11], v[172:175]\n\t"
    "ds_read_b64_tr_b16 v[236:237], v248 offset:9728\n\t"
    "ds_read_b64_tr_b16 v[238:239], v248 offset:9984\n\t"
    "ds_read_b64_tr_b16 v[240:241], v249 offset:1536\n\t"
    "s_waitcnt lgkmcnt(9)\n\t"
    "v_mfma_f32_16x16x32_bf16 v[184:187], v[216:219], v[0:3], 0\n\t"
    "ds_read_b64_tr_b16 v[242:243], v249 offset:1792\n\t"
    "v_mfma_f32_16x16x32_bf16 v[184:187], v[220:223], v[8:11], v[184:187]\n\t"
    "ds_read_b64_tr_b16 v[244:245], v249 offset:9728\n\t"
    "ds_read_b64_tr_b16 v[246:247], v249 offset:9984\n\t"
    "s_waitcnt lgkmcnt(8)\n\t"
    "v_mfma_f32_16x16x32_bf16 v[188:191], v[224:227], v[0:3], 0\n\t"
    "v_mfma_f32_16x16x32_bf16 v[188:191], v[228:231], v[8:11], v[188:191]\n\t"
    "s_waitcnt lgkmcnt(0)\n\t"
    "s_barrier\n\t"
    "ds_read_b128 v[16:19], v250 offset:49152\n\t"
    "ds_read_b128 v[20:23], v250 offset:51200\n\t"
    "ds_read_b128 v[24:27], v250 offset:49664\n\t"
    "v_mfma_f32_16x16x32_bf16 v[200:203], v[232:235], v[0:3], 0\n\t"
    "ds_read_b128 v[28:31], v250 offset:51712\n\t"
    "ds_read_b64_tr_b16 v[216:217], v248 offset:32768\n\t"
    "ds_read_b64_tr_b16 v[218:219], v248 offset:33024\n\t"
    "v_mfma_f32_16x16x32_bf16 v[200:203], v[236:239], v[8:11], v[200:203]\n\t"
    "ds_read_b64_tr_b16 v[220:221], v248 offset:40960\n\t"
    "ds_read_b64_tr_b16 v[222:223], v248 offset:41216\n\t"
    "ds_read_b64_tr_b16 v[224:225], v249 offset:32768\n\t"
    "ds_read_b64_tr_b16 v[226:227], v249 offset:33024\n\t"
    "ds_read_b64_tr_b16 v[228:229], v249 offset:40960\n\t"
    "ds_read_b64_tr_b16 v[230:231], v249 offset:41216\n\t"
    "ds_read_b64_tr_b16 v[232:233], v248 offset:33280\n\t"
    "v_mfma_f32_16x16x32_bf16 v[204:207], v[240:243], v[0:3], 0\n\t"
    "ds_read_b64_tr_b16 v[234:235], v248 offset:33536\n\t"
    "v_mfma_f32_16x16x32_bf16 v[204:207], v[244:247], v[8:11], v[204:207]\n\t"
    "ds_read_b64_tr_b16 v[236:237], v248 offset:41472\n\t"
    "ds_read_b64_tr_b16 v[238:239], v248 offset:41728\n\t"
    "ds_read_b64_tr_b16 v[240:241], v249 offset:33280\n\t"
    "s_waitcnt lgkmcnt(9)\n\t"
    "v_mfma_f32_16x16x32_bf16 v[152:155], v[216:219], v[16:19], v[152:155]\n\t"
    "ds_read_b64_tr_b16 v[242:243], v249 offset:33536\n\t"
    "v_mfma_f32_16x16x32_bf16 v[152:155], v[220:223], v[24:27], v[152:155]\n\t"
    "ds_read_b64_tr_b16 v[244:245], v249 offset:41472\n\t"
    "ds_read_b64_tr_b16 v[246:247], v249 offset:41728\n\t"
    "ds_read_b64_tr_b16 v[216:217], v248 offset:33792\n\t"
    "s_waitcnt lgkmcnt(9)\n\t"
    "v_mfma_f32_16x16x32_bf16 v[156:159], v[224:227], v[16:19], v[156:159]\n\t"
    "ds_read_b64_tr_b16 v[218:219], v248 offset:34048\n\t"
    "v_mfma_f32_16x16x32_bf16 v[156:159], v[228:231], v[24:27], v[156:159]\n\t"
    "ds_read_b64_tr_b16 v[220:221], v248 offset:41984\n\t"
    "ds_read_b64_tr_b16 v[222:223], v248 offset:42240\n\t"
    "ds_read_b64_tr_b16 v[224:225], v249 offset:33792\n\t"
    "s_waitcnt lgkmcnt(9)\n\t"
    "v_mfma_f32_16x16x32_bf16 v[168:171], v[232:235], v[16:19], v[168:171]\n\t"
    "ds_read_b64_tr_b16 v[226:227], v249 offset:34048\n\t"
    "v_mfma_f32_16x16x32_bf16 v[168:171], v[236:239], v[24:27], v[168:171]\n\t"
    "ds_read_b64_tr_b16 v[228:229], v249 offset:41984\n\t"
    "ds_read_b64_tr_b16 v[230:231], v249 offset:42240\n\t"
    "ds_read_b64_tr_b16 v[232:233], v248 offset:34304\n\t"
    "s_waitcnt lgkmcnt(9)\n\t"
    "v_mfma_f32_16x16x32_bf16 v[172:175], v[240:243], v[16:19], v[172:175]\n\t"
    "ds_read_b64_tr_b16 v[234:235], v248 offset:34560\n\t"
    "v_mfma_f32_16x16x32_bf16 v[172:175], v[244:247], v[24:27], v[172:175]\n\t"
    "ds_read_b64_tr_b16 v[236:237], v248 offset:42496\n\t"
    "ds_read_b64_tr_b16 v[238:239], v248 offset:42752\n\t"
    "ds_read_b64_tr_b16 v[240:241], v249 offset:34304\n\t"
    "s_waitcnt lgkmcnt(9)\n\t"
    "v_mfma_f32_16x16x32_bf16 v[184:187], v[216:219], v[16:19], v[184:187]\n\t"
    "ds_read_b64_tr_b16 v[242:243], v249 offset:34560\n\t"
    "v_mfma_f32_16x16x32_bf16 v[184:187], v[220:223], v[24:27], v[184:187]\n\t"
    "ds_read_b64_tr_b16 v[244:245], v249 offset:42496\n\t"
    "ds_read_b64_tr_b16 v[246:247], v249 offset:42752\n\t"
    "s_waitcnt lgkmcnt(8)\n\t"
    "v_mfma_f32_16x16x32_bf16 v[188:191], v[224:227], v[16:19], v[188:191]\n\t"
    "v_mfma_f32_16x16x32_bf16 v[188:191], v[228:231], v[24:27], v[188:191]\n\t"
    "s_waitcnt lgkmcnt(0)\n\t"
    "s_barrier\n\t"
    "v_add_u32 v250, 0x10000, v250\n\t"
    "ds_read_b128 v[32:35], v250 offset:32768\n\t"
    "ds_read_b128 v[36:39], v250 offset:34816\n\t"
    "ds_read_b128 v[40:43], v250 offset:33280\n\t"
    "v_mfma_f32_16x16x32_bf16 v[200:203], v[232:235], v[16:19], v[200:203]\n\t"
    "ds_read_b128 v[44:47], v250 offset:35328\n\t"
    "v_add_u32 v248, 0x10000, v248\n\t"
    "v_add_u32 v249, 0x10000, v249\n\t"
    "ds_read_b64_tr_b16 v[216:217], v248 offset:16384\n\t"
    "v_mfma_f32_16x16x32_bf16 v[200:203], v[236:239], v[24:27], v[200:203]\n\t"
    "ds_read_b64_tr_b16 v[218:219], v248 offset:16640\n\t"
    "ds_read_b64_tr_b16 v[220:221], v248 offset:24576\n\t"
    "ds_read_b64_tr_b16 v[222:223], v248 offset:24832\n\t"
    "ds_read_b64_tr_b16 v[224:225], v249 offset:16384\n\t"
    "ds_read_b64_tr_b16 v[226:227], v249 offset:16640\n\t"
    "ds_read_b64_tr_b16 v[228:229], v249 offset:24576\n\t"
    "ds_read_b64_tr_b16 v[230:231], v249 offset:24832\n\t"
    "ds_read_b64_tr_b16 v[232:233], v248 offset:16896\n\t"
    "v_mfma_f32_16x16x32_bf16 v[204:207], v[240:243], v[16:19], v[204:207]\n\t"
    "ds_read_b64_tr_b16 v[234:235], v248 offset:17152\n\t"
    "v_mfma_f32_16x16x32_bf16 v[204:207], v[244:247], v[24:27], v[204:207]\n\t"
    "ds_read_b64_tr_b16 v[236:237], v248 offset:25088\n\t"
    "ds_read_b64_tr_b16 v[238:239], v248 offset:25344\n\t"
    "ds_read_b64_tr_b16 v[240:241], v249 offset:16896\n\t"
    "s_waitcnt lgkmcnt(9)\n\t"
    "v_mfma_f32_16x16x32_bf16 v[152:155], v[216:219], v[32:35], v[152:155]\n\t"
    "ds_read_b64_tr_b16 v[242:243], v249 offset:17152\n\t"
    "v_mfma_f32_16x16x32_bf16 v[152:155], v[220:223], v[40:43], v[152:155]\n\t"
    "ds_read_b64_tr_b16 v[244:245], v249 offset:25088\n\t"
    "ds_read_b64_tr_b16 v[246:247], v249 offset:25344\n\t"
    "ds_read_b64_tr_b16 v[216:217], v248 offset:17408\n\t"
    "s_waitcnt lgkmcnt(9)\n\t"
    "v_mfma_f32_16x16x32_bf16 v[156:159], v[224:227], v[32:35], v[156:159]\n\t"
    "ds_read_b64_tr_b16 v[218:219], v248 offset:17664\n\t"
    "v_mfma_f32_16x16x32_bf16 v[156:159], v[228:231], v[40:43], v[156:159]\n\t"
    "ds_read_b64_tr_b16 v[220:221], v248 offset:25600\n\t"
    "ds_read_b64_tr_b16 v[222:223], v248 offset:25856\n\t"
    "ds_read_b64_tr_b16 v[224:225], v249 offset:17408\n\t"
    "s_waitcnt lgkmcnt(9)\n\t"
    "v_mfma_f32_16x16x32_bf16 v[168:171], v[232:235], v[32:35], v[168:171]\n\t"
    "ds_read_b64_tr_b16 v[226:227], v249 offset:17664\n\t"
    "v_mfma_f32_16x16x32_bf16 v[168:171], v[236:239], v[40:43], v[168:171]\n\t"
    "ds_read_b64_tr_b16 v[228:229], v249 offset:25600\n\t"
    "ds_read_b64_tr_b16 v[230:231], v249 offset:25856\n\t"
    "ds_read_b64_tr_b16 v[232:233], v248 offset:17920\n\t"
    "s_waitcnt lgkmcnt(9)\n\t"
    "v_mfma_f32_16x16x32_bf16 v[172:175], v[240:243], v[32:35], v[172:175]\n\t"
    "ds_read_b64_tr_b16 v[234:235], v248 offset:18176\n\t"
    "v_mfma_f32_16x16x32_bf16 v[172:175], v[244:247], v[40:43], v[172:175]\n\t"
    "ds_read_b64_tr_b16 v[236:237], v248 offset:26112\n\t"
    "ds_read_b64_tr_b16 v[238:239], v248 offset:26368\n\t"
    "ds_read_b64_tr_b16 v[240:241], v249 offset:17920\n\t"
    "s_waitcnt lgkmcnt(9)\n\t"
    "v_mfma_f32_16x16x32_bf16 v[184:187], v[216:219], v[32:35], v[184:187]\n\t"
    "ds_read_b64_tr_b16 v[242:243], v249 offset:18176\n\t"
    "v_mfma_f32_16x16x32_bf16 v[184:187], v[220:223], v[40:43], v[184:187]\n\t"
    "ds_read_b64_tr_b16 v[244:245], v249 offset:26112\n\t"
    "ds_read_b64_tr_b16 v[246:247], v249 offset:26368\n\t"
    "s_waitcnt lgkmcnt(8)\n\t"
    "v_mfma_f32_16x16x32_bf16 v[188:191], v[224:227], v[32:35], v[188:191]\n\t"
    "v_mfma_f32_16x16x32_bf16 v[188:191], v[228:231], v[40:43], v[188:191]\n\t"
    "s_waitcnt lgkmcnt(0)\n\t"
    "s_barrier\n\t"
    "v_add_u32 v250, 0x10000, v250\n\t"
    "ds_read_b128 v[48:51], v250 offset:0\n\t"
    "ds_read_b128 v[52:55], v250 offset:2048\n\t"
    "ds_read_b128 v[56:59], v250 offset:512\n\t"
    "v_mfma_f32_16x16x32_bf16 v[200:203], v[232:235], v[32:35], v[200:203]\n\t"
    "ds_read_b128 v[60:63], v250 offset:2560\n\t"
    "ds_read_b64_tr_b16 v[216:217], v248 offset:49152\n\t"
    "ds_read_b64_tr_b16 v[218:219], v248 offset:49408\n\t"
    "v_mfma_f32_16x16x32_bf16 v[200:203], v[236:239], v[40:43], v[200:203]\n\t"
    "ds_read_b64_tr_b16 v[220:221], v248 offset:57344\n\t"
    "ds_read_b64_tr_b16 v[222:223], v248 offset:57600\n\t"
    "ds_read_b64_tr_b16 v[224:225], v249 offset:49152\n\t"
    "ds_read_b64_tr_b16 v[226:227], v249 offset:49408\n\t"
    "ds_read_b64_tr_b16 v[228:229], v249 offset:57344\n\t"
    "ds_read_b64_tr_b16 v[230:231], v249 offset:57600\n\t"
    "ds_read_b64_tr_b16 v[232:233], v248 offset:49664\n\t"
    "v_mfma_f32_16x16x32_bf16 v[204:207], v[240:243], v[32:35], v[204:207]\n\t"
    "ds_read_b64_tr_b16 v[234:235], v248 offset:49920\n\t"
    "v_mfma_f32_16x16x32_bf16 v[204:207], v[244:247], v[40:43], v[204:207]\n\t"
    "ds_read_b64_tr_b16 v[236:237], v248 offset:57856\n\t"
    "ds_read_b64_tr_b16 v[238:239], v248 offset:58112\n\t"
    "ds_read_b64_tr_b16 v[240:241], v249 offset:49664\n\t"
    "s_waitcnt lgkmcnt(9)\n\t"
    "v_mfma_f32_16x16x32_bf16 v[152:155], v[216:219], v[48:51], v[152:155]\n\t"
    "ds_read_b64_tr_b16 v[242:243], v249 offset:49920\n\t"
    "v_mfma_f32_16x16x32_bf16 v[152:155], v[220:223], v[56:59], v[152:155]\n\t"
    "ds_read_b64_tr_b16 v[244:245], v249 offset:57856\n\t"
    "ds_read_b64_tr_b16 v[246:247], v249 offset:58112\n\t"
    "ds_read_b64_tr_b16 v[216:217], v248 offset:50176\n\t"
    "s_waitcnt lgkmcnt(9)\n\t"
    "v_mfma_f32_16x16x32_bf16 v[156:159], v[224:227], v[48:51], v[156:159]\n\t"
    "ds_read_b64_tr_b16 v[218:219], v248 offset:50432\n\t"
    "v_mfma_f32_16x16x32_bf16 v[156:159], v[228:231], v[56:59], v[156:159]\n\t"
    "ds_read_b64_tr_b16 v[220:221], v248 offset:58368\n\t"
    "ds_read_b64_tr_b16 v[222:223], v248 offset:58624\n\t"
    "ds_read_b64_tr_b16 v[224:225], v249 offset:50176\n\t"
    "s_waitcnt lgkmcnt(9)\n\t"
    "v_mfma_f32_16x16x32_bf16 v[168:171], v[232:235], v[48:51], v[168:171]\n\t"
    "ds_read_b64_tr_b16 v[226:227], v249 offset:50432\n\t"
    "v_mfma_f32_16x16x32_bf16 v[168:171], v[236:239], v[56:59], v[168:171]\n\t"
    "ds_read_b64_tr_b16 v[228:229], v249 offset:58368\n\t"
    "ds_read_b64_tr_b16 v[230:231], v249 offset:58624\n\t"
    "ds_read_b64_tr_b16 v[232:233], v248 offset:50688\n\t"
    "s_waitcnt lgkmcnt(9)\n\t"
    "v_mfma_f32_16x16x32_bf16 v[172:175], v[240:243], v[48:51], v[172:175]\n\t"
    "ds_read_b64_tr_b16 v[234:235], v248 offset:50944\n\t"
    "v_mfma_f32_16x16x32_bf16 v[172:175], v[244:247], v[56:59], v[172:175]\n\t"
    "ds_read_b64_tr_b16 v[236:237], v248 offset:58880\n\t"
    "ds_read_b64_tr_b16 v[238:239], v248 offset:59136\n\t"
    "ds_read_b64_tr_b16 v[240:241], v249 offset:50688\n\t"
    "s_waitcnt lgkmcnt(9)\n\t"
    "v_mfma_f32_16x16x32_bf16 v[184:187], v[216:219], v[48:51], v[184:187]\n\t"
    "ds_read_b64_tr_b16 v[242:243], v249 offset:50944\n\t"
    "v_mfma_f32_16x16x32_bf16 v[184:187], v[220:223], v[56:59], v[184:187]\n\t"
    "ds_read_b64_tr_b16 v[244:245], v249 offset:58880\n\t"
    "ds_read_b64_tr_b16 v[246:247], v249 offset:59136\n\t"
    "s_waitcnt lgkmcnt(8)\n\t"
    "v_mfma_f32_16x16x32_bf16 v[188:191], v[224:227], v[48:51], v[188:191]\n\t"
    "v_mfma_f32_16x16x32_bf16 v[188:191], v[228:231], v[56:59], v[188:191]\n\t"
    "s_waitcnt lgkmcnt(0)\n\t"
    "s_barrier\n\t"
    "v_add_u32 v250, 0xfffe0000, v250\n\t"
    "ds_read_b128 v[64:67], v250 offset:49152\n\t"
    "ds_read_b128 v[68:71], v250 offset:51200\n\t"
    "ds_read_b128 v[72:75], v250 offset:49664\n\t"
    "v_mfma_f32_16x16x32_bf16 v[200:203], v[232:235], v[48:51], v[200:203]\n\t"
    "ds_read_b128 v[76:79], v250 offset:51712\n\t"
    "v_add_u32 v248, 0xffff0000, v248\n\t"
    "v_add_u32 v249, 0xffff0000, v249\n\t"
    "ds_read_b64_tr_b16 v[216:217], v248 offset:32768\n\t"
    "v_mfma_f32_16x16x32_bf16 v[200:203], v[236:239], v[56:59], v[200:203]\n\t"
    "ds_read_b64_tr_b16 v[218:219], v248 offset:33024\n\t"
    "ds_read_b64_tr_b16 v[220:221], v248 offset:40960\n\t"
    "ds_read_b64_tr_b16 v[222:223], v248 offset:41216\n\t"
    "ds_read_b64_tr_b16 v[224:225], v249 offset:32768\n\t"
    "ds_read_b64_tr_b16 v[226:227], v249 offset:33024\n\t"
    "ds_read_b64_tr_b16 v[228:229], v249 offset:40960\n\t"
    "ds_read_b64_tr_b16 v[230:231], v249 offset:41216\n\t"
    "ds_read_b64_tr_b16 v[232:233], v248 offset:33280\n\t"
    "v_mfma_f32_16x16x32_bf16 v[204:207], v[240:243], v[48:51], v[204:207]\n\t"
    "ds_read_b64_tr_b16 v[234:235], v248 offset:33536\n\t"
    "v_mfma_f32_16x16x32_bf16 v[204:207], v[244:247], v[56:59], v[204:207]\n\t"
    "ds_read_b64_tr_b16 v[236:237], v248 offset:41472\n\t"
    "ds_read_b64_tr_b16 v[238:239], v248 offset:41728\n\t"
    "ds_read_b64_tr_b16 v[240:241], v249 offset:33280\n\t"
    "s_waitcnt lgkmcnt(9)\n\t"
    "v_mfma_f32_16x16x32_bf16 v[152:155], v[216:219], v[64:67], v[152:155]\n\t"
    "ds_read_b64_tr_b16 v[242:243], v249 offset:33536\n\t"
    "v_mfma_f32_16x16x32_bf16 v[152:155], v[220:223], v[72:75], v[152:155]\n\t"
    "ds_read_b64_tr_b16 v[244:245], v249 offset:41472\n\t"
    "ds_read_b64_tr_b16 v[246:247], v249 offset:41728\n\t"
    "ds_read_b64_tr_b16 v[216:217], v248 offset:33792\n\t"
    "s_waitcnt lgkmcnt(9)\n\t"
    "v_mfma_f32_16x16x32_bf16 v[156:159], v[224:227], v[64:67], v[156:159]\n\t"
    "ds_read_b64_tr_b16 v[218:219], v248 offset:34048\n\t"
    "v_mfma_f32_16x16x32_bf16 v[156:159], v[228:231], v[72:75], v[156:159]\n\t"
    "ds_read_b64_tr_b16 v[220:221], v248 offset:41984\n\t"
    "ds_read_b64_tr_b16 v[222:223], v248 offset:42240\n\t"
    "ds_read_b64_tr_b16 v[224:225], v249 offset:33792\n\t"
    "s_waitcnt lgkmcnt(9)\n\t"
    "v_mfma_f32_16x16x32_bf16 v[168:171], v[232:235], v[64:67], v[168:171]\n\t"
    "ds_read_b64_tr_b16 v[226:227], v249 offset:34048\n\t"
    "v_mfma_f32_16x16x32_bf16 v[168:171], v[236:239], v[72:75], v[168:171]\n\t"
    "ds_read_b64_tr_b16 v[228:229], v249 offset:41984\n\t"
    "ds_read_b64_tr_b16 v[230:231], v249 offset:42240\n\t"
    "ds_read_b64_tr_b16 v[232:233], v248 offset:34304\n\t"
    "s_waitcnt lgkmcnt(9)\n\t"
    "v_mfma_f32_16x16x32_bf16 v[172:175], v[240:243], v[64:67], v[172:175]\n\t"
    "ds_read_b64_tr_b16 v[234:235], v248 offset:34560\n\t"
    "v_mfma_f32_16x16x32_bf16 v[172:175], v[244:247], v[72:75], v[172:175]\n\t"
    "ds_read_b64_tr_b16 v[236:237], v248 offset:42496\n\t"
    "ds_read_b64_tr_b16 v[238:239], v248 offset:42752\n\t"
    "ds_read_b64_tr_b16 v[240:241], v249 offset:34304\n\t"
    "s_waitcnt lgkmcnt(9)\n\t"
    "v_mfma_f32_16x16x32_bf16 v[184:187], v[216:219], v[64:67], v[184:187]\n\t"
    "ds_read_b64_tr_b16 v[242:243], v249 offset:34560\n\t"
    "v_mfma_f32_16x16x32_bf16 v[184:187], v[220:223], v[72:75], v[184:187]\n\t"
    "ds_read_b64_tr_b16 v[244:245], v249 offset:42496\n\t"
    "ds_read_b64_tr_b16 v[246:247], v249 offset:42752\n\t"
    "s_waitcnt lgkmcnt(8)\n\t"
    "v_mfma_f32_16x16x32_bf16 v[188:191], v[224:227], v[64:67], v[188:191]\n\t"
    "v_mfma_f32_16x16x32_bf16 v[188:191], v[228:231], v[72:75], v[188:191]\n\t"
    "s_waitcnt lgkmcnt(0)\n\t"
    "s_barrier\n\t"
    "v_add_u32 v250, 0x10000, v250\n\t"
    "ds_read_b128 v[80:83], v250 offset:32768\n\t"
    "ds_read_b128 v[84:87], v250 offset:34816\n\t"
    "ds_read_b128 v[88:91], v250 offset:33280\n\t"
    "v_mfma_f32_16x16x32_bf16 v[200:203], v[232:235], v[64:67], v[200:203]\n\t"
    "ds_read_b128 v[92:95], v250 offset:35328\n\t"
    "v_add_u32 v248, 0x10000, v248\n\t"
    "v_add_u32 v249, 0x10000, v249\n\t"
    "ds_read_b64_tr_b16 v[216:217], v248 offset:16384\n\t"
    "v_mfma_f32_16x16x32_bf16 v[200:203], v[236:239], v[72:75], v[200:203]\n\t"
    "ds_read_b64_tr_b16 v[218:219], v248 offset:16640\n\t"
    "ds_read_b64_tr_b16 v[220:221], v248 offset:24576\n\t"
    "ds_read_b64_tr_b16 v[222:223], v248 offset:24832\n\t"
    "ds_read_b64_tr_b16 v[224:225], v249 offset:16384\n\t"
    "ds_read_b64_tr_b16 v[226:227], v249 offset:16640\n\t"
    "ds_read_b64_tr_b16 v[228:229], v249 offset:24576\n\t"
    "ds_read_b64_tr_b16 v[230:231], v249 offset:24832\n\t"
    "ds_read_b64_tr_b16 v[232:233], v248 offset:16896\n\t"
    "v_mfma_f32_16x16x32_bf16 v[204:207], v[240:243], v[64:67], v[204:207]\n\t"
    "ds_read_b64_tr_b16 v[234:235], v248 offset:17152\n\t"
    "v_mfma_f32_16x16x32_bf16 v[204:207], v[244:247], v[72:75], v[204:207]\n\t"
    "ds_read_b64_tr_b16 v[236:237], v248 offset:25088\n\t"
    "ds_read_b64_tr_b16 v[238:239], v248 offset:25344\n\t"
    "ds_read_b64_tr_b16 v[240:241], v249 offset:16896\n\t"
    "s_waitcnt lgkmcnt(9)\n\t"
    "v_mfma_f32_16x16x32_bf16 v[152:155], v[216:219], v[80:83], v[152:155]\n\t"
    "ds_read_b64_tr_b16 v[242:243], v249 offset:17152\n\t"
    "v_mfma_f32_16x16x32_bf16 v[152:155], v[220:223], v[88:91], v[152:155]\n\t"
    "ds_read_b64_tr_b16 v[244:245], v249 offset:25088\n\t"
    "ds_read_b64_tr_b16 v[246:247], v249 offset:25344\n\t"
    "ds_read_b64_tr_b16 v[216:217], v248 offset:17408\n\t"
    "s_waitcnt lgkmcnt(9)\n\t"
    "v_mfma_f32_16x16x32_bf16 v[156:159], v[224:227], v[80:83], v[156:159]\n\t"
    "ds_read_b64_tr_b16 v[218:219], v248 offset:17664\n\t"
    "v_mfma_f32_16x16x32_bf16 v[156:159], v[228:231], v[88:91], v[156:159]\n\t"
    "ds_read_b64_tr_b16 v[220:221], v248 offset:25600\n\t"
    "ds_read_b64_tr_b16 v[222:223], v248 offset:25856\n\t"
    "ds_read_b64_tr_b16 v[224:225], v249 offset:17408\n\t"
    "s_waitcnt lgkmcnt(9)\n\t"
    "v_mfma_f32_16x16x32_bf16 v[168:171], v[232:235], v[80:83], v[168:171]\n\t"
    "ds_read_b64_tr_b16 v[226:227], v249 offset:17664\n\t"
    "v_mfma_f32_16x16x32_bf16 v[168:171], v[236:239], v[88:91], v[168:171]\n\t"
    "ds_read_b64_tr_b16 v[228:229], v249 offset:25600\n\t"
    "ds_read_b64_tr_b16 v[230:231], v249 offset:25856\n\t"
    "ds_read_b64_tr_b16 v[232:233], v248 offset:17920\n\t"
    "s_waitcnt lgkmcnt(9)\n\t"
    "v_mfma_f32_16x16x32_bf16 v[172:175], v[240:243], v[80:83], v[172:175]\n\t"
    "ds_read_b64_tr_b16 v[234:235], v248 offset:18176\n\t"
    "v_mfma_f32_16x16x32_bf16 v[172:175], v[244:247], v[88:91], v[172:175]\n\t"
    "ds_read_b64_tr_b16 v[236:237], v248 offset:26112\n\t"
    "ds_read_b64_tr_b16 v[238:239], v248 offset:26368\n\t"
    "ds_read_b64_tr_b16 v[240:241], v249 offset:17920\n\t"
    "s_waitcnt lgkmcnt(9)\n\t"
    "v_mfma_f32_16x16x32_bf16 v[184:187], v[216:219], v[80:83], v[184:187]\n\t"
    "ds_read_b64_tr_b16 v[242:243], v249 offset:18176\n\t"
    "v_mfma_f32_16x16x32_bf16 v[184:187], v[220:223], v[88:91], v[184:187]\n\t"
    "ds_read_b64_tr_b16 v[244:245], v249 offset:26112\n\t"
    "ds_read_b64_tr_b16 v[246:247], v249 offset:26368\n\t"
    "s_waitcnt lgkmcnt(8)\n\t"
    "v_mfma_f32_16x16x32_bf16 v[188:191], v[224:227], v[80:83], v[188:191]\n\t"
    "v_mfma_f32_16x16x32_bf16 v[188:191], v[228:231], v[88:91], v[188:191]\n\t"
    "s_waitcnt lgkmcnt(0)\n\t"
    "s_barrier\n\t"
    "v_add_u32 v250, 0x10000, v250\n\t"
    "ds_read_b128 v[96:99], v250 offset:0\n\t"
    "ds_read_b128 v[100:103], v250 offset:2048\n\t"
    "ds_read_b128 v[104:107], v250 offset:512\n\t"
    "v_mfma_f32_16x16x32_bf16 v[200:203], v[232:235], v[80:83], v[200:203]\n\t"
    "ds_read_b128 v[108:111], v250 offset:2560\n\t"
    "ds_read_b64_tr_b16 v[216:217], v248 offset:49152\n\t"
    "ds_read_b64_tr_b16 v[218:219], v248 offset:49408\n\t"
    "v_mfma_f32_16x16x32_bf16 v[200:203], v[236:239], v[88:91], v[200:203]\n\t"
    "ds_read_b64_tr_b16 v[220:221], v248 offset:57344\n\t"
    "ds_read_b64_tr_b16 v[222:223], v248 offset:57600\n\t"
    "ds_read_b64_tr_b16 v[224:225], v249 offset:49152\n\t"
    "ds_read_b64_tr_b16 v[226:227], v249 offset:49408\n\t"
    "ds_read_b64_tr_b16 v[228:229], v249 offset:57344\n\t"
    "ds_read_b64_tr_b16 v[230:231], v249 offset:57600\n\t"
    "ds_read_b64_tr_b16 v[232:233], v248 offset:49664\n\t"
    "v_mfma_f32_16x16x32_bf16 v[204:207], v[240:243], v[80:83], v[204:207]\n\t"
    "ds_read_b64_tr_b16 v[234:235], v248 offset:49920\n\t"
    "v_mfma_f32_16x16x32_bf16 v[204:207], v[244:247], v[88:91], v[204:207]\n\t"
    "ds_read_b64_tr_b16 v[236:237], v248 offset:57856\n\t"
    "ds_read_b64_tr_b16 v[238:239], v248 offset:58112\n\t"
    "ds_read_b64_tr_b16 v[240:241], v249 offset:49664\n\t"
    "s_waitcnt lgkmcnt(9)\n\t"
    "v_mfma_f32_16x16x32_bf16 v[152:155], v[216:219], v[96:99], v[152:155]\n\t"
    "ds_read_b64_tr_b16 v[242:243], v249 offset:49920\n\t"
    "v_mfma_f32_16x16x32_bf16 v[152:155], v[220:223], v[104:107], v[152:155]\n\t"
    "ds_read_b64_tr_b16 v[244:245], v249 offset:57856\n\t"
    "ds_read_b64_tr_b16 v[246:247], v249 offset:58112\n\t"
    "ds_read_b64_tr_b16 v[216:217], v248 offset:50176\n\t"
    "s_waitcnt lgkmcnt(9)\n\t"
    "v_mfma_f32_16x16x32_bf16 v[156:159], v[224:227], v[96:99], v[156:159]\n\t"
    "ds_read_b64_tr_b16 v[218:219], v248 offset:50432\n\t"
    "v_mfma_f32_16x16x32_bf16 v[156:159], v[228:231], v[104:107], v[156:159]\n\t"
    "ds_read_b64_tr_b16 v[220:221], v248 offset:58368\n\t"
    "ds_read_b64_tr_b16 v[222:223], v248 offset:58624\n\t"
    "ds_read_b64_tr_b16 v[224:225], v249 offset:50176\n\t"
    "s_waitcnt lgkmcnt(9)\n\t"
    "v_mfma_f32_16x16x32_bf16 v[168:171], v[232:235], v[96:99], v[168:171]\n\t"
    "ds_read_b64_tr_b16 v[226:227], v249 offset:50432\n\t"
    "v_mfma_f32_16x16x32_bf16 v[168:171], v[236:239], v[104:107], v[168:171]\n\t"
    "ds_read_b64_tr_b16 v[228:229], v249 offset:58368\n\t"
    "ds_read_b64_tr_b16 v[230:231], v249 offset:58624\n\t"
    "ds_read_b64_tr_b16 v[232:233], v248 offset:50688\n\t"
    "s_waitcnt lgkmcnt(9)\n\t"
    "v_mfma_f32_16x16x32_bf16 v[172:175], v[240:243], v[96:99], v[172:175]\n\t"
    "ds_read_b64_tr_b16 v[234:235], v248 offset:50944\n\t"
    "v_mfma_f32_16x16x32_bf16 v[172:175], v[244:247], v[104:107], v[172:175]\n\t"
    "ds_read_b64_tr_b16 v[236:237], v248 offset:58880\n\t"
    "ds_read_b64_tr_b16 v[238:239], v248 offset:59136\n\t"
    "ds_read_b64_tr_b16 v[240:241], v249 offset:50688\n\t"
    "s_waitcnt lgkmcnt(9)\n\t"
    "v_mfma_f32_16x16x32_bf16 v[184:187], v[216:219], v[96:99], v[184:187]\n\t"
    "ds_read_b64_tr_b16 v[242:243], v249 offset:50944\n\t"
    "v_mfma_f32_16x16x32_bf16 v[184:187], v[220:223], v[104:107], v[184:187]\n\t"
    "ds_read_b64_tr_b16 v[244:245], v249 offset:58880\n\t"
    "ds_read_b64_tr_b16 v[246:247], v249 offset:59136\n\t"
    "s_waitcnt lgkmcnt(8)\n\t"
    "v_mfma_f32_16x16x32_bf16 v[188:191], v[224:227], v[96:99], v[188:191]\n\t"
    "v_mfma_f32_16x16x32_bf16 v[188:191], v[228:231], v[104:107], v[188:191]\n\t"
    "s_waitcnt lgkmcnt(0)\n\t"
    "s_barrier\n\t"
    "v_add_u32 v250, 0xfffe0000, v250\n\t"
    "ds_read_b128 v[112:115], v250 offset:49152\n\t"
    "ds_read_b128 v[116:119], v250 offset:51200\n\t"
    "ds_read_b128 v[120:123], v250 offset:49664\n\t"
    "v_mfma_f32_16x16x32_bf16 v[200:203], v[232:235], v[96:99], v[200:203]\n\t"
    "ds_read_b128 v[124:127], v250 offset:51712\n\t"
    "v_add_u32 v248, 0xffff0000, v248\n\t"
    "v_add_u32 v249, 0xffff0000, v249\n\t"
    "ds_read_b64_tr_b16 v[216:217], v248 offset:32768\n\t"
    "v_mfma_f32_16x16x32_bf16 v[200:203], v[236:239], v[104:107], v[200:203]\n\t"
    "ds_read_b64_tr_b16 v[218:219], v248 offset:33024\n\t"
    "ds_read_b64_tr_b16 v[220:221], v248 offset:40960\n\t"
    "ds_read_b64_tr_b16 v[222:223], v248 offset:41216\n\t"
    "ds_read_b64_tr_b16 v[224:225], v249 offset:32768\n\t"
    "ds_read_b64_tr_b16 v[226:227], v249 offset:33024\n\t"
    "ds_read_b64_tr_b16 v[228:229], v249 offset:40960\n\t"
    "ds_read_b64_tr_b16 v[230:231], v249 offset:41216\n\t"
    "ds_read_b64_tr_b16 v[232:233], v248 offset:33280\n\t"
    "v_mfma_f32_16x16x32_bf16 v[204:207], v[240:243], v[96:99], v[204:207]\n\t"
    "ds_read_b64_tr_b16 v[234:235], v248 offset:33536\n\t"
    "v_mfma_f32_16x16x32_bf16 v[204:207], v[244:247], v[104:107], v[204:207]\n\t"
    "ds_read_b64_tr_b16 v[236:237], v248 offset:41472\n\t"
    "ds_read_b64_tr_b16 v[238:239], v248 offset:41728\n\t"
    "ds_read_b64_tr_b16 v[240:241], v249 offset:33280\n\t"
    "s_waitcnt lgkmcnt(9)\n\t"
    "v_mfma_f32_16x16x32_bf16 v[152:155], v[216:219], v[112:115], v[152:155]\n\t"
    "ds_read_b64_tr_b16 v[242:243], v249 offset:33536\n\t"
    "v_mfma_f32_16x16x32_bf16 v[152:155], v[220:223], v[120:123], v[152:155]\n\t"
    "ds_read_b64_tr_b16 v[244:245], v249 offset:41472\n\t"
    "ds_read_b64_tr_b16 v[246:247], v249 offset:41728\n\t"
    "ds_read_b64_tr_b16 v[216:217], v248 offset:33792\n\t"
    "s_waitcnt lgkmcnt(9)\n\t"
    "v_mfma_f32_16x16x32_bf16 v[156:159], v[224:227], v[112:115], v[156:159]\n\t"
    "ds_read_b64_tr_b16 v[218:219], v248 offset:34048\n\t"
    "v_mfma_f32_16x16x32_bf16 v[156:159], v[228:231], v[120:123], v[156:159]\n\t"
    "ds_read_b64_tr_b16 v[220:221], v248 offset:41984\n\t"
    "ds_read_b64_tr_b16 v[222:223], v248 offset:42240\n\t"
    "ds_read_b64_tr_b16 v[224:225], v249 offset:33792\n\t"
    "s_waitcnt lgkmcnt(9)\n\t"
    "v_mfma_f32_16x16x32_bf16 v[168:171], v[232:235], v[112:115], v[168:171]\n\t"
    "ds_read_b64_tr_b16 v[226:227], v249 offset:34048\n\t"
    "v_mfma_f32_16x16x32_bf16 v[168:171], v[236:239], v[120:123], v[168:171]\n\t"
    "ds_read_b64_tr_b16 v[228:229], v249 offset:41984\n\t"
    "v_mul_f32 v152, 0x3e0293ee, v152\n\t"
    "v_mul_f32 v153, 0x3e0293ee, v153\n\t"
    "v_cvt_pk_bf16_f32 v152, v152, v153\n\t"
    "ds_read_b64_tr_b16 v[230:231], v249 offset:42240\n\t"
    "v_mul_f32 v154, 0x3e0293ee, v154\n\t"
    "v_mul_f32 v155, 0x3e0293ee, v155\n\t"
    "v_cvt_pk_bf16_f32 v153, v154, v155\n\t"
    "ds_read_b64_tr_b16 v[232:233], v248 offset:34304\n\t"
    "v_mul_f32 v156, 0x3e0293ee, v156\n\t"
    "v_mul_f32 v157, 0x3e0293ee, v157\n\t"
    "v_cvt_pk_bf16_f32 v154, v156, v157\n\t"
    "s_waitcnt lgkmcnt(9)\n\t"
    "v_mfma_f32_16x16x32_bf16 v[172:175], v[240:243], v[112:115], v[172:175]\n\t"
    "ds_read_b64_tr_b16 v[234:235], v248 offset:34560\n\t"
    "v_mul_f32 v158, 0x3e0293ee, v158\n\t"
    "v_mul_f32 v159, 0x3e0293ee, v159\n\t"
    "v_cvt_pk_bf16_f32 v155, v158, v159\n\t"
    "v_mfma_f32_16x16x32_bf16 v[172:175], v[244:247], v[120:123], v[172:175]\n\t"
    "ds_read_b64_tr_b16 v[236:237], v248 offset:42496\n\t"
    "ds_read_b64_tr_b16 v[238:239], v248 offset:42752\n\t"
    "ds_read_b64_tr_b16 v[240:241], v249 offset:34304\n\t"
    "s_waitcnt lgkmcnt(9)\n\t"
    "v_mfma_f32_16x16x32_bf16 v[184:187], v[216:219], v[112:115], v[184:187]\n\t"
    "ds_read_b64_tr_b16 v[242:243], v249 offset:34560\n\t"
    "v_mfma_f32_16x16x32_bf16 v[184:187], v[220:223], v[120:123], v[184:187]\n\t"
    "ds_read_b64_tr_b16 v[244:245], v249 offset:42496\n\t"
    "v_permlane16_swap_b32 v152, v154\n\t"
    "v_permlane16_swap_b32 v153, v155\n\t"
    "ds_read_b64_tr_b16 v[246:247], v249 offset:42752\n\t"
    "v_mul_f32 v168, 0x3e0293ee, v168\n\t"
    "v_mul_f32 v169, 0x3e0293ee, v169\n\t"
    "v_cvt_pk_bf16_f32 v160, v168, v169\n\t"
    "v_mul_f32 v170, 0x3e0293ee, v170\n\t"
    "v_mul_f32 v171, 0x3e0293ee, v171\n\t"
    "v_cvt_pk_bf16_f32 v161, v170, v171\n\t"
    "s_waitcnt lgkmcnt(8)\n\t"
    "v_mfma_f32_16x16x32_bf16 v[188:191], v[224:227], v[112:115], v[188:191]\n\t"
    "v_mul_f32 v172, 0x3e0293ee, v172\n\t"
    "v_mul_f32 v173, 0x3e0293ee, v173\n\t"
    "v_cvt_pk_bf16_f32 v162, v172, v173\n\t"
    "v_mul_f32 v174, 0x3e0293ee, v174\n\t"
    "v_mfma_f32_16x16x32_bf16 v[188:191], v[228:231], v[120:123], v[188:191]\n\t"
    "v_mul_f32 v175, 0x3e0293ee, v175\n\t"
    "v_cvt_pk_bf16_f32 v163, v174, v175\n\t"
    "s_waitcnt lgkmcnt(0)\n\t"
    "s_barrier\n\t"
    "v_mov_b32 v250, %[lds]\n\t"
    "v_bfe_u32 v254, v253, 3, 1\n\t"
    "v_lshl_add_u32 v250, v254, 11, v250\n\t"
    "v_bfe_u32 v254, v253, 0, 3\n\t"
    "v_lshl_add_u32 v250, v254, 6, v250\n\t"
    "v_mfma_f32_16x16x32_bf16 v[200:203], v[232:235], v[112:115], v[200:203]\n\t"
    "v_bfe_u32 v254, v253, 4, 1\n\t"
    "v_lshl_add_u32 v250, v254, 5, v250\n\t"
    "v_bfe_u32 v254, v253, 5, 1\n\t"
    "v_lshl_add_u32 v250, v254, 4, v250\n\t"
    "v_mfma_f32_16x16x32_bf16 v[200:203], v[236:239], v[120:123], v[200:203]\n\t"
    "v_bfe_u32 v254, v253, 3, 1\n\t"
    "v_lshlrev_b32 v254, 4, v254\n\t"
    "v_xor_b32 v250, v250, v254\n\t"
    "v_add_u32 v250, 0x20000, v250\n\t"
    "ds_read_b128 v[216:219], v250 offset:16384\n\t"
    "ds_read_b128 v[220:223], v250 offset:16896\n\t"
    "ds_read_b128 v[224:227], v250 offset:17408\n\t"
    "ds_read_b128 v[228:231], v250 offset:17920\n\t"
    "ds_read_b128 v[232:235], v250 offset:20480\n\t"
    "v_mfma_f32_16x16x32_bf16 v[204:207], v[240:243], v[112:115], v[204:207]\n\t"
    "ds_read_b128 v[236:239], v250 offset:20992\n\t"
    "v_mfma_f32_16x16x32_bf16 v[204:207], v[244:247], v[120:123], v[204:207]\n\t"
    "v_permlane16_swap_b32 v160, v162\n\t"
    "v_permlane16_swap_b32 v161, v163\n\t"
    "v_mul_f32 v184, 0x3e0293ee, v184\n\t"
    "v_mul_f32 v185, 0x3e0293ee, v185\n\t"
    "v_cvt_pk_bf16_f32 v168, v184, v185\n\t"
    "v_mul_f32 v186, 0x3e0293ee, v186\n\t"
    "ds_read_b128 v[240:243], v250 offset:21504\n\t"
    "v_mul_f32 v187, 0x3e0293ee, v187\n\t"
    "v_cvt_pk_bf16_f32 v169, v186, v187\n\t"
    "v_mul_f32 v188, 0x3e0293ee, v188\n\t"
    "s_waitcnt lgkmcnt(5)\n\t"
    "v_mfma_f32_16x16x32_bf16 v[120:123], v[216:219], v[152:155], 0\n\t"
    "ds_read_b128 v[244:247], v250 offset:22016\n\t"
    "v_mul_f32 v189, 0x3e0293ee, v189\n\t"
    "v_cvt_pk_bf16_f32 v170, v188, v189\n\t"
    "v_mul_f32 v190, 0x3e0293ee, v190\n\t"
    "v_mfma_f32_16x16x32_bf16 v[120:123], v[220:223], v[160:163], v[120:123]\n\t"
    "v_mul_f32 v191, 0x3e0293ee, v191\n\t"
    "v_cvt_pk_bf16_f32 v171, v190, v191\n\t"
    "v_permlane16_swap_b32 v168, v170\n\t"
    "s_nop 0\n\t"
    "v_permlane16_swap_b32 v169, v171\n\t"
    "s_waitcnt lgkmcnt(4)\n\t"
    "s_nop 0\n\t"
    "v_mfma_f32_16x16x32_bf16 v[120:123], v[224:227], v[168:171], v[120:123]\n\t"
    "v_mul_f32 v200, 0x3e0293ee, v200\n\t"
    "v_mul_f32 v201, 0x3e0293ee, v201\n\t"
    "v_cvt_pk_bf16_f32 v176, v200, v201\n\t"
    "v_mul_f32 v202, 0x3e0293ee, v202\n\t"
    "v_mul_f32 v203, 0x3e0293ee, v203\n\t"
    "v_cvt_pk_bf16_f32 v177, v202, v203\n\t"
    "v_mul_f32 v204, 0x3e0293ee, v204\n\t"
    "v_mul_f32 v205, 0x3e0293ee, v205\n\t"
    "v_cvt_pk_bf16_f32 v178, v204, v205\n\t"
    "v_mul_f32 v206, 0x3e0293ee, v206\n\t"
    "v_mul_f32 v207, 0x3e0293ee, v207\n\t"
    "v_cvt_pk_bf16_f32 v179, v206, v207\n\t"
    "v_permlane16_swap_b32 v176, v178\n\t"
    "s_nop 0\n\t"
    "v_permlane16_swap_b32 v177, v179\n\t"
    "s_nop 1\n\t"
    "v_mfma_f32_16x16x32_bf16 v[120:123], v[228:231], v[176:179], v[120:123]\n\t"
    "s_waitcnt vmcnt(0)\n\t"
    "ds_read_b128 v[216:219], v250 offset:24576\n\t"
    "s_waitcnt lgkmcnt(3)\n\t"
    "v_mfma_f32_16x16x32_bf16 v[124:127], v[232:235], v[152:155], 0\n\t"
    "ds_read_b128 v[220:223], v250 offset:25088\n\t"
    "v_mfma_f32_16x16x32_bf16 v[124:127], v[236:239], v[160:163], v[124:127]\n\t"
    "ds_read_b128 v[224:227], v250 offset:25600\n\t"
    "s_waitcnt lgkmcnt(3)\n\t"
    "v_mfma_f32_16x16x32_bf16 v[124:127], v[240:243], v[168:171], v[124:127]\n\t"
    "ds_read_b128 v[228:231], v250 offset:26112\n\t"
    "v_mfma_f32_16x16x32_bf16 v[124:127], v[244:247], v[176:179], v[124:127]\n\t"
    "ds_read_b128 v[232:235], v250 offset:28672\n\t"
    "s_waitcnt lgkmcnt(3)\n\t"
    "v_mfma_f32_16x16x32_bf16 v[104:107], v[216:219], v[152:155], 0\n\t"
    "ds_read_b128 v[236:239], v250 offset:29184\n\t"
    "v_mfma_f32_16x16x32_bf16 v[104:107], v[220:223], v[160:163], v[104:107]\n\t"
    "ds_read_b128 v[240:243], v250 offset:29696\n\t"
    "v_max3_f32 v192, v120, v121, v122\n\t"
    "v_max3_f32 v193, v128, v129, v130\n\t"
    "v_max3_f32 v192, v192, v123, v124\n\t"
    "s_waitcnt lgkmcnt(3)\n\t"
    "v_mfma_f32_16x16x32_bf16 v[104:107], v[224:227], v[168:171], v[104:107]\n\t"
    "ds_read_b128 v[244:247], v250 offset:30208\n\t"
    "v_max3_f32 v193, v193, v131, v132\n\t"
    "v_max3_f32 v192, v192, v125, v126\n\t"
    "v_max3_f32 v193, v193, v133, v134\n\t"
    "v_mfma_f32_16x16x32_bf16 v[104:107], v[228:231], v[176:179], v[104:107]\n\t"
    "v_max_f32 v192, v192, v127\n\t"
    "v_max_f32 v193, v193, v135\n\t"
    "v_mov_b32 v194, v192\n\t"
    "v_mov_b32 v195, v193\n\t"
    "s_nop 0\n\t"
    "v_permlane32_swap_b32 v192, v194\n\t"
    "v_permlane32_swap_b32 v193, v195\n\t"
    "v_max_f32 v192, v192, v194\n\t"
    "v_max_f32 v193, v193, v195\n\t"
    "v_mov_b32 v194, v192\n\t"
    "v_mov_b32 v195, v193\n\t"
    "s_nop 0\n\t"
    "v_permlane16_swap_b32 v192, v194\n\t"
    "v_permlane16_swap_b32 v193, v195\n\t"
    "v_max_f32 v192, v192, v194\n\t"
    "v_max_f32 v193, v193, v195\n\t"
    "v_mov_b32 v251, 0\n\t"
    "v_mov_b32 v252, 0\n\t"
    "s_waitcnt lgkmcnt(0)\n\t"
    "s_barrier\n\t"
    "v_mov_b32 v248, %[lds]\n\t"
    "v_bfe_u32 v254, v253, 5, 1\n\t"
    "v_sub_f32 v184, 0xc1c00000, v192\n\t"
    "v_sub_f32 v188, s60, v193\n\t"
    "v_mfma_f32_16x16x32_bf16 v[108:111], v[232:235], v[152:155], 0\n\t"
    "v_lshl_add_u32 v248, v254, 11, v248\n\t"
    "v_bfe_u32 v254, v253, 2, 3\n\t"
    "v_mov_b32 v185, v184\n\t"
    "v_mov_b32 v186, v184\n\t"
    "v_mfma_f32_16x16x32_bf16 v[108:111], v[236:239], v[160:163], v[108:111]\n\t"
    "v_lshl_add_u32 v248, v254, 6, v248\n\t"
    "v_bfe_u32 v254, v253, 4, 1\n\t"
    "v_mov_b32 v187, v184\n\t"
    "v_mov_b32 v189, v188\n\t"
    "v_mfma_f32_16x16x32_bf16 v[108:111], v[240:243], v[168:171], v[108:111]\n\t"
    "v_lshl_add_u32 v248, v254, 5, v248\n\t"
    "v_bfe_u32 v254, v253, 0, 2\n\t"
    "v_mov_b32 v190, v188\n\t"
    "v_mov_b32 v191, v188\n\t"
    "v_mfma_f32_16x16x32_bf16 v[108:111], v[244:247], v[176:179], v[108:111]\n\t"
    "v_lshl_add_u32 v248, v254, 3, v248\n\t"
    "v_xor_b32 v249, 32, v248\n\t"
    "v_add_f32 v120, v184, v120\n\t"
    "v_add_f32 v121, v184, v121\n\t"
    "v_add_u32 v250, 0xfffe0000, v250\n\t"
    "ds_read_b128 v[216:219], v250 offset:0\n\t"
    "v_add_f32 v122, v184, v122\n\t"
    "v_add_f32 v123, v184, v123\n\t"
    "ds_read_b128 v[220:223], v250 offset:512\n\t"
    "ds_read_b128 v[224:227], v250 offset:1024\n\t"
    "v_add_f32 v124, v184, v124\n\t"
    "v_add_f32 v125, v184, v125\n\t"
    "ds_read_b128 v[228:231], v250 offset:1536\n\t"
    "v_add_f32 v126, v184, v126\n\t"
    "v_add_f32 v127, v184, v127\n\t"
    "ds_read_b128 v[232:235], v250 offset:4096\n\t"
    "s_waitcnt lgkmcnt(3)\n\t"
    "v_mfma_f32_16x16x32_bf16 v[136:139], v[216:219], v[152:155], v[184:187]\n\t"
    "ds_read_b128 v[236:239], v250 offset:4608\n\t"
    "v_mfma_f32_16x16x32_bf16 v[136:139], v[220:223], v[160:163], v[136:139]\n\t"
    "ds_read_b128 v[240:243], v250 offset:5120\n\t"
    "v_exp_f32 v120, v120\n\t"
    "s_waitcnt lgkmcnt(3)\n\t"
    "v_mfma_f32_16x16x32_bf16 v[136:139], v[224:227], v[168:171], v[136:139]\n\t"
    "ds_read_b128 v[244:247], v250 offset:5632\n\t"
    "v_exp_f32 v121, v121\n\t"
    "v_exp_f32 v122, v122\n\t"
    "v_mfma_f32_16x16x32_bf16 v[136:139], v[228:231], v[176:179], v[136:139]\n\t"
    "v_exp_f32 v123, v123\n\t"
    "v_cvt_pk_bf16_f32 v0, v120, v121\n\t"
    "v_exp_f32 v124, v124\n\t"
    "v_exp_f32 v125, v125\n\t"
    "v_cvt_pk_bf16_f32 v1, v122, v123\n\t"
    "v_exp_f32 v126, v126\n\t"
    "v_exp_f32 v127, v127\n\t"
    "v_cvt_pk_bf16_f32 v2, v124, v125\n\t"
    "v_or3_b32 v251, v251, v0, v1\n\t"
    "ds_read_b128 v[216:219], v250 offset:8192\n\t"
    "v_cvt_pk_bf16_f32 v3, v126, v127\n\t"
    "s_waitcnt lgkmcnt(3)\n\t"
    "v_mfma_f32_16x16x32_bf16 v[140:143], v[232:235], v[152:155], v[184:187]\n\t"
    "ds_read_b128 v[220:223], v250 offset:8704\n\t"
    "v_mfma_f32_16x16x32_bf16 v[140:143], v[236:239], v[160:163], v[140:143]\n\t"
    "ds_read_b128 v[224:227], v250 offset:9216\n\t"
    "v_or3_b32 v251, v251, v2, v3\n\t"
    "s_waitcnt lgkmcnt(3)\n\t"
    "v_mfma_f32_16x16x32_bf16 v[140:143], v[240:243], v[168:171], v[140:143]\n\t"
    "ds_read_b128 v[228:231], v250 offset:9728\n\t"
    "v_mfma_f32_16x16x32_bf16 v[140:143], v[244:247], v[176:179], v[140:143]\n\t"
    "v_add_f32 v104, v184, v104\n\t"
    "v_add_f32 v105, v184, v105\n\t"
    "v_add_f32 v106, v184, v106\n\t"
    "v_add_f32 v107, v184, v107\n\t"
    "v_add_f32 v108, v184, v108\n\t"
    "v_add_f32 v109, v184, v109\n\t"
    "v_add_f32 v110, v184, v110\n\t"
    "v_add_f32 v111, v184, v111\n\t"
    "ds_read_b128 v[232:235], v250 offset:12288\n\t"
    "s_waitcnt lgkmcnt(3)\n\t"
    "v_mfma_f32_16x16x32_bf16 v[120:123], v[216:219], v[152:155], v[184:187]\n\t"
    "ds_read_b128 v[236:239], v250 offset:12800\n\t"
    "v_mfma_f32_16x16x32_bf16 v[120:123], v[220:223], v[160:163], v[120:123]\n\t"
    "ds_read_b128 v[240:243], v250 offset:13312\n\t"
    "v_exp_f32 v104, v104\n\t"
    "s_waitcnt lgkmcnt(3)\n\t"
    "v_mfma_f32_16x16x32_bf16 v[120:123], v[224:227], v[168:171], v[120:123]\n\t"
    "ds_read_b128 v[244:247], v250 offset:13824\n\t"
    "v_exp_f32 v105, v105\n\t"
    "v_exp_f32 v106, v106\n\t"
    "v_mfma_f32_16x16x32_bf16 v[120:123], v[228:231], v[176:179], v[120:123]\n\t"
    "v_exp_f32 v107, v107\n\t"
    "v_cvt_pk_bf16_f32 v8, v104, v105\n\t"
    "v_exp_f32 v108, v108\n\t"
    "v_exp_f32 v109, v109\n\t"
    "v_cvt_pk_bf16_f32 v9, v106, v107\n\t"
    "v_exp_f32 v110, v110\n\t"
    "v_exp_f32 v111, v111\n\t"
    "v_cvt_pk_bf16_f32 v10, v108, v109\n\t"
    "v_or3_b32 v251, v251, v8, v9\n\t"
    "s_waitcnt lgkmcnt(0)\n\t"
    "s_barrier\n\t"
    "ds_read_b128 v[216:219], v250 offset:16384\n\t"
    "v_cvt_pk_bf16_f32 v11, v110, v111\n\t"
    "v_mfma_f32_16x16x32_bf16 v[124:127], v[232:235], v[152:155], v[184:187]\n\t"
    "ds_read_b128 v[220:223], v250 offset:16896\n\t"
    "v_mfma_f32_16x16x32_bf16 v[124:127], v[236:239], v[160:163], v[124:127]\n\t"
    "ds_read_b128 v[224:227], v250 offset:17408\n\t"
    "v_or3_b32 v251, v251, v10, v11\n\t"
    "v_mfma_f32_16x16x32_bf16 v[124:127], v[240:243], v[168:171], v[124:127]\n\t"
    "ds_read_b128 v[228:231], v250 offset:17920\n\t"
    "v_mfma_f32_16x16x32_bf16 v[124:127], v[244:247], v[176:179], v[124:127]\n\t"
    "v_exp_f32 v136, v136\n\t"
    "v_exp_f32 v137, v137\n\t"
    "v_exp_f32 v138, v138\n\t"
    "v_exp_f32 v139, v139\n\t"
    "ds_read_b128 v[232:235], v250 offset:20480\n\t"
    "v_cvt_pk_bf16_f32 v16, v136, v137\n\t"
    "v_exp_f32 v140, v140\n\t"
    "s_waitcnt lgkmcnt(3)\n\t"
    "v_mfma_f32_16x16x32_bf16 v[104:107], v[216:219], v[152:155], v[184:187]\n\t"
    "ds_read_b128 v[236:239], v250 offset:20992\n\t"
    "v_exp_f32 v141, v141\n\t"
    "v_cvt_pk_bf16_f32 v17, v138, v139\n\t"
    "v_mfma_f32_16x16x32_bf16 v[104:107], v[220:223], v[160:163], v[104:107]\n\t"
    "ds_read_b128 v[240:243], v250 offset:21504\n\t"
    "v_exp_f32 v142, v142\n\t"
    "v_exp_f32 v143, v143\n\t"
    "s_waitcnt lgkmcnt(3)\n\t"
    "v_mfma_f32_16x16x32_bf16 v[104:107], v[224:227], v[168:171], v[104:107]\n\t"
    "ds_read_b128 v[244:247], v250 offset:22016\n\t"
    "v_cvt_pk_bf16_f32 v18, v140, v141\n\t"
    "v_or3_b32 v251, v251, v16, v17\n\t"
    "v_mfma_f32_16x16x32_bf16 v[104:107], v[228:231], v[176:179], v[104:107]\n\t"
    "v_cvt_pk_bf16_f32 v19, v142, v143\n\t"
    "v_or3_b32 v251, v251, v18, v19\n\t"
    "ds_read_b128 v[216:219], v250 offset:24576\n\t"
    "s_waitcnt lgkmcnt(3)\n\t"
    "v_mfma_f32_16x16x32_bf16 v[108:111], v[232:235], v[152:155], v[184:187]\n\t"
    "ds_read_b128 v[220:223], v250 offset:25088\n\t"
    "v_exp_f32 v120, v120\n\t"
    "v_mfma_f32_16x16x32_bf16 v[108:111], v[236:239], v[160:163], v[108:111]\n\t"
    "ds_read_b128 v[224:227], v250 offset:25600\n\t"
    "v_exp_f32 v121, v121\n\t"
    "v_exp_f32 v122, v122\n\t"
    "s_waitcnt lgkmcnt(3)\n\t"
    "v_mfma_f32_16x16x32_bf16 v[108:111], v[240:243], v[168:171], v[108:111]\n\t"
    "ds_read_b128 v[228:231], v250 offset:26112\n\t"
    "v_exp_f32 v123, v123\n\t"
    "v_cvt_pk_bf16_f32 v24, v120, v121\n\t"
    "v_mfma_f32_16x16x32_bf16 v[108:111], v[244:247], v[176:179], v[108:111]\n\t"
    "v_exp_f32 v124, v124\n\t"
    "v_exp_f32 v125, v125\n\t"
    "v_cvt_pk_bf16_f32 v25, v122, v123\n\t"
    "v_exp_f32 v126, v126\n\t"
    "v_exp_f32 v127, v127\n\t"
    "v_cvt_pk_bf16_f32 v26, v124, v125\n\t"
    "v_or3_b32 v251, v251, v24, v25\n\t"
    "v_cvt_pk_bf16_f32 v27, v126, v127\n\t"
    "ds_read_b128 v[232:235], v250 offset:28672\n\t"
    "s_waitcnt lgkmcnt(3)\n\t"
    "v_mfma_f32_16x16x32_bf16 v[136:139], v[216:219], v[152:155], v[184:187]\n\t"
    "ds_read_b128 v[236:239], v250 offset:29184\n\t"
    "v_or3_b32 v251, v251, v26, v27\n\t"
    "v_mfma_f32_16x16x32_bf16 v[136:139], v[220:223], v[160:163], v[136:139]\n\t"
    "ds_read_b128 v[240:243], v250 offset:29696\n\t"
    "s_waitcnt lgkmcnt(3)\n\t"
    "v_mfma_f32_16x16x32_bf16 v[136:139], v[224:227], v[168:171], v[136:139]\n\t"
    "ds_read_b128 v[244:247], v250 offset:30208\n\t"
    "v_mfma_f32_16x16x32_bf16 v[136:139], v[228:231], v[176:179], v[136:139]\n\t"
    "v_exp_f32 v104, v104\n\t"
    "v_exp_f32 v105, v105\n\t"
    "v_exp_f32 v106, v106\n\t"
    "v_exp_f32 v107, v107\n\t"
    "v_cvt_pk_bf16_f32 v32, v104, v105\n\t"
    "v_exp_f32 v108, v108\n\t"
    "v_exp_f32 v109, v109\n\t"
    "s_waitcnt lgkmcnt(0)\n\t"
    "s_barrier\n\t"
    "v_add_u32 v250, 0x10000, v250\n\t"
    "v_cvt_pk_bf16_f32 v33, v106, v107\n\t"
    "v_exp_f32 v110, v110\n\t"
    "v_mfma_f32_16x16x32_bf16 v[140:143], v[232:235], v[152:155], v[184:187]\n\t"
    "ds_read_b128 v[216:219], v250 offset:16384\n\t"
    "v_exp_f32 v111, v111\n\t"
    "v_cvt_pk_bf16_f32 v34, v108, v109\n\t"
    "v_mfma_f32_16x16x32_bf16 v[140:143], v[236:239], v[160:163], v[140:143]\n\t"
    "ds_read_b128 v[220:223], v250 offset:16896\n\t"
    "v_or3_b32 v251, v251, v32, v33\n\t"
    "v_mfma_f32_16x16x32_bf16 v[140:143], v[240:243], v[168:171], v[140:143]\n\t"
    "ds_read_b128 v[224:227], v250 offset:17408\n\t"
    "v_cvt_pk_bf16_f32 v35, v110, v111\n\t"
    "v_mfma_f32_16x16x32_bf16 v[140:143], v[244:247], v[176:179], v[140:143]\n\t"
    "ds_read_b128 v[228:231], v250 offset:17920\n\t"
    "v_or3_b32 v251, v251, v34, v35\n\t"
    "ds_read_b128 v[232:235], v250 offset:20480\n\t"
    "s_waitcnt lgkmcnt(3)\n\t"
    "v_mfma_f32_16x16x32_bf16 v[120:123], v[216:219], v[152:155], v[184:187]\n\t"
    "ds_read_b128 v[236:239], v250 offset:20992\n\t"
    "v_mfma_f32_16x16x32_bf16 v[120:123], v[220:223], v[160:163], v[120:123]\n\t"
    "ds_read_b128 v[240:243], v250 offset:21504\n\t"
    "v_exp_f32 v136, v136\n\t"
    "v_exp_f32 v137, v137\n\t"
    "s_waitcnt lgkmcnt(3)\n\t"
    "v_mfma_f32_16x16x32_bf16 v[120:123], v[224:227], v[168:171], v[120:123]\n\t"
    "ds_read_b128 v[244:247], v250 offset:22016\n\t"
    "v_exp_f32 v138, v138\n\t"
    "v_exp_f32 v139, v139\n\t"
    "v_mfma_f32_16x16x32_bf16 v[120:123], v[228:231], v[176:179], v[120:123]\n\t"
    "v_cvt_pk_bf16_f32 v40, v136, v137\n\t"
    "v_exp_f32 v140, v140\n\t"
    "v_exp_f32 v141, v141\n\t"
    "v_cvt_pk_bf16_f32 v41, v138, v139\n\t"
    "v_exp_f32 v142, v142\n\t"
    "v_exp_f32 v143, v143\n\t"
    "v_cvt_pk_bf16_f32 v42, v140, v141\n\t"
    "v_or3_b32 v251, v251, v40, v41\n\t"
    "v_cvt_pk_bf16_f32 v43, v142, v143\n\t"
    "ds_read_b128 v[216:219], v250 offset:24576\n\t"
    "s_waitcnt lgkmcnt(3)\n\t"
    "v_mfma_f32_16x16x32_bf16 v[124:127], v[232:235], v[152:155], v[184:187]\n\t"
    "ds_read_b128 v[220:223], v250 offset:25088\n\t"
    "v_or3_b32 v251, v251, v42, v43\n\t"
    "v_mfma_f32_16x16x32_bf16 v[124:127], v[236:239], v[160:163], v[124:127]\n\t"
    "ds_read_b128 v[224:227], v250 offset:25600\n\t"
    "s_waitcnt lgkmcnt(3)\n\t"
    "v_mfma_f32_16x16x32_bf16 v[124:127], v[240:243], v[168:171], v[124:127]\n\t"
    "ds_read_b128 v[228:231], v250 offset:26112\n\t"
    "v_mfma_f32_16x16x32_bf16 v[124:127], v[244:247], v[176:179], v[124:127]\n\t"
    "ds_read_b128 v[232:235], v250 offset:28672\n\t"
    "s_waitcnt lgkmcnt(3)\n\t"
    "v_mfma_f32_16x16x32_bf16 v[104:107], v[216:219], v[152:155], v[184:187]\n\t"
    "ds_read_b128 v[236:239], v250 offset:29184\n\t"
    "v_mfma_f32_16x16x32_bf16 v[104:107], v[220:223], v[160:163], v[104:107]\n\t"
    "ds_read_b128 v[240:243], v250 offset:29696\n\t"
    "v_exp_f32 v120, v120\n\t"
    "v_exp_f32 v121, v121\n\t"
    "s_waitcnt lgkmcnt(3)\n\t"
    "v_mfma_f32_16x16x32_bf16 v[104:107], v[224:227], v[168:171], v[104:107]\n\t"
    "ds_read_b128 v[244:247], v250 offset:30208\n\t"
    "v_exp_f32 v122, v122\n\t"
    "v_exp_f32 v123, v123\n\t"
    "v_mfma_f32_16x16x32_bf16 v[104:107], v[228:231], v[176:179], v[104:107]\n\t"
    "v_cvt_pk_bf16_f32 v48, v120, v121\n\t"
    "v_exp_f32 v124, v124\n\t"
    "v_exp_f32 v125, v125\n\t"
    "v_cvt_pk_bf16_f32 v49, v122, v123\n\t"
    "v_exp_f32 v126, v126\n\t"
    "v_exp_f32 v127, v127\n\t"
    "v_cvt_pk_bf16_f32 v50, v124, v125\n\t"
    "v_or3_b32 v251, v251, v48, v49\n\t"
    "v_cvt_pk_bf16_f32 v51, v126, v127\n\t"
    "s_waitcnt lgkmcnt(0)\n\t"
    "s_barrier\n\t"
    "ds_read_b128 v[216:219], v250 offset:32768\n\t"
    "v_mfma_f32_16x16x32_bf16 v[108:111], v[232:235], v[152:155], v[184:187]\n\t"
    "ds_read_b128 v[220:223], v250 offset:33280\n\t"
    "v_or3_b32 v251, v251, v50, v51\n\t"
    "v_mfma_f32_16x16x32_bf16 v[108:111], v[236:239], v[160:163], v[108:111]\n\t"
    "ds_read_b128 v[224:227], v250 offset:33792\n\t"
    "v_mfma_f32_16x16x32_bf16 v[108:111], v[240:243], v[168:171], v[108:111]\n\t"
    "ds_read_b128 v[228:231], v250 offset:34304\n\t"
    "v_mfma_f32_16x16x32_bf16 v[108:111], v[244:247], v[176:179], v[108:111]\n\t"
    "ds_read_b128 v[232:235], v250 offset:36864\n\t"
    "s_waitcnt lgkmcnt(3)\n\t"
    "v_mfma_f32_16x16x32_bf16 v[136:139], v[216:219], v[152:155], v[184:187]\n\t"
    "ds_read_b128 v[236:239], v250 offset:37376\n\t"
    "v_mfma_f32_16x16x32_bf16 v[136:139], v[220:223], v[160:163], v[136:139]\n\t"
    "ds_read_b128 v[240:243], v250 offset:37888\n\t"
    "v_exp_f32 v104, v104\n\t"
    "v_exp_f32 v105, v105\n\t"
    "s_waitcnt lgkmcnt(3)\n\t"
    "v_mfma_f32_16x16x32_bf16 v[136:139], v[224:227], v[168:171], v[136:139]\n\t"
    "ds_read_b128 v[244:247], v250 offset:38400\n\t"
    "v_exp_f32 v106, v106\n\t"
    "v_exp_f32 v107, v107\n\t"
    "v_mfma_f32_16x16x32_bf16 v[136:139], v[228:231], v[176:179], v[136:139]\n\t"
    "v_cvt_pk_bf16_f32 v56, v104, v105\n\t"
    "v_exp_f32 v108, v108\n\t"
    "v_exp_f32 v109, v109\n\t"
    "v_cvt_pk_bf16_f32 v57, v106, v107\n\t"
    "v_exp_f32 v110, v110\n\t"
    "v_exp_f32 v111, v111\n\t"
    "v_cvt_pk_bf16_f32 v58, v108, v109\n\t"
    "v_or3_b32 v251, v251, v56, v57\n\t"
    "v_cvt_pk_bf16_f32 v59, v110, v111\n\t"
    "ds_read_b128 v[216:219], v250 offset:40960\n\t"
    "s_waitcnt lgkmcnt(3)\n\t"
    "v_mfma_f32_16x16x32_bf16 v[140:143], v[232:235], v[152:155], v[184:187]\n\t"
    "ds_read_b128 v[220:223], v250 offset:41472\n\t"
    "v_or3_b32 v251, v251, v58, v59\n\t"
    "v_mfma_f32_16x16x32_bf16 v[140:143], v[236:239], v[160:163], v[140:143]\n\t"
    "ds_read_b128 v[224:227], v250 offset:41984\n\t"
    "s_waitcnt lgkmcnt(3)\n\t"
    "v_mfma_f32_16x16x32_bf16 v[140:143], v[240:243], v[168:171], v[140:143]\n\t"
    "ds_read_b128 v[228:231], v250 offset:42496\n\t"
    "v_mfma_f32_16x16x32_bf16 v[140:143], v[244:247], v[176:179], v[140:143]\n\t"
    "ds_read_b128 v[232:235], v250 offset:45056\n\t"
    "s_waitcnt lgkmcnt(3)\n\t"
    "v_mfma_f32_16x16x32_bf16 v[120:123], v[216:219], v[152:155], v[184:187]\n\t"
    "ds_read_b128 v[236:239], v250 offset:45568\n\t"
    "v_mfma_f32_16x16x32_bf16 v[120:123], v[220:223], v[160:163], v[120:123]\n\t"
    "ds_read_b128 v[240:243], v250 offset:46080\n\t"
    "v_exp_f32 v136, v136\n\t"
    "v_exp_f32 v137, v137\n\t"
    "s_waitcnt lgkmcnt(3)\n\t"
    "v_mfma_f32_16x16x32_bf16 v[120:123], v[224:227], v[168:171], v[120:123]\n\t"
    "ds_read_b128 v[244:247], v250 offset:46592\n\t"
    "v_exp_f32 v138, v138\n\t"
    "v_exp_f32 v139, v139\n\t"
    "v_mfma_f32_16x16x32_bf16 v[120:123], v[228:231], v[176:179], v[120:123]\n\t"
    "v_cvt_pk_bf16_f32 v64, v136, v137\n\t"
    "v_exp_f32 v140, v140\n\t"
    "v_exp_f32 v141, v141\n\t"
    "v_cvt_pk_bf16_f32 v65, v138, v139\n\t"
    "v_exp_f32 v142, v142\n\t"
    "v_exp_f32 v143, v143\n\t"
    "v_cvt_pk_bf16_f32 v66, v140, v141\n\t"
    "v_or3_b32 v251, v251, v64, v65\n\t"
    "v_cvt_pk_bf16_f32 v67, v142, v143\n\t"
    "s_waitcnt lgkmcnt(0)\n\t"
    "s_barrier\n\t"
    "ds_read_b128 v[216:219], v250 offset:49152\n\t"
    "v_mfma_f32_16x16x32_bf16 v[124:127], v[232:235], v[152:155], v[184:187]\n\t"
    "ds_read_b128 v[220:223], v250 offset:49664\n\t"
    "v_or3_b32 v251, v251, v66, v67\n\t"
    "v_mfma_f32_16x16x32_bf16 v[124:127], v[236:239], v[160:163], v[124:127]\n\t"
    "ds_read_b128 v[224:227], v250 offset:50176\n\t"
    "v_mfma_f32_16x16x32_bf16 v[124:127], v[240:243], v[168:171], v[124:127]\n\t"
    "ds_read_b128 v[228:231], v250 offset:50688\n\t"
    "v_mfma_f32_16x16x32_bf16 v[124:127], v[244:247], v[176:179], v[124:127]\n\t"
    "ds_read_b128 v[232:235], v250 offset:53248\n\t"
    "s_waitcnt lgkmcnt(3)\n\t"
    "v_mfma_f32_16x16x32_bf16 v[104:107], v[216:219], v[152:155], v[184:187]\n\t"
    "ds_read_b128 v[236:239], v250 offset:53760\n\t"
    "v_mfma_f32_16x16x32_bf16 v[104:107], v[220:223], v[160:163], v[104:107]\n\t"
    "ds_read_b128 v[240:243], v250 offset:54272\n\t"
    "v_exp_f32 v120, v120\n\t"
    "v_exp_f32 v121, v121\n\t"
    "s_waitcnt lgkmcnt(3)\n\t"
    "v_mfma_f32_16x16x32_bf16 v[104:107], v[224:227], v[168:171], v[104:107]\n\t"
    "ds_read_b128 v[244:247], v250 offset:54784\n\t"
    "v_exp_f32 v122, v122\n\t"
    "v_exp_f32 v123, v123\n\t"
    "v_mfma_f32_16x16x32_bf16 v[104:107], v[228:231], v[176:179], v[104:107]\n\t"
    "v_cvt_pk_bf16_f32 v72, v120, v121\n\t"
    "v_exp_f32 v124, v124\n\t"
    "v_exp_f32 v125, v125\n\t"
    "v_cvt_pk_bf16_f32 v73, v122, v123\n\t"
    "v_exp_f32 v126, v126\n\t"
    "v_exp_f32 v127, v127\n\t"
    "v_cvt_pk_bf16_f32 v74, v124, v125\n\t"
    "v_or3_b32 v251, v251, v72, v73\n\t"
    "v_cvt_pk_bf16_f32 v75, v126, v127\n\t"
    "ds_read_b128 v[216:219], v250 offset:57344\n\t"
    "s_waitcnt lgkmcnt(3)\n\t"
    "v_mfma_f32_16x16x32_bf16 v[108:111], v[232:235], v[152:155], v[184:187]\n\t"
    "ds_read_b128 v[220:223], v250 offset:57856\n\t"
    "v_or3_b32 v251, v251, v74, v75\n\t"
    "v_mfma_f32_16x16x32_bf16 v[108:111], v[236:239], v[160:163], v[108:111]\n\t"
    "ds_read_b128 v[224:227], v250 offset:58368\n\t"
    "s_waitcnt lgkmcnt(3)\n\t"
    "v_mfma_f32_16x16x32_bf16 v[108:111], v[240:243], v[168:171], v[108:111]\n\t"
    "ds_read_b128 v[228:231], v250 offset:58880\n\t"
    "v_mfma_f32_16x16x32_bf16 v[108:111], v[244:247], v[176:179], v[108:111]\n\t"
    "ds_read_b128 v[232:235], v250 offset:61440\n\t"
    "s_waitcnt lgkmcnt(3)\n\t"
    "v_mfma_f32_16x16x32_bf16 v[136:139], v[216:219], v[152:155], v[184:187]\n\t"
    "ds_read_b128 v[236:239], v250 offset:61952\n\t"
    "v_mfma_f32_16x16x32_bf16 v[136:139], v[220:223], v[160:163], v[136:139]\n\t"
    "ds_read_b128 v[240:243], v250 offset:62464\n\t"
    "v_exp_f32 v104, v104\n\t"
    "v_exp_f32 v105, v105\n\t"
    "s_waitcnt lgkmcnt(3)\n\t"
    "v_mfma_f32_16x16x32_bf16 v[136:139], v[224:227], v[168:171], v[136:139]\n\t"
    "ds_read_b128 v[244:247], v250 offset:62976\n\t"
    "v_exp_f32 v106, v106\n\t"
    "v_exp_f32 v107, v107\n\t"
    "v_mfma_f32_16x16x32_bf16 v[136:139], v[228:231], v[176:179], v[136:139]\n\t"
    "v_cvt_pk_bf16_f32 v80, v104, v105\n\t"
    "v_exp_f32 v108, v108\n\t"
    "v_exp_f32 v109, v109\n\t"
    "v_cvt_pk_bf16_f32 v81, v106, v107\n\t"
    "v_exp_f32 v110, v110\n\t"
    "v_exp_f32 v111, v111\n\t"
    "v_cvt_pk_bf16_f32 v82, v108, v109\n\t"
    "v_or3_b32 v251, v251, v80, v81\n\t"
    "v_cvt_pk_bf16_f32 v83, v110, v111\n\t"
    "s_waitcnt lgkmcnt(0)\n\t"
    "s_barrier\n\t"
    "v_add_u32 v250, 0xffff0000, v250\n\t"
    "v_mfma_f32_16x16x32_bf16 v[140:143], v[232:235], v[152:155], v[184:187]\n\t"
    "ds_read_b128 v[216:219], v250 offset:32768\n\t"
    "v_or3_b32 v251, v251, v82, v83\n\t"
    "v_mfma_f32_16x16x32_bf16 v[140:143], v[236:239], v[160:163], v[140:143]\n\t"
    "ds_read_b128 v[220:223], v250 offset:33280\n\t"
    "v_mfma_f32_16x16x32_bf16 v[140:143], v[240:243], v[168:171], v[140:143]\n\t"
    "ds_read_b128 v[224:227], v250 offset:33792\n\t"
    "v_mfma_f32_16x16x32_bf16 v[140:143], v[244:247], v[176:179], v[140:143]\n\t"
    "ds_read_b128 v[228:231], v250 offset:34304\n\t"
    "ds_read_b128 v[232:235], v250 offset:36864\n\t"
    "s_waitcnt lgkmcnt(3)\n\t"
    "v_mfma_f32_16x16x32_bf16 v[120:123], v[216:219], v[152:155], v[184:187]\n\t"
    "ds_read_b128 v[236:239], v250 offset:37376\n\t"
    "v_mfma_f32_16x16x32_bf16 v[120:123], v[220:223], v[160:163], v[120:123]\n\t"
    "ds_read_b128 v[240:243], v250 offset:37888\n\t"
    "v_exp_f32 v136, v136\n\t"
    "v_exp_f32 v137, v137\n\t"
    "s_waitcnt lgkmcnt(3)\n\t"
    "v_mfma_f32_16x16x32_bf16 v[120:123], v[224:227], v[168:171], v[120:123]\n\t"
    "ds_read_b128 v[244:247], v250 offset:38400\n\t"
    "v_exp_f32 v138, v138\n\t"
    "v_exp_f32 v139, v139\n\t"
    "v_mfma_f32_16x16x32_bf16 v[120:123], v[228:231], v[176:179], v[120:123]\n\t"
    "v_cvt_pk_bf16_f32 v88, v136, v137\n\t"
    "v_exp_f32 v140, v140\n\t"
    "v_exp_f32 v141, v141\n\t"
    "v_cvt_pk_bf16_f32 v89, v138, v139\n\t"
    "v_exp_f32 v142, v142\n\t"
    "v_exp_f32 v143, v143\n\t"
    "v_cvt_pk_bf16_f32 v90, v140, v141\n\t"
    "v_or3_b32 v251, v251, v88, v89\n\t"
    "v_cvt_pk_bf16_f32 v91, v142, v143\n\t"
    "ds_read_b128 v[216:219], v250 offset:40960\n\t"
    "s_waitcnt lgkmcnt(3)\n\t"
    "v_mfma_f32_16x16x32_bf16 v[124:127], v[232:235], v[152:155], v[184:187]\n\t"
    "ds_read_b128 v[220:223], v250 offset:41472\n\t"
    "v_or3_b32 v251, v251, v90, v91\n\t"
    "v_mfma_f32_16x16x32_bf16 v[124:127], v[236:239], v[160:163], v[124:127]\n\t"
    "ds_read_b128 v[224:227], v250 offset:41984\n\t"
    "s_waitcnt lgkmcnt(3)\n\t"
    "v_mfma_f32_16x16x32_bf16 v[124:127], v[240:243], v[168:171], v[124:127]\n\t"
    "ds_read_b128 v[228:231], v250 offset:42496\n\t"
    "v_mfma_f32_16x16x32_bf16 v[124:127], v[244:247], v[176:179], v[124:127]\n\t"
    "ds_read_b128 v[232:235], v250 offset:45056\n\t"
    "s_waitcnt lgkmcnt(3)\n\t"
    "v_mfma_f32_16x16x32_bf16 v[104:107], v[216:219], v[152:155], v[184:187]\n\t"
    "ds_read_b128 v[236:239], v250 offset:45568\n\t"
    "v_mfma_f32_16x16x32_bf16 v[104:107], v[220:223], v[160:163], v[104:107]\n\t"
    "ds_read_b128 v[240:243], v250 offset:46080\n\t"
    "v_exp_f32 v120, v120\n\t"
    "v_exp_f32 v121, v121\n\t"
    "s_waitcnt lgkmcnt(3)\n\t"
    "v_mfma_f32_16x16x32_bf16 v[104:107], v[224:227], v[168:171], v[104:107]\n\t"
    "ds_read_b128 v[244:247], v250 offset:46592\n\t"
    "v_exp_f32 v122, v122\n\t"
    "v_exp_f32 v123, v123\n\t"
    "v_mfma_f32_16x16x32_bf16 v[104:107], v[228:231], v[176:179], v[104:107]\n\t"
    "v_cvt_pk_bf16_f32 v96, v120, v121\n\t"
    "v_exp_f32 v124, v124\n\t"
    "v_exp_f32 v125, v125\n\t"
    "v_cvt_pk_bf16_f32 v97, v122, v123\n\t"
    "v_exp_f32 v126, v126\n\t"
    "v_exp_f32 v127, v127\n\t"
    "v_cvt_pk_bf16_f32 v98, v124, v125\n\t"
    "v_or3_b32 v251, v251, v96, v97\n\t"
    "v_cvt_pk_bf16_f32 v99, v126, v127\n\t"
    "s_waitcnt lgkmcnt(0)\n\t"
    "s_barrier\n\t"
    "ds_read_b128 v[216:219], v250 offset:49152\n\t"
    "v_mfma_f32_16x16x32_bf16 v[108:111], v[232:235], v[152:155], v[184:187]\n\t"
    "ds_read_b128 v[220:223], v250 offset:49664\n\t"
    "v_or3_b32 v251, v251, v98, v99\n\t"
    "v_mfma_f32_16x16x32_bf16 v[108:111], v[236:239], v[160:163], v[108:111]\n\t"
    "ds_read_b128 v[224:227], v250 offset:50176\n\t"
    "v_mfma_f32_16x16x32_bf16 v[108:111], v[240:243], v[168:171], v[108:111]\n\t"
    "ds_read_b128 v[228:231], v250 offset:50688\n\t"
    "v_mfma_f32_16x16x32_bf16 v[108:111], v[244:247], v[176:179], v[108:111]\n\t"
    "ds_read_b128 v[232:235], v250 offset:53248\n\t"
    "s_waitcnt lgkmcnt(3)\n\t"
    "v_mfma_f32_16x16x32_bf16 v[136:139], v[216:219], v[152:155], v[184:187]\n\t"
    "ds_read_b128 v[236:239], v250 offset:53760\n\t"
    "v_mfma_f32_16x16x32_bf16 v[136:139], v[220:223], v[160:163], v[136:139]\n\t"
    "ds_read_b128 v[240:243], v250 offset:54272\n\t"
    "v_exp_f32 v104, v104\n\t"
    "v_exp_f32 v105, v105\n\t"
    "s_waitcnt lgkmcnt(3)\n\t"
    "v_mfma_f32_16x16x32_bf16 v[136:139], v[224:227], v[168:171], v[136:139]\n\t"
    "ds_read_b128 v[244:247], v250 offset:54784\n\t"
    "v_exp_f32 v106, v106\n\t"
    "v_exp_f32 v107, v107\n\t"
    "v_mfma_f32_16x16x32_bf16 v[136:139], v[228:231], v[176:179], v[136:139]\n\t"
    "v_cvt_pk_bf16_f32 v104, v104, v105\n\t"
    "v_exp_f32 v108, v108\n\t"
    "v_exp_f32 v109, v109\n\t"
    "v_cvt_pk_bf16_f32 v105, v106, v107\n\t"
    "v_exp_f32 v110, v110\n\t"
    "v_exp_f32 v111, v111\n\t"
    "v_cvt_pk_bf16_f32 v106, v108, v109\n\t"
    "v_or3_b32 v251, v251, v104, v105\n\t"
    "v_cvt_pk_bf16_f32 v107, v110, v111\n\t"
    "ds_read_b128 v[216:219], v250 offset:57344\n\t"
    "s_waitcnt lgkmcnt(3)\n\t"
    "v_mfma_f32_16x16x32_bf16 v[140:143], v[232:235], v[152:155], v[184:187]\n\t"
    "ds_read_b128 v[220:223], v250 offset:57856\n\t"
    "v_or3_b32 v251, v251, v106, v107\n\t"
    "v_mfma_f32_16x16x32_bf16 v[140:143], v[236:239], v[160:163], v[140:143]\n\t"
    "ds_read_b128 v[224:227], v250 offset:58368\n\t"
    "s_waitcnt lgkmcnt(3)\n\t"
    "v_mfma_f32_16x16x32_bf16 v[140:143], v[240:243], v[168:171], v[140:143]\n\t"
    "ds_read_b128 v[228:231], v250 offset:58880\n\t"
    "v_mfma_f32_16x16x32_bf16 v[140:143], v[244:247], v[176:179], v[140:143]\n\t"
    "ds_read_b128 v[232:235], v250 offset:61440\n\t"
    "s_waitcnt lgkmcnt(3)\n\t"
    "v_mfma_f32_16x16x32_bf16 v[120:123], v[216:219], v[152:155], v[184:187]\n\t"
    "ds_read_b128 v[236:239], v250 offset:61952\n\t"
    "v_mfma_f32_16x16x32_bf16 v[120:123], v[220:223], v[160:163], v[120:123]\n\t"
    "ds_read_b128 v[240:243], v250 offset:62464\n\t"
    "v_exp_f32 v136, v136\n\t"
    "v_exp_f32 v137, v137\n\t"
    "s_waitcnt lgkmcnt(3)\n\t"
    "v_mfma_f32_16x16x32_bf16 v[120:123], v[224:227], v[168:171], v[120:123]\n\t"
    "ds_read_b128 v[244:247], v250 offset:62976\n\t"
    "v_exp_f32 v138, v138\n\t"
    "v_exp_f32 v139, v139\n\t"
    "v_mfma_f32_16x16x32_bf16 v[120:123], v[228:231], v[176:179], v[120:123]\n\t"
    "v_cvt_pk_bf16_f32 v112, v136, v137\n\t"
    "v_exp_f32 v140, v140\n\t"
    "v_exp_f32 v141, v141\n\t"
    "v_cvt_pk_bf16_f32 v113, v138, v139\n\t"
    "v_exp_f32 v142, v142\n\t"
    "v_exp_f32 v143, v143\n\t"
    "v_cvt_pk_bf16_f32 v114, v140, v141\n\t"
    "v_or3_b32 v251, v251, v112, v113\n\t"
    "v_cvt_pk_bf16_f32 v115, v142, v143\n\t"
    "s_waitcnt lgkmcnt(0)\n\t"
    "s_barrier\n\t"
    "v_add_u32 v248, 0x10000, v248\n\t"
    "v_add_u32 v249, 0x10000, v249\n\t"
    "v_mfma_f32_16x16x32_bf16 v[124:127], v[232:235], v[152:155], v[184:187]\n\t"
    "ds_read_b64_tr_b16 v[216:217], v248 offset:0\n\t"
    "ds_read_b64_tr_b16 v[218:219], v248 offset:4096\n\t"
    "v_or3_b32 v251, v251, v114, v115\n\t"
    "v_mfma_f32_16x16x32_bf16 v[124:127], v[236:239], v[160:163], v[124:127]\n\t"
    "ds_read_b64_tr_b16 v[220:221], v248 offset:8192\n\t"
    "v_mfma_f32_16x16x32_bf16 v[124:127], v[240:243], v[168:171], v[124:127]\n\t"
    "ds_read_b64_tr_b16 v[222:223], v248 offset:12288\n\t"
    "v_mfma_f32_16x16x32_bf16 v[124:127], v[244:247], v[176:179], v[124:127]\n\t"
    "ds_read_b64_tr_b16 v[224:225], v249 offset:0\n\t"
    "ds_read_b64_tr_b16 v[226:227], v249 offset:4096\n\t"
    "ds_read_b64_tr_b16 v[228:229], v249 offset:8192\n\t"
    "ds_read_b64_tr_b16 v[230:231], v249 offset:12288\n\t"
    "ds_read_b64_tr_b16 v[232:233], v248 offset:512\n\t"
    "v_exp_f32 v120, v120\n\t"
    "v_exp_f32 v121, v121\n\t"
    "v_exp_f32 v122, v122\n\t"
    "v_exp_f32 v123, v123\n\t"
    "v_cvt_pk_bf16_f32 v120, v120, v121\n\t"
    "v_exp_f32 v124, v124\n\t"
    "v_exp_f32 v125, v125\n\t"
    "v_cvt_pk_bf16_f32 v121, v122, v123\n\t"
    "v_exp_f32 v126, v126\n\t"
    "v_exp_f32 v127, v127\n\t"
    "v_cvt_pk_bf16_f32 v122, v124, v125\n\t"
    "v_or3_b32 v251, v251, v120, v121\n\t"
    "v_cvt_pk_bf16_f32 v123, v126, v127\n\t"
    "v_or3_b32 v251, v251, v122, v123\n\t"
    "v_and_b32 v251, 0x40004000, v251\n\t"
    "v_cmp_ne_u32 vcc, 0, v251\n\t"
    "s_nop 3\n\t"
    "s_cmp_lg_u64 vcc, 0\n\t"
    "s_cbranch_scc1 FB%=\n\t"
    "FBRET2_%=:\n\t"
    "v_and_b32 v138, 3, v253\n\t"
    "v_cmp_eq_u32 vcc, 0, v138\n\t"
    "v_mov_b32 v139, 0x3f803f80\n\t"
    "v_mov_b32 v140, 0\n\t"
    "v_cndmask_b32 v138, v140, v139, vcc\n\t"
    "v_mov_b32 v139, v138\n\t"
    "v_mov_b32 v140, v138\n\t"
    "v_mov_b32 v141, v138\n\t"
    "s_waitcnt lgkmcnt(5)\n\t"
    "v_mfma_f32_16x16x32_bf16 v[152:155], v[216:219], v[0:3], 0\n\t"
    "ds_read_b64_tr_b16 v[234:235], v248 offset:4608\n\t"
    "ds_read_b64_tr_b16 v[236:237], v248 offset:8704\n\t"
    "ds_read_b64_tr_b16 v[238:239], v248 offset:12800\n\t"
    "v_mfma_f32_16x16x32_bf16 v[152:155], v[220:223], v[8:11], v[152:155]\n\t"
    "ds_read_b64_tr_b16 v[240:241], v249 offset:512\n\t"
    "ds_read_b64_tr_b16 v[242:243], v249 offset:4608\n\t"
    "ds_read_b64_tr_b16 v[244:245], v249 offset:8704\n\t"
    "ds_read_b64_tr_b16 v[246:247], v249 offset:12800\n\t"
    "v_mfma_f32_16x16x32_bf16 v[144:147], v[138:141], v[0:3], 0\n\t"
    "ds_read_b64_tr_b16 v[216:217], v248 offset:1024\n\t"
    "s_waitcnt lgkmcnt(9)\n\t"
    "v_mfma_f32_16x16x32_bf16 v[156:159], v[224:227], v[0:3], 0\n\t"
    "ds_read_b64_tr_b16 v[218:219], v248 offset:5120\n\t"
    "v_mfma_f32_16x16x32_bf16 v[156:159], v[228:231], v[8:11], v[156:159]\n\t"
    "ds_read_b64_tr_b16 v[220:221], v248 offset:9216\n\t"
    "ds_read_b64_tr_b16 v[222:223], v248 offset:13312\n\t"
    "v_lshlrev_b32 v142, 4, v253\n\t"
    "s_lshl_b32 s52, s50, 12\n\t"
    "ds_read_b64_tr_b16 v[224:225], v249 offset:1024\n\t"
    "v_add_u32 v142, s52, v142\n\t"
    "s_waitcnt lgkmcnt(9)\n\t"
    "v_mfma_f32_16x16x32_bf16 v[168:171], v[232:235], v[0:3], 0\n\t"
    "ds_read_b64_tr_b16 v[226:227], v249 offset:5120\n\t"
    "v_or_b32 v143, s61, v142\n\t"
    "v_mfma_f32_16x16x32_bf16 v[168:171], v[236:239], v[8:11], v[168:171]\n\t"
    "ds_read_b64_tr_b16 v[228:229], v249 offset:9216\n\t"
    "v_bfe_u32 v254, v253, 2, 3\n\t"
    "ds_read_b64_tr_b16 v[230:231], v249 offset:13312\n\t"
    "v_lshlrev_b32 v128, 8, v254\n\t"
    "ds_read_b64_tr_b16 v[232:233], v248 offset:1536\n\t"
    "v_bfe_u32 v254, v253, 5, 1\n\t"
    "s_waitcnt lgkmcnt(9)\n\t"
    "v_mfma_f32_16x16x32_bf16 v[172:175], v[240:243], v[0:3], 0\n\t"
    "ds_read_b64_tr_b16 v[234:235], v248 offset:5632\n\t"
    "v_lshl_add_u32 v128, v254, 6, v128\n\t"
    "v_mfma_f32_16x16x32_bf16 v[172:175], v[244:247], v[8:11], v[172:175]\n\t"
    "ds_read_b64_tr_b16 v[236:237], v248 offset:9728\n\t"
    "v_bfe_u32 v254, v253, 0, 2\n\t"
    "ds_read_b64_tr_b16 v[238:239], v248 offset:13824\n\t"
    "v_lshl_add_u32 v128, v254, 4, v128\n\t"
    "s_lshl_b32 s52, s50, 12\n\t"
    "s_add_u32 s52, s52, 0x400\n\t"
    "v_mfma_f32_16x16x32_bf16 v[144:147], v[138:141], v[8:11], v[144:147]\n\t"
    "ds_read_b64_tr_b16 v[240:241], v249 offset:1536\n\t"
    "v_add_u32 v128, s52, v128\n\t"
    "s_waitcnt lgkmcnt(9)\n\t"
    "v_mfma_f32_16x16x32_bf16 v[184:187], v[216:219], v[0:3], 0\n\t"
    "ds_read_b64_tr_b16 v[242:243], v249 offset:5632\n\t"
    "v_add_u32 v129, 0xfffffc80, v128\n\t"
    "v_mfma_f32_16x16x32_bf16 v[184:187], v[220:223], v[8:11], v[184:187]\n\t"
    "ds_read_b64_tr_b16 v[244:245], v249 offset:9728\n\t"
    "v_xor_b32 v130, 16, v128\n\t"
    "ds_read_b64_tr_b16 v[246:247], v249 offset:13824\n\t"
    "v_add_u32 v131, 0xfffffc80, v130\n\t"
    "v_mov_b32 v132, v128\n\t"
    "v_bfe_u32 v254, v253, 4, 1\n\t"
    "s_waitcnt lgkmcnt(8)\n\t"
    "v_mfma_f32_16x16x32_bf16 v[188:191], v[224:227], v[0:3], 0\n\t"
    "v_lshlrev_b32 v254, 5, v254\n\t"
    "v_xor_b32 v132, v132, v254\n\t"
    "v_mfma_f32_16x16x32_bf16 v[188:191], v[228:231], v[8:11], v[188:191]\n\t"
    "v_add_u32 v133, 0xfffffc80, v132\n\t"
    "v_bfe_u32 v254, v253, 2, 3\n\t"
    "v_lshlrev_b32 v134, 10, v254\n\t"
    "v_bfe_u32 v254, v253, 5, 1\n\t"
    "v_lshl_add_u32 v134, v254, 6, v134\n\t"
    "v_bfe_u32 v254, v253, 0, 2\n\t"
    "s_waitcnt lgkmcnt(0)\n\t"
    "s_barrier\n\t"
    "v_add_u32 v248, 0xffff0000, v248\n\t"
    "v_add_u32 v249, 0xffff0000, v249\n\t"
    "ds_read_b64_tr_b16 v[216:217], v248 offset:0\n\t"
    "v_mfma_f32_16x16x32_bf16 v[200:203], v[232:235], v[0:3], 0\n\t"
    "ds_read_b64_tr_b16 v[218:219], v248 offset:4096\n\t"
    "ds_read_b64_tr_b16 v[220:221], v248 offset:8192\n\t"
    "ds_read_b64_tr_b16 v[222:223], v248 offset:12288\n\t"
    "v_mfma_f32_16x16x32_bf16 v[200:203], v[236:239], v[8:11], v[200:203]\n\t"
    "ds_read_b64_tr_b16 v[224:225], v249 offset:0\n\t"
    "ds_read_b64_tr_b16 v[226:227], v249 offset:4096\n\t"
    "ds_read_b64_tr_b16 v[228:229], v249 offset:8192\n\t"
    "ds_read_b64_tr_b16 v[230:231], v249 offset:12288\n\t"
    "v_lshl_add_u32 v134, v254, 4, v134\n\t"
    "v_bfe_u32 v254, v253, 4, 1\n\t"
    "v_mfma_f32_16x16x32_bf16 v[144:147], v[138:141], v[16:19], v[144:147]\n\t"
    "ds_read_b64_tr_b16 v[232:233], v248 offset:512\n\t"
    "v_lshlrev_b32 v254, 5, v254\n\t"
    "v_mfma_f32_16x16x32_bf16 v[204:207], v[240:243], v[0:3], 0\n\t"
    "ds_read_b64_tr_b16 v[234:235], v248 offset:4608\n\t"
    "v_xor_b32 v134, v134, v254\n\t"
    "v_mfma_f32_16x16x32_bf16 v[204:207], v[244:247], v[8:11], v[204:207]\n\t"
    "ds_read_b64_tr_b16 v[236:237], v248 offset:8704\n\t"
    "v_xor_b32 v134, %[octo], v134\n\t"
    "ds_read_b64_tr_b16 v[238:239], v248 offset:12800\n\t"
    "s_lshl_b32 s52, s50, 14\n\t"
    "ds_read_b64_tr_b16 v[240:241], v249 offset:512\n\t"
    "v_add_u32 v134, s52, v134\n\t"
    "s_waitcnt lgkmcnt(9)\n\t"
    "v_mfma_f32_16x16x32_bf16 v[152:155], v[216:219], v[16:19], v[152:155]\n\t"
    "ds_read_b64_tr_b16 v[242:243], v249 offset:4608\n\t"
    "v_xor_b32 v135, 0x80, v134\n\t"
    "v_mfma_f32_16x16x32_bf16 v[152:155], v[220:223], v[24:27], v[152:155]\n\t"
    "ds_read_b64_tr_b16 v[244:245], v249 offset:8704\n\t"
    "v_add_u32 v136, 0x1c00, v134\n\t"
    "ds_read_b64_tr_b16 v[246:247], v249 offset:12800\n\t"
    "v_add_u32 v137, 0x1800, v135\n\t"
    "v_add_u32 v134, 0x400, v134\n\t"
    "ds_read_b64_tr_b16 v[216:217], v248 offset:1024\n\t"
    "s_waitcnt lgkmcnt(9)\n\t"
    "v_mfma_f32_16x16x32_bf16 v[156:159], v[224:227], v[16:19], v[156:159]\n\t"
    "ds_read_b64_tr_b16 v[218:219], v248 offset:5120\n\t"
    "v_mfma_f32_16x16x32_bf16 v[156:159], v[228:231], v[24:27], v[156:159]\n\t"
    "ds_read_b64_tr_b16 v[220:221], v248 offset:9216\n\t"
    "ds_read_b64_tr_b16 v[222:223], v248 offset:13312\n\t"
    "ds_read_b64_tr_b16 v[224:225], v249 offset:1024\n\t"
    "s_waitcnt lgkmcnt(9)\n\t"
    "v_mfma_f32_16x16x32_bf16 v[168:171], v[232:235], v[16:19], v[168:171]\n\t"
    "ds_read_b64_tr_b16 v[226:227], v249 offset:5120\n\t"
    "v_mfma_f32_16x16x32_bf16 v[168:171], v[236:239], v[24:27], v[168:171]\n\t"
    "ds_read_b64_tr_b16 v[228:229], v249 offset:9216\n\t"
    "ds_read_b64_tr_b16 v[230:231], v249 offset:13312\n\t"
    "v_mfma_f32_16x16x32_bf16 v[144:147], v[138:141], v[24:27], v[144:147]\n\t"
    "ds_read_b64_tr_b16 v[232:233], v248 offset:1536\n\t"
    "s_waitcnt lgkmcnt(9)\n\t"
    "v_mfma_f32_16x16x32_bf16 v[172:175], v[240:243], v[16:19], v[172:175]\n\t"
    "ds_read_b64_tr_b16 v[234:235], v248 offset:5632\n\t"
    "v_mfma_f32_16x16x32_bf16 v[172:175], v[244:247], v[24:27], v[172:175]\n\t"
    "ds_read_b64_tr_b16 v[236:237], v248 offset:9728\n\t"
    "ds_read_b64_tr_b16 v[238:239], v248 offset:13824\n\t"
    "ds_read_b64_tr_b16 v[240:241], v249 offset:1536\n\t"
    "s_waitcnt lgkmcnt(9)\n\t"
    "v_mfma_f32_16x16x32_bf16 v[184:187], v[216:219], v[16:19], v[184:187]\n\t"
    "ds_read_b64_tr_b16 v[242:243], v249 offset:5632\n\t"
    "v_mfma_f32_16x16x32_bf16 v[184:187], v[220:223], v[24:27], v[184:187]\n\t"
    "ds_read_b64_tr_b16 v[244:245], v249 offset:9728\n\t"
    "ds_read_b64_tr_b16 v[246:247], v249 offset:13824\n\t"
    "s_waitcnt lgkmcnt(8)\n\t"
    "v_mfma_f32_16x16x32_bf16 v[188:191], v[224:227], v[16:19], v[188:191]\n\t"
    "v_mfma_f32_16x16x32_bf16 v[188:191], v[228:231], v[24:27], v[188:191]\n\t"
    "v_mfma_f32_16x16x32_bf16 v[144:147], v[138:141], v[32:35], v[144:147]\n\t"
    "s_waitcnt lgkmcnt(0)\n\t"
    "s_barrier\n\t"
    "ds_read_b64_tr_b16 v[216:217], v248 offset:16384\n\t"
    "ds_read_b64_tr_b16 v[218:219], v248 offset:20480\n\t"
    "v_mfma_f32_16x16x32_bf16 v[200:203], v[232:235], v[16:19], v[200:203]\n\t"
    "ds_read_b64_tr_b16 v[220:221], v248 offset:24576\n\t"
    "ds_read_b64_tr_b16 v[222:223], v248 offset:28672\n\t"
    "v_mfma_f32_16x16x32_bf16 v[200:203], v[236:239], v[24:27], v[200:203]\n\t"
    "ds_read_b64_tr_b16 v[224:225], v249 offset:16384\n\t"
    "ds_read_b64_tr_b16 v[226:227], v249 offset:20480\n\t"
    "ds_read_b64_tr_b16 v[228:229], v249 offset:24576\n\t"
    "ds_read_b64_tr_b16 v[230:231], v249 offset:28672\n\t"
    "ds_read_b64_tr_b16 v[232:233], v248 offset:16896\n\t"
    "v_mfma_f32_16x16x32_bf16 v[204:207], v[240:243], v[16:19], v[204:207]\n\t"
    "ds_read_b64_tr_b16 v[234:235], v248 offset:20992\n\t"
    "v_mfma_f32_16x16x32_bf16 v[204:207], v[244:247], v[24:27], v[204:207]\n\t"
    "ds_read_b64_tr_b16 v[236:237], v248 offset:25088\n\t"
    "ds_read_b64_tr_b16 v[238:239], v248 offset:29184\n\t"
    "ds_read_b64_tr_b16 v[240:241], v249 offset:16896\n\t"
    "s_waitcnt lgkmcnt(9)\n\t"
    "v_mfma_f32_16x16x32_bf16 v[152:155], v[216:219], v[32:35], v[152:155]\n\t"
    "ds_read_b64_tr_b16 v[242:243], v249 offset:20992\n\t"
    "v_mfma_f32_16x16x32_bf16 v[152:155], v[220:223], v[40:43], v[152:155]\n\t"
    "ds_read_b64_tr_b16 v[244:245], v249 offset:25088\n\t"
    "ds_read_b64_tr_b16 v[246:247], v249 offset:29184\n\t"
    "v_mfma_f32_16x16x32_bf16 v[144:147], v[138:141], v[40:43], v[144:147]\n\t"
    "ds_read_b64_tr_b16 v[216:217], v248 offset:17408\n\t"
    "s_waitcnt lgkmcnt(9)\n\t"
    "v_mfma_f32_16x16x32_bf16 v[156:159], v[224:227], v[32:35], v[156:159]\n\t"
    "ds_read_b64_tr_b16 v[218:219], v248 offset:21504\n\t"
    "v_mfma_f32_16x16x32_bf16 v[156:159], v[228:231], v[40:43], v[156:159]\n\t"
    "ds_read_b64_tr_b16 v[220:221], v248 offset:25600\n\t"
    "ds_read_b64_tr_b16 v[222:223], v248 offset:29696\n\t"
    "ds_read_b64_tr_b16 v[224:225], v249 offset:17408\n\t"
    "s_waitcnt lgkmcnt(9)\n\t"
    "v_mfma_f32_16x16x32_bf16 v[168:171], v[232:235], v[32:35], v[168:171]\n\t"
    "ds_read_b64_tr_b16 v[226:227], v249 offset:21504\n\t"
    "v_mfma_f32_16x16x32_bf16 v[168:171], v[236:239], v[40:43], v[168:171]\n\t"
    "ds_read_b64_tr_b16 v[228:229], v249 offset:25600\n\t"
    "ds_read_b64_tr_b16 v[230:231], v249 offset:29696\n\t"
    "ds_read_b64_tr_b16 v[232:233], v248 offset:17920\n\t"
    "s_waitcnt lgkmcnt(9)\n\t"
    "v_mfma_f32_16x16x32_bf16 v[172:175], v[240:243], v[32:35], v[172:175]\n\t"
    "ds_read_b64_tr_b16 v[234:235], v248 offset:22016\n\t"
    "v_mfma_f32_16x16x32_bf16 v[172:175], v[244:247], v[40:43], v[172:175]\n\t"
    "ds_read_b64_tr_b16 v[236:237], v248 offset:26112\n\t"
    "ds_read_b64_tr_b16 v[238:239], v248 offset:30208\n\t"
    "ds_read_b64_tr_b16 v[240:241], v249 offset:17920\n\t"
    "s_waitcnt lgkmcnt(9)\n\t"
    "v_mfma_f32_16x16x32_bf16 v[184:187], v[216:219], v[32:35], v[184:187]\n\t"
    "ds_read_b64_tr_b16 v[242:243], v249 offset:22016\n\t"
    "v_mfma_f32_16x16x32_bf16 v[184:187], v[220:223], v[40:43], v[184:187]\n\t"
    "ds_read_b64_tr_b16 v[244:245], v249 offset:26112\n\t"
    "ds_read_b64_tr_b16 v[246:247], v249 offset:30208\n\t"
    "v_mfma_f32_16x16x32_bf16 v[144:147], v[138:141], v[48:51], v[144:147]\n\t"
    "s_waitcnt lgkmcnt(8)\n\t"
    "v_mfma_f32_16x16x32_bf16 v[188:191], v[224:227], v[32:35], v[188:191]\n\t"
    "v_mfma_f32_16x16x32_bf16 v[188:191], v[228:231], v[40:43], v[188:191]\n\t"
    "s_waitcnt lgkmcnt(0)\n\t"
    "s_barrier\n\t"
    "v_add_u32 v248, 0x10000, v248\n\t"
    "v_add_u32 v249, 0x10000, v249\n\t"
    "ds_read_b64_tr_b16 v[216:217], v248 offset:16384\n\t"
    "v_mfma_f32_16x16x32_bf16 v[200:203], v[232:235], v[32:35], v[200:203]\n\t"
    "ds_read_b64_tr_b16 v[218:219], v248 offset:20480\n\t"
    "ds_read_b64_tr_b16 v[220:221], v248 offset:24576\n\t"
    "ds_read_b64_tr_b16 v[222:223], v248 offset:28672\n\t"
    "v_mfma_f32_16x16x32_bf16 v[200:203], v[236:239], v[40:43], v[200:203]\n\t"
    "ds_read_b64_tr_b16 v[224:225], v249 offset:16384\n\t"
    "ds_read_b64_tr_b16 v[226:227], v249 offset:20480\n\t"
    "ds_read_b64_tr_b16 v[228:229], v249 offset:24576\n\t"
    "ds_read_b64_tr_b16 v[230:231], v249 offset:28672\n\t"
    "ds_read_b64_tr_b16 v[232:233], v248 offset:16896\n\t"
    "v_mfma_f32_16x16x32_bf16 v[204:207], v[240:243], v[32:35], v[204:207]\n\t"
    "ds_read_b64_tr_b16 v[234:235], v248 offset:20992\n\t"
    "v_mfma_f32_16x16x32_bf16 v[204:207], v[244:247], v[40:43], v[204:207]\n\t"
    "ds_read_b64_tr_b16 v[236:237], v248 offset:25088\n\t"
    "ds_read_b64_tr_b16 v[238:239], v248 offset:29184\n\t"
    "v_mfma_f32_16x16x32_bf16 v[144:147], v[138:141], v[56:59], v[144:147]\n\t"
    "ds_read_b64_tr_b16 v[240:241], v249 offset:16896\n\t"
    "s_waitcnt lgkmcnt(9)\n\t"
    "v_mfma_f32_16x16x32_bf16 v[152:155], v[216:219], v[48:51], v[152:155]\n\t"
    "ds_read_b64_tr_b16 v[242:243], v249 offset:20992\n\t"
    "v_mfma_f32_16x16x32_bf16 v[152:155], v[220:223], v[56:59], v[152:155]\n\t"
    "ds_read_b64_tr_b16 v[244:245], v249 offset:25088\n\t"
    "ds_read_b64_tr_b16 v[246:247], v249 offset:29184\n\t"
    "ds_read_b64_tr_b16 v[216:217], v248 offset:17408\n\t"
    "s_waitcnt lgkmcnt(9)\n\t"
    "v_mfma_f32_16x16x32_bf16 v[156:159], v[224:227], v[48:51], v[156:159]\n\t"
    "ds_read_b64_tr_b16 v[218:219], v248 offset:21504\n\t"
    "v_mfma_f32_16x16x32_bf16 v[156:159], v[228:231], v[56:59], v[156:159]\n\t"
    "ds_read_b64_tr_b16 v[220:221], v248 offset:25600\n\t"
    "ds_read_b64_tr_b16 v[222:223], v248 offset:29696\n\t"
    "ds_read_b64_tr_b16 v[224:225], v249 offset:17408\n\t"
    "s_waitcnt lgkmcnt(9)\n\t"
    "v_mfma_f32_16x16x32_bf16 v[168:171], v[232:235], v[48:51], v[168:171]\n\t"
    "ds_read_b64_tr_b16 v[226:227], v249 offset:21504\n\t"
    "v_mfma_f32_16x16x32_bf16 v[168:171], v[236:239], v[56:59], v[168:171]\n\t"
    "ds_read_b64_tr_b16 v[228:229], v249 offset:25600\n\t"
    "ds_read_b64_tr_b16 v[230:231], v249 offset:29696\n\t"
    "ds_read_b64_tr_b16 v[232:233], v248 offset:17920\n\t"
    "s_waitcnt lgkmcnt(9)\n\t"
    "v_mfma_f32_16x16x32_bf16 v[172:175], v[240:243], v[48:51], v[172:175]\n\t"
    "ds_read_b64_tr_b16 v[234:235], v248 offset:22016\n\t"
    "v_mfma_f32_16x16x32_bf16 v[172:175], v[244:247], v[56:59], v[172:175]\n\t"
    "ds_read_b64_tr_b16 v[236:237], v248 offset:26112\n\t"
    "ds_read_b64_tr_b16 v[238:239], v248 offset:30208\n\t"
    "v_mfma_f32_16x16x32_bf16 v[144:147], v[138:141], v[64:67], v[144:147]\n\t"
    "ds_read_b64_tr_b16 v[240:241], v249 offset:17920\n\t"
    "s_waitcnt lgkmcnt(9)\n\t"
    "v_mfma_f32_16x16x32_bf16 v[184:187], v[216:219], v[48:51], v[184:187]\n\t"
    "ds_read_b64_tr_b16 v[242:243], v249 offset:22016\n\t"
    "v_mfma_f32_16x16x32_bf16 v[184:187], v[220:223], v[56:59], v[184:187]\n\t"
    "ds_read_b64_tr_b16 v[244:245], v249 offset:26112\n\t"
    "ds_read_b64_tr_b16 v[246:247], v249 offset:30208\n\t"
    "s_waitcnt lgkmcnt(8)\n\t"
    "v_mfma_f32_16x16x32_bf16 v[188:191], v[224:227], v[48:51], v[188:191]\n\t"
    "v_mfma_f32_16x16x32_bf16 v[188:191], v[228:231], v[56:59], v[188:191]\n\t"
    "s_waitcnt lgkmcnt(0)\n\t"
    "s_barrier\n\t"
    "ds_read_b64_tr_b16 v[216:217], v248 offset:32768\n\t"
    "ds_read_b64_tr_b16 v[218:219], v248 offset:36864\n\t"
    "v_mfma_f32_16x16x32_bf16 v[200:203], v[232:235], v[48:51], v[200:203]\n\t"
    "ds_read_b64_tr_b16 v[220:221], v248 offset:40960\n\t"
    "ds_read_b64_tr_b16 v[222:223], v248 offset:45056\n\t"
    "v_mfma_f32_16x16x32_bf16 v[200:203], v[236:239], v[56:59], v[200:203]\n\t"
    "ds_read_b64_tr_b16 v[224:225], v249 offset:32768\n\t"
    "ds_read_b64_tr_b16 v[226:227], v249 offset:36864\n\t"
    "ds_read_b64_tr_b16 v[228:229], v249 offset:40960\n\t"
    "ds_read_b64_tr_b16 v[230:231], v249 offset:45056\n\t"
    "v_mfma_f32_16x16x32_bf16 v[144:147], v[138:141], v[72:75], v[144:147]\n\t"
    "ds_read_b64_tr_b16 v[232:233], v248 offset:33280\n\t"
    "v_mfma_f32_16x16x32_bf16 v[204:207], v[240:243], v[48:51], v[204:207]\n\t"
    "ds_read_b64_tr_b16 v[234:235], v248 offset:37376\n\t"
    "v_mfma_f32_16x16x32_bf16 v[204:207], v[244:247], v[56:59], v[204:207]\n\t"
    "ds_read_b64_tr_b16 v[236:237], v248 offset:41472\n\t"
    "ds_read_b64_tr_b16 v[238:239], v248 offset:45568\n\t"
    "ds_read_b64_tr_b16 v[240:241], v249 offset:33280\n\t"
    "s_waitcnt lgkmcnt(9)\n\t"
    "v_mfma_f32_16x16x32_bf16 v[152:155], v[216:219], v[64:67], v[152:155]\n\t"
    "ds_read_b64_tr_b16 v[242:243], v249 offset:37376\n\t"
    "v_mfma_f32_16x16x32_bf16 v[152:155], v[220:223], v[72:75], v[152:155]\n\t"
    "ds_read_b64_tr_b16 v[244:245], v249 offset:41472\n\t"
    "ds_read_b64_tr_b16 v[246:247], v249 offset:45568\n\t"
    "ds_read_b64_tr_b16 v[216:217], v248 offset:33792\n\t"
    "s_waitcnt lgkmcnt(9)\n\t"
    "v_mfma_f32_16x16x32_bf16 v[156:159], v[224:227], v[64:67], v[156:159]\n\t"
    "ds_read_b64_tr_b16 v[218:219], v248 offset:37888\n\t"
    "v_mfma_f32_16x16x32_bf16 v[156:159], v[228:231], v[72:75], v[156:159]\n\t"
    "ds_read_b64_tr_b16 v[220:221], v248 offset:41984\n\t"
    "ds_read_b64_tr_b16 v[222:223], v248 offset:46080\n\t"
    "v_mfma_f32_16x16x32_bf16 v[144:147], v[138:141], v[80:83], v[144:147]\n\t"
    "ds_read_b64_tr_b16 v[224:225], v249 offset:33792\n\t"
    "s_waitcnt lgkmcnt(9)\n\t"
    "v_mfma_f32_16x16x32_bf16 v[168:171], v[232:235], v[64:67], v[168:171]\n\t"
    "ds_read_b64_tr_b16 v[226:227], v249 offset:37888\n\t"
    "v_mfma_f32_16x16x32_bf16 v[168:171], v[236:239], v[72:75], v[168:171]\n\t"
    "ds_read_b64_tr_b16 v[228:229], v249 offset:41984\n\t"
    "ds_read_b64_tr_b16 v[230:231], v249 offset:46080\n\t"
    "ds_read_b64_tr_b16 v[232:233], v248 offset:34304\n\t"
    "s_waitcnt lgkmcnt(9)\n\t"
    "v_mfma_f32_16x16x32_bf16 v[172:175], v[240:243], v[64:67], v[172:175]\n\t"
    "ds_read_b64_tr_b16 v[234:235], v248 offset:38400\n\t"
    "v_mfma_f32_16x16x32_bf16 v[172:175], v[244:247], v[72:75], v[172:175]\n\t"
    "ds_read_b64_tr_b16 v[236:237], v248 offset:42496\n\t"
    "ds_read_b64_tr_b16 v[238:239], v248 offset:46592\n\t"
    "ds_read_b64_tr_b16 v[240:241], v249 offset:34304\n\t"
    "s_waitcnt lgkmcnt(9)\n\t"
    "v_mfma_f32_16x16x32_bf16 v[184:187], v[216:219], v[64:67], v[184:187]\n\t"
    "ds_read_b64_tr_b16 v[242:243], v249 offset:38400\n\t"
    "v_mfma_f32_16x16x32_bf16 v[184:187], v[220:223], v[72:75], v[184:187]\n\t"
    "ds_read_b64_tr_b16 v[244:245], v249 offset:42496\n\t"
    "ds_read_b64_tr_b16 v[246:247], v249 offset:46592\n\t"
    "s_waitcnt lgkmcnt(8)\n\t"
    "v_mfma_f32_16x16x32_bf16 v[188:191], v[224:227], v[64:67], v[188:191]\n\t"
    "v_mfma_f32_16x16x32_bf16 v[188:191], v[228:231], v[72:75], v[188:191]\n\t"
    "v_mfma_f32_16x16x32_bf16 v[144:147], v[138:141], v[88:91], v[144:147]\n\t"
    "s_waitcnt lgkmcnt(0)\n\t"
    "s_barrier\n\t"
    "ds_read_b64_tr_b16 v[216:217], v248 offset:49152\n\t"
    "ds_read_b64_tr_b16 v[218:219], v248 offset:53248\n\t"
    "v_mfma_f32_16x16x32_bf16 v[200:203], v[232:235], v[64:67], v[200:203]\n\t"
    "ds_read_b64_tr_b16 v[220:221], v248 offset:57344\n\t"
    "ds_read_b64_tr_b16 v[222:223], v248 offset:61440\n\t"
    "v_mfma_f32_16x16x32_bf16 v[200:203], v[236:239], v[72:75], v[200:203]\n\t"
    "ds_read_b64_tr_b16 v[224:225], v249 offset:49152\n\t"
    "ds_read_b64_tr_b16 v[226:227], v249 offset:53248\n\t"
    "ds_read_b64_tr_b16 v[228:229], v249 offset:57344\n\t"
    "ds_read_b64_tr_b16 v[230:231], v249 offset:61440\n\t"
    "ds_read_b64_tr_b16 v[232:233], v248 offset:49664\n\t"
    "v_mfma_f32_16x16x32_bf16 v[204:207], v[240:243], v[64:67], v[204:207]\n\t"
    "ds_read_b64_tr_b16 v[234:235], v248 offset:53760\n\t"
    "v_mfma_f32_16x16x32_bf16 v[204:207], v[244:247], v[72:75], v[204:207]\n\t"
    "ds_read_b64_tr_b16 v[236:237], v248 offset:57856\n\t"
    "ds_read_b64_tr_b16 v[238:239], v248 offset:61952\n\t"
    "ds_read_b64_tr_b16 v[240:241], v249 offset:49664\n\t"
    "s_waitcnt lgkmcnt(9)\n\t"
    "v_mfma_f32_16x16x32_bf16 v[152:155], v[216:219], v[80:83], v[152:155]\n\t"
    "ds_read_b64_tr_b16 v[242:243], v249 offset:53760\n\t"
    "v_mfma_f32_16x16x32_bf16 v[152:155], v[220:223], v[88:91], v[152:155]\n\t"
    "ds_read_b64_tr_b16 v[244:245], v249 offset:57856\n\t"
    "ds_read_b64_tr_b16 v[246:247], v249 offset:61952\n\t"
    "v_mfma_f32_16x16x32_bf16 v[144:147], v[138:141], v[96:99], v[144:147]\n\t"
    "ds_read_b64_tr_b16 v[216:217], v248 offset:50176\n\t"
    "s_waitcnt lgkmcnt(9)\n\t"
    "v_mfma_f32_16x16x32_bf16 v[156:159], v[224:227], v[80:83], v[156:159]\n\t"
    "ds_read_b64_tr_b16 v[218:219], v248 offset:54272\n\t"
    "v_mfma_f32_16x16x32_bf16 v[156:159], v[228:231], v[88:91], v[156:159]\n\t"
    "ds_read_b64_tr_b16 v[220:221], v248 offset:58368\n\t"
    "ds_read_b64_tr_b16 v[222:223], v248 offset:62464\n\t"
    "ds_read_b64_tr_b16 v[224:225], v249 offset:50176\n\t"
    "s_waitcnt lgkmcnt(9)\n\t"
    "v_mfma_f32_16x16x32_bf16 v[168:171], v[232:235], v[80:83], v[168:171]\n\t"
    "ds_read_b64_tr_b16 v[226:227], v249 offset:54272\n\t"
    "v_mfma_f32_16x16x32_bf16 v[168:171], v[236:239], v[88:91], v[168:171]\n\t"
    "ds_read_b64_tr_b16 v[228:229], v249 offset:58368\n\t"
    "ds_read_b64_tr_b16 v[230:231], v249 offset:62464\n\t"
    "ds_read_b64_tr_b16 v[232:233], v248 offset:50688\n\t"
    "s_waitcnt lgkmcnt(9)\n\t"
    "v_mfma_f32_16x16x32_bf16 v[172:175], v[240:243], v[80:83], v[172:175]\n\t"
    "ds_read_b64_tr_b16 v[234:235], v248 offset:54784\n\t"
    "v_mfma_f32_16x16x32_bf16 v[172:175], v[244:247], v[88:91], v[172:175]\n\t"
    "ds_read_b64_tr_b16 v[236:237], v248 offset:58880\n\t"
    "ds_read_b64_tr_b16 v[238:239], v248 offset:62976\n\t"
    "v_mfma_f32_16x16x32_bf16 v[144:147], v[138:141], v[104:107], v[144:147]\n\t"
    "ds_read_b64_tr_b16 v[240:241], v249 offset:50688\n\t"
    "s_waitcnt lgkmcnt(9)\n\t"
    "v_mfma_f32_16x16x32_bf16 v[184:187], v[216:219], v[80:83], v[184:187]\n\t"
    "ds_read_b64_tr_b16 v[242:243], v249 offset:54784\n\t"
    "v_mfma_f32_16x16x32_bf16 v[184:187], v[220:223], v[88:91], v[184:187]\n\t"
    "ds_read_b64_tr_b16 v[244:245], v249 offset:58880\n\t"
    "ds_read_b64_tr_b16 v[246:247], v249 offset:62976\n\t"
    "s_waitcnt lgkmcnt(8)\n\t"
    "v_mfma_f32_16x16x32_bf16 v[188:191], v[224:227], v[80:83], v[188:191]\n\t"
    "v_mfma_f32_16x16x32_bf16 v[188:191], v[228:231], v[88:91], v[188:191]\n\t"
    "s_waitcnt lgkmcnt(0)\n\t"
    "s_barrier\n\t"
    "v_add_u32 v248, 0xffff0000, v248\n\t"
    "v_add_u32 v249, 0xffff0000, v249\n\t"
    "ds_read_b64_tr_b16 v[216:217], v248 offset:32768\n\t"
    "v_mfma_f32_16x16x32_bf16 v[200:203], v[232:235], v[80:83], v[200:203]\n\t"
    "ds_read_b64_tr_b16 v[218:219], v248 offset:36864\n\t"
    "ds_read_b64_tr_b16 v[220:221], v248 offset:40960\n\t"
    "ds_read_b64_tr_b16 v[222:223], v248 offset:45056\n\t"
    "v_mfma_f32_16x16x32_bf16 v[200:203], v[236:239], v[88:91], v[200:203]\n\t"
    "ds_read_b64_tr_b16 v[224:225], v249 offset:32768\n\t"
    "ds_read_b64_tr_b16 v[226:227], v249 offset:36864\n\t"
    "ds_read_b64_tr_b16 v[228:229], v249 offset:40960\n\t"
    "ds_read_b64_tr_b16 v[230:231], v249 offset:45056\n\t"
    "ds_read_b64_tr_b16 v[232:233], v248 offset:33280\n\t"
    "v_mfma_f32_16x16x32_bf16 v[204:207], v[240:243], v[80:83], v[204:207]\n\t"
    "ds_read_b64_tr_b16 v[234:235], v248 offset:37376\n\t"
    "v_mfma_f32_16x16x32_bf16 v[204:207], v[244:247], v[88:91], v[204:207]\n\t"
    "ds_read_b64_tr_b16 v[236:237], v248 offset:41472\n\t"
    "ds_read_b64_tr_b16 v[238:239], v248 offset:45568\n\t"
    "v_mfma_f32_16x16x32_bf16 v[144:147], v[138:141], v[112:115], v[144:147]\n\t"
    "ds_read_b64_tr_b16 v[240:241], v249 offset:33280\n\t"
    "s_waitcnt lgkmcnt(9)\n\t"
    "v_mfma_f32_16x16x32_bf16 v[152:155], v[216:219], v[96:99], v[152:155]\n\t"
    "ds_read_b64_tr_b16 v[242:243], v249 offset:37376\n\t"
    "v_mfma_f32_16x16x32_bf16 v[152:155], v[220:223], v[104:107], v[152:155]\n\t"
    "ds_read_b64_tr_b16 v[244:245], v249 offset:41472\n\t"
    "ds_read_b64_tr_b16 v[246:247], v249 offset:45568\n\t"
    "ds_read_b64_tr_b16 v[216:217], v248 offset:33792\n\t"
    "s_waitcnt lgkmcnt(9)\n\t"
    "v_mfma_f32_16x16x32_bf16 v[156:159], v[224:227], v[96:99], v[156:159]\n\t"
    "ds_read_b64_tr_b16 v[218:219], v248 offset:37888\n\t"
    "v_mfma_f32_16x16x32_bf16 v[156:159], v[228:231], v[104:107], v[156:159]\n\t"
    "ds_read_b64_tr_b16 v[220:221], v248 offset:41984\n\t"
    "ds_read_b64_tr_b16 v[222:223], v248 offset:46080\n\t"
    "ds_read_b64_tr_b16 v[224:225], v249 offset:33792\n\t"
    "s_waitcnt lgkmcnt(9)\n\t"
    "v_mfma_f32_16x16x32_bf16 v[168:171], v[232:235], v[96:99], v[168:171]\n\t"
    "ds_read_b64_tr_b16 v[226:227], v249 offset:37888\n\t"
    "v_mfma_f32_16x16x32_bf16 v[168:171], v[236:239], v[104:107], v[168:171]\n\t"
    "ds_read_b64_tr_b16 v[228:229], v249 offset:41984\n\t"
    "ds_read_b64_tr_b16 v[230:231], v249 offset:46080\n\t"
    "v_mfma_f32_16x16x32_bf16 v[144:147], v[138:141], v[120:123], v[144:147]\n\t"
    "ds_read_b64_tr_b16 v[232:233], v248 offset:34304\n\t"
    "s_waitcnt lgkmcnt(9)\n\t"
    "v_mfma_f32_16x16x32_bf16 v[172:175], v[240:243], v[96:99], v[172:175]\n\t"
    "ds_read_b64_tr_b16 v[234:235], v248 offset:38400\n\t"
    "v_mfma_f32_16x16x32_bf16 v[172:175], v[244:247], v[104:107], v[172:175]\n\t"
    "ds_read_b64_tr_b16 v[236:237], v248 offset:42496\n\t"
    "ds_read_b64_tr_b16 v[238:239], v248 offset:46592\n\t"
    "ds_read_b64_tr_b16 v[240:241], v249 offset:34304\n\t"
    "s_waitcnt lgkmcnt(9)\n\t"
    "v_mfma_f32_16x16x32_bf16 v[184:187], v[216:219], v[96:99], v[184:187]\n\t"
    "ds_read_b64_tr_b16 v[242:243], v249 offset:38400\n\t"
    "v_mfma_f32_16x16x32_bf16 v[184:187], v[220:223], v[104:107], v[184:187]\n\t"
    "ds_read_b64_tr_b16 v[244:245], v249 offset:42496\n\t"
    "ds_read_b64_tr_b16 v[246:247], v249 offset:46592\n\t"
    "s_waitcnt lgkmcnt(8)\n\t"
    "v_mfma_f32_16x16x32_bf16 v[188:191], v[224:227], v[96:99], v[188:191]\n\t"
    "v_mfma_f32_16x16x32_bf16 v[188:191], v[228:231], v[104:107], v[188:191]\n\t"
    "s_waitcnt lgkmcnt(0)\n\t"
    "s_barrier\n\t"
    "ds_read_b64_tr_b16 v[216:217], v248 offset:49152\n\t"
    "ds_read_b64_tr_b16 v[218:219], v248 offset:53248\n\t"
    "v_mfma_f32_16x16x32_bf16 v[200:203], v[232:235], v[96:99], v[200:203]\n\t"
    "ds_read_b64_tr_b16 v[220:221], v248 offset:57344\n\t"
    "ds_read_b64_tr_b16 v[222:223], v248 offset:61440\n\t"
    "v_mfma_f32_16x16x32_bf16 v[200:203], v[236:239], v[104:107], v[200:203]\n\t"
    "ds_read_b64_tr_b16 v[224:225], v249 offset:49152\n\t"
    "ds_read_b64_tr_b16 v[226:227], v249 offset:53248\n\t"
    "ds_read_b64_tr_b16 v[228:229], v249 offset:57344\n\t"
    "ds_read_b64_tr_b16 v[230:231], v249 offset:61440\n\t"
    "ds_read_b64_tr_b16 v[232:233], v248 offset:49664\n\t"
    "v_mfma_f32_16x16x32_bf16 v[204:207], v[240:243], v[96:99], v[204:207]\n\t"
    "ds_read_b64_tr_b16 v[234:235], v248 offset:53760\n\t"
    "v_mfma_f32_16x16x32_bf16 v[204:207], v[244:247], v[104:107], v[204:207]\n\t"
    "ds_read_b64_tr_b16 v[236:237], v248 offset:57856\n\t"
    "ds_read_b64_tr_b16 v[238:239], v248 offset:61952\n\t"
    "ds_read_b64_tr_b16 v[240:241], v249 offset:49664\n\t"
    "v_max_f32 v252, 0xda24260, v148\n\t"
    "s_waitcnt lgkmcnt(9)\n\t"
    "v_mfma_f32_16x16x32_bf16 v[152:155], v[216:219], v[112:115], v[152:155]\n\t"
    "ds_read_b64_tr_b16 v[242:243], v249 offset:53760\n\t"
    "v_rcp_f32 v251, v144\n\t"
    "v_rcp_f32 v252, v252\n\t"
    "v_mfma_f32_16x16x32_bf16 v[152:155], v[220:223], v[120:123], v[152:155]\n\t"
    "ds_read_b64_tr_b16 v[244:245], v249 offset:57856\n\t"
    "ds_read_b64_tr_b16 v[246:247], v249 offset:61952\n\t"
    "ds_read_b64_tr_b16 v[216:217], v248 offset:50176\n\t"
    "v_lshlrev_b32 v145, 5, v253\n\t"
    "v_cmp_gt_u32 vcc, 4, v253\n\t"
    "v_mov_b32 v254, 0x80000000\n\t"
    "s_waitcnt lgkmcnt(9)\n\t"
    "v_mfma_f32_16x16x32_bf16 v[156:159], v[224:227], v[112:115], v[156:159]\n\t"
    "ds_read_b64_tr_b16 v[218:219], v248 offset:54272\n\t"
    "v_cndmask_b32 v145, v254, v145, vcc\n\t"
    "v_mov_b32 v144, 0\n\t"
    "v_cmp_eq_u32 vcc, 0, v253\n\t"
    "v_mfma_f32_16x16x32_bf16 v[156:159], v[228:231], v[120:123], v[156:159]\n\t"
    "ds_read_b64_tr_b16 v[220:221], v248 offset:58368\n\t"
    "v_mov_b32 v254, 0x80000000\n\t"
    "v_cndmask_b32 v144, v254, v144, vcc\n\t"
    "v_mov_b32 v148, s44\n\t"
    "ds_read_b64_tr_b16 v[222:223], v248 offset:62464\n\t"
    "v_mov_b32 v149, s45\n\t"
    "ds_read_b64_tr_b16 v[224:225], v249 offset:50176\n\t"
    "s_waitcnt lgkmcnt(9)\n\t"
    "v_mfma_f32_16x16x32_bf16 v[168:171], v[232:235], v[112:115], v[168:171]\n\t"
    "ds_read_b64_tr_b16 v[226:227], v249 offset:54272\n\t"
    "v_mfma_f32_16x16x32_bf16 v[168:171], v[236:239], v[120:123], v[168:171]\n\t"
    "ds_read_b64_tr_b16 v[228:229], v249 offset:58368\n\t"
    "v_mul_f32 v152, v251, v152\n\t"
    "v_mul_f32 v153, v251, v153\n\t"
    "v_cvt_pk_bf16_f32 v0, v152, v153\n\t"
    "ds_read_b64_tr_b16 v[230:231], v249 offset:62464\n\t"
    "v_mul_f32 v154, v251, v154\n\t"
    "v_mul_f32 v155, v251, v155\n\t"
    "v_cvt_pk_bf16_f32 v1, v154, v155\n\t"
    "ds_read_b64_tr_b16 v[232:233], v248 offset:50688\n\t"
    "v_mul_f32 v156, v251, v156\n\t"
    "v_mul_f32 v157, v251, v157\n\t"
    "v_cvt_pk_bf16_f32 v2, v156, v157\n\t"
    "s_waitcnt lgkmcnt(9)\n\t"
    "v_mfma_f32_16x16x32_bf16 v[172:175], v[240:243], v[112:115], v[172:175]\n\t"
    "ds_read_b64_tr_b16 v[234:235], v248 offset:54784\n\t"
    "v_mul_f32 v158, v251, v158\n\t"
    "v_mul_f32 v159, v251, v159\n\t"
    "v_cvt_pk_bf16_f32 v3, v158, v159\n\t"
    "v_mfma_f32_16x16x32_bf16 v[172:175], v[244:247], v[120:123], v[172:175]\n\t"
    "ds_read_b64_tr_b16 v[236:237], v248 offset:58880\n\t"
    "ds_read_b64_tr_b16 v[238:239], v248 offset:62976\n\t"
    "ds_read_b64_tr_b16 v[240:241], v249 offset:50688\n\t"
    "s_waitcnt lgkmcnt(9)\n\t"
    "v_mfma_f32_16x16x32_bf16 v[184:187], v[216:219], v[112:115], v[184:187]\n\t"
    "ds_read_b64_tr_b16 v[242:243], v249 offset:54784\n\t"
    "v_mfma_f32_16x16x32_bf16 v[184:187], v[220:223], v[120:123], v[184:187]\n\t"
    "ds_read_b64_tr_b16 v[244:245], v249 offset:58880\n\t"
    "v_mul_f32 v168, v251, v168\n\t"
    "v_mul_f32 v169, v251, v169\n\t"
    "v_cvt_pk_bf16_f32 v8, v168, v169\n\t"
    "ds_read_b64_tr_b16 v[246:247], v249 offset:62976\n\t"
    "v_mul_f32 v170, v251, v170\n\t"
    "v_mul_f32 v171, v251, v171\n\t"
    "v_cvt_pk_bf16_f32 v9, v170, v171\n\t"
    "v_mul_f32 v172, v251, v172\n\t"
    "v_mul_f32 v173, v251, v173\n\t"
    "v_cvt_pk_bf16_f32 v10, v172, v173\n\t"
    "v_mul_f32 v174, v251, v174\n\t"
    "s_waitcnt lgkmcnt(8)\n\t"
    "v_mfma_f32_16x16x32_bf16 v[188:191], v[224:227], v[112:115], v[188:191]\n\t"
    "v_mul_f32 v175, v251, v175\n\t"
    "v_cvt_pk_bf16_f32 v11, v174, v175\n\t"
    "v_mfma_f32_16x16x32_bf16 v[188:191], v[228:231], v[120:123], v[188:191]\n\t"
    "s_waitcnt lgkmcnt(0)\n\t"
    "s_barrier\n\t"
    "v_mfma_f32_16x16x32_bf16 v[200:203], v[232:235], v[112:115], v[200:203]\n\t"
    "v_mfma_f32_16x16x32_bf16 v[200:203], v[236:239], v[120:123], v[200:203]\n\t"
    "v_mul_f32 v184, v251, v184\n\t"
    "v_mul_f32 v185, v251, v185\n\t"
    "v_cvt_pk_bf16_f32 v16, v184, v185\n\t"
    "v_mul_f32 v186, v251, v186\n\t"
    "v_mul_f32 v187, v251, v187\n\t"
    "v_cvt_pk_bf16_f32 v17, v186, v187\n\t"
    "v_mul_f32 v188, v251, v188\n\t"
    "v_mul_f32 v189, v251, v189\n\t"
    "v_cvt_pk_bf16_f32 v18, v188, v189\n\t"
    "v_mul_f32 v190, v251, v190\n\t"
    "v_mul_f32 v191, v251, v191\n\t"
    "v_cvt_pk_bf16_f32 v19, v190, v191\n\t"
    "v_mfma_f32_16x16x32_bf16 v[204:207], v[240:243], v[112:115], v[204:207]\n\t"
    "v_mfma_f32_16x16x32_bf16 v[204:207], v[244:247], v[120:123], v[204:207]\n\t"
    "v_mul_f32 v200, v251, v200\n\t"
    "v_mul_f32 v201, v251, v201\n\t"
    "v_cvt_pk_bf16_f32 v24, v200, v201\n\t"
    "v_mul_f32 v202, v251, v202\n\t"
    "v_mul_f32 v203, v251, v203\n\t"
    "v_cvt_pk_bf16_f32 v25, v202, v203\n\t"
    "s_nop 1\n\t"
    "v_mul_f32 v204, v251, v204\n\t"
    "v_mul_f32 v205, v251, v205\n\t"
    "v_cvt_pk_bf16_f32 v26, v204, v205\n\t"
    "v_mul_f32 v206, v251, v206\n\t"
    "v_mul_f32 v207, v251, v207\n\t"
    "v_cvt_pk_bf16_f32 v27, v206, v207\n\t"
    "s_nop 7\n\t"
    "s_branch W3J0_%=\n\t"
    "W3S1_%=:\n\t"
    "s_waitcnt vmcnt(8)\n\t"
    "s_waitcnt lgkmcnt(0)\n\t"
    "s_barrier\n\t"
    "s_add_u32 m0, s47, 0x1c000\n\t"
    "s_add_u32 s46, s59, 0x14000\n\t"
    "s_and_b32 s46, s46, 0x1ffff\n\t"
    "s_or_b32 s46, s46, s48\n\t"
    "buffer_load_dwordx4 v128, %[srdk], s46 offen nt lds\n\t"
    "buffer_load_dwordx4 v129, %[srdk], s46 offen offset:1024 nt lds\n\t"
    "buffer_load_dwordx4 v130, %[srdk], s46 offen offset:2048 nt lds\n\t"
    "buffer_load_dwordx4 v131, %[srdk], s46 offen offset:3072 nt lds\n\t"
    "v_add_u32 v250, 0xfffe0000, v250\n\t"
    "ds_read_b128 v[112:115], v250 offset:49152\n\t"
    "ds_read_b128 v[116:119], v250 offset:51200\n\t"
    "ds_read_b128 v[120:123], v250 offset:49664\n\t"
    "ds_read_b128 v[124:127], v250 offset:51712\n\t"
    "v_add_u32 v248, 0x10000, v248\n\t"
    "v_add_u32 v249, 0x10000, v249\n\t"
    "ds_read_b64_tr_b16 v[216:217], v248 offset:0\n\t"
    "ds_read_b64_tr_b16 v[218:219], v248 offset:256\n\t"
    "ds_read_b64_tr_b16 v[220:221], v248 offset:8192\n\t"
    "ds_read_b64_tr_b16 v[222:223], v248 offset:8448\n\t"
    "ds_read_b64_tr_b16 v[224:225], v249 offset:0\n\t"
    "ds_read_b64_tr_b16 v[226:227], v249 offset:256\n\t"
    "ds_read_b64_tr_b16 v[228:229], v249 offset:8192\n\t"
    "ds_read_b64_tr_b16 v[230:231], v249 offset:8448\n\t"
    "ds_read_b64_tr_b16 v[232:233], v248 offset:512\n\t"
    "ds_read_b64_tr_b16 v[234:235], v248 offset:768\n\t"
    "ds_read_b64_tr_b16 v[236:237], v248 offset:8704\n\t"
    "ds_read_b64_tr_b16 v[238:239], v248 offset:8960\n\t"
    "ds_read_b64_tr_b16 v[240:241], v249 offset:512\n\t"
    "ds_read_b64_tr_b16 v[242:243], v249 offset:768\n\t"
    "ds_read_b64_tr_b16 v[244:245], v249 offset:8704\n\t"
    "ds_read_b64_tr_b16 v[246:247], v249 offset:8960\n\t"
    "s_waitcnt lgkmcnt(12)\n\t"
    "v_mfma_f32_16x16x32_bf16 v[152:155], v[216:219], v[0:3], 0\n\t"
    "v_mfma_f32_16x16x32_bf16 v[152:155], v[220:223], v[8:11], v[152:155]\n\t"
    "s_waitcnt lgkmcnt(8)\n\t"
    "v_mfma_f32_16x16x32_bf16 v[156:159], v[224:227], v[0:3], 0\n\t"
    "v_mfma_f32_16x16x32_bf16 v[156:159], v[228:231], v[8:11], v[156:159]\n\t"
    "s_waitcnt lgkmcnt(0)\n\t"
    "s_barrier\n\t"
    "s_add_u32 m0, s47, 0x20000\n\t"
    "s_lshr_b32 s46, s49, 9\n\t"
    "s_add_u32 s46, s46, s58\n\t"
    "s_add_u32 s46, s46, 0x80000\n\t"
    "s_and_b32 s46, s46, 0x3fffff\n\t"
    "buffer_load_dwordx4 v134, %[srdwq], s46 offen lds\n\t"
    "buffer_load_dwordx4 v135, %[srdwq], s46 offen offset:1024 lds\n\t"
    "buffer_load_dwordx4 v136, %[srdwq], s46 offen offset:2048 lds\n\t"
    "buffer_load_dwordx4 v137, %[srdwq], s46 offen offset:3072 lds\n\t"
    "s_add_u32 m0, s47, 0x8000\n\t"
    "s_add_u32 s46, s59, 0x18000\n\t"
    "s_and_b32 s46, s46, 0x1ffff\n\t"
    "s_or_b32 s46, s46, s48\n\t"
    "s_waitcnt vmcnt(12)\n\t"
    "buffer_load_dwordx4 v128, %[srdk], s46 offen nt lds\n\t"
    "s_waitcnt vmcnt(12)\n\t"
    "buffer_load_dwordx4 v129, %[srdk], s46 offen offset:1024 nt lds\n\t"
    "s_waitcnt vmcnt(12)\n\t"
    "buffer_load_dwordx4 v130, %[srdk], s46 offen offset:2048 nt lds\n\t"
    "s_waitcnt vmcnt(12)\n\t"
    "buffer_load_dwordx4 v131, %[srdk], s46 offen offset:3072 nt lds\n\t"
    "s_add_u32 m0, s47, 0xc000\n\t"
    "s_add_u32 s46, s59, 0x1c000\n\t"
    "s_and_b32 s46, s46, 0x1ffff\n\t"
    "s_or_b32 s46, s46, s48\n\t"
    "s_waitcnt vmcnt(12)\n\t"
    "buffer_load_dwordx4 v128, %[srdk], s46 offen nt lds\n\t"
    "ds_read_b64_tr_b16 v[216:217], v248 offset:1024\n\t"
    "ds_read_b64_tr_b16 v[218:219], v248 offset:1280\n\t"
    "v_mfma_f32_16x16x32_bf16 v[168:171], v[232:235], v[0:3], 0\n\t"
    "ds_read_b64_tr_b16 v[220:221], v248 offset:9216\n\t"
    "ds_read_b64_tr_b16 v[222:223], v248 offset:9472\n\t"
    "v_mfma_f32_16x16x32_bf16 v[168:171], v[236:239], v[8:11], v[168:171]\n\t"
    "ds_read_b64_tr_b16 v[224:225], v249 offset:1024\n\t"
    "ds_read_b64_tr_b16 v[226:227], v249 offset:1280\n\t"
    "ds_read_b64_tr_b16 v[228:229], v249 offset:9216\n\t"
    "ds_read_b64_tr_b16 v[230:231], v249 offset:9472\n\t"
    "ds_read_b64_tr_b16 v[232:233], v248 offset:1536\n\t"
    "v_mfma_f32_16x16x32_bf16 v[172:175], v[240:243], v[0:3], 0\n\t"
    "ds_read_b64_tr_b16 v[234:235], v248 offset:1792\n\t"
    "v_mfma_f32_16x16x32_bf16 v[172:175], v[244:247], v[8:11], v[172:175]\n\t"
    "ds_read_b64_tr_b16 v[236:237], v248 offset:9728\n\t"
    "ds_read_b64_tr_b16 v[238:239], v248 offset:9984\n\t"
    "ds_read_b64_tr_b16 v[240:241], v249 offset:1536\n\t"
    "s_waitcnt lgkmcnt(9)\n\t"
    "v_mfma_f32_16x16x32_bf16 v[184:187], v[216:219], v[0:3], 0\n\t"
    "ds_read_b64_tr_b16 v[242:243], v249 offset:1792\n\t"
    "v_mfma_f32_16x16x32_bf16 v[184:187], v[220:223], v[8:11], v[184:187]\n\t"
    "ds_read_b64_tr_b16 v[244:245], v249 offset:9728\n\t"
    "ds_read_b64_tr_b16 v[246:247], v249 offset:9984\n\t"
    "s_waitcnt lgkmcnt(8)\n\t"
    "v_mfma_f32_16x16x32_bf16 v[188:191], v[224:227], v[0:3], 0\n\t"
    "v_mfma_f32_16x16x32_bf16 v[188:191], v[228:231], v[8:11], v[188:191]\n\t"
    "s_waitcnt vmcnt(5)\n\t"
    "s_waitcnt lgkmcnt(0)\n\t"
    "s_barrier\n\t"
    "s_add_u32 m0, s47, 0xc000\n\t"
    "s_add_u32 s46, s59, 0x1c000\n\t"
    "s_and_b32 s46, s46, 0x1ffff\n\t"
    "s_or_b32 s46, s46, s48\n\t"
    "buffer_load_dwordx4 v129, %[srdk], s46 offen offset:1024 nt lds\n\t"
    "buffer_load_dwordx4 v130, %[srdk], s46 offen offset:2048 nt lds\n\t"
    "buffer_load_dwordx4 v131, %[srdk], s46 offen offset:3072 nt lds\n\t"
    "s_add_u32 m0, s47, 0x10000\n\t"
    "s_add_u32 s46, s59, 0x0\n\t"
    "s_and_b32 s46, s46, 0x1ffff\n\t"
    "s_or_b32 s46, s46, s48\n\t"
    "buffer_load_dwordx4 v132, %[srdv], s46 offen nt lds\n\t"
    "v_add_u32 v248, 0x10000, v248\n\t"
    "v_add_u32 v249, 0x10000, v249\n\t"
    "ds_read_b64_tr_b16 v[216:217], v248 offset:0\n\t"
    "v_mfma_f32_16x16x32_bf16 v[200:203], v[232:235], v[0:3], 0\n\t"
    "ds_read_b64_tr_b16 v[218:219], v248 offset:256\n\t"
    "ds_read_b64_tr_b16 v[220:221], v248 offset:8192\n\t"
    "ds_read_b64_tr_b16 v[222:223], v248 offset:8448\n\t"
    "v_mfma_f32_16x16x32_bf16 v[200:203], v[236:239], v[8:11], v[200:203]\n\t"
    "ds_read_b64_tr_b16 v[224:225], v249 offset:0\n\t"
    "ds_read_b64_tr_b16 v[226:227], v249 offset:256\n\t"
    "ds_read_b64_tr_b16 v[228:229], v249 offset:8192\n\t"
    "ds_read_b64_tr_b16 v[230:231], v249 offset:8448\n\t"
    "ds_read_b64_tr_b16 v[232:233], v248 offset:512\n\t"
    "v_mfma_f32_16x16x32_bf16 v[204:207], v[240:243], v[0:3], 0\n\t"
    "ds_read_b64_tr_b16 v[234:235], v248 offset:768\n\t"
    "v_mfma_f32_16x16x32_bf16 v[204:207], v[244:247], v[8:11], v[204:207]\n\t"
    "ds_read_b64_tr_b16 v[236:237], v248 offset:8704\n\t"
    "ds_read_b64_tr_b16 v[238:239], v248 offset:8960\n\t"
    "ds_read_b64_tr_b16 v[240:241], v249 offset:512\n\t"
    "s_waitcnt lgkmcnt(9)\n\t"
    "v_mfma_f32_16x16x32_bf16 v[152:155], v[216:219], v[16:19], v[152:155]\n\t"
    "ds_read_b64_tr_b16 v[242:243], v249 offset:768\n\t"
    "v_mfma_f32_16x16x32_bf16 v[152:155], v[220:223], v[24:27], v[152:155]\n\t"
    "ds_read_b64_tr_b16 v[244:245], v249 offset:8704\n\t"
    "ds_read_b64_tr_b16 v[246:247], v249 offset:8960\n\t"
    "s_waitcnt lgkmcnt(8)\n\t"
    "v_mfma_f32_16x16x32_bf16 v[156:159], v[224:227], v[16:19], v[156:159]\n\t"
    "v_mfma_f32_16x16x32_bf16 v[156:159], v[228:231], v[24:27], v[156:159]\n\t"
    "s_waitcnt lgkmcnt(0)\n\t"
    "s_barrier\n\t"
    "s_add_u32 m0, s47, 0x24000\n\t"
    "s_lshr_b32 s46, s49, 9\n\t"
    "s_add_u32 s46, s46, s58\n\t"
    "s_add_u32 s46, s46, 0x100000\n\t"
    "s_and_b32 s46, s46, 0x3fffff\n\t"
    "buffer_load_dwordx4 v134, %[srdwq], s46 offen lds\n\t"
    "buffer_load_dwordx4 v135, %[srdwq], s46 offen offset:1024 lds\n\t"
    "buffer_load_dwordx4 v136, %[srdwq], s46 offen offset:2048 lds\n\t"
    "buffer_load_dwordx4 v137, %[srdwq], s46 offen offset:3072 lds\n\t"
    "s_add_u32 m0, s47, 0x10000\n\t"
    "s_add_u32 s46, s59, 0x0\n\t"
    "s_and_b32 s46, s46, 0x1ffff\n\t"
    "s_or_b32 s46, s46, s48\n\t"
    "s_waitcnt vmcnt(12)\n\t"
    "buffer_load_dwordx4 v133, %[srdv], s46 offen offset:1024 nt lds\n\t"
    "s_waitcnt vmcnt(12)\n\t"
    "buffer_load_dwordx4 v132, %[srdv], s46 offen offset:2048 nt lds\n\t"
    "s_waitcnt vmcnt(12)\n\t"
    "buffer_load_dwordx4 v133, %[srdv], s46 offen offset:3072 nt lds\n\t"
    "s_add_u32 m0, s47, 0x0\n\t"
    "s_add_u32 s46, s59, 0x4000\n\t"
    "s_and_b32 s46, s46, 0x1ffff\n\t"
    "s_or_b32 s46, s46, s48\n\t"
    "s_waitcnt vmcnt(12)\n\t"
    "buffer_load_dwordx4 v132, %[srdv], s46 offen nt lds\n\t"
    "s_waitcnt vmcnt(12)\n\t"
    "buffer_load_dwordx4 v133, %[srdv], s46 offen offset:1024 nt lds\n\t"
    "ds_read_b64_tr_b16 v[216:217], v248 offset:1024\n\t"
    "ds_read_b64_tr_b16 v[218:219], v248 offset:1280\n\t"
    "v_mfma_f32_16x16x32_bf16 v[168:171], v[232:235], v[16:19], v[168:171]\n\t"
    "ds_read_b64_tr_b16 v[220:221], v248 offset:9216\n\t"
    "ds_read_b64_tr_b16 v[222:223], v248 offset:9472\n\t"
    "v_mfma_f32_16x16x32_bf16 v[168:171], v[236:239], v[24:27], v[168:171]\n\t"
    "ds_read_b64_tr_b16 v[224:225], v249 offset:1024\n\t"
    "ds_read_b64_tr_b16 v[226:227], v249 offset:1280\n\t"
    "ds_read_b64_tr_b16 v[228:229], v249 offset:9216\n\t"
    "ds_read_b64_tr_b16 v[230:231], v249 offset:9472\n\t"
    "ds_read_b64_tr_b16 v[232:233], v248 offset:1536\n\t"
    "v_mfma_f32_16x16x32_bf16 v[172:175], v[240:243], v[16:19], v[172:175]\n\t"
    "ds_read_b64_tr_b16 v[234:235], v248 offset:1792\n\t"
    "v_mfma_f32_16x16x32_bf16 v[172:175], v[244:247], v[24:27], v[172:175]\n\t"
    "ds_read_b64_tr_b16 v[236:237], v248 offset:9728\n\t"
    "ds_read_b64_tr_b16 v[238:239], v248 offset:9984\n\t"
    "ds_read_b64_tr_b16 v[240:241], v249 offset:1536\n\t"
    "s_waitcnt lgkmcnt(9)\n\t"
    "v_mfma_f32_16x16x32_bf16 v[184:187], v[216:219], v[16:19], v[184:187]\n\t"
    "ds_read_b64_tr_b16 v[242:243], v249 offset:1792\n\t"
    "v_mfma_f32_16x16x32_bf16 v[184:187], v[220:223], v[24:27], v[184:187]\n\t"
    "ds_read_b64_tr_b16 v[244:245], v249 offset:9728\n\t"
    "ds_read_b64_tr_b16 v[246:247], v249 offset:9984\n\t"
    "s_waitcnt lgkmcnt(8)\n\t"
    "v_mfma_f32_16x16x32_bf16 v[188:191], v[224:227], v[16:19], v[188:191]\n\t"
    "v_mfma_f32_16x16x32_bf16 v[188:191], v[228:231], v[24:27], v[188:191]\n\t"
    "s_waitcnt vmcnt(5)\n\t"
    "s_waitcnt lgkmcnt(0)\n\t"
    "s_barrier\n\t"
    "s_add_u32 m0, s47, 0x0\n\t"
    "s_add_u32 s46, s59, 0x4000\n\t"
    "s_and_b32 s46, s46, 0x1ffff\n\t"
    "s_or_b32 s46, s46, s48\n\t"
    "buffer_load_dwordx4 v132, %[srdv], s46 offen offset:2048 nt lds\n\t"
    "buffer_load_dwordx4 v133, %[srdv], s46 offen offset:3072 nt lds\n\t"
    "s_add_u32 m0, s47, 0x4000\n\t"
    "s_add_u32 s46, s59, 0x8000\n\t"
    "s_and_b32 s46, s46, 0x1ffff\n\t"
    "s_or_b32 s46, s46, s48\n\t"
    "buffer_load_dwordx4 v132, %[srdv], s46 offen nt lds\n\t"
    "buffer_load_dwordx4 v133, %[srdv], s46 offen offset:1024 nt lds\n\t"
    "ds_read_b64_tr_b16 v[216:217], v248 offset:16384\n\t"
    "ds_read_b64_tr_b16 v[218:219], v248 offset:16640\n\t"
    "v_mfma_f32_16x16x32_bf16 v[200:203], v[232:235], v[16:19], v[200:203]\n\t"
    "ds_read_b64_tr_b16 v[220:221], v248 offset:24576\n\t"
    "ds_read_b64_tr_b16 v[222:223], v248 offset:24832\n\t"
    "v_mfma_f32_16x16x32_bf16 v[200:203], v[236:239], v[24:27], v[200:203]\n\t"
    "ds_read_b64_tr_b16 v[224:225], v249 offset:16384\n\t"
    "ds_read_b64_tr_b16 v[226:227], v249 offset:16640\n\t"
    "ds_read_b64_tr_b16 v[228:229], v249 offset:24576\n\t"
    "ds_read_b64_tr_b16 v[230:231], v249 offset:24832\n\t"
    "ds_read_b64_tr_b16 v[232:233], v248 offset:16896\n\t"
    "v_mfma_f32_16x16x32_bf16 v[204:207], v[240:243], v[16:19], v[204:207]\n\t"
    "ds_read_b64_tr_b16 v[234:235], v248 offset:17152\n\t"
    "v_mfma_f32_16x16x32_bf16 v[204:207], v[244:247], v[24:27], v[204:207]\n\t"
    "ds_read_b64_tr_b16 v[236:237], v248 offset:25088\n\t"
    "ds_read_b64_tr_b16 v[238:239], v248 offset:25344\n\t"
    "ds_read_b64_tr_b16 v[240:241], v249 offset:16896\n\t"
    "s_waitcnt lgkmcnt(9)\n\t"
    "v_mfma_f32_16x16x32_bf16 v[152:155], v[216:219], v[32:35], v[152:155]\n\t"
    "ds_read_b64_tr_b16 v[242:243], v249 offset:17152\n\t"
    "v_mfma_f32_16x16x32_bf16 v[152:155], v[220:223], v[40:43], v[152:155]\n\t"
    "ds_read_b64_tr_b16 v[244:245], v249 offset:25088\n\t"
    "ds_read_b64_tr_b16 v[246:247], v249 offset:25344\n\t"
    "s_waitcnt lgkmcnt(8)\n\t"
    "v_mfma_f32_16x16x32_bf16 v[156:159], v[224:227], v[32:35], v[156:159]\n\t"
    "v_mfma_f32_16x16x32_bf16 v[156:159], v[228:231], v[40:43], v[156:159]\n\t"
    "s_waitcnt lgkmcnt(0)\n\t"
    "s_barrier\n\t"
    "s_add_u32 m0, s47, 0x20000\n\t"
    "s_lshr_b32 s46, s49, 9\n\t"
    "s_add_u32 s46, s46, s58\n\t"
    "s_add_u32 s46, s46, 0x180000\n\t"
    "s_and_b32 s46, s46, 0x3fffff\n\t"
    "buffer_load_dwordx4 v134, %[srdwq], s46 offen lds\n\t"
    "buffer_load_dwordx4 v135, %[srdwq], s46 offen offset:1024 lds\n\t"
    "buffer_load_dwordx4 v136, %[srdwq], s46 offen offset:2048 lds\n\t"
    "buffer_load_dwordx4 v137, %[srdwq], s46 offen offset:3072 lds\n\t"
    "s_add_u32 m0, s47, 0x4000\n\t"
    "s_add_u32 s46, s59, 0x8000\n\t"
    "s_and_b32 s46, s46, 0x1ffff\n\t"
    "s_or_b32 s46, s46, s48\n\t"
    "s_waitcnt vmcnt(12)\n\t"
    "buffer_load_dwordx4 v132, %[srdv], s46 offen offset:2048 nt lds\n\t"
    "s_waitcnt vmcnt(12)\n\t"
    "buffer_load_dwordx4 v133, %[srdv], s46 offen offset:3072 nt lds\n\t"
    "s_add_u32 m0, s47, 0x14000\n\t"
    "s_add_u32 s46, s59, 0xc000\n\t"
    "s_and_b32 s46, s46, 0x1ffff\n\t"
    "s_or_b32 s46, s46, s48\n\t"
    "s_waitcnt vmcnt(12)\n\t"
    "buffer_load_dwordx4 v132, %[srdv], s46 offen nt lds\n\t"
    "s_waitcnt vmcnt(12)\n\t"
    "buffer_load_dwordx4 v133, %[srdv], s46 offen offset:1024 nt lds\n\t"
    "s_waitcnt vmcnt(12)\n\t"
    "buffer_load_dwordx4 v132, %[srdv], s46 offen offset:2048 nt lds\n\t"
    "ds_read_b64_tr_b16 v[216:217], v248 offset:17408\n\t"
    "ds_read_b64_tr_b16 v[218:219], v248 offset:17664\n\t"
    "v_mfma_f32_16x16x32_bf16 v[168:171], v[232:235], v[32:35], v[168:171]\n\t"
    "ds_read_b64_tr_b16 v[220:221], v248 offset:25600\n\t"
    "ds_read_b64_tr_b16 v[222:223], v248 offset:25856\n\t"
    "v_mfma_f32_16x16x32_bf16 v[168:171], v[236:239], v[40:43], v[168:171]\n\t"
    "ds_read_b64_tr_b16 v[224:225], v249 offset:17408\n\t"
    "ds_read_b64_tr_b16 v[226:227], v249 offset:17664\n\t"
    "ds_read_b64_tr_b16 v[228:229], v249 offset:25600\n\t"
    "ds_read_b64_tr_b16 v[230:231], v249 offset:25856\n\t"
    "ds_read_b64_tr_b16 v[232:233], v248 offset:17920\n\t"
    "v_mfma_f32_16x16x32_bf16 v[172:175], v[240:243], v[32:35], v[172:175]\n\t"
    "ds_read_b64_tr_b16 v[234:235], v248 offset:18176\n\t"
    "v_mfma_f32_16x16x32_bf16 v[172:175], v[244:247], v[40:43], v[172:175]\n\t"
    "ds_read_b64_tr_b16 v[236:237], v248 offset:26112\n\t"
    "ds_read_b64_tr_b16 v[238:239], v248 offset:26368\n\t"
    "ds_read_b64_tr_b16 v[240:241], v249 offset:17920\n\t"
    "s_waitcnt lgkmcnt(9)\n\t"
    "v_mfma_f32_16x16x32_bf16 v[184:187], v[216:219], v[32:35], v[184:187]\n\t"
    "ds_read_b64_tr_b16 v[242:243], v249 offset:18176\n\t"
    "v_mfma_f32_16x16x32_bf16 v[184:187], v[220:223], v[40:43], v[184:187]\n\t"
    "ds_read_b64_tr_b16 v[244:245], v249 offset:26112\n\t"
    "ds_read_b64_tr_b16 v[246:247], v249 offset:26368\n\t"
    "s_waitcnt lgkmcnt(8)\n\t"
    "v_mfma_f32_16x16x32_bf16 v[188:191], v[224:227], v[32:35], v[188:191]\n\t"
    "v_mfma_f32_16x16x32_bf16 v[188:191], v[228:231], v[40:43], v[188:191]\n\t"
    "s_waitcnt vmcnt(5)\n\t"
    "s_waitcnt lgkmcnt(0)\n\t"
    "s_barrier\n\t"
    "s_add_u32 m0, s47, 0x14000\n\t"
    "s_add_u32 s46, s59, 0xc000\n\t"
    "s_and_b32 s46, s46, 0x1ffff\n\t"
    "s_or_b32 s46, s46, s48\n\t"
    "buffer_load_dwordx4 v133, %[srdv], s46 offen offset:3072 nt lds\n\t"
    "s_add_u32 m0, s47, 0x18000\n\t"
    "s_add_u32 s46, s59, 0x10000\n\t"
    "s_and_b32 s46, s46, 0x1ffff\n\t"
    "s_or_b32 s46, s46, s48\n\t"
    "buffer_load_dwordx4 v132, %[srdv], s46 offen nt lds\n\t"
    "buffer_load_dwordx4 v133, %[srdv], s46 offen offset:1024 nt lds\n\t"
    "buffer_load_dwordx4 v132, %[srdv], s46 offen offset:2048 nt lds\n\t"
    "ds_read_b64_tr_b16 v[216:217], v248 offset:0\n\t"
    "ds_read_b64_tr_b16 v[218:219], v248 offset:256\n\t"
    "v_mfma_f32_16x16x32_bf16 v[200:203], v[232:235], v[32:35], v[200:203]\n\t"
    "ds_read_b64_tr_b16 v[220:221], v248 offset:8192\n\t"
    "ds_read_b64_tr_b16 v[222:223], v248 offset:8448\n\t"
    "v_mfma_f32_16x16x32_bf16 v[200:203], v[236:239], v[40:43], v[200:203]\n\t"
    "ds_read_b64_tr_b16 v[224:225], v249 offset:0\n\t"
    "ds_read_b64_tr_b16 v[226:227], v249 offset:256\n\t"
    "ds_read_b64_tr_b16 v[228:229], v249 offset:8192\n\t"
    "ds_read_b64_tr_b16 v[230:231], v249 offset:8448\n\t"
    "ds_read_b64_tr_b16 v[232:233], v248 offset:512\n\t"
    "v_mfma_f32_16x16x32_bf16 v[204:207], v[240:243], v[32:35], v[204:207]\n\t"
    "ds_read_b64_tr_b16 v[234:235], v248 offset:768\n\t"
    "v_mfma_f32_16x16x32_bf16 v[204:207], v[244:247], v[40:43], v[204:207]\n\t"
    "ds_read_b64_tr_b16 v[236:237], v248 offset:8704\n\t"
    "ds_read_b64_tr_b16 v[238:239], v248 offset:8960\n\t"
    "ds_read_b64_tr_b16 v[240:241], v249 offset:512\n\t"
    "s_waitcnt lgkmcnt(9)\n\t"
    "v_mfma_f32_16x16x32_bf16 v[152:155], v[216:219], v[48:51], v[152:155]\n\t"
    "ds_read_b64_tr_b16 v[242:243], v249 offset:768\n\t"
    "v_mfma_f32_16x16x32_bf16 v[152:155], v[220:223], v[56:59], v[152:155]\n\t"
    "ds_read_b64_tr_b16 v[244:245], v249 offset:8704\n\t"
    "ds_read_b64_tr_b16 v[246:247], v249 offset:8960\n\t"
    "s_waitcnt lgkmcnt(8)\n\t"
    "v_mfma_f32_16x16x32_bf16 v[156:159], v[224:227], v[48:51], v[156:159]\n\t"
    "v_mfma_f32_16x16x32_bf16 v[156:159], v[228:231], v[56:59], v[156:159]\n\t"
    "s_waitcnt lgkmcnt(0)\n\t"
    "s_barrier\n\t"
    "s_add_u32 m0, s47, 0x24000\n\t"
    "s_lshr_b32 s46, s49, 9\n\t"
    "s_add_u32 s46, s46, s58\n\t"
    "s_add_u32 s46, s46, 0x200000\n\t"
    "s_and_b32 s46, s46, 0x3fffff\n\t"
    "buffer_load_dwordx4 v134, %[srdwq], s46 offen lds\n\t"
    "buffer_load_dwordx4 v135, %[srdwq], s46 offen offset:1024 lds\n\t"
    "buffer_load_dwordx4 v136, %[srdwq], s46 offen offset:2048 lds\n\t"
    "buffer_load_dwordx4 v137, %[srdwq], s46 offen offset:3072 lds\n\t"
    "s_add_u32 m0, s47, 0x18000\n\t"
    "s_add_u32 s46, s59, 0x10000\n\t"
    "s_and_b32 s46, s46, 0x1ffff\n\t"
    "s_or_b32 s46, s46, s48\n\t"
    "s_waitcnt vmcnt(12)\n\t"
    "buffer_load_dwordx4 v133, %[srdv], s46 offen offset:3072 nt lds\n\t"
    "s_add_u32 m0, s47, 0x1c000\n\t"
    "s_add_u32 s46, s59, 0x14000\n\t"
    "s_and_b32 s46, s46, 0x1ffff\n\t"
    "s_or_b32 s46, s46, s48\n\t"
    "s_waitcnt vmcnt(12)\n\t"
    "buffer_load_dwordx4 v132, %[srdv], s46 offen nt lds\n\t"
    "s_waitcnt vmcnt(12)\n\t"
    "buffer_load_dwordx4 v133, %[srdv], s46 offen offset:1024 nt lds\n\t"
    "s_waitcnt vmcnt(12)\n\t"
    "buffer_load_dwordx4 v132, %[srdv], s46 offen offset:2048 nt lds\n\t"
    "ds_read_b64_tr_b16 v[216:217], v248 offset:1024\n\t"
    "ds_read_b64_tr_b16 v[218:219], v248 offset:1280\n\t"
    "v_mfma_f32_16x16x32_bf16 v[168:171], v[232:235], v[48:51], v[168:171]\n\t"
    "ds_read_b64_tr_b16 v[220:221], v248 offset:9216\n\t"
    "ds_read_b64_tr_b16 v[222:223], v248 offset:9472\n\t"
    "v_mfma_f32_16x16x32_bf16 v[168:171], v[236:239], v[56:59], v[168:171]\n\t"
    "ds_read_b64_tr_b16 v[224:225], v249 offset:1024\n\t"
    "ds_read_b64_tr_b16 v[226:227], v249 offset:1280\n\t"
    "ds_read_b64_tr_b16 v[228:229], v249 offset:9216\n\t"
    "ds_read_b64_tr_b16 v[230:231], v249 offset:9472\n\t"
    "ds_read_b64_tr_b16 v[232:233], v248 offset:1536\n\t"
    "v_mfma_f32_16x16x32_bf16 v[172:175], v[240:243], v[48:51], v[172:175]\n\t"
    "ds_read_b64_tr_b16 v[234:235], v248 offset:1792\n\t"
    "v_mfma_f32_16x16x32_bf16 v[172:175], v[244:247], v[56:59], v[172:175]\n\t"
    "ds_read_b64_tr_b16 v[236:237], v248 offset:9728\n\t"
    "ds_read_b64_tr_b16 v[238:239], v248 offset:9984\n\t"
    "ds_read_b64_tr_b16 v[240:241], v249 offset:1536\n\t"
    "s_waitcnt lgkmcnt(9)\n\t"
    "v_mfma_f32_16x16x32_bf16 v[184:187], v[216:219], v[48:51], v[184:187]\n\t"
    "ds_read_b64_tr_b16 v[242:243], v249 offset:1792\n\t"
    "v_mfma_f32_16x16x32_bf16 v[184:187], v[220:223], v[56:59], v[184:187]\n\t"
    "ds_read_b64_tr_b16 v[244:245], v249 offset:9728\n\t"
    "ds_read_b64_tr_b16 v[246:247], v249 offset:9984\n\t"
    "s_waitcnt lgkmcnt(8)\n\t"
    "v_mfma_f32_16x16x32_bf16 v[188:191], v[224:227], v[48:51], v[188:191]\n\t"
    "v_mfma_f32_16x16x32_bf16 v[188:191], v[228:231], v[56:59], v[188:191]\n\t"
    "s_waitcnt vmcnt(4)\n\t"
    "s_waitcnt lgkmcnt(0)\n\t"
    "s_barrier\n\t"
    "s_add_u32 m0, s47, 0x1c000\n\t"
    "s_add_u32 s46, s59, 0x14000\n\t"
    "s_and_b32 s46, s46, 0x1ffff\n\t"
    "s_or_b32 s46, s46, s48\n\t"
    "buffer_load_dwordx4 v133, %[srdv], s46 offen offset:3072 nt lds\n\t"
    "s_add_u32 m0, s47, 0x8000\n\t"
    "s_add_u32 s46, s59, 0x18000\n\t"
    "s_and_b32 s46, s46, 0x1ffff\n\t"
    "s_or_b32 s46, s46, s48\n\t"
    "buffer_load_dwordx4 v132, %[srdv], s46 offen nt lds\n\t"
    "buffer_load_dwordx4 v133, %[srdv], s46 offen offset:1024 nt lds\n\t"
    "buffer_load_dwordx4 v132, %[srdv], s46 offen offset:2048 nt lds\n\t"
    "buffer_load_dwordx4 v133, %[srdv], s46 offen offset:3072 nt lds\n\t"
    "ds_read_b64_tr_b16 v[216:217], v248 offset:16384\n\t"
    "ds_read_b64_tr_b16 v[218:219], v248 offset:16640\n\t"
    "v_mfma_f32_16x16x32_bf16 v[200:203], v[232:235], v[48:51], v[200:203]\n\t"
    "ds_read_b64_tr_b16 v[220:221], v248 offset:24576\n\t"
    "ds_read_b64_tr_b16 v[222:223], v248 offset:24832\n\t"
    "v_mfma_f32_16x16x32_bf16 v[200:203], v[236:239], v[56:59], v[200:203]\n\t"
    "ds_read_b64_tr_b16 v[224:225], v249 offset:16384\n\t"
    "ds_read_b64_tr_b16 v[226:227], v249 offset:16640\n\t"
    "ds_read_b64_tr_b16 v[228:229], v249 offset:24576\n\t"
    "ds_read_b64_tr_b16 v[230:231], v249 offset:24832\n\t"
    "ds_read_b64_tr_b16 v[232:233], v248 offset:16896\n\t"
    "v_mfma_f32_16x16x32_bf16 v[204:207], v[240:243], v[48:51], v[204:207]\n\t"
    "ds_read_b64_tr_b16 v[234:235], v248 offset:17152\n\t"
    "v_mfma_f32_16x16x32_bf16 v[204:207], v[244:247], v[56:59], v[204:207]\n\t"
    "ds_read_b64_tr_b16 v[236:237], v248 offset:25088\n\t"
    "ds_read_b64_tr_b16 v[238:239], v248 offset:25344\n\t"
    "ds_read_b64_tr_b16 v[240:241], v249 offset:16896\n\t"
    "s_waitcnt lgkmcnt(9)\n\t"
    "v_mfma_f32_16x16x32_bf16 v[152:155], v[216:219], v[64:67], v[152:155]\n\t"
    "ds_read_b64_tr_b16 v[242:243], v249 offset:17152\n\t"
    "v_mfma_f32_16x16x32_bf16 v[152:155], v[220:223], v[72:75], v[152:155]\n\t"
    "ds_read_b64_tr_b16 v[244:245], v249 offset:25088\n\t"
    "ds_read_b64_tr_b16 v[246:247], v249 offset:25344\n\t"
    "s_waitcnt lgkmcnt(8)\n\t"
    "v_mfma_f32_16x16x32_bf16 v[156:159], v[224:227], v[64:67], v[156:159]\n\t"
    "v_mfma_f32_16x16x32_bf16 v[156:159], v[228:231], v[72:75], v[156:159]\n\t"
    "s_waitcnt lgkmcnt(0)\n\t"
    "s_barrier\n\t"
    "s_add_u32 m0, s47, 0x20000\n\t"
    "s_lshr_b32 s46, s49, 9\n\t"
    "s_add_u32 s46, s46, s58\n\t"
    "s_add_u32 s46, s46, 0x280000\n\t"
    "s_and_b32 s46, s46, 0x3fffff\n\t"
    "buffer_load_dwordx4 v134, %[srdwq], s46 offen lds\n\t"
    "buffer_load_dwordx4 v135, %[srdwq], s46 offen offset:1024 lds\n\t"
    "buffer_load_dwordx4 v136, %[srdwq], s46 offen offset:2048 lds\n\t"
    "buffer_load_dwordx4 v137, %[srdwq], s46 offen offset:3072 lds\n\t"
    "s_add_u32 m0, s47, 0xc000\n\t"
    "s_add_u32 s46, s59, 0x1c000\n\t"
    "s_and_b32 s46, s46, 0x1ffff\n\t"
    "s_or_b32 s46, s46, s48\n\t"
    "s_waitcnt vmcnt(12)\n\t"
    "buffer_load_dwordx4 v132, %[srdv], s46 offen nt lds\n\t"
    "s_waitcnt vmcnt(12)\n\t"
    "buffer_load_dwordx4 v133, %[srdv], s46 offen offset:1024 nt lds\n\t"
    "s_waitcnt vmcnt(12)\n\t"
    "buffer_load_dwordx4 v132, %[srdv], s46 offen offset:2048 nt lds\n\t"
    "s_waitcnt vmcnt(12)\n\t"
    "buffer_load_dwordx4 v133, %[srdv], s46 offen offset:3072 nt lds\n\t"
    "ds_read_b64_tr_b16 v[216:217], v248 offset:17408\n\t"
    "ds_read_b64_tr_b16 v[218:219], v248 offset:17664\n\t"
    "v_mfma_f32_16x16x32_bf16 v[168:171], v[232:235], v[64:67], v[168:171]\n\t"
    "ds_read_b64_tr_b16 v[220:221], v248 offset:25600\n\t"
    "ds_read_b64_tr_b16 v[222:223], v248 offset:25856\n\t"
    "v_mfma_f32_16x16x32_bf16 v[168:171], v[236:239], v[72:75], v[168:171]\n\t"
    "ds_read_b64_tr_b16 v[224:225], v249 offset:17408\n\t"
    "ds_read_b64_tr_b16 v[226:227], v249 offset:17664\n\t"
    "ds_read_b64_tr_b16 v[228:229], v249 offset:25600\n\t"
    "ds_read_b64_tr_b16 v[230:231], v249 offset:25856\n\t"
    "ds_read_b64_tr_b16 v[232:233], v248 offset:17920\n\t"
    "v_mfma_f32_16x16x32_bf16 v[172:175], v[240:243], v[64:67], v[172:175]\n\t"
    "ds_read_b64_tr_b16 v[234:235], v248 offset:18176\n\t"
    "v_mfma_f32_16x16x32_bf16 v[172:175], v[244:247], v[72:75], v[172:175]\n\t"
    "ds_read_b64_tr_b16 v[236:237], v248 offset:26112\n\t"
    "ds_read_b64_tr_b16 v[238:239], v248 offset:26368\n\t"
    "ds_read_b64_tr_b16 v[240:241], v249 offset:17920\n\t"
    "s_waitcnt lgkmcnt(9)\n\t"
    "v_mfma_f32_16x16x32_bf16 v[184:187], v[216:219], v[64:67], v[184:187]\n\t"
    "ds_read_b64_tr_b16 v[242:243], v249 offset:18176\n\t"
    "v_mfma_f32_16x16x32_bf16 v[184:187], v[220:223], v[72:75], v[184:187]\n\t"
    "ds_read_b64_tr_b16 v[244:245], v249 offset:26112\n\t"
    "ds_read_b64_tr_b16 v[246:247], v249 offset:26368\n\t"
    "s_waitcnt lgkmcnt(8)\n\t"
    "v_mfma_f32_16x16x32_bf16 v[188:191], v[224:227], v[64:67], v[188:191]\n\t"
    "v_mfma_f32_16x16x32_bf16 v[188:191], v[228:231], v[72:75], v[188:191]\n\t"
    "s_waitcnt vmcnt(4)\n\t"
    "s_waitcnt lgkmcnt(0)\n\t"
    "s_barrier\n\t"
    "s_add_u32 m0, s47, 0x10000\n\t"
    "s_add_u32 s46, s59, 0x0\n\t"
    "s_and_b32 s46, s46, 0x1ffff\n\t"
    "s_or_b32 s46, s46, s49\n\t"
    "buffer_load_dwordx4 v128, %[srdk], s46 offen nt lds\n\t"
    "buffer_load_dwordx4 v129, %[srdk], s46 offen offset:1024 nt lds\n\t"
    "buffer_load_dwordx4 v130, %[srdk], s46 offen offset:2048 nt lds\n\t"
    "buffer_load_dwordx4 v131, %[srdk], s46 offen offset:3072 nt lds\n\t"
    "ds_read_b64_tr_b16 v[216:217], v248 offset:0\n\t"
    "ds_read_b64_tr_b16 v[218:219], v248 offset:256\n\t"
    "v_mfma_f32_16x16x32_bf16 v[200:203], v[232:235], v[64:67], v[200:203]\n\t"
    "ds_read_b64_tr_b16 v[220:221], v248 offset:8192\n\t"
    "ds_read_b64_tr_b16 v[222:223], v248 offset:8448\n\t"
    "v_mfma_f32_16x16x32_bf16 v[200:203], v[236:239], v[72:75], v[200:203]\n\t"
    "ds_read_b64_tr_b16 v[224:225], v249 offset:0\n\t"
    "ds_read_b64_tr_b16 v[226:227], v249 offset:256\n\t"
    "ds_read_b64_tr_b16 v[228:229], v249 offset:8192\n\t"
    "ds_read_b64_tr_b16 v[230:231], v249 offset:8448\n\t"
    "ds_read_b64_tr_b16 v[232:233], v248 offset:512\n\t"
    "v_mfma_f32_16x16x32_bf16 v[204:207], v[240:243], v[64:67], v[204:207]\n\t"
    "ds_read_b64_tr_b16 v[234:235], v248 offset:768\n\t"
    "v_mfma_f32_16x16x32_bf16 v[204:207], v[244:247], v[72:75], v[204:207]\n\t"
    "ds_read_b64_tr_b16 v[236:237], v248 offset:8704\n\t"
    "ds_read_b64_tr_b16 v[238:239], v248 offset:8960\n\t"
    "ds_read_b64_tr_b16 v[240:241], v249 offset:512\n\t"
    "s_waitcnt lgkmcnt(9)\n\t"
    "v_mfma_f32_16x16x32_bf16 v[152:155], v[216:219], v[80:83], v[152:155]\n\t"
    "ds_read_b64_tr_b16 v[242:243], v249 offset:768\n\t"
    "v_mfma_f32_16x16x32_bf16 v[152:155], v[220:223], v[88:91], v[152:155]\n\t"
    "ds_read_b64_tr_b16 v[244:245], v249 offset:8704\n\t"
    "ds_read_b64_tr_b16 v[246:247], v249 offset:8960\n\t"
    "s_waitcnt lgkmcnt(8)\n\t"
    "v_mfma_f32_16x16x32_bf16 v[156:159], v[224:227], v[80:83], v[156:159]\n\t"
    "v_mfma_f32_16x16x32_bf16 v[156:159], v[228:231], v[88:91], v[156:159]\n\t"
    "s_waitcnt lgkmcnt(0)\n\t"
    "s_barrier\n\t"
    "s_add_u32 m0, s47, 0x24000\n\t"
    "s_lshr_b32 s46, s49, 9\n\t"
    "s_add_u32 s46, s46, s58\n\t"
    "s_add_u32 s46, s46, 0x300000\n\t"
    "s_and_b32 s46, s46, 0x3fffff\n\t"
    "buffer_load_dwordx4 v134, %[srdwq], s46 offen lds\n\t"
    "buffer_load_dwordx4 v135, %[srdwq], s46 offen offset:1024 lds\n\t"
    "buffer_load_dwordx4 v136, %[srdwq], s46 offen offset:2048 lds\n\t"
    "buffer_load_dwordx4 v137, %[srdwq], s46 offen offset:3072 lds\n\t"
    "s_add_u32 m0, s47, 0x0\n\t"
    "s_add_u32 s46, s59, 0x4000\n\t"
    "s_and_b32 s46, s46, 0x1ffff\n\t"
    "s_or_b32 s46, s46, s49\n\t"
    "buffer_load_dwordx4 v128, %[srdk], s46 offen nt lds\n\t"
    "s_waitcnt vmcnt(12)\n\t"
    "buffer_load_dwordx4 v129, %[srdk], s46 offen offset:1024 nt lds\n\t"
    "s_waitcnt vmcnt(12)\n\t"
    "buffer_load_dwordx4 v130, %[srdk], s46 offen offset:2048 nt lds\n\t"
    "s_waitcnt vmcnt(12)\n\t"
    "buffer_load_dwordx4 v131, %[srdk], s46 offen offset:3072 nt lds\n\t"
    "ds_read_b64_tr_b16 v[216:217], v248 offset:1024\n\t"
    "ds_read_b64_tr_b16 v[218:219], v248 offset:1280\n\t"
    "v_mfma_f32_16x16x32_bf16 v[168:171], v[232:235], v[80:83], v[168:171]\n\t"
    "ds_read_b64_tr_b16 v[220:221], v248 offset:9216\n\t"
    "ds_read_b64_tr_b16 v[222:223], v248 offset:9472\n\t"
    "v_mfma_f32_16x16x32_bf16 v[168:171], v[236:239], v[88:91], v[168:171]\n\t"
    "ds_read_b64_tr_b16 v[224:225], v249 offset:1024\n\t"
    "ds_read_b64_tr_b16 v[226:227], v249 offset:1280\n\t"
    "ds_read_b64_tr_b16 v[228:229], v249 offset:9216\n\t"
    "ds_read_b64_tr_b16 v[230:231], v249 offset:9472\n\t"
    "ds_read_b64_tr_b16 v[232:233], v248 offset:1536\n\t"
    "v_mfma_f32_16x16x32_bf16 v[172:175], v[240:243], v[80:83], v[172:175]\n\t"
    "ds_read_b64_tr_b16 v[234:235], v248 offset:1792\n\t"
    "v_mfma_f32_16x16x32_bf16 v[172:175], v[244:247], v[88:91], v[172:175]\n\t"
    "ds_read_b64_tr_b16 v[236:237], v248 offset:9728\n\t"
    "ds_read_b64_tr_b16 v[238:239], v248 offset:9984\n\t"
    "ds_read_b64_tr_b16 v[240:241], v249 offset:1536\n\t"
    "s_waitcnt lgkmcnt(9)\n\t"
    "v_mfma_f32_16x16x32_bf16 v[184:187], v[216:219], v[80:83], v[184:187]\n\t"
    "ds_read_b64_tr_b16 v[242:243], v249 offset:1792\n\t"
    "v_mfma_f32_16x16x32_bf16 v[184:187], v[220:223], v[88:91], v[184:187]\n\t"
    "ds_read_b64_tr_b16 v[244:245], v249 offset:9728\n\t"
    "ds_read_b64_tr_b16 v[246:247], v249 offset:9984\n\t"
    "s_waitcnt lgkmcnt(8)\n\t"
    "v_mfma_f32_16x16x32_bf16 v[188:191], v[224:227], v[80:83], v[188:191]\n\t"
    "v_mfma_f32_16x16x32_bf16 v[188:191], v[228:231], v[88:91], v[188:191]\n\t"
    "s_waitcnt vmcnt(4)\n\t"
    "s_waitcnt lgkmcnt(0)\n\t"
    "s_barrier\n\t"
    "s_add_u32 m0, s47, 0x4000\n\t"
    "s_add_u32 s46, s59, 0x8000\n\t"
    "s_and_b32 s46, s46, 0x1ffff\n\t"
    "s_or_b32 s46, s46, s49\n\t"
    "buffer_load_dwordx4 v128, %[srdk], s46 offen nt lds\n\t"
    "buffer_load_dwordx4 v129, %[srdk], s46 offen offset:1024 nt lds\n\t"
    "buffer_load_dwordx4 v130, %[srdk], s46 offen offset:2048 nt lds\n\t"
    "buffer_load_dwordx4 v131, %[srdk], s46 offen offset:3072 nt lds\n\t"
    "ds_read_b64_tr_b16 v[216:217], v248 offset:16384\n\t"
    "ds_read_b64_tr_b16 v[218:219], v248 offset:16640\n\t"
    "v_mfma_f32_16x16x32_bf16 v[200:203], v[232:235], v[80:83], v[200:203]\n\t"
    "ds_read_b64_tr_b16 v[220:221], v248 offset:24576\n\t"
    "ds_read_b64_tr_b16 v[222:223], v248 offset:24832\n\t"
    "v_mfma_f32_16x16x32_bf16 v[200:203], v[236:239], v[88:91], v[200:203]\n\t"
    "ds_read_b64_tr_b16 v[224:225], v249 offset:16384\n\t"
    "ds_read_b64_tr_b16 v[226:227], v249 offset:16640\n\t"
    "ds_read_b64_tr_b16 v[228:229], v249 offset:24576\n\t"
    "ds_read_b64_tr_b16 v[230:231], v249 offset:24832\n\t"
    "ds_read_b64_tr_b16 v[232:233], v248 offset:16896\n\t"
    "v_mfma_f32_16x16x32_bf16 v[204:207], v[240:243], v[80:83], v[204:207]\n\t"
    "ds_read_b64_tr_b16 v[234:235], v248 offset:17152\n\t"
    "v_mfma_f32_16x16x32_bf16 v[204:207], v[244:247], v[88:91], v[204:207]\n\t"
    "ds_read_b64_tr_b16 v[236:237], v248 offset:25088\n\t"
    "ds_read_b64_tr_b16 v[238:239], v248 offset:25344\n\t"
    "ds_read_b64_tr_b16 v[240:241], v249 offset:16896\n\t"
    "s_waitcnt lgkmcnt(9)\n\t"
    "v_mfma_f32_16x16x32_bf16 v[152:155], v[216:219], v[96:99], v[152:155]\n\t"
    "ds_read_b64_tr_b16 v[242:243], v249 offset:17152\n\t"
    "v_mfma_f32_16x16x32_bf16 v[152:155], v[220:223], v[104:107], v[152:155]\n\t"
    "ds_read_b64_tr_b16 v[244:245], v249 offset:25088\n\t"
    "ds_read_b64_tr_b16 v[246:247], v249 offset:25344\n\t"
    "s_waitcnt lgkmcnt(8)\n\t"
    "v_mfma_f32_16x16x32_bf16 v[156:159], v[224:227], v[96:99], v[156:159]\n\t"
    "v_mfma_f32_16x16x32_bf16 v[156:159], v[228:231], v[104:107], v[156:159]\n\t"
    "s_waitcnt lgkmcnt(0)\n\t"
    "s_barrier\n\t"
    "s_add_u32 m0, s47, 0x20000\n\t"
    "s_lshr_b32 s46, s49, 9\n\t"
    "s_add_u32 s46, s46, s58\n\t"
    "s_add_u32 s46, s46, 0x380000\n\t"
    "s_and_b32 s46, s46, 0x3fffff\n\t"
    "buffer_load_dwordx4 v134, %[srdwq], s46 offen lds\n\t"
    "buffer_load_dwordx4 v135, %[srdwq], s46 offen offset:1024 lds\n\t"
    "buffer_load_dwordx4 v136, %[srdwq], s46 offen offset:2048 lds\n\t"
    "buffer_load_dwordx4 v137, %[srdwq], s46 offen offset:3072 lds\n\t"
    "s_add_u32 m0, s47, 0x14000\n\t"
    "s_add_u32 s46, s59, 0xc000\n\t"
    "s_and_b32 s46, s46, 0x1ffff\n\t"
    "s_or_b32 s46, s46, s49\n\t"
    "buffer_load_dwordx4 v128, %[srdk], s46 offen nt lds\n\t"
    "s_waitcnt vmcnt(12)\n\t"
    "buffer_load_dwordx4 v129, %[srdk], s46 offen offset:1024 nt lds\n\t"
    "s_waitcnt vmcnt(12)\n\t"
    "buffer_load_dwordx4 v130, %[srdk], s46 offen offset:2048 nt lds\n\t"
    "s_waitcnt vmcnt(12)\n\t"
    "buffer_load_dwordx4 v131, %[srdk], s46 offen offset:3072 nt lds\n\t"
    "ds_read_b64_tr_b16 v[216:217], v248 offset:17408\n\t"
    "ds_read_b64_tr_b16 v[218:219], v248 offset:17664\n\t"
    "v_mfma_f32_16x16x32_bf16 v[168:171], v[232:235], v[96:99], v[168:171]\n\t"
    "ds_read_b64_tr_b16 v[220:221], v248 offset:25600\n\t"
    "ds_read_b64_tr_b16 v[222:223], v248 offset:25856\n\t"
    "v_mfma_f32_16x16x32_bf16 v[168:171], v[236:239], v[104:107], v[168:171]\n\t"
    "ds_read_b64_tr_b16 v[224:225], v249 offset:17408\n\t"
    "ds_read_b64_tr_b16 v[226:227], v249 offset:17664\n\t"
    "ds_read_b64_tr_b16 v[228:229], v249 offset:25600\n\t"
    "ds_read_b64_tr_b16 v[230:231], v249 offset:25856\n\t"
    "ds_read_b64_tr_b16 v[232:233], v248 offset:17920\n\t"
    "v_mfma_f32_16x16x32_bf16 v[172:175], v[240:243], v[96:99], v[172:175]\n\t"
    "ds_read_b64_tr_b16 v[234:235], v248 offset:18176\n\t"
    "v_mfma_f32_16x16x32_bf16 v[172:175], v[244:247], v[104:107], v[172:175]\n\t"
    "ds_read_b64_tr_b16 v[236:237], v248 offset:26112\n\t"
    "ds_read_b64_tr_b16 v[238:239], v248 offset:26368\n\t"
    "ds_read_b64_tr_b16 v[240:241], v249 offset:17920\n\t"
    "s_waitcnt lgkmcnt(9)\n\t"
    "v_mfma_f32_16x16x32_bf16 v[184:187], v[216:219], v[96:99], v[184:187]\n\t"
    "ds_read_b64_tr_b16 v[242:243], v249 offset:18176\n\t"
    "v_mfma_f32_16x16x32_bf16 v[184:187], v[220:223], v[104:107], v[184:187]\n\t"
    "ds_read_b64_tr_b16 v[244:245], v249 offset:26112\n\t"
    "ds_read_b64_tr_b16 v[246:247], v249 offset:26368\n\t"
    "s_waitcnt lgkmcnt(8)\n\t"
    "v_mfma_f32_16x16x32_bf16 v[188:191], v[224:227], v[96:99], v[188:191]\n\t"
    "v_mfma_f32_16x16x32_bf16 v[188:191], v[228:231], v[104:107], v[188:191]\n\t"
    "s_waitcnt vmcnt(4)\n\t"
    "s_waitcnt lgkmcnt(0)\n\t"
    "s_barrier\n\t"
    "s_add_u32 m0, s47, 0x18000\n\t"
    "s_add_u32 s46, s59, 0x10000\n\t"
    "s_and_b32 s46, s46, 0x1ffff\n\t"
    "s_or_b32 s46, s46, s49\n\t"
    "buffer_load_dwordx4 v128, %[srdk], s46 offen nt lds\n\t"
    "buffer_load_dwordx4 v129, %[srdk], s46 offen offset:1024 nt lds\n\t"
    "buffer_load_dwordx4 v130, %[srdk], s46 offen offset:2048 nt lds\n\t"
    "buffer_load_dwordx4 v131, %[srdk], s46 offen offset:3072 nt lds\n\t"
    "ds_read_b64_tr_b16 v[216:217], v248 offset:0\n\t"
    "ds_read_b64_tr_b16 v[218:219], v248 offset:256\n\t"
    "v_mfma_f32_16x16x32_bf16 v[200:203], v[232:235], v[96:99], v[200:203]\n\t"
    "ds_read_b64_tr_b16 v[220:221], v248 offset:8192\n\t"
    "ds_read_b64_tr_b16 v[222:223], v248 offset:8448\n\t"
    "v_mfma_f32_16x16x32_bf16 v[200:203], v[236:239], v[104:107], v[200:203]\n\t"
    "ds_read_b64_tr_b16 v[224:225], v249 offset:0\n\t"
    "ds_read_b64_tr_b16 v[226:227], v249 offset:256\n\t"
    "ds_read_b64_tr_b16 v[228:229], v249 offset:8192\n\t"
    "ds_read_b64_tr_b16 v[230:231], v249 offset:8448\n\t"
    "ds_read_b64_tr_b16 v[232:233], v248 offset:512\n\t"
    "v_mfma_f32_16x16x32_bf16 v[204:207], v[240:243], v[96:99], v[204:207]\n\t"
    "ds_read_b64_tr_b16 v[234:235], v248 offset:768\n\t"
    "v_mfma_f32_16x16x32_bf16 v[204:207], v[244:247], v[104:107], v[204:207]\n\t"
    "ds_read_b64_tr_b16 v[236:237], v248 offset:8704\n\t"
    "ds_read_b64_tr_b16 v[238:239], v248 offset:8960\n\t"
    "ds_read_b64_tr_b16 v[240:241], v249 offset:512\n\t"
    "s_waitcnt lgkmcnt(9)\n\t"
    "v_mfma_f32_16x16x32_bf16 v[152:155], v[216:219], v[112:115], v[152:155]\n\t"
    "ds_read_b64_tr_b16 v[242:243], v249 offset:768\n\t"
    "v_mfma_f32_16x16x32_bf16 v[152:155], v[220:223], v[120:123], v[152:155]\n\t"
    "ds_read_b64_tr_b16 v[244:245], v249 offset:8704\n\t"
    "ds_read_b64_tr_b16 v[246:247], v249 offset:8960\n\t"
    "s_waitcnt lgkmcnt(8)\n\t"
    "v_mfma_f32_16x16x32_bf16 v[156:159], v[224:227], v[112:115], v[156:159]\n\t"
    "v_mfma_f32_16x16x32_bf16 v[156:159], v[228:231], v[120:123], v[156:159]\n\t"
    "s_waitcnt lgkmcnt(0)\n\t"
    "s_barrier\n\t"
    "s_add_u32 m0, s47, 0x24000\n\t"
    "s_add_u32 s46, s59, 0x14000\n\t"
    "s_and_b32 s46, s46, 0x1ffff\n\t"
    "s_or_b32 s46, s46, s49\n\t"
    "buffer_load_dwordx4 v128, %[srdk], s46 offen nt lds\n\t"
    "buffer_load_dwordx4 v129, %[srdk], s46 offen offset:1024 nt lds\n\t"
    "buffer_load_dwordx4 v130, %[srdk], s46 offen offset:2048 nt lds\n\t"
    "buffer_load_dwordx4 v131, %[srdk], s46 offen offset:3072 nt lds\n\t"
    "ds_read_b64_tr_b16 v[216:217], v248 offset:1024\n\t"
    "ds_read_b64_tr_b16 v[218:219], v248 offset:1280\n\t"
    "v_mfma_f32_16x16x32_bf16 v[168:171], v[232:235], v[112:115], v[168:171]\n\t"
    "ds_read_b64_tr_b16 v[220:221], v248 offset:9216\n\t"
    "ds_read_b64_tr_b16 v[222:223], v248 offset:9472\n\t"
    "v_mfma_f32_16x16x32_bf16 v[168:171], v[236:239], v[120:123], v[168:171]\n\t"
    "ds_read_b64_tr_b16 v[224:225], v249 offset:1024\n\t"
    "ds_read_b64_tr_b16 v[226:227], v249 offset:1280\n\t"
    "v_mul_f32 v152, 0x3e0293ee, v152\n\t"
    "v_mul_f32 v153, 0x3e0293ee, v153\n\t"
    "ds_read_b64_tr_b16 v[228:229], v249 offset:9216\n\t"
    "ds_read_b64_tr_b16 v[230:231], v249 offset:9472\n\t"
    "v_cvt_pk_bf16_f32 v152, v152, v153\n\t"
    "v_mul_f32 v154, 0x3e0293ee, v154\n\t"
    "ds_read_b64_tr_b16 v[232:233], v248 offset:1536\n\t"
    "v_mul_f32 v155, 0x3e0293ee, v155\n\t"
    "v_cvt_pk_bf16_f32 v153, v154, v155\n\t"
    "v_mul_f32 v156, 0x3e0293ee, v156\n\t"
    "v_mfma_f32_16x16x32_bf16 v[172:175], v[240:243], v[112:115], v[172:175]\n\t"
    "ds_read_b64_tr_b16 v[234:235], v248 offset:1792\n\t"
    "v_mul_f32 v157, 0x3e0293ee, v157\n\t"
    "v_cvt_pk_bf16_f32 v154, v156, v157\n\t"
    "v_mul_f32 v158, 0x3e0293ee, v158\n\t"
    "v_mfma_f32_16x16x32_bf16 v[172:175], v[244:247], v[120:123], v[172:175]\n\t"
    "ds_read_b64_tr_b16 v[236:237], v248 offset:9728\n\t"
    "v_mul_f32 v159, 0x3e0293ee, v159\n\t"
    "v_cvt_pk_bf16_f32 v155, v158, v159\n\t"
    "ds_read_b64_tr_b16 v[238:239], v248 offset:9984\n\t"
    "ds_read_b64_tr_b16 v[240:241], v249 offset:1536\n\t"
    "s_waitcnt lgkmcnt(9)\n\t"
    "v_mfma_f32_16x16x32_bf16 v[184:187], v[216:219], v[112:115], v[184:187]\n\t"
    "ds_read_b64_tr_b16 v[242:243], v249 offset:1792\n\t"
    "v_mfma_f32_16x16x32_bf16 v[184:187], v[220:223], v[120:123], v[184:187]\n\t"
    "ds_read_b64_tr_b16 v[244:245], v249 offset:9728\n\t"
    "v_permlane16_swap_b32 v152, v154\n\t"
    "ds_read_b64_tr_b16 v[246:247], v249 offset:9984\n\t"
    "v_permlane16_swap_b32 v153, v155\n\t"
    "v_mul_f32 v168, 0x3e0293ee, v168\n\t"
    "v_mul_f32 v169, 0x3e0293ee, v169\n\t"
    "v_cvt_pk_bf16_f32 v160, v168, v169\n\t"
    "v_mul_f32 v170, 0x3e0293ee, v170\n\t"
    "s_waitcnt lgkmcnt(8)\n\t"
    "v_mfma_f32_16x16x32_bf16 v[188:191], v[224:227], v[112:115], v[188:191]\n\t"
    "v_mul_f32 v171, 0x3e0293ee, v171\n\t"
    "v_cvt_pk_bf16_f32 v161, v170, v171\n\t"
    "v_mul_f32 v172, 0x3e0293ee, v172\n\t"
    "v_mul_f32 v173, 0x3e0293ee, v173\n\t"
    "v_mfma_f32_16x16x32_bf16 v[188:191], v[228:231], v[120:123], v[188:191]\n\t"
    "v_cvt_pk_bf16_f32 v162, v172, v173\n\t"
    "v_mul_f32 v174, 0x3e0293ee, v174\n\t"
    "v_mul_f32 v175, 0x3e0293ee, v175\n\t"
    "v_cvt_pk_bf16_f32 v163, v174, v175\n\t"
    "s_waitcnt lgkmcnt(0)\n\t"
    "s_barrier\n\t"
    "s_add_u32 m0, s47, 0x1c000\n\t"
    "s_add_u32 s46, s59, 0x18000\n\t"
    "s_and_b32 s46, s46, 0x1ffff\n\t"
    "s_or_b32 s46, s46, s49\n\t"
    "buffer_load_dwordx4 v128, %[srdk], s46 offen nt lds\n\t"
    "s_waitcnt vmcnt(12)\n\t"
    "buffer_load_dwordx4 v129, %[srdk], s46 offen offset:1024 nt lds\n\t"
    "s_waitcnt vmcnt(12)\n\t"
    "buffer_load_dwordx4 v130, %[srdk], s46 offen offset:2048 nt lds\n\t"
    "s_waitcnt vmcnt(12)\n\t"
    "buffer_load_dwordx4 v131, %[srdk], s46 offen offset:3072 nt lds\n\t"
    "v_mfma_f32_16x16x32_bf16 v[200:203], v[232:235], v[112:115], v[200:203]\n\t"
    "v_mfma_f32_16x16x32_bf16 v[200:203], v[236:239], v[120:123], v[200:203]\n\t"
    "v_permlane16_swap_b32 v160, v162\n\t"
    "v_permlane16_swap_b32 v161, v163\n\t"
    "v_mul_f32 v184, 0x3e0293ee, v184\n\t"
    "v_mul_f32 v185, 0x3e0293ee, v185\n\t"
    "v_cvt_pk_bf16_f32 v168, v184, v185\n\t"
    "v_mul_f32 v186, 0x3e0293ee, v186\n\t"
    "v_mul_f32 v187, 0x3e0293ee, v187\n\t"
    "v_cvt_pk_bf16_f32 v169, v186, v187\n\t"
    "v_mul_f32 v188, 0x3e0293ee, v188\n\t"
    "v_mul_f32 v189, 0x3e0293ee, v189\n\t"
    "v_mfma_f32_16x16x32_bf16 v[204:207], v[240:243], v[112:115], v[204:207]\n\t"
    "v_cvt_pk_bf16_f32 v170, v188, v189\n\t"
    "v_mul_f32 v190, 0x3e0293ee, v190\n\t"
    "v_mul_f32 v191, 0x3e0293ee, v191\n\t"
    "v_cvt_pk_bf16_f32 v171, v190, v191\n\t"
    "v_mfma_f32_16x16x32_bf16 v[204:207], v[244:247], v[120:123], v[204:207]\n\t"
    "v_permlane16_swap_b32 v168, v170\n\t"
    "v_permlane16_swap_b32 v169, v171\n\t"
    "v_mul_f32 v200, 0x3e0293ee, v200\n\t"
    "v_mul_f32 v201, 0x3e0293ee, v201\n\t"
    "v_cvt_pk_bf16_f32 v176, v200, v201\n\t"
    "v_mul_f32 v202, 0x3e0293ee, v202\n\t"
    "v_mul_f32 v203, 0x3e0293ee, v203\n\t"
    "v_cvt_pk_bf16_f32 v177, v202, v203\n\t"
    "v_mul_f32 v204, 0x3e0293ee, v204\n\t"
    "v_mul_f32 v205, 0x3e0293ee, v205\n\t"
    "v_cvt_pk_bf16_f32 v178, v204, v205\n\t"
    "v_mul_f32 v206, 0x3e0293ee, v206\n\t"
    "v_mul_f32 v207, 0x3e0293ee, v207\n\t"
    "v_cvt_pk_bf16_f32 v179, v206, v207\n\t"
    "v_permlane16_swap_b32 v176, v178\n\t"
    "s_nop 0\n\t"
    "v_permlane16_swap_b32 v177, v179\n\t"
    "v_mov_b32 v250, %[lds]\n\t"
    "v_bfe_u32 v254, v253, 3, 1\n\t"
    "v_lshl_add_u32 v250, v254, 11, v250\n\t"
    "v_bfe_u32 v254, v253, 0, 3\n\t"
    "v_lshl_add_u32 v250, v254, 6, v250\n\t"
    "v_bfe_u32 v254, v253, 4, 1\n\t"
    "v_lshl_add_u32 v250, v254, 5, v250\n\t"
    "v_bfe_u32 v254, v253, 5, 1\n\t"
    "v_lshl_add_u32 v250, v254, 4, v250\n\t"
    "v_bfe_u32 v254, v253, 3, 1\n\t"
    "v_lshlrev_b32 v254, 4, v254\n\t"
    "v_xor_b32 v250, v250, v254\n\t"
    "s_setprio 0\n\t"
    "s_barrier\n\t"
    "v_add_u32 v250, 0x10000, v250\n\t"
    "ds_read_b128 v[216:219], v250 offset:0\n\t"
    "ds_read_b128 v[220:223], v250 offset:512\n\t"
    "ds_read_b128 v[224:227], v250 offset:1024\n\t"
    "ds_read_b128 v[228:231], v250 offset:1536\n\t"
    "ds_read_b128 v[232:235], v250 offset:4096\n\t"
    "ds_read_b128 v[236:239], v250 offset:4608\n\t"
    "ds_read_b128 v[240:243], v250 offset:5120\n\t"
    "ds_read_b128 v[244:247], v250 offset:5632\n\t"
    "s_waitcnt lgkmcnt(6)\n\t"
    "v_mfma_f32_16x16x32_bf16 v[120:123], v[216:219], v[152:155], 0\n\t"
    "v_mfma_f32_16x16x32_bf16 v[120:123], v[220:223], v[160:163], v[120:123]\n\t"
    "s_waitcnt lgkmcnt(4)\n\t"
    "v_mfma_f32_16x16x32_bf16 v[120:123], v[224:227], v[168:171], v[120:123]\n\t"
    "v_mfma_f32_16x16x32_bf16 v[120:123], v[228:231], v[176:179], v[120:123]\n\t"
    "ds_read_b128 v[216:219], v250 offset:8192\n\t"
    "s_waitcnt lgkmcnt(3)\n\t"
    "v_mfma_f32_16x16x32_bf16 v[124:127], v[232:235], v[152:155], 0\n\t"
    "ds_read_b128 v[220:223], v250 offset:8704\n\t"
    "v_mfma_f32_16x16x32_bf16 v[124:127], v[236:239], v[160:163], v[124:127]\n\t"
    "ds_read_b128 v[224:227], v250 offset:9216\n\t"
    "s_waitcnt lgkmcnt(3)\n\t"
    "v_mfma_f32_16x16x32_bf16 v[124:127], v[240:243], v[168:171], v[124:127]\n\t"
    "ds_read_b128 v[228:231], v250 offset:9728\n\t"
    "v_mfma_f32_16x16x32_bf16 v[124:127], v[244:247], v[176:179], v[124:127]\n\t"
    "ds_read_b128 v[232:235], v250 offset:12288\n\t"
    "s_waitcnt lgkmcnt(3)\n\t"
    "v_mfma_f32_16x16x32_bf16 v[104:107], v[216:219], v[152:155], 0\n\t"
    "ds_read_b128 v[236:239], v250 offset:12800\n\t"
    "v_mfma_f32_16x16x32_bf16 v[104:107], v[220:223], v[160:163], v[104:107]\n\t"
    "ds_read_b128 v[240:243], v250 offset:13312\n\t"
    "v_max3_f32 v192, v120, v121, v122\n\t"
    "v_max3_f32 v193, v128, v129, v130\n\t"
    "v_max3_f32 v192, v192, v123, v124\n\t"
    "s_waitcnt lgkmcnt(3)\n\t"
    "v_mfma_f32_16x16x32_bf16 v[104:107], v[224:227], v[168:171], v[104:107]\n\t"
    "ds_read_b128 v[244:247], v250 offset:13824\n\t"
    "v_max3_f32 v193, v193, v131, v132\n\t"
    "v_max3_f32 v192, v192, v125, v126\n\t"
    "v_max3_f32 v193, v193, v133, v134\n\t"
    "v_mfma_f32_16x16x32_bf16 v[104:107], v[228:231], v[176:179], v[104:107]\n\t"
    "v_max_f32 v192, v192, v127\n\t"
    "v_max_f32 v193, v193, v135\n\t"
    "v_mov_b32 v194, v192\n\t"
    "v_mov_b32 v195, v193\n\t"
    "s_nop 0\n\t"
    "v_permlane32_swap_b32 v192, v194\n\t"
    "v_permlane32_swap_b32 v193, v195\n\t"
    "v_max_f32 v192, v192, v194\n\t"
    "v_max_f32 v193, v193, v195\n\t"
    "v_mov_b32 v194, v192\n\t"
    "v_mov_b32 v195, v193\n\t"
    "s_nop 0\n\t"
    "v_permlane16_swap_b32 v192, v194\n\t"
    "v_permlane16_swap_b32 v193, v195\n\t"
    "v_max_f32 v192, v192, v194\n\t"
    "v_max_f32 v193, v193, v195\n\t"
    "v_mov_b32 v251, 0\n\t"
    "v_mov_b32 v252, 0\n\t"
    "s_waitcnt lgkmcnt(0)\n\t"
    "s_barrier\n\t"
    "v_mov_b32 v248, %[lds]\n\t"
    "v_bfe_u32 v254, v253, 5, 1\n\t"
    "v_sub_f32 v184, 0xc1c00000, v192\n\t"
    "v_sub_f32 v188, s60, v193\n\t"
    "v_mfma_f32_16x16x32_bf16 v[108:111], v[232:235], v[152:155], 0\n\t"
    "v_lshl_add_u32 v248, v254, 11, v248\n\t"
    "v_bfe_u32 v254, v253, 2, 3\n\t"
    "v_mov_b32 v185, v184\n\t"
    "v_mov_b32 v186, v184\n\t"
    "v_mfma_f32_16x16x32_bf16 v[108:111], v[236:239], v[160:163], v[108:111]\n\t"
    "v_lshl_add_u32 v248, v254, 6, v248\n\t"
    "v_bfe_u32 v254, v253, 4, 1\n\t"
    "v_mov_b32 v187, v184\n\t"
    "v_mov_b32 v189, v188\n\t"
    "v_mfma_f32_16x16x32_bf16 v[108:111], v[240:243], v[168:171], v[108:111]\n\t"
    "v_lshl_add_u32 v248, v254, 5, v248\n\t"
    "v_bfe_u32 v254, v253, 0, 2\n\t"
    "v_mov_b32 v190, v188\n\t"
    "v_mov_b32 v191, v188\n\t"
    "v_mfma_f32_16x16x32_bf16 v[108:111], v[244:247], v[176:179], v[108:111]\n\t"
    "v_lshl_add_u32 v248, v254, 3, v248\n\t"
    "v_xor_b32 v249, 32, v248\n\t"
    "v_add_f32 v120, v184, v120\n\t"
    "v_add_f32 v121, v184, v121\n\t"
    "v_add_u32 v250, 0xffff0000, v250\n\t"
    "ds_read_b128 v[216:219], v250 offset:0\n\t"
    "v_add_f32 v122, v184, v122\n\t"
    "v_add_f32 v123, v184, v123\n\t"
    "ds_read_b128 v[220:223], v250 offset:512\n\t"
    "ds_read_b128 v[224:227], v250 offset:1024\n\t"
    "v_add_f32 v124, v184, v124\n\t"
    "v_add_f32 v125, v184, v125\n\t"
    "ds_read_b128 v[228:231], v250 offset:1536\n\t"
    "v_add_f32 v126, v184, v126\n\t"
    "v_add_f32 v127, v184, v127\n\t"
    "ds_read_b128 v[232:235], v250 offset:4096\n\t"
    "s_waitcnt lgkmcnt(3)\n\t"
    "v_mfma_f32_16x16x32_bf16 v[136:139], v[216:219], v[152:155], v[184:187]\n\t"
    "ds_read_b128 v[236:239], v250 offset:4608\n\t"
    "v_mfma_f32_16x16x32_bf16 v[136:139], v[220:223], v[160:163], v[136:139]\n\t"
    "ds_read_b128 v[240:243], v250 offset:5120\n\t"
    "v_exp_f32 v120, v120\n\t"
    "s_waitcnt lgkmcnt(3)\n\t"
    "v_mfma_f32_16x16x32_bf16 v[136:139], v[224:227], v[168:171], v[136:139]\n\t"
    "ds_read_b128 v[244:247], v250 offset:5632\n\t"
    "v_exp_f32 v121, v121\n\t"
    "v_exp_f32 v122, v122\n\t"
    "v_mfma_f32_16x16x32_bf16 v[136:139], v[228:231], v[176:179], v[136:139]\n\t"
    "v_exp_f32 v123, v123\n\t"
    "v_cvt_pk_bf16_f32 v0, v120, v121\n\t"
    "v_exp_f32 v124, v124\n\t"
    "v_exp_f32 v125, v125\n\t"
    "v_cvt_pk_bf16_f32 v1, v122, v123\n\t"
    "v_exp_f32 v126, v126\n\t"
    "v_exp_f32 v127, v127\n\t"
    "v_cvt_pk_bf16_f32 v2, v124, v125\n\t"
    "v_or3_b32 v251, v251, v0, v1\n\t"
    "ds_read_b128 v[216:219], v250 offset:8192\n\t"
    "v_cvt_pk_bf16_f32 v3, v126, v127\n\t"
    "s_waitcnt lgkmcnt(3)\n\t"
    "v_mfma_f32_16x16x32_bf16 v[140:143], v[232:235], v[152:155], v[184:187]\n\t"
    "ds_read_b128 v[220:223], v250 offset:8704\n\t"
    "v_mfma_f32_16x16x32_bf16 v[140:143], v[236:239], v[160:163], v[140:143]\n\t"
    "ds_read_b128 v[224:227], v250 offset:9216\n\t"
    "v_or3_b32 v251, v251, v2, v3\n\t"
    "s_waitcnt lgkmcnt(3)\n\t"
    "v_mfma_f32_16x16x32_bf16 v[140:143], v[240:243], v[168:171], v[140:143]\n\t"
    "ds_read_b128 v[228:231], v250 offset:9728\n\t"
    "v_mfma_f32_16x16x32_bf16 v[140:143], v[244:247], v[176:179], v[140:143]\n\t"
    "v_add_f32 v104, v184, v104\n\t"
    "v_add_f32 v105, v184, v105\n\t"
    "v_add_f32 v106, v184, v106\n\t"
    "v_add_f32 v107, v184, v107\n\t"
    "v_add_f32 v108, v184, v108\n\t"
    "v_add_f32 v109, v184, v109\n\t"
    "v_add_f32 v110, v184, v110\n\t"
    "v_add_f32 v111, v184, v111\n\t"
    "ds_read_b128 v[232:235], v250 offset:12288\n\t"
    "s_waitcnt lgkmcnt(3)\n\t"
    "v_mfma_f32_16x16x32_bf16 v[120:123], v[216:219], v[152:155], v[184:187]\n\t"
    "ds_read_b128 v[236:239], v250 offset:12800\n\t"
    "v_mfma_f32_16x16x32_bf16 v[120:123], v[220:223], v[160:163], v[120:123]\n\t"
    "ds_read_b128 v[240:243], v250 offset:13312\n\t"
    "v_exp_f32 v104, v104\n\t"
    "s_waitcnt lgkmcnt(3)\n\t"
    "v_mfma_f32_16x16x32_bf16 v[120:123], v[224:227], v[168:171], v[120:123]\n\t"
    "ds_read_b128 v[244:247], v250 offset:13824\n\t"
    "v_exp_f32 v105, v105\n\t"
    "v_exp_f32 v106, v106\n\t"
    "v_mfma_f32_16x16x32_bf16 v[120:123], v[228:231], v[176:179], v[120:123]\n\t"
    "v_exp_f32 v107, v107\n\t"
    "v_cvt_pk_bf16_f32 v8, v104, v105\n\t"
    "v_exp_f32 v108, v108\n\t"
    "v_exp_f32 v109, v109\n\t"
    "v_cvt_pk_bf16_f32 v9, v106, v107\n\t"
    "v_exp_f32 v110, v110\n\t"
    "v_exp_f32 v111, v111\n\t"
    "v_cvt_pk_bf16_f32 v10, v108, v109\n\t"
    "v_or3_b32 v251, v251, v8, v9\n\t"
    "s_waitcnt lgkmcnt(0)\n\t"
    "s_barrier\n\t"
    "ds_read_b128 v[216:219], v250 offset:16384\n\t"
    "v_cvt_pk_bf16_f32 v11, v110, v111\n\t"
    "v_mfma_f32_16x16x32_bf16 v[124:127], v[232:235], v[152:155], v[184:187]\n\t"
    "ds_read_b128 v[220:223], v250 offset:16896\n\t"
    "v_mfma_f32_16x16x32_bf16 v[124:127], v[236:239], v[160:163], v[124:127]\n\t"
    "ds_read_b128 v[224:227], v250 offset:17408\n\t"
    "v_or3_b32 v251, v251, v10, v11\n\t"
    "v_mfma_f32_16x16x32_bf16 v[124:127], v[240:243], v[168:171], v[124:127]\n\t"
    "ds_read_b128 v[228:231], v250 offset:17920\n\t"
    "v_mfma_f32_16x16x32_bf16 v[124:127], v[244:247], v[176:179], v[124:127]\n\t"
    "v_exp_f32 v136, v136\n\t"
    "v_exp_f32 v137, v137\n\t"
    "v_exp_f32 v138, v138\n\t"
    "v_exp_f32 v139, v139\n\t"
    "ds_read_b128 v[232:235], v250 offset:20480\n\t"
    "v_cvt_pk_bf16_f32 v16, v136, v137\n\t"
    "v_exp_f32 v140, v140\n\t"
    "s_waitcnt lgkmcnt(3)\n\t"
    "v_mfma_f32_16x16x32_bf16 v[104:107], v[216:219], v[152:155], v[184:187]\n\t"
    "ds_read_b128 v[236:239], v250 offset:20992\n\t"
    "v_exp_f32 v141, v141\n\t"
    "v_cvt_pk_bf16_f32 v17, v138, v139\n\t"
    "v_mfma_f32_16x16x32_bf16 v[104:107], v[220:223], v[160:163], v[104:107]\n\t"
    "ds_read_b128 v[240:243], v250 offset:21504\n\t"
    "v_exp_f32 v142, v142\n\t"
    "v_exp_f32 v143, v143\n\t"
    "s_waitcnt lgkmcnt(3)\n\t"
    "v_mfma_f32_16x16x32_bf16 v[104:107], v[224:227], v[168:171], v[104:107]\n\t"
    "ds_read_b128 v[244:247], v250 offset:22016\n\t"
    "v_cvt_pk_bf16_f32 v18, v140, v141\n\t"
    "v_or3_b32 v251, v251, v16, v17\n\t"
    "v_mfma_f32_16x16x32_bf16 v[104:107], v[228:231], v[176:179], v[104:107]\n\t"
    "v_cvt_pk_bf16_f32 v19, v142, v143\n\t"
    "v_or3_b32 v251, v251, v18, v19\n\t"
    "ds_read_b128 v[216:219], v250 offset:24576\n\t"
    "s_waitcnt lgkmcnt(3)\n\t"
    "v_mfma_f32_16x16x32_bf16 v[108:111], v[232:235], v[152:155], v[184:187]\n\t"
    "ds_read_b128 v[220:223], v250 offset:25088\n\t"
    "v_exp_f32 v120, v120\n\t"
    "v_mfma_f32_16x16x32_bf16 v[108:111], v[236:239], v[160:163], v[108:111]\n\t"
    "ds_read_b128 v[224:227], v250 offset:25600\n\t"
    "v_exp_f32 v121, v121\n\t"
    "v_exp_f32 v122, v122\n\t"
    "s_waitcnt lgkmcnt(3)\n\t"
    "v_mfma_f32_16x16x32_bf16 v[108:111], v[240:243], v[168:171], v[108:111]\n\t"
    "ds_read_b128 v[228:231], v250 offset:26112\n\t"
    "v_exp_f32 v123, v123\n\t"
    "v_cvt_pk_bf16_f32 v24, v120, v121\n\t"
    "v_mfma_f32_16x16x32_bf16 v[108:111], v[244:247], v[176:179], v[108:111]\n\t"
    "v_exp_f32 v124, v124\n\t"
    "v_exp_f32 v125, v125\n\t"
    "v_cvt_pk_bf16_f32 v25, v122, v123\n\t"
    "v_exp_f32 v126, v126\n\t"
    "v_exp_f32 v127, v127\n\t"
    "v_cvt_pk_bf16_f32 v26, v124, v125\n\t"
    "v_or3_b32 v251, v251, v24, v25\n\t"
    "v_cvt_pk_bf16_f32 v27, v126, v127\n\t"
    "ds_read_b128 v[232:235], v250 offset:28672\n\t"
    "s_waitcnt lgkmcnt(3)\n\t"
    "v_mfma_f32_16x16x32_bf16 v[136:139], v[216:219], v[152:155], v[184:187]\n\t"
    "ds_read_b128 v[236:239], v250 offset:29184\n\t"
    "v_or3_b32 v251, v251, v26, v27\n\t"
    "v_mfma_f32_16x16x32_bf16 v[136:139], v[220:223], v[160:163], v[136:139]\n\t"
    "ds_read_b128 v[240:243], v250 offset:29696\n\t"
    "s_waitcnt lgkmcnt(3)\n\t"
    "v_mfma_f32_16x16x32_bf16 v[136:139], v[224:227], v[168:171], v[136:139]\n\t"
    "ds_read_b128 v[244:247], v250 offset:30208\n\t"
    "v_mfma_f32_16x16x32_bf16 v[136:139], v[228:231], v[176:179], v[136:139]\n\t"
    "v_exp_f32 v104, v104\n\t"
    "v_exp_f32 v105, v105\n\t"
    "v_exp_f32 v106, v106\n\t"
    "v_exp_f32 v107, v107\n\t"
    "v_cvt_pk_bf16_f32 v32, v104, v105\n\t"
    "v_exp_f32 v108, v108\n\t"
    "v_exp_f32 v109, v109\n\t"
    "s_waitcnt vmcnt(12)\n\t"
    "s_waitcnt lgkmcnt(0)\n\t"
    "s_barrier\n\t"
    "v_add_u32 v250, 0x10000, v250\n\t"
    "v_cvt_pk_bf16_f32 v33, v106, v107\n\t"
    "v_exp_f32 v110, v110\n\t"
    "v_mfma_f32_16x16x32_bf16 v[140:143], v[232:235], v[152:155], v[184:187]\n\t"
    "ds_read_b128 v[216:219], v250 offset:16384\n\t"
    "v_exp_f32 v111, v111\n\t"
    "v_cvt_pk_bf16_f32 v34, v108, v109\n\t"
    "v_mfma_f32_16x16x32_bf16 v[140:143], v[236:239], v[160:163], v[140:143]\n\t"
    "ds_read_b128 v[220:223], v250 offset:16896\n\t"
    "v_or3_b32 v251, v251, v32, v33\n\t"
    "v_mfma_f32_16x16x32_bf16 v[140:143], v[240:243], v[168:171], v[140:143]\n\t"
    "ds_read_b128 v[224:227], v250 offset:17408\n\t"
    "v_cvt_pk_bf16_f32 v35, v110, v111\n\t"
    "v_mfma_f32_16x16x32_bf16 v[140:143], v[244:247], v[176:179], v[140:143]\n\t"
    "ds_read_b128 v[228:231], v250 offset:17920\n\t"
    "v_or3_b32 v251, v251, v34, v35\n\t"
    "ds_read_b128 v[232:235], v250 offset:20480\n\t"
    "s_waitcnt lgkmcnt(3)\n\t"
    "v_mfma_f32_16x16x32_bf16 v[120:123], v[216:219], v[152:155], v[184:187]\n\t"
    "ds_read_b128 v[236:239], v250 offset:20992\n\t"
    "v_mfma_f32_16x16x32_bf16 v[120:123], v[220:223], v[160:163], v[120:123]\n\t"
    "ds_read_b128 v[240:243], v250 offset:21504\n\t"
    "v_exp_f32 v136, v136\n\t"
    "v_exp_f32 v137, v137\n\t"
    "s_waitcnt lgkmcnt(3)\n\t"
    "v_mfma_f32_16x16x32_bf16 v[120:123], v[224:227], v[168:171], v[120:123]\n\t"
    "ds_read_b128 v[244:247], v250 offset:22016\n\t"
    "v_exp_f32 v138, v138\n\t"
    "v_exp_f32 v139, v139\n\t"
    "v_mfma_f32_16x16x32_bf16 v[120:123], v[228:231], v[176:179], v[120:123]\n\t"
    "v_cvt_pk_bf16_f32 v40, v136, v137\n\t"
    "v_exp_f32 v140, v140\n\t"
    "v_exp_f32 v141, v141\n\t"
    "v_cvt_pk_bf16_f32 v41, v138, v139\n\t"
    "v_exp_f32 v142, v142\n\t"
    "v_exp_f32 v143, v143\n\t"
    "v_cvt_pk_bf16_f32 v42, v140, v141\n\t"
    "v_or3_b32 v251, v251, v40, v41\n\t"
    "v_cvt_pk_bf16_f32 v43, v142, v143\n\t"
    "ds_read_b128 v[216:219], v250 offset:24576\n\t"
    "s_waitcnt lgkmcnt(3)\n\t"
    "v_mfma_f32_16x16x32_bf16 v[124:127], v[232:235], v[152:155], v[184:187]\n\t"
    "ds_read_b128 v[220:223], v250 offset:25088\n\t"
    "v_or3_b32 v251, v251, v42, v43\n\t"
    "v_mfma_f32_16x16x32_bf16 v[124:127], v[236:239], v[160:163], v[124:127]\n\t"
    "ds_read_b128 v[224:227], v250 offset:25600\n\t"
    "s_waitcnt lgkmcnt(3)\n\t"
    "v_mfma_f32_16x16x32_bf16 v[124:127], v[240:243], v[168:171], v[124:127]\n\t"
    "ds_read_b128 v[228:231], v250 offset:26112\n\t"
    "v_mfma_f32_16x16x32_bf16 v[124:127], v[244:247], v[176:179], v[124:127]\n\t"
    "ds_read_b128 v[232:235], v250 offset:28672\n\t"
    "s_waitcnt lgkmcnt(3)\n\t"
    "v_mfma_f32_16x16x32_bf16 v[104:107], v[216:219], v[152:155], v[184:187]\n\t"
    "ds_read_b128 v[236:239], v250 offset:29184\n\t"
    "v_mfma_f32_16x16x32_bf16 v[104:107], v[220:223], v[160:163], v[104:107]\n\t"
    "ds_read_b128 v[240:243], v250 offset:29696\n\t"
    "v_exp_f32 v120, v120\n\t"
    "v_exp_f32 v121, v121\n\t"
    "s_waitcnt lgkmcnt(3)\n\t"
    "v_mfma_f32_16x16x32_bf16 v[104:107], v[224:227], v[168:171], v[104:107]\n\t"
    "ds_read_b128 v[244:247], v250 offset:30208\n\t"
    "v_exp_f32 v122, v122\n\t"
    "v_exp_f32 v123, v123\n\t"
    "v_mfma_f32_16x16x32_bf16 v[104:107], v[228:231], v[176:179], v[104:107]\n\t"
    "v_cvt_pk_bf16_f32 v48, v120, v121\n\t"
    "v_exp_f32 v124, v124\n\t"
    "v_exp_f32 v125, v125\n\t"
    "v_cvt_pk_bf16_f32 v49, v122, v123\n\t"
    "v_exp_f32 v126, v126\n\t"
    "v_exp_f32 v127, v127\n\t"
    "v_cvt_pk_bf16_f32 v50, v124, v125\n\t"
    "v_or3_b32 v251, v251, v48, v49\n\t"
    "v_cvt_pk_bf16_f32 v51, v126, v127\n\t"
    "s_waitcnt vmcnt(8)\n\t"
    "s_waitcnt lgkmcnt(0)\n\t"
    "s_barrier\n\t"
    "ds_read_b128 v[216:219], v250 offset:32768\n\t"
    "v_mfma_f32_16x16x32_bf16 v[108:111], v[232:235], v[152:155], v[184:187]\n\t"
    "ds_read_b128 v[220:223], v250 offset:33280\n\t"
    "v_or3_b32 v251, v251, v50, v51\n\t"
    "v_mfma_f32_16x16x32_bf16 v[108:111], v[236:239], v[160:163], v[108:111]\n\t"
    "ds_read_b128 v[224:227], v250 offset:33792\n\t"
    "v_mfma_f32_16x16x32_bf16 v[108:111], v[240:243], v[168:171], v[108:111]\n\t"
    "ds_read_b128 v[228:231], v250 offset:34304\n\t"
    "v_mfma_f32_16x16x32_bf16 v[108:111], v[244:247], v[176:179], v[108:111]\n\t"
    "ds_read_b128 v[232:235], v250 offset:36864\n\t"
    "s_waitcnt lgkmcnt(3)\n\t"
    "v_mfma_f32_16x16x32_bf16 v[136:139], v[216:219], v[152:155], v[184:187]\n\t"
    "ds_read_b128 v[236:239], v250 offset:37376\n\t"
    "v_mfma_f32_16x16x32_bf16 v[136:139], v[220:223], v[160:163], v[136:139]\n\t"
    "ds_read_b128 v[240:243], v250 offset:37888\n\t"
    "v_exp_f32 v104, v104\n\t"
    "v_exp_f32 v105, v105\n\t"
    "s_waitcnt lgkmcnt(3)\n\t"
    "v_mfma_f32_16x16x32_bf16 v[136:139], v[224:227], v[168:171], v[136:139]\n\t"
    "ds_read_b128 v[244:247], v250 offset:38400\n\t"
    "v_exp_f32 v106, v106\n\t"
    "v_exp_f32 v107, v107\n\t"
    "v_mfma_f32_16x16x32_bf16 v[136:139], v[228:231], v[176:179], v[136:139]\n\t"
    "v_cvt_pk_bf16_f32 v56, v104, v105\n\t"
    "v_exp_f32 v108, v108\n\t"
    "v_exp_f32 v109, v109\n\t"
    "v_cvt_pk_bf16_f32 v57, v106, v107\n\t"
    "v_exp_f32 v110, v110\n\t"
    "v_exp_f32 v111, v111\n\t"
    "v_cvt_pk_bf16_f32 v58, v108, v109\n\t"
    "v_or3_b32 v251, v251, v56, v57\n\t"
    "v_cvt_pk_bf16_f32 v59, v110, v111\n\t"
    "ds_read_b128 v[216:219], v250 offset:40960\n\t"
    "s_waitcnt lgkmcnt(3)\n\t"
    "v_mfma_f32_16x16x32_bf16 v[140:143], v[232:235], v[152:155], v[184:187]\n\t"
    "ds_read_b128 v[220:223], v250 offset:41472\n\t"
    "v_or3_b32 v251, v251, v58, v59\n\t"
    "v_mfma_f32_16x16x32_bf16 v[140:143], v[236:239], v[160:163], v[140:143]\n\t"
    "ds_read_b128 v[224:227], v250 offset:41984\n\t"
    "s_waitcnt lgkmcnt(3)\n\t"
    "v_mfma_f32_16x16x32_bf16 v[140:143], v[240:243], v[168:171], v[140:143]\n\t"
    "ds_read_b128 v[228:231], v250 offset:42496\n\t"
    "v_mfma_f32_16x16x32_bf16 v[140:143], v[244:247], v[176:179], v[140:143]\n\t"
    "ds_read_b128 v[232:235], v250 offset:45056\n\t"
    "s_waitcnt lgkmcnt(3)\n\t"
    "v_mfma_f32_16x16x32_bf16 v[120:123], v[216:219], v[152:155], v[184:187]\n\t"
    "ds_read_b128 v[236:239], v250 offset:45568\n\t"
    "v_mfma_f32_16x16x32_bf16 v[120:123], v[220:223], v[160:163], v[120:123]\n\t"
    "ds_read_b128 v[240:243], v250 offset:46080\n\t"
    "v_exp_f32 v136, v136\n\t"
    "v_exp_f32 v137, v137\n\t"
    "s_waitcnt lgkmcnt(3)\n\t"
    "v_mfma_f32_16x16x32_bf16 v[120:123], v[224:227], v[168:171], v[120:123]\n\t"
    "ds_read_b128 v[244:247], v250 offset:46592\n\t"
    "v_exp_f32 v138, v138\n\t"
    "v_exp_f32 v139, v139\n\t"
    "v_mfma_f32_16x16x32_bf16 v[120:123], v[228:231], v[176:179], v[120:123]\n\t"
    "v_cvt_pk_bf16_f32 v64, v136, v137\n\t"
    "v_exp_f32 v140, v140\n\t"
    "v_exp_f32 v141, v141\n\t"
    "v_cvt_pk_bf16_f32 v65, v138, v139\n\t"
    "v_exp_f32 v142, v142\n\t"
    "v_exp_f32 v143, v143\n\t"
    "v_cvt_pk_bf16_f32 v66, v140, v141\n\t"
    "v_or3_b32 v251, v251, v64, v65\n\t"
    "v_cvt_pk_bf16_f32 v67, v142, v143\n\t"
    "s_waitcnt vmcnt(4)\n\t"
    "s_waitcnt lgkmcnt(0)\n\t"
    "s_barrier\n\t"
    "v_add_u32 v250, 0x10000, v250\n\t"
    "v_mfma_f32_16x16x32_bf16 v[124:127], v[232:235], v[152:155], v[184:187]\n\t"
    "ds_read_b128 v[216:219], v250 offset:16384\n\t"
    "v_or3_b32 v251, v251, v66, v67\n\t"
    "v_mfma_f32_16x16x32_bf16 v[124:127], v[236:239], v[160:163], v[124:127]\n\t"
    "ds_read_b128 v[220:223], v250 offset:16896\n\t"
    "v_mfma_f32_16x16x32_bf16 v[124:127], v[240:243], v[168:171], v[124:127]\n\t"
    "ds_read_b128 v[224:227], v250 offset:17408\n\t"
    "v_mfma_f32_16x16x32_bf16 v[124:127], v[244:247], v[176:179], v[124:127]\n\t"
    "ds_read_b128 v[228:231], v250 offset:17920\n\t"
    "ds_read_b128 v[232:235], v250 offset:20480\n\t"
    "s_waitcnt lgkmcnt(3)\n\t"
    "v_mfma_f32_16x16x32_bf16 v[104:107], v[216:219], v[152:155], v[184:187]\n\t"
    "ds_read_b128 v[236:239], v250 offset:20992\n\t"
    "v_mfma_f32_16x16x32_bf16 v[104:107], v[220:223], v[160:163], v[104:107]\n\t"
    "ds_read_b128 v[240:243], v250 offset:21504\n\t"
    "v_exp_f32 v120, v120\n\t"
    "v_exp_f32 v121, v121\n\t"
    "s_waitcnt lgkmcnt(3)\n\t"
    "v_mfma_f32_16x16x32_bf16 v[104:107], v[224:227], v[168:171], v[104:107]\n\t"
    "ds_read_b128 v[244:247], v250 offset:22016\n\t"
    "v_exp_f32 v122, v122\n\t"
    "v_exp_f32 v123, v123\n\t"
    "v_mfma_f32_16x16x32_bf16 v[104:107], v[228:231], v[176:179], v[104:107]\n\t"
    "v_cvt_pk_bf16_f32 v72, v120, v121\n\t"
    "v_exp_f32 v124, v124\n\t"
    "v_exp_f32 v125, v125\n\t"
    "v_cvt_pk_bf16_f32 v73, v122, v123\n\t"
    "v_exp_f32 v126, v126\n\t"
    "v_exp_f32 v127, v127\n\t"
    "v_cvt_pk_bf16_f32 v74, v124, v125\n\t"
    "v_or3_b32 v251, v251, v72, v73\n\t"
    "v_cvt_pk_bf16_f32 v75, v126, v127\n\t"
    "ds_read_b128 v[216:219], v250 offset:24576\n\t"
    "s_waitcnt lgkmcnt(3)\n\t"
    "v_mfma_f32_16x16x32_bf16 v[108:111], v[232:235], v[152:155], v[184:187]\n\t"
    "ds_read_b128 v[220:223], v250 offset:25088\n\t"
    "v_or3_b32 v251, v251, v74, v75\n\t"
    "v_mfma_f32_16x16x32_bf16 v[108:111], v[236:239], v[160:163], v[108:111]\n\t"
    "ds_read_b128 v[224:227], v250 offset:25600\n\t"
    "s_waitcnt lgkmcnt(3)\n\t"
    "v_mfma_f32_16x16x32_bf16 v[108:111], v[240:243], v[168:171], v[108:111]\n\t"
    "ds_read_b128 v[228:231], v250 offset:26112\n\t"
    "v_mfma_f32_16x16x32_bf16 v[108:111], v[244:247], v[176:179], v[108:111]\n\t"
    "ds_read_b128 v[232:235], v250 offset:28672\n\t"
    "s_waitcnt lgkmcnt(3)\n\t"
    "v_mfma_f32_16x16x32_bf16 v[136:139], v[216:219], v[152:155], v[184:187]\n\t"
    "ds_read_b128 v[236:239], v250 offset:29184\n\t"
    "v_mfma_f32_16x16x32_bf16 v[136:139], v[220:223], v[160:163], v[136:139]\n\t"
    "ds_read_b128 v[240:243], v250 offset:29696\n\t"
    "v_exp_f32 v104, v104\n\t"
    "v_exp_f32 v105, v105\n\t"
    "s_waitcnt lgkmcnt(3)\n\t"
    "v_mfma_f32_16x16x32_bf16 v[136:139], v[224:227], v[168:171], v[136:139]\n\t"
    "ds_read_b128 v[244:247], v250 offset:30208\n\t"
    "v_exp_f32 v106, v106\n\t"
    "v_exp_f32 v107, v107\n\t"
    "v_mfma_f32_16x16x32_bf16 v[136:139], v[228:231], v[176:179], v[136:139]\n\t"
    "v_cvt_pk_bf16_f32 v80, v104, v105\n\t"
    "v_exp_f32 v108, v108\n\t"
    "v_exp_f32 v109, v109\n\t"
    "v_cvt_pk_bf16_f32 v81, v106, v107\n\t"
    "v_exp_f32 v110, v110\n\t"
    "v_exp_f32 v111, v111\n\t"
    "v_cvt_pk_bf16_f32 v82, v108, v109\n\t"
    "v_or3_b32 v251, v251, v80, v81\n\t"
    "v_cvt_pk_bf16_f32 v83, v110, v111\n\t"
    "s_waitcnt vmcnt(0)\n\t"
    "s_waitcnt lgkmcnt(0)\n\t"
    "s_barrier\n\t"
    "v_add_u32 v250, 0xffff0000, v250\n\t"
    "v_mfma_f32_16x16x32_bf16 v[140:143], v[232:235], v[152:155], v[184:187]\n\t"
    "ds_read_b128 v[216:219], v250 offset:49152\n\t"
    "v_or3_b32 v251, v251, v82, v83\n\t"
    "v_mfma_f32_16x16x32_bf16 v[140:143], v[236:239], v[160:163], v[140:143]\n\t"
    "ds_read_b128 v[220:223], v250 offset:49664\n\t"
    "v_mfma_f32_16x16x32_bf16 v[140:143], v[240:243], v[168:171], v[140:143]\n\t"
    "ds_read_b128 v[224:227], v250 offset:50176\n\t"
    "v_mfma_f32_16x16x32_bf16 v[140:143], v[244:247], v[176:179], v[140:143]\n\t"
    "ds_read_b128 v[228:231], v250 offset:50688\n\t"
    "ds_read_b128 v[232:235], v250 offset:53248\n\t"
    "s_waitcnt lgkmcnt(3)\n\t"
    "v_mfma_f32_16x16x32_bf16 v[120:123], v[216:219], v[152:155], v[184:187]\n\t"
    "ds_read_b128 v[236:239], v250 offset:53760\n\t"
    "v_mfma_f32_16x16x32_bf16 v[120:123], v[220:223], v[160:163], v[120:123]\n\t"
    "ds_read_b128 v[240:243], v250 offset:54272\n\t"
    "v_exp_f32 v136, v136\n\t"
    "v_exp_f32 v137, v137\n\t"
    "s_waitcnt lgkmcnt(3)\n\t"
    "v_mfma_f32_16x16x32_bf16 v[120:123], v[224:227], v[168:171], v[120:123]\n\t"
    "ds_read_b128 v[244:247], v250 offset:54784\n\t"
    "v_exp_f32 v138, v138\n\t"
    "v_exp_f32 v139, v139\n\t"
    "v_mfma_f32_16x16x32_bf16 v[120:123], v[228:231], v[176:179], v[120:123]\n\t"
    "v_cvt_pk_bf16_f32 v88, v136, v137\n\t"
    "v_exp_f32 v140, v140\n\t"
    "v_exp_f32 v141, v141\n\t"
    "v_cvt_pk_bf16_f32 v89, v138, v139\n\t"
    "v_exp_f32 v142, v142\n\t"
    "v_exp_f32 v143, v143\n\t"
    "v_cvt_pk_bf16_f32 v90, v140, v141\n\t"
    "v_or3_b32 v251, v251, v88, v89\n\t"
    "v_cvt_pk_bf16_f32 v91, v142, v143\n\t"
    "ds_read_b128 v[216:219], v250 offset:57344\n\t"
    "s_waitcnt lgkmcnt(3)\n\t"
    "v_mfma_f32_16x16x32_bf16 v[124:127], v[232:235], v[152:155], v[184:187]\n\t"
    "ds_read_b128 v[220:223], v250 offset:57856\n\t"
    "v_or3_b32 v251, v251, v90, v91\n\t"
    "v_mfma_f32_16x16x32_bf16 v[124:127], v[236:239], v[160:163], v[124:127]\n\t"
    "ds_read_b128 v[224:227], v250 offset:58368\n\t"
    "s_waitcnt lgkmcnt(3)\n\t"
    "v_mfma_f32_16x16x32_bf16 v[124:127], v[240:243], v[168:171], v[124:127]\n\t"
    "ds_read_b128 v[228:231], v250 offset:58880\n\t"
    "v_mfma_f32_16x16x32_bf16 v[124:127], v[244:247], v[176:179], v[124:127]\n\t"
    "ds_read_b128 v[232:235], v250 offset:61440\n\t"
    "s_waitcnt lgkmcnt(3)\n\t"
    "v_mfma_f32_16x16x32_bf16 v[104:107], v[216:219], v[152:155], v[184:187]\n\t"
    "ds_read_b128 v[236:239], v250 offset:61952\n\t"
    "v_mfma_f32_16x16x32_bf16 v[104:107], v[220:223], v[160:163], v[104:107]\n\t"
    "ds_read_b128 v[240:243], v250 offset:62464\n\t"
    "v_exp_f32 v120, v120\n\t"
    "v_exp_f32 v121, v121\n\t"
    "s_waitcnt lgkmcnt(3)\n\t"
    "v_mfma_f32_16x16x32_bf16 v[104:107], v[224:227], v[168:171], v[104:107]\n\t"
    "ds_read_b128 v[244:247], v250 offset:62976\n\t"
    "v_exp_f32 v122, v122\n\t"
    "v_exp_f32 v123, v123\n\t"
    "v_mfma_f32_16x16x32_bf16 v[104:107], v[228:231], v[176:179], v[104:107]\n\t"
    "v_cvt_pk_bf16_f32 v96, v120, v121\n\t"
    "v_exp_f32 v124, v124\n\t"
    "v_exp_f32 v125, v125\n\t"
    "v_cvt_pk_bf16_f32 v97, v122, v123\n\t"
    "v_exp_f32 v126, v126\n\t"
    "v_exp_f32 v127, v127\n\t"
    "v_cvt_pk_bf16_f32 v98, v124, v125\n\t"
    "v_or3_b32 v251, v251, v96, v97\n\t"
    "v_cvt_pk_bf16_f32 v99, v126, v127\n\t"
    "s_waitcnt lgkmcnt(0)\n\t"
    "s_barrier\n\t"
    "v_add_u32 v250, 0xffff0000, v250\n\t"
    "v_mfma_f32_16x16x32_bf16 v[108:111], v[232:235], v[152:155], v[184:187]\n\t"
    "ds_read_b128 v[216:219], v250 offset:32768\n\t"
    "v_or3_b32 v251, v251, v98, v99\n\t"
    "v_mfma_f32_16x16x32_bf16 v[108:111], v[236:239], v[160:163], v[108:111]\n\t"
    "ds_read_b128 v[220:223], v250 offset:33280\n\t"
    "v_mfma_f32_16x16x32_bf16 v[108:111], v[240:243], v[168:171], v[108:111]\n\t"
    "ds_read_b128 v[224:227], v250 offset:33792\n\t"
    "v_mfma_f32_16x16x32_bf16 v[108:111], v[244:247], v[176:179], v[108:111]\n\t"
    "ds_read_b128 v[228:231], v250 offset:34304\n\t"
    "ds_read_b128 v[232:235], v250 offset:36864\n\t"
    "s_waitcnt lgkmcnt(3)\n\t"
    "v_mfma_f32_16x16x32_bf16 v[136:139], v[216:219], v[152:155], v[184:187]\n\t"
    "ds_read_b128 v[236:239], v250 offset:37376\n\t"
    "v_mfma_f32_16x16x32_bf16 v[136:139], v[220:223], v[160:163], v[136:139]\n\t"
    "ds_read_b128 v[240:243], v250 offset:37888\n\t"
    "v_exp_f32 v104, v104\n\t"
    "v_exp_f32 v105, v105\n\t"
    "s_waitcnt lgkmcnt(3)\n\t"
    "v_mfma_f32_16x16x32_bf16 v[136:139], v[224:227], v[168:171], v[136:139]\n\t"
    "ds_read_b128 v[244:247], v250 offset:38400\n\t"
    "v_exp_f32 v106, v106\n\t"
    "v_exp_f32 v107, v107\n\t"
    "v_mfma_f32_16x16x32_bf16 v[136:139], v[228:231], v[176:179], v[136:139]\n\t"
    "v_cvt_pk_bf16_f32 v104, v104, v105\n\t"
    "v_exp_f32 v108, v108\n\t"
    "v_exp_f32 v109, v109\n\t"
    "v_cvt_pk_bf16_f32 v105, v106, v107\n\t"
    "v_exp_f32 v110, v110\n\t"
    "v_exp_f32 v111, v111\n\t"
    "v_cvt_pk_bf16_f32 v106, v108, v109\n\t"
    "v_or3_b32 v251, v251, v104, v105\n\t"
    "v_cvt_pk_bf16_f32 v107, v110, v111\n\t"
    "ds_read_b128 v[216:219], v250 offset:40960\n\t"
    "s_waitcnt lgkmcnt(3)\n\t"
    "v_mfma_f32_16x16x32_bf16 v[140:143], v[232:235], v[152:155], v[184:187]\n\t"
    "ds_read_b128 v[220:223], v250 offset:41472\n\t"
    "v_or3_b32 v251, v251, v106, v107\n\t"
    "v_mfma_f32_16x16x32_bf16 v[140:143], v[236:239], v[160:163], v[140:143]\n\t"
    "ds_read_b128 v[224:227], v250 offset:41984\n\t"
    "s_waitcnt lgkmcnt(3)\n\t"
    "v_mfma_f32_16x16x32_bf16 v[140:143], v[240:243], v[168:171], v[140:143]\n\t"
    "ds_read_b128 v[228:231], v250 offset:42496\n\t"
    "v_mfma_f32_16x16x32_bf16 v[140:143], v[244:247], v[176:179], v[140:143]\n\t"
    "ds_read_b128 v[232:235], v250 offset:45056\n\t"
    "s_waitcnt lgkmcnt(3)\n\t"
    "v_mfma_f32_16x16x32_bf16 v[120:123], v[216:219], v[152:155], v[184:187]\n\t"
    "ds_read_b128 v[236:239], v250 offset:45568\n\t"
    "v_mfma_f32_16x16x32_bf16 v[120:123], v[220:223], v[160:163], v[120:123]\n\t"
    "ds_read_b128 v[240:243], v250 offset:46080\n\t"
    "v_exp_f32 v136, v136\n\t"
    "v_exp_f32 v137, v137\n\t"
    "s_waitcnt lgkmcnt(3)\n\t"
    "v_mfma_f32_16x16x32_bf16 v[120:123], v[224:227], v[168:171], v[120:123]\n\t"
    "ds_read_b128 v[244:247], v250 offset:46592\n\t"
    "v_exp_f32 v138, v138\n\t"
    "v_exp_f32 v139, v139\n\t"
    "v_mfma_f32_16x16x32_bf16 v[120:123], v[228:231], v[176:179], v[120:123]\n\t"
    "v_cvt_pk_bf16_f32 v112, v136, v137\n\t"
    "v_exp_f32 v140, v140\n\t"
    "v_exp_f32 v141, v141\n\t"
    "v_cvt_pk_bf16_f32 v113, v138, v139\n\t"
    "v_exp_f32 v142, v142\n\t"
    "v_exp_f32 v143, v143\n\t"
    "v_cvt_pk_bf16_f32 v114, v140, v141\n\t"
    "v_or3_b32 v251, v251, v112, v113\n\t"
    "v_cvt_pk_bf16_f32 v115, v142, v143\n\t"
    "s_waitcnt lgkmcnt(0)\n\t"
    "s_barrier\n\t"
    "v_add_u32 v248, 0x20000, v248\n\t"
    "v_add_u32 v249, 0x20000, v249\n\t"
    "v_mfma_f32_16x16x32_bf16 v[124:127], v[232:235], v[152:155], v[184:187]\n\t"
    "ds_read_b64_tr_b16 v[216:217], v248 offset:0\n\t"
    "ds_read_b64_tr_b16 v[218:219], v248 offset:4096\n\t"
    "v_or3_b32 v251, v251, v114, v115\n\t"
    "v_mfma_f32_16x16x32_bf16 v[124:127], v[236:239], v[160:163], v[124:127]\n\t"
    "ds_read_b64_tr_b16 v[220:221], v248 offset:8192\n\t"
    "v_mfma_f32_16x16x32_bf16 v[124:127], v[240:243], v[168:171], v[124:127]\n\t"
    "ds_read_b64_tr_b16 v[222:223], v248 offset:12288\n\t"
    "v_mfma_f32_16x16x32_bf16 v[124:127], v[244:247], v[176:179], v[124:127]\n\t"
    "ds_read_b64_tr_b16 v[224:225], v249 offset:0\n\t"
    "ds_read_b64_tr_b16 v[226:227], v249 offset:4096\n\t"
    "ds_read_b64_tr_b16 v[228:229], v249 offset:8192\n\t"
    "ds_read_b64_tr_b16 v[230:231], v249 offset:12288\n\t"
    "ds_read_b64_tr_b16 v[232:233], v248 offset:512\n\t"
    "v_exp_f32 v120, v120\n\t"
    "v_exp_f32 v121, v121\n\t"
    "v_exp_f32 v122, v122\n\t"
    "v_exp_f32 v123, v123\n\t"
    "v_cvt_pk_bf16_f32 v120, v120, v121\n\t"
    "v_exp_f32 v124, v124\n\t"
    "v_exp_f32 v125, v125\n\t"
    "v_cvt_pk_bf16_f32 v121, v122, v123\n\t"
    "v_exp_f32 v126, v126\n\t"
    "v_exp_f32 v127, v127\n\t"
    "v_cvt_pk_bf16_f32 v122, v124, v125\n\t"
    "v_or3_b32 v251, v251, v120, v121\n\t"
    "v_cvt_pk_bf16_f32 v123, v126, v127\n\t"
    "v_or3_b32 v251, v251, v122, v123\n\t"
    "v_and_b32 v251, 0x40004000, v251\n\t"
    "v_cmp_ne_u32 vcc, 0, v251\n\t"
    "s_nop 3\n\t"
    "s_cmp_lg_u64 vcc, 0\n\t"
    "s_cbranch_scc1 FB%=\n\t"
    "FBRET3_%=:\n\t"
    "v_and_b32 v138, 3, v253\n\t"
    "v_cmp_eq_u32 vcc, 0, v138\n\t"
    "v_mov_b32 v139, 0x3f803f80\n\t"
    "v_mov_b32 v140, 0\n\t"
    "v_cndmask_b32 v138, v140, v139, vcc\n\t"
    "v_mov_b32 v139, v138\n\t"
    "v_mov_b32 v140, v138\n\t"
    "v_mov_b32 v141, v138\n\t"
    "s_waitcnt lgkmcnt(5)\n\t"
    "v_mfma_f32_16x16x32_bf16 v[152:155], v[216:219], v[0:3], 0\n\t"
    "ds_read_b64_tr_b16 v[234:235], v248 offset:4608\n\t"
    "ds_read_b64_tr_b16 v[236:237], v248 offset:8704\n\t"
    "ds_read_b64_tr_b16 v[238:239], v248 offset:12800\n\t"
    "v_mfma_f32_16x16x32_bf16 v[152:155], v[220:223], v[8:11], v[152:155]\n\t"
    "ds_read_b64_tr_b16 v[240:241], v249 offset:512\n\t"
    "ds_read_b64_tr_b16 v[242:243], v249 offset:4608\n\t"
    "ds_read_b64_tr_b16 v[244:245], v249 offset:8704\n\t"
    "ds_read_b64_tr_b16 v[246:247], v249 offset:12800\n\t"
    "v_mfma_f32_16x16x32_bf16 v[144:147], v[138:141], v[0:3], 0\n\t"
    "ds_read_b64_tr_b16 v[216:217], v248 offset:1024\n\t"
    "s_waitcnt lgkmcnt(9)\n\t"
    "v_mfma_f32_16x16x32_bf16 v[156:159], v[224:227], v[0:3], 0\n\t"
    "ds_read_b64_tr_b16 v[218:219], v248 offset:5120\n\t"
    "v_mfma_f32_16x16x32_bf16 v[156:159], v[228:231], v[8:11], v[156:159]\n\t"
    "ds_read_b64_tr_b16 v[220:221], v248 offset:9216\n\t"
    "ds_read_b64_tr_b16 v[222:223], v248 offset:13312\n\t"
    "v_lshlrev_b32 v142, 4, v253\n\t"
    "s_lshl_b32 s52, s50, 12\n\t"
    "ds_read_b64_tr_b16 v[224:225], v249 offset:1024\n\t"
    "v_add_u32 v142, s52, v142\n\t"
    "s_waitcnt lgkmcnt(9)\n\t"
    "v_mfma_f32_16x16x32_bf16 v[168:171], v[232:235], v[0:3], 0\n\t"
    "ds_read_b64_tr_b16 v[226:227], v249 offset:5120\n\t"
    "v_or_b32 v143, s61, v142\n\t"
    "v_mfma_f32_16x16x32_bf16 v[168:171], v[236:239], v[8:11], v[168:171]\n\t"
    "ds_read_b64_tr_b16 v[228:229], v249 offset:9216\n\t"
    "v_bfe_u32 v254, v253, 2, 3\n\t"
    "ds_read_b64_tr_b16 v[230:231], v249 offset:13312\n\t"
    "v_lshlrev_b32 v134, 10, v254\n\t"
    "ds_read_b64_tr_b16 v[232:233], v248 offset:1536\n\t"
    "v_bfe_u32 v254, v253, 5, 1\n\t"
    "s_waitcnt lgkmcnt(9)\n\t"
    "v_mfma_f32_16x16x32_bf16 v[172:175], v[240:243], v[0:3], 0\n\t"
    "ds_read_b64_tr_b16 v[234:235], v248 offset:5632\n\t"
    "v_lshl_add_u32 v134, v254, 6, v134\n\t"
    "v_mfma_f32_16x16x32_bf16 v[172:175], v[244:247], v[8:11], v[172:175]\n\t"
    "ds_read_b64_tr_b16 v[236:237], v248 offset:9728\n\t"
    "v_bfe_u32 v254, v253, 0, 2\n\t"
    "ds_read_b64_tr_b16 v[238:239], v248 offset:13824\n\t"
    "v_lshl_add_u32 v134, v254, 4, v134\n\t"
    "v_bfe_u32 v254, v253, 4, 1\n\t"
    "v_lshlrev_b32 v254, 5, v254\n\t"
    "v_mfma_f32_16x16x32_bf16 v[144:147], v[138:141], v[8:11], v[144:147]\n\t"
    "ds_read_b64_tr_b16 v[240:241], v249 offset:1536\n\t"
    "v_xor_b32 v134, v134, v254\n\t"
    "s_waitcnt lgkmcnt(9)\n\t"
    "v_mfma_f32_16x16x32_bf16 v[184:187], v[216:219], v[0:3], 0\n\t"
    "ds_read_b64_tr_b16 v[242:243], v249 offset:5632\n\t"
    "v_xor_b32 v134, %[octo], v134\n\t"
    "v_mfma_f32_16x16x32_bf16 v[184:187], v[220:223], v[8:11], v[184:187]\n\t"
    "ds_read_b64_tr_b16 v[244:245], v249 offset:9728\n\t"
    "s_lshl_b32 s52, s50, 14\n\t"
    "ds_read_b64_tr_b16 v[246:247], v249 offset:13824\n\t"
    "v_add_u32 v134, s52, v134\n\t"
    "v_xor_b32 v135, 0x80, v134\n\t"
    "v_add_u32 v136, 0x1c00, v134\n\t"
    "s_waitcnt lgkmcnt(8)\n\t"
    "v_mfma_f32_16x16x32_bf16 v[188:191], v[224:227], v[0:3], 0\n\t"
    "v_add_u32 v137, 0x1800, v135\n\t"
    "v_add_u32 v134, 0x400, v134\n\t"
    "v_mfma_f32_16x16x32_bf16 v[188:191], v[228:231], v[8:11], v[188:191]\n\t"
    "s_waitcnt lgkmcnt(0)\n\t"
    "s_barrier\n\t"
    "v_add_u32 v248, 0xfffe0000, v248\n\t"
    "v_add_u32 v249, 0xfffe0000, v249\n\t"
    "ds_read_b64_tr_b16 v[216:217], v248 offset:49152\n\t"
    "v_mfma_f32_16x16x32_bf16 v[200:203], v[232:235], v[0:3], 0\n\t"
    "ds_read_b64_tr_b16 v[218:219], v248 offset:53248\n\t"
    "ds_read_b64_tr_b16 v[220:221], v248 offset:57344\n\t"
    "ds_read_b64_tr_b16 v[222:223], v248 offset:61440\n\t"
    "v_mfma_f32_16x16x32_bf16 v[200:203], v[236:239], v[8:11], v[200:203]\n\t"
    "ds_read_b64_tr_b16 v[224:225], v249 offset:49152\n\t"
    "ds_read_b64_tr_b16 v[226:227], v249 offset:53248\n\t"
    "ds_read_b64_tr_b16 v[228:229], v249 offset:57344\n\t"
    "ds_read_b64_tr_b16 v[230:231], v249 offset:61440\n\t"
    "v_mfma_f32_16x16x32_bf16 v[144:147], v[138:141], v[16:19], v[144:147]\n\t"
    "ds_read_b64_tr_b16 v[232:233], v248 offset:49664\n\t"
    "v_mfma_f32_16x16x32_bf16 v[204:207], v[240:243], v[0:3], 0\n\t"
    "ds_read_b64_tr_b16 v[234:235], v248 offset:53760\n\t"
    "v_mfma_f32_16x16x32_bf16 v[204:207], v[244:247], v[8:11], v[204:207]\n\t"
    "ds_read_b64_tr_b16 v[236:237], v248 offset:57856\n\t"
    "ds_read_b64_tr_b16 v[238:239], v248 offset:61952\n\t"
    "ds_read_b64_tr_b16 v[240:241], v249 offset:49664\n\t"
    "s_waitcnt lgkmcnt(9)\n\t"
    "v_mfma_f32_16x16x32_bf16 v[152:155], v[216:219], v[16:19], v[152:155]\n\t"
    "ds_read_b64_tr_b16 v[242:243], v249 offset:53760\n\t"
    "v_mfma_f32_16x16x32_bf16 v[152:155], v[220:223], v[24:27], v[152:155]\n\t"
    "ds_read_b64_tr_b16 v[244:245], v249 offset:57856\n\t"
    "ds_read_b64_tr_b16 v[246:247], v249 offset:61952\n\t"
    "ds_read_b64_tr_b16 v[216:217], v248 offset:50176\n\t"
    "s_waitcnt lgkmcnt(9)\n\t"
    "v_mfma_f32_16x16x32_bf16 v[156:159], v[224:227], v[16:19], v[156:159]\n\t"
    "ds_read_b64_tr_b16 v[218:219], v248 offset:54272\n\t"
    "v_mfma_f32_16x16x32_bf16 v[156:159], v[228:231], v[24:27], v[156:159]\n\t"
    "ds_read_b64_tr_b16 v[220:221], v248 offset:58368\n\t"
    "ds_read_b64_tr_b16 v[222:223], v248 offset:62464\n\t"
    "ds_read_b64_tr_b16 v[224:225], v249 offset:50176\n\t"
    "s_waitcnt lgkmcnt(9)\n\t"
    "v_mfma_f32_16x16x32_bf16 v[168:171], v[232:235], v[16:19], v[168:171]\n\t"
    "ds_read_b64_tr_b16 v[226:227], v249 offset:54272\n\t"
    "v_mfma_f32_16x16x32_bf16 v[168:171], v[236:239], v[24:27], v[168:171]\n\t"
    "ds_read_b64_tr_b16 v[228:229], v249 offset:58368\n\t"
    "ds_read_b64_tr_b16 v[230:231], v249 offset:62464\n\t"
    "v_mfma_f32_16x16x32_bf16 v[144:147], v[138:141], v[24:27], v[144:147]\n\t"
    "ds_read_b64_tr_b16 v[232:233], v248 offset:50688\n\t"
    "s_waitcnt lgkmcnt(9)\n\t"
    "v_mfma_f32_16x16x32_bf16 v[172:175], v[240:243], v[16:19], v[172:175]\n\t"
    "ds_read_b64_tr_b16 v[234:235], v248 offset:54784\n\t"
    "v_mfma_f32_16x16x32_bf16 v[172:175], v[244:247], v[24:27], v[172:175]\n\t"
    "ds_read_b64_tr_b16 v[236:237], v248 offset:58880\n\t"
    "ds_read_b64_tr_b16 v[238:239], v248 offset:62976\n\t"
    "ds_read_b64_tr_b16 v[240:241], v249 offset:50688\n\t"
    "s_waitcnt lgkmcnt(9)\n\t"
    "v_mfma_f32_16x16x32_bf16 v[184:187], v[216:219], v[16:19], v[184:187]\n\t"
    "ds_read_b64_tr_b16 v[242:243], v249 offset:54784\n\t"
    "v_mfma_f32_16x16x32_bf16 v[184:187], v[220:223], v[24:27], v[184:187]\n\t"
    "ds_read_b64_tr_b16 v[244:245], v249 offset:58880\n\t"
    "ds_read_b64_tr_b16 v[246:247], v249 offset:62976\n\t"
    "s_waitcnt lgkmcnt(8)\n\t"
    "v_mfma_f32_16x16x32_bf16 v[188:191], v[224:227], v[16:19], v[188:191]\n\t"
    "v_mfma_f32_16x16x32_bf16 v[188:191], v[228:231], v[24:27], v[188:191]\n\t"
    "v_mfma_f32_16x16x32_bf16 v[144:147], v[138:141], v[32:35], v[144:147]\n\t"
    "s_waitcnt lgkmcnt(0)\n\t"
    "s_barrier\n\t"
    "v_add_u32 v248, 0x10000, v248\n\t"
    "v_add_u32 v249, 0x10000, v249\n\t"
    "ds_read_b64_tr_b16 v[216:217], v248 offset:0\n\t"
    "v_mfma_f32_16x16x32_bf16 v[200:203], v[232:235], v[16:19], v[200:203]\n\t"
    "ds_read_b64_tr_b16 v[218:219], v248 offset:4096\n\t"
    "ds_read_b64_tr_b16 v[220:221], v248 offset:8192\n\t"
    "ds_read_b64_tr_b16 v[222:223], v248 offset:12288\n\t"
    "v_mfma_f32_16x16x32_bf16 v[200:203], v[236:239], v[24:27], v[200:203]\n\t"
    "ds_read_b64_tr_b16 v[224:225], v249 offset:0\n\t"
    "ds_read_b64_tr_b16 v[226:227], v249 offset:4096\n\t"
    "ds_read_b64_tr_b16 v[228:229], v249 offset:8192\n\t"
    "ds_read_b64_tr_b16 v[230:231], v249 offset:12288\n\t"
    "ds_read_b64_tr_b16 v[232:233], v248 offset:512\n\t"
    "v_mfma_f32_16x16x32_bf16 v[204:207], v[240:243], v[16:19], v[204:207]\n\t"
    "ds_read_b64_tr_b16 v[234:235], v248 offset:4608\n\t"
    "v_mfma_f32_16x16x32_bf16 v[204:207], v[244:247], v[24:27], v[204:207]\n\t"
    "ds_read_b64_tr_b16 v[236:237], v248 offset:8704\n\t"
    "ds_read_b64_tr_b16 v[238:239], v248 offset:12800\n\t"
    "ds_read_b64_tr_b16 v[240:241], v249 offset:512\n\t"
    "s_waitcnt lgkmcnt(9)\n\t"
    "v_mfma_f32_16x16x32_bf16 v[152:155], v[216:219], v[32:35], v[152:155]\n\t"
    "ds_read_b64_tr_b16 v[242:243], v249 offset:4608\n\t"
    "v_mfma_f32_16x16x32_bf16 v[152:155], v[220:223], v[40:43], v[152:155]\n\t"
    "ds_read_b64_tr_b16 v[244:245], v249 offset:8704\n\t"
    "ds_read_b64_tr_b16 v[246:247], v249 offset:12800\n\t"
    "v_mfma_f32_16x16x32_bf16 v[144:147], v[138:141], v[40:43], v[144:147]\n\t"
    "ds_read_b64_tr_b16 v[216:217], v248 offset:1024\n\t"
    "s_waitcnt lgkmcnt(9)\n\t"
    "v_mfma_f32_16x16x32_bf16 v[156:159], v[224:227], v[32:35], v[156:159]\n\t"
    "ds_read_b64_tr_b16 v[218:219], v248 offset:5120\n\t"
    "v_mfma_f32_16x16x32_bf16 v[156:159], v[228:231], v[40:43], v[156:159]\n\t"
    "ds_read_b64_tr_b16 v[220:221], v248 offset:9216\n\t"
    "ds_read_b64_tr_b16 v[222:223], v248 offset:13312\n\t"
    "ds_read_b64_tr_b16 v[224:225], v249 offset:1024\n\t"
    "s_waitcnt lgkmcnt(9)\n\t"
    "v_mfma_f32_16x16x32_bf16 v[168:171], v[232:235], v[32:35], v[168:171]\n\t"
    "ds_read_b64_tr_b16 v[226:227], v249 offset:5120\n\t"
    "v_mfma_f32_16x16x32_bf16 v[168:171], v[236:239], v[40:43], v[168:171]\n\t"
    "ds_read_b64_tr_b16 v[228:229], v249 offset:9216\n\t"
    "ds_read_b64_tr_b16 v[230:231], v249 offset:13312\n\t"
    "ds_read_b64_tr_b16 v[232:233], v248 offset:1536\n\t"
    "s_waitcnt lgkmcnt(9)\n\t"
    "v_mfma_f32_16x16x32_bf16 v[172:175], v[240:243], v[32:35], v[172:175]\n\t"
    "ds_read_b64_tr_b16 v[234:235], v248 offset:5632\n\t"
    "v_mfma_f32_16x16x32_bf16 v[172:175], v[244:247], v[40:43], v[172:175]\n\t"
    "ds_read_b64_tr_b16 v[236:237], v248 offset:9728\n\t"
    "ds_read_b64_tr_b16 v[238:239], v248 offset:13824\n\t"
    "ds_read_b64_tr_b16 v[240:241], v249 offset:1536\n\t"
    "s_waitcnt lgkmcnt(9)\n\t"
    "v_mfma_f32_16x16x32_bf16 v[184:187], v[216:219], v[32:35], v[184:187]\n\t"
    "ds_read_b64_tr_b16 v[242:243], v249 offset:5632\n\t"
    "v_mfma_f32_16x16x32_bf16 v[184:187], v[220:223], v[40:43], v[184:187]\n\t"
    "ds_read_b64_tr_b16 v[244:245], v249 offset:9728\n\t"
    "ds_read_b64_tr_b16 v[246:247], v249 offset:13824\n\t"
    "v_mfma_f32_16x16x32_bf16 v[144:147], v[138:141], v[48:51], v[144:147]\n\t"
    "s_waitcnt lgkmcnt(8)\n\t"
    "v_mfma_f32_16x16x32_bf16 v[188:191], v[224:227], v[32:35], v[188:191]\n\t"
    "v_mfma_f32_16x16x32_bf16 v[188:191], v[228:231], v[40:43], v[188:191]\n\t"
    "s_waitcnt lgkmcnt(0)\n\t"
    "s_barrier\n\t"
    "v_add_u32 v248, 0xffff0000, v248\n\t"
    "v_add_u32 v249, 0xffff0000, v249\n\t"
    "ds_read_b64_tr_b16 v[216:217], v248 offset:0\n\t"
    "v_mfma_f32_16x16x32_bf16 v[200:203], v[232:235], v[32:35], v[200:203]\n\t"
    "ds_read_b64_tr_b16 v[218:219], v248 offset:4096\n\t"
    "ds_read_b64_tr_b16 v[220:221], v248 offset:8192\n\t"
    "ds_read_b64_tr_b16 v[222:223], v248 offset:12288\n\t"
    "v_mfma_f32_16x16x32_bf16 v[200:203], v[236:239], v[40:43], v[200:203]\n\t"
    "ds_read_b64_tr_b16 v[224:225], v249 offset:0\n\t"
    "ds_read_b64_tr_b16 v[226:227], v249 offset:4096\n\t"
    "ds_read_b64_tr_b16 v[228:229], v249 offset:8192\n\t"
    "ds_read_b64_tr_b16 v[230:231], v249 offset:12288\n\t"
    "ds_read_b64_tr_b16 v[232:233], v248 offset:512\n\t"
    "v_mfma_f32_16x16x32_bf16 v[204:207], v[240:243], v[32:35], v[204:207]\n\t"
    "ds_read_b64_tr_b16 v[234:235], v248 offset:4608\n\t"
    "v_mfma_f32_16x16x32_bf16 v[204:207], v[244:247], v[40:43], v[204:207]\n\t"
    "ds_read_b64_tr_b16 v[236:237], v248 offset:8704\n\t"
    "ds_read_b64_tr_b16 v[238:239], v248 offset:12800\n\t"
    "v_mfma_f32_16x16x32_bf16 v[144:147], v[138:141], v[56:59], v[144:147]\n\t"
    "ds_read_b64_tr_b16 v[240:241], v249 offset:512\n\t"
    "s_waitcnt lgkmcnt(9)\n\t"
    "v_mfma_f32_16x16x32_bf16 v[152:155], v[216:219], v[48:51], v[152:155]\n\t"
    "ds_read_b64_tr_b16 v[242:243], v249 offset:4608\n\t"
    "v_mfma_f32_16x16x32_bf16 v[152:155], v[220:223], v[56:59], v[152:155]\n\t"
    "ds_read_b64_tr_b16 v[244:245], v249 offset:8704\n\t"
    "ds_read_b64_tr_b16 v[246:247], v249 offset:12800\n\t"
    "ds_read_b64_tr_b16 v[216:217], v248 offset:1024\n\t"
    "s_waitcnt lgkmcnt(9)\n\t"
    "v_mfma_f32_16x16x32_bf16 v[156:159], v[224:227], v[48:51], v[156:159]\n\t"
    "ds_read_b64_tr_b16 v[218:219], v248 offset:5120\n\t"
    "v_mfma_f32_16x16x32_bf16 v[156:159], v[228:231], v[56:59], v[156:159]\n\t"
    "ds_read_b64_tr_b16 v[220:221], v248 offset:9216\n\t"
    "ds_read_b64_tr_b16 v[222:223], v248 offset:13312\n\t"
    "ds_read_b64_tr_b16 v[224:225], v249 offset:1024\n\t"
    "s_waitcnt lgkmcnt(9)\n\t"
    "v_mfma_f32_16x16x32_bf16 v[168:171], v[232:235], v[48:51], v[168:171]\n\t"
    "ds_read_b64_tr_b16 v[226:227], v249 offset:5120\n\t"
    "v_mfma_f32_16x16x32_bf16 v[168:171], v[236:239], v[56:59], v[168:171]\n\t"
    "ds_read_b64_tr_b16 v[228:229], v249 offset:9216\n\t"
    "ds_read_b64_tr_b16 v[230:231], v249 offset:13312\n\t"
    "ds_read_b64_tr_b16 v[232:233], v248 offset:1536\n\t"
    "s_waitcnt lgkmcnt(9)\n\t"
    "v_mfma_f32_16x16x32_bf16 v[172:175], v[240:243], v[48:51], v[172:175]\n\t"
    "ds_read_b64_tr_b16 v[234:235], v248 offset:5632\n\t"
    "v_mfma_f32_16x16x32_bf16 v[172:175], v[244:247], v[56:59], v[172:175]\n\t"
    "ds_read_b64_tr_b16 v[236:237], v248 offset:9728\n\t"
    "ds_read_b64_tr_b16 v[238:239], v248 offset:13824\n\t"
    "v_mfma_f32_16x16x32_bf16 v[144:147], v[138:141], v[64:67], v[144:147]\n\t"
    "ds_read_b64_tr_b16 v[240:241], v249 offset:1536\n\t"
    "s_waitcnt lgkmcnt(9)\n\t"
    "v_mfma_f32_16x16x32_bf16 v[184:187], v[216:219], v[48:51], v[184:187]\n\t"
    "ds_read_b64_tr_b16 v[242:243], v249 offset:5632\n\t"
    "v_mfma_f32_16x16x32_bf16 v[184:187], v[220:223], v[56:59], v[184:187]\n\t"
    "ds_read_b64_tr_b16 v[244:245], v249 offset:9728\n\t"
    "ds_read_b64_tr_b16 v[246:247], v249 offset:13824\n\t"
    "s_waitcnt lgkmcnt(8)\n\t"
    "v_mfma_f32_16x16x32_bf16 v[188:191], v[224:227], v[48:51], v[188:191]\n\t"
    "v_mfma_f32_16x16x32_bf16 v[188:191], v[228:231], v[56:59], v[188:191]\n\t"
    "s_waitcnt lgkmcnt(0)\n\t"
    "s_barrier\n\t"
    "v_add_u32 v248, 0x10000, v248\n\t"
    "v_add_u32 v249, 0x10000, v249\n\t"
    "ds_read_b64_tr_b16 v[216:217], v248 offset:16384\n\t"
    "v_mfma_f32_16x16x32_bf16 v[200:203], v[232:235], v[48:51], v[200:203]\n\t"
    "ds_read_b64_tr_b16 v[218:219], v248 offset:20480\n\t"
    "ds_read_b64_tr_b16 v[220:221], v248 offset:24576\n\t"
    "ds_read_b64_tr_b16 v[222:223], v248 offset:28672\n\t"
    "v_mfma_f32_16x16x32_bf16 v[200:203], v[236:239], v[56:59], v[200:203]\n\t"
    "ds_read_b64_tr_b16 v[224:225], v249 offset:16384\n\t"
    "ds_read_b64_tr_b16 v[226:227], v249 offset:20480\n\t"
    "ds_read_b64_tr_b16 v[228:229], v249 offset:24576\n\t"
    "ds_read_b64_tr_b16 v[230:231], v249 offset:28672\n\t"
    "v_mfma_f32_16x16x32_bf16 v[144:147], v[138:141], v[72:75], v[144:147]\n\t"
    "ds_read_b64_tr_b16 v[232:233], v248 offset:16896\n\t"
    "v_mfma_f32_16x16x32_bf16 v[204:207], v[240:243], v[48:51], v[204:207]\n\t"
    "ds_read_b64_tr_b16 v[234:235], v248 offset:20992\n\t"
    "v_mfma_f32_16x16x32_bf16 v[204:207], v[244:247], v[56:59], v[204:207]\n\t"
    "ds_read_b64_tr_b16 v[236:237], v248 offset:25088\n\t"
    "ds_read_b64_tr_b16 v[238:239], v248 offset:29184\n\t"
    "ds_read_b64_tr_b16 v[240:241], v249 offset:16896\n\t"
    "s_waitcnt lgkmcnt(9)\n\t"
    "v_mfma_f32_16x16x32_bf16 v[152:155], v[216:219], v[64:67], v[152:155]\n\t"
    "ds_read_b64_tr_b16 v[242:243], v249 offset:20992\n\t"
    "v_mfma_f32_16x16x32_bf16 v[152:155], v[220:223], v[72:75], v[152:155]\n\t"
    "ds_read_b64_tr_b16 v[244:245], v249 offset:25088\n\t"
    "ds_read_b64_tr_b16 v[246:247], v249 offset:29184\n\t"
    "ds_read_b64_tr_b16 v[216:217], v248 offset:17408\n\t"
    "s_waitcnt lgkmcnt(9)\n\t"
    "v_mfma_f32_16x16x32_bf16 v[156:159], v[224:227], v[64:67], v[156:159]\n\t"
    "ds_read_b64_tr_b16 v[218:219], v248 offset:21504\n\t"
    "v_mfma_f32_16x16x32_bf16 v[156:159], v[228:231], v[72:75], v[156:159]\n\t"
    "ds_read_b64_tr_b16 v[220:221], v248 offset:25600\n\t"
    "ds_read_b64_tr_b16 v[222:223], v248 offset:29696\n\t"
    "v_mfma_f32_16x16x32_bf16 v[144:147], v[138:141], v[80:83], v[144:147]\n\t"
    "ds_read_b64_tr_b16 v[224:225], v249 offset:17408\n\t"
    "s_waitcnt lgkmcnt(9)\n\t"
    "v_mfma_f32_16x16x32_bf16 v[168:171], v[232:235], v[64:67], v[168:171]\n\t"
    "ds_read_b64_tr_b16 v[226:227], v249 offset:21504\n\t"
    "v_mfma_f32_16x16x32_bf16 v[168:171], v[236:239], v[72:75], v[168:171]\n\t"
    "ds_read_b64_tr_b16 v[228:229], v249 offset:25600\n\t"
    "ds_read_b64_tr_b16 v[230:231], v249 offset:29696\n\t"
    "ds_read_b64_tr_b16 v[232:233], v248 offset:17920\n\t"
    "s_waitcnt lgkmcnt(9)\n\t"
    "v_mfma_f32_16x16x32_bf16 v[172:175], v[240:243], v[64:67], v[172:175]\n\t"
    "ds_read_b64_tr_b16 v[234:235], v248 offset:22016\n\t"
    "v_mfma_f32_16x16x32_bf16 v[172:175], v[244:247], v[72:75], v[172:175]\n\t"
    "ds_read_b64_tr_b16 v[236:237], v248 offset:26112\n\t"
    "ds_read_b64_tr_b16 v[238:239], v248 offset:30208\n\t"
    "ds_read_b64_tr_b16 v[240:241], v249 offset:17920\n\t"
    "s_waitcnt lgkmcnt(9)\n\t"
    "v_mfma_f32_16x16x32_bf16 v[184:187], v[216:219], v[64:67], v[184:187]\n\t"
    "ds_read_b64_tr_b16 v[242:243], v249 offset:22016\n\t"
    "v_mfma_f32_16x16x32_bf16 v[184:187], v[220:223], v[72:75], v[184:187]\n\t"
    "ds_read_b64_tr_b16 v[244:245], v249 offset:26112\n\t"
    "ds_read_b64_tr_b16 v[246:247], v249 offset:30208\n\t"
    "s_waitcnt lgkmcnt(8)\n\t"
    "v_mfma_f32_16x16x32_bf16 v[188:191], v[224:227], v[64:67], v[188:191]\n\t"
    "v_mfma_f32_16x16x32_bf16 v[188:191], v[228:231], v[72:75], v[188:191]\n\t"
    "v_mfma_f32_16x16x32_bf16 v[144:147], v[138:141], v[88:91], v[144:147]\n\t"
    "s_waitcnt lgkmcnt(0)\n\t"
    "s_barrier\n\t"
    "ds_read_b64_tr_b16 v[216:217], v248 offset:32768\n\t"
    "ds_read_b64_tr_b16 v[218:219], v248 offset:36864\n\t"
    "v_mfma_f32_16x16x32_bf16 v[200:203], v[232:235], v[64:67], v[200:203]\n\t"
    "ds_read_b64_tr_b16 v[220:221], v248 offset:40960\n\t"
    "ds_read_b64_tr_b16 v[222:223], v248 offset:45056\n\t"
    "v_mfma_f32_16x16x32_bf16 v[200:203], v[236:239], v[72:75], v[200:203]\n\t"
    "ds_read_b64_tr_b16 v[224:225], v249 offset:32768\n\t"
    "ds_read_b64_tr_b16 v[226:227], v249 offset:36864\n\t"
    "ds_read_b64_tr_b16 v[228:229], v249 offset:40960\n\t"
    "ds_read_b64_tr_b16 v[230:231], v249 offset:45056\n\t"
    "ds_read_b64_tr_b16 v[232:233], v248 offset:33280\n\t"
    "v_mfma_f32_16x16x32_bf16 v[204:207], v[240:243], v[64:67], v[204:207]\n\t"
    "ds_read_b64_tr_b16 v[234:235], v248 offset:37376\n\t"
    "v_mfma_f32_16x16x32_bf16 v[204:207], v[244:247], v[72:75], v[204:207]\n\t"
    "ds_read_b64_tr_b16 v[236:237], v248 offset:41472\n\t"
    "ds_read_b64_tr_b16 v[238:239], v248 offset:45568\n\t"
    "ds_read_b64_tr_b16 v[240:241], v249 offset:33280\n\t"
    "s_waitcnt lgkmcnt(9)\n\t"
    "v_mfma_f32_16x16x32_bf16 v[152:155], v[216:219], v[80:83], v[152:155]\n\t"
    "ds_read_b64_tr_b16 v[242:243], v249 offset:37376\n\t"
    "v_mfma_f32_16x16x32_bf16 v[152:155], v[220:223], v[88:91], v[152:155]\n\t"
    "ds_read_b64_tr_b16 v[244:245], v249 offset:41472\n\t"
    "ds_read_b64_tr_b16 v[246:247], v249 offset:45568\n\t"
    "v_mfma_f32_16x16x32_bf16 v[144:147], v[138:141], v[96:99], v[144:147]\n\t"
    "ds_read_b64_tr_b16 v[216:217], v248 offset:33792\n\t"
    "s_waitcnt lgkmcnt(9)\n\t"
    "v_mfma_f32_16x16x32_bf16 v[156:159], v[224:227], v[80:83], v[156:159]\n\t"
    "ds_read_b64_tr_b16 v[218:219], v248 offset:37888\n\t"
    "v_mfma_f32_16x16x32_bf16 v[156:159], v[228:231], v[88:91], v[156:159]\n\t"
    "ds_read_b64_tr_b16 v[220:221], v248 offset:41984\n\t"
    "ds_read_b64_tr_b16 v[222:223], v248 offset:46080\n\t"
    "ds_read_b64_tr_b16 v[224:225], v249 offset:33792\n\t"
    "s_waitcnt lgkmcnt(9)\n\t"
    "v_mfma_f32_16x16x32_bf16 v[168:171], v[232:235], v[80:83], v[168:171]\n\t"
    "ds_read_b64_tr_b16 v[226:227], v249 offset:37888\n\t"
    "v_mfma_f32_16x16x32_bf16 v[168:171], v[236:239], v[88:91], v[168:171]\n\t"
    "ds_read_b64_tr_b16 v[228:229], v249 offset:41984\n\t"
    "ds_read_b64_tr_b16 v[230:231], v249 offset:46080\n\t"
    "ds_read_b64_tr_b16 v[232:233], v248 offset:34304\n\t"
    "s_waitcnt lgkmcnt(9)\n\t"
    "v_mfma_f32_16x16x32_bf16 v[172:175], v[240:243], v[80:83], v[172:175]\n\t"
    "ds_read_b64_tr_b16 v[234:235], v248 offset:38400\n\t"
    "v_mfma_f32_16x16x32_bf16 v[172:175], v[244:247], v[88:91], v[172:175]\n\t"
    "ds_read_b64_tr_b16 v[236:237], v248 offset:42496\n\t"
    "ds_read_b64_tr_b16 v[238:239], v248 offset:46592\n\t"
    "v_mfma_f32_16x16x32_bf16 v[144:147], v[138:141], v[104:107], v[144:147]\n\t"
    "ds_read_b64_tr_b16 v[240:241], v249 offset:34304\n\t"
    "s_waitcnt lgkmcnt(9)\n\t"
    "v_mfma_f32_16x16x32_bf16 v[184:187], v[216:219], v[80:83], v[184:187]\n\t"
    "ds_read_b64_tr_b16 v[242:243], v249 offset:38400\n\t"
    "v_mfma_f32_16x16x32_bf16 v[184:187], v[220:223], v[88:91], v[184:187]\n\t"
    "ds_read_b64_tr_b16 v[244:245], v249 offset:42496\n\t"
    "ds_read_b64_tr_b16 v[246:247], v249 offset:46592\n\t"
    "s_waitcnt lgkmcnt(8)\n\t"
    "v_mfma_f32_16x16x32_bf16 v[188:191], v[224:227], v[80:83], v[188:191]\n\t"
    "v_mfma_f32_16x16x32_bf16 v[188:191], v[228:231], v[88:91], v[188:191]\n\t"
    "s_waitcnt lgkmcnt(0)\n\t"
    "s_barrier\n\t"
    "v_add_u32 v248, 0xffff0000, v248\n\t"
    "v_add_u32 v249, 0xffff0000, v249\n\t"
    "ds_read_b64_tr_b16 v[216:217], v248 offset:32768\n\t"
    "v_mfma_f32_16x16x32_bf16 v[200:203], v[232:235], v[80:83], v[200:203]\n\t"
    "ds_read_b64_tr_b16 v[218:219], v248 offset:36864\n\t"
    "ds_read_b64_tr_b16 v[220:221], v248 offset:40960\n\t"
    "ds_read_b64_tr_b16 v[222:223], v248 offset:45056\n\t"
    "v_mfma_f32_16x16x32_bf16 v[200:203], v[236:239], v[88:91], v[200:203]\n\t"
    "ds_read_b64_tr_b16 v[224:225], v249 offset:32768\n\t"
    "ds_read_b64_tr_b16 v[226:227], v249 offset:36864\n\t"
    "ds_read_b64_tr_b16 v[228:229], v249 offset:40960\n\t"
    "ds_read_b64_tr_b16 v[230:231], v249 offset:45056\n\t"
    "ds_read_b64_tr_b16 v[232:233], v248 offset:33280\n\t"
    "v_mfma_f32_16x16x32_bf16 v[204:207], v[240:243], v[80:83], v[204:207]\n\t"
    "ds_read_b64_tr_b16 v[234:235], v248 offset:37376\n\t"
    "v_mfma_f32_16x16x32_bf16 v[204:207], v[244:247], v[88:91], v[204:207]\n\t"
    "ds_read_b64_tr_b16 v[236:237], v248 offset:41472\n\t"
    "ds_read_b64_tr_b16 v[238:239], v248 offset:45568\n\t"
    "v_mfma_f32_16x16x32_bf16 v[144:147], v[138:141], v[112:115], v[144:147]\n\t"
    "ds_read_b64_tr_b16 v[240:241], v249 offset:33280\n\t"
    "s_waitcnt lgkmcnt(9)\n\t"
    "v_mfma_f32_16x16x32_bf16 v[152:155], v[216:219], v[96:99], v[152:155]\n\t"
    "ds_read_b64_tr_b16 v[242:243], v249 offset:37376\n\t"
    "v_mfma_f32_16x16x32_bf16 v[152:155], v[220:223], v[104:107], v[152:155]\n\t"
    "ds_read_b64_tr_b16 v[244:245], v249 offset:41472\n\t"
    "ds_read_b64_tr_b16 v[246:247], v249 offset:45568\n\t"
    "ds_read_b64_tr_b16 v[216:217], v248 offset:33792\n\t"
    "s_waitcnt lgkmcnt(9)\n\t"
    "v_mfma_f32_16x16x32_bf16 v[156:159], v[224:227], v[96:99], v[156:159]\n\t"
    "ds_read_b64_tr_b16 v[218:219], v248 offset:37888\n\t"
    "v_mfma_f32_16x16x32_bf16 v[156:159], v[228:231], v[104:107], v[156:159]\n\t"
    "ds_read_b64_tr_b16 v[220:221], v248 offset:41984\n\t"
    "ds_read_b64_tr_b16 v[222:223], v248 offset:46080\n\t"
    "ds_read_b64_tr_b16 v[224:225], v249 offset:33792\n\t"
    "s_waitcnt lgkmcnt(9)\n\t"
    "v_mfma_f32_16x16x32_bf16 v[168:171], v[232:235], v[96:99], v[168:171]\n\t"
    "ds_read_b64_tr_b16 v[226:227], v249 offset:37888\n\t"
    "v_mfma_f32_16x16x32_bf16 v[168:171], v[236:239], v[104:107], v[168:171]\n\t"
    "ds_read_b64_tr_b16 v[228:229], v249 offset:41984\n\t"
    "ds_read_b64_tr_b16 v[230:231], v249 offset:46080\n\t"
    "v_mfma_f32_16x16x32_bf16 v[144:147], v[138:141], v[120:123], v[144:147]\n\t"
    "ds_read_b64_tr_b16 v[232:233], v248 offset:34304\n\t"
    "s_waitcnt lgkmcnt(9)\n\t"
    "v_mfma_f32_16x16x32_bf16 v[172:175], v[240:243], v[96:99], v[172:175]\n\t"
    "ds_read_b64_tr_b16 v[234:235], v248 offset:38400\n\t"
    "v_mfma_f32_16x16x32_bf16 v[172:175], v[244:247], v[104:107], v[172:175]\n\t"
    "ds_read_b64_tr_b16 v[236:237], v248 offset:42496\n\t"
    "ds_read_b64_tr_b16 v[238:239], v248 offset:46592\n\t"
    "ds_read_b64_tr_b16 v[240:241], v249 offset:34304\n\t"
    "s_waitcnt lgkmcnt(9)\n\t"
    "v_mfma_f32_16x16x32_bf16 v[184:187], v[216:219], v[96:99], v[184:187]\n\t"
    "ds_read_b64_tr_b16 v[242:243], v249 offset:38400\n\t"
    "v_mfma_f32_16x16x32_bf16 v[184:187], v[220:223], v[104:107], v[184:187]\n\t"
    "ds_read_b64_tr_b16 v[244:245], v249 offset:42496\n\t"
    "ds_read_b64_tr_b16 v[246:247], v249 offset:46592\n\t"
    "s_waitcnt lgkmcnt(8)\n\t"
    "v_mfma_f32_16x16x32_bf16 v[188:191], v[224:227], v[96:99], v[188:191]\n\t"
    "v_mfma_f32_16x16x32_bf16 v[188:191], v[228:231], v[104:107], v[188:191]\n\t"
    "s_waitcnt lgkmcnt(0)\n\t"
    "s_barrier\n\t"
    "s_setprio 2\n\t"
    "ds_read_b64_tr_b16 v[216:217], v248 offset:49152\n\t"
    "ds_read_b64_tr_b16 v[218:219], v248 offset:53248\n\t"
    "v_mfma_f32_16x16x32_bf16 v[200:203], v[232:235], v[96:99], v[200:203]\n\t"
    "ds_read_b64_tr_b16 v[220:221], v248 offset:57344\n\t"
    "ds_read_b64_tr_b16 v[222:223], v248 offset:61440\n\t"
    "v_mfma_f32_16x16x32_bf16 v[200:203], v[236:239], v[104:107], v[200:203]\n\t"
    "ds_read_b64_tr_b16 v[224:225], v249 offset:49152\n\t"
    "ds_read_b64_tr_b16 v[226:227], v249 offset:53248\n\t"
    "ds_read_b64_tr_b16 v[228:229], v249 offset:57344\n\t"
    "ds_read_b64_tr_b16 v[230:231], v249 offset:61440\n\t"
    "ds_read_b64_tr_b16 v[232:233], v248 offset:49664\n\t"
    "v_mfma_f32_16x16x32_bf16 v[204:207], v[240:243], v[96:99], v[204:207]\n\t"
    "ds_read_b64_tr_b16 v[234:235], v248 offset:53760\n\t"
    "v_mfma_f32_16x16x32_bf16 v[204:207], v[244:247], v[104:107], v[204:207]\n\t"
    "ds_read_b64_tr_b16 v[236:237], v248 offset:57856\n\t"
    "ds_read_b64_tr_b16 v[238:239], v248 offset:61952\n\t"
    "ds_read_b64_tr_b16 v[240:241], v249 offset:49664\n\t"
    "v_max_f32 v252, 0xda24260, v148\n\t"
    "s_waitcnt lgkmcnt(9)\n\t"
    "v_mfma_f32_16x16x32_bf16 v[152:155], v[216:219], v[112:115], v[152:155]\n\t"
    "ds_read_b64_tr_b16 v[242:243], v249 offset:53760\n\t"
    "v_rcp_f32 v251, v144\n\t"
    "v_rcp_f32 v252, v252\n\t"
    "v_mfma_f32_16x16x32_bf16 v[152:155], v[220:223], v[120:123], v[152:155]\n\t"
    "ds_read_b64_tr_b16 v[244:245], v249 offset:57856\n\t"
    "ds_read_b64_tr_b16 v[246:247], v249 offset:61952\n\t"
    "ds_read_b64_tr_b16 v[216:217], v248 offset:50176\n\t"
    "v_lshlrev_b32 v145, 5, v253\n\t"
    "v_cmp_gt_u32 vcc, 4, v253\n\t"
    "v_mov_b32 v254, 0x80000000\n\t"
    "s_waitcnt lgkmcnt(9)\n\t"
    "v_mfma_f32_16x16x32_bf16 v[156:159], v[224:227], v[112:115], v[156:159]\n\t"
    "ds_read_b64_tr_b16 v[218:219], v248 offset:54272\n\t"
    "v_cndmask_b32 v145, v254, v145, vcc\n\t"
    "v_mov_b32 v144, 0\n\t"
    "v_cmp_eq_u32 vcc, 0, v253\n\t"
    "v_mfma_f32_16x16x32_bf16 v[156:159], v[228:231], v[120:123], v[156:159]\n\t"
    "ds_read_b64_tr_b16 v[220:221], v248 offset:58368\n\t"
    "v_mov_b32 v254, 0x80000000\n\t"
    "v_cndmask_b32 v144, v254, v144, vcc\n\t"
    "v_mov_b32 v148, s44\n\t"
    "ds_read_b64_tr_b16 v[222:223], v248 offset:62464\n\t"
    "v_mov_b32 v149, s45\n\t"
    "ds_read_b64_tr_b16 v[224:225], v249 offset:50176\n\t"
    "s_waitcnt lgkmcnt(9)\n\t"
    "v_mfma_f32_16x16x32_bf16 v[168:171], v[232:235], v[112:115], v[168:171]\n\t"
    "ds_read_b64_tr_b16 v[226:227], v249 offset:54272\n\t"
    "v_mfma_f32_16x16x32_bf16 v[168:171], v[236:239], v[120:123], v[168:171]\n\t"
    "ds_read_b64_tr_b16 v[228:229], v249 offset:58368\n\t"
    "v_mul_f32 v152, v251, v152\n\t"
    "v_mul_f32 v153, v251, v153\n\t"
    "v_cvt_pk_bf16_f32 v0, v152, v153\n\t"
    "ds_read_b64_tr_b16 v[230:231], v249 offset:62464\n\t"
    "v_mul_f32 v154, v251, v154\n\t"
    "v_mul_f32 v155, v251, v155\n\t"
    "v_cvt_pk_bf16_f32 v1, v154, v155\n\t"
    "ds_read_b64_tr_b16 v[232:233], v248 offset:50688\n\t"
    "v_mul_f32 v156, v251, v156\n\t"
    "v_mul_f32 v157, v251, v157\n\t"
    "v_cvt_pk_bf16_f32 v2, v156, v157\n\t"
    "s_waitcnt lgkmcnt(9)\n\t"
    "v_mfma_f32_16x16x32_bf16 v[172:175], v[240:243], v[112:115], v[172:175]\n\t"
    "ds_read_b64_tr_b16 v[234:235], v248 offset:54784\n\t"
    "v_mul_f32 v158, v251, v158\n\t"
    "v_mul_f32 v159, v251, v159\n\t"
    "v_cvt_pk_bf16_f32 v3, v158, v159\n\t"
    "v_mfma_f32_16x16x32_bf16 v[172:175], v[244:247], v[120:123], v[172:175]\n\t"
    "ds_read_b64_tr_b16 v[236:237], v248 offset:58880\n\t"
    "ds_read_b64_tr_b16 v[238:239], v248 offset:62976\n\t"
    "ds_read_b64_tr_b16 v[240:241], v249 offset:50688\n\t"
    "s_waitcnt lgkmcnt(9)\n\t"
    "v_mfma_f32_16x16x32_bf16 v[184:187], v[216:219], v[112:115], v[184:187]\n\t"
    "ds_read_b64_tr_b16 v[242:243], v249 offset:54784\n\t"
    "v_mfma_f32_16x16x32_bf16 v[184:187], v[220:223], v[120:123], v[184:187]\n\t"
    "ds_read_b64_tr_b16 v[244:245], v249 offset:58880\n\t"
    "s_add_u32 s54, s47, 0x14000\n\t"
    "v_lshl_add_u32 v146, v253, 4, s54\n\t"
    "ds_write_b128 v146, v[0:3] offset:0\n\t"
    "ds_read_b64_tr_b16 v[246:247], v249 offset:62976\n\t"
    "ds_write_b128 v146, v[4:7] offset:1024\n\t"
    "s_xor_b32 s53, s43, 0x0\n\t"
    "s_lshr_b32 s53, s53, 2\n\t"
    "s_add_u32 s53, s53, %[obase]\n\t"
    "buffer_store_dwordx4 v[0:3], v142, %[srdws], s53 offen sc1\n\t"
    "s_waitcnt lgkmcnt(10)\n\t"
    "v_mfma_f32_16x16x32_bf16 v[188:191], v[224:227], v[112:115], v[188:191]\n\t"
    "v_mul_f32 v168, v251, v168\n\t"
    "v_mfma_f32_16x16x32_bf16 v[188:191], v[228:231], v[120:123], v[188:191]\n\t"
    "v_mul_f32 v169, v251, v169\n\t"
    "v_cvt_pk_bf16_f32 v8, v168, v169\n\t"
    "v_mul_f32 v170, v251, v170\n\t"
    "v_mul_f32 v171, v251, v171\n\t"
    "v_cvt_pk_bf16_f32 v9, v170, v171\n\t"
    "v_mul_f32 v172, v251, v172\n\t"
    "v_mul_f32 v173, v251, v173\n\t"
    "v_cvt_pk_bf16_f32 v10, v172, v173\n\t"
    "v_mul_f32 v174, v251, v174\n\t"
    "v_mul_f32 v175, v251, v175\n\t"
    "v_cvt_pk_bf16_f32 v11, v174, v175\n\t"
    "s_waitcnt lgkmcnt(6)\n\t"
    "v_mfma_f32_16x16x32_bf16 v[200:203], v[232:235], v[112:115], v[200:203]\n\t"
    "v_mfma_f32_16x16x32_bf16 v[200:203], v[236:239], v[120:123], v[200:203]\n\t"
    "ds_write_b128 v146, v[8:11] offset:2048\n\t"
    "ds_write_b128 v146, v[12:15] offset:3072\n\t"
    "buffer_store_dwordx4 v[8:11], v142, %[srdws], s53 offen offset:2048 sc1\n\t"
    "s_waitcnt lgkmcnt(3)\n\t"
    "v_mfma_f32_16x16x32_bf16 v[204:207], v[240:243], v[112:115], v[204:207]\n\t"
    "v_mul_f32 v184, v251, v184\n\t"
    "v_mfma_f32_16x16x32_bf16 v[204:207], v[244:247], v[120:123], v[204:207]\n\t"
    "v_mul_f32 v185, v251, v185\n\t"
    "v_cvt_pk_bf16_f32 v16, v184, v185\n\t"
    "v_mul_f32 v186, v251, v186\n\t"
    "v_mul_f32 v187, v251, v187\n\t"
    "v_cvt_pk_bf16_f32 v17, v186, v187\n\t"
    "v_mul_f32 v188, v251, v188\n\t"
    "v_mul_f32 v189, v251, v189\n\t"
    "v_cvt_pk_bf16_f32 v18, v188, v189\n\t"
    "v_mul_f32 v190, v251, v190\n\t"
    "v_mul_f32 v191, v251, v191\n\t"
    "v_cvt_pk_bf16_f32 v19, v190, v191\n\t"
    "s_add_u32 s54, s47, 0x18000\n\t"
    "v_lshl_add_u32 v146, v253, 4, s54\n\t"
    "ds_write_b128 v146, v[16:19] offset:0\n\t"
    "ds_write_b128 v146, v[20:23] offset:1024\n\t"
    "s_xor_b32 s53, s43, 0x10000\n\t"
    "s_lshr_b32 s53, s53, 2\n\t"
    "s_add_u32 s53, s53, %[obase]\n\t"
    "buffer_store_dwordx4 v[16:19], v142, %[srdws], s53 offen sc1\n\t"
    "v_mul_f32 v200, v251, v200\n\t"
    "v_mul_f32 v201, v251, v201\n\t"
    "v_cvt_pk_bf16_f32 v24, v200, v201\n\t"
    "v_mul_f32 v202, v251, v202\n\t"
    "v_mul_f32 v203, v251, v203\n\t"
    "v_cvt_pk_bf16_f32 v25, v202, v203\n\t"
    "v_mul_f32 v204, v251, v204\n\t"
    "v_mul_f32 v205, v251, v205\n\t"
    "v_cvt_pk_bf16_f32 v26, v204, v205\n\t"
    "v_mul_f32 v206, v251, v206\n\t"
    "v_mul_f32 v207, v251, v207\n\t"
    "v_cvt_pk_bf16_f32 v27, v206, v207\n\t"
    "ds_write_b128 v146, v[24:27] offset:2048\n\t"
    "ds_write_b128 v146, v[28:31] offset:3072\n\t"
    "buffer_store_dwordx4 v[24:27], v142, %[srdws], s53 offen offset:2048 sc1\n\t"
    "s_waitcnt lgkmcnt(0)\n\t"
    "s_barrier\n\t"
    "s_add_u32 m0, s47, 0x4000\n\t"
    "s_bfe_u32 s46, %[htab], 0x30004\n\t"
    "s_lshl_b32 s46, s46, 17\n\t"
    "s_or_b32 s46, s46, 0x10000\n\t"
    "buffer_load_dwordx4 v134, %[srdwo], s46 offen lds\n\t"
    "buffer_load_dwordx4 v135, %[srdwo], s46 offen offset:1024 lds\n\t"
    "buffer_load_dwordx4 v136, %[srdwo], s46 offen offset:2048 lds\n\t"
    "buffer_load_dwordx4 v137, %[srdwo], s46 offen offset:3072 lds\n\t"
    "s_add_u32 m0, s47, 0x24000\n\t"
    "s_bfe_u32 s46, %[htab], 0x30008\n\t"
    "s_lshl_b32 s46, s46, 17\n\t"
    "buffer_load_dwordx4 v134, %[srdwo], s46 offen lds\n\t"
    "buffer_load_dwordx4 v135, %[srdwo], s46 offen offset:1024 lds\n\t"
    "buffer_load_dwordx4 v136, %[srdwo], s46 offen offset:2048 lds\n\t"
    "buffer_load_dwordx4 v137, %[srdwo], s46 offen offset:3072 lds\n\t"
    "s_add_u32 m0, s47, 0xc000\n\t"
    "s_bfe_u32 s46, %[htab], 0x30008\n\t"
    "s_lshl_b32 s46, s46, 17\n\t"
    "s_or_b32 s46, s46, 0x10000\n\t"
    "buffer_load_dwordx4 v134, %[srdwo], s46 offen lds\n\t"
    "buffer_load_dwordx4 v135, %[srdwo], s46 offen offset:1024 lds\n\t"
    "buffer_load_dwordx4 v136, %[srdwo], s46 offen offset:2048 lds\n\t"
    "buffer_load_dwordx4 v137, %[srdwo], s46 offen offset:3072 lds\n\t"
    "s_barrier\n\t"
    "s_lshl_b32 s55, s50, 3\n\t"
    "s_add_u32 s55, s55, %[f1base]\n\t"
    "s_lshr_b32 s52, s43, 12\n\t"
    "s_add_u32 s55, s55, s52\n\t"
    "s_waitcnt vmcnt(12)\n\t"
    "buffer_store_dwordx2 v[148:149], v144, %[srdws], s55 offen sc1\n\t"
    "s_lshl_b32 s55, s50, 3\n\t"
    "s_add_u32 s55, s55, %[f1base]\n\t"
    "s_add_u32 s55, s55, 0x80\n\t"
    "s_add_u32 m0, s47, 0x20000\n\t"
    "s_bfe_u32 s46, %[htab], 0x3000c\n\t"
    "s_lshl_b32 s46, s46, 17\n\t"
    "buffer_load_dwordx4 v134, %[srdwo], s46 offen lds\n\t"
    "buffer_load_dwordx4 v135, %[srdwo], s46 offen offset:1024 lds\n\t"
    "buffer_load_dwordx4 v136, %[srdwo], s46 offen offset:2048 lds\n\t"
    "buffer_load_dwordx4 v137, %[srdwo], s46 offen offset:3072 lds\n\t"
    "s_waitcnt vmcnt(13)\n\t"
    "s_barrier\n\t"
    "s_add_u32 m0, s47, 0x14000\n\t"
    "s_xor_b32 s46, s42, 0x50000\n\t"
    "s_lshr_b32 s46, s46, 2\n\t"
    "s_add_u32 s46, s46, %[obase]\n\t"
    "buffer_load_dwordx4 v142, %[srdws], s46 offen sc1 lds\n\t"
    "buffer_load_dwordx4 v142, %[srdws], s46 offen offset:2048 sc1 lds\n\t"
    "s_add_u32 m0, s47, 0x18000\n\t"
    "s_xor_b32 s46, s42, 0x60000\n\t"
    "s_lshr_b32 s46, s46, 2\n\t"
    "s_add_u32 s46, s46, %[obase]\n\t"
    "buffer_load_dwordx4 v142, %[srdws], s46 offen sc1 lds\n\t"
    "buffer_load_dwordx4 v142, %[srdws], s46 offen offset:2048 sc1 lds\n\t"
    "s_waitcnt vmcnt(2)\n\t"
    "s_barrier\n\t"
    "buffer_load_dwordx2 v[150:151], v145, %[srdws], s55 offen sc1\n\t"
    "s_add_u32 m0, s47, 0x8000\n\t"
    "s_xor_b32 s46, s42, 0x70000\n\t"
    "s_lshr_b32 s46, s46, 2\n\t"
    "s_add_u32 s46, s46, %[obase]\n\t"
    "buffer_load_dwordx4 v142, %[srdws], s46 offen sc1 lds\n\t"
    "buffer_load_dwordx4 v142, %[srdws], s46 offen offset:2048 sc1 lds\n\t"
    "s_add_u32 m0, s47, 0x4000\n\t"
    "s_bfe_u32 s46, %[htab], 0x3000c\n\t"
    "s_lshl_b32 s46, s46, 17\n\t"
    "s_or_b32 s46, s46, 0x10000\n\t"
    "buffer_load_dwordx4 v134, %[srdwo], s46 offen lds\n\t"
    "buffer_load_dwordx4 v135, %[srdwo], s46 offen offset:1024 lds\n\t"
    "buffer_load_dwordx4 v136, %[srdwo], s46 offen offset:2048 lds\n\t"
    "buffer_load_dwordx4 v137, %[srdwo], s46 offen offset:3072 lds\n\t"
    "s_add_u32 m0, s47, 0x10000\n\t"
    "s_bfe_u32 s46, %[htab], 0x30014\n\t"
    "s_lshl_b32 s46, s46, 17\n\t"
    "buffer_load_dwordx4 v134, %[srdwo], s46 offen lds\n\t"
    "buffer_load_dwordx4 v135, %[srdwo], s46 offen offset:1024 lds\n\t"
    "buffer_load_dwordx4 v136, %[srdwo], s46 offen offset:2048 lds\n\t"
    "buffer_load_dwordx4 v137, %[srdwo], s46 offen offset:3072 lds\n\t"
    "s_mov_b32 s54, 2000000\n\t"
    "s_waitcnt vmcnt(10)\n\t"
    "v_cmp_ne_u64 vcc, s[44:45], v[150:151]\n\t"
    "s_nop 3\n\t"
    "s_and_b64 vcc, vcc, 15\n\t"
    "s_cbranch_scc0 WPB_done%=\n\t"
    "WPB_loop%=:\n\t"
    "buffer_load_dwordx2 v[150:151], v145, %[srdws], s55 offen sc1\n\t"
    "s_sub_u32 s54, s54, 1\n\t"
    "s_cbranch_scc1 WPB_tmo%=\n\t"
    "s_waitcnt vmcnt(0)\n\t"
    "v_cmp_ne_u64 vcc, s[44:45], v[150:151]\n\t"
    "s_nop 3\n\t"
    "s_and_b64 vcc, vcc, 15\n\t"
    "s_cbranch_scc1 WPB_loop%=\n\t"
    "WPB_tmo%=:\n\t"
    "s_waitcnt vmcnt(0)\n\t"
    "WPB_done%=:\n\t"
    "s_add_u32 m0, s47, 0x0\n\t"
    "s_xor_b32 s46, s43, 0x20000\n\t"
    "s_lshr_b32 s46, s46, 2\n\t"
    "s_add_u32 s46, s46, %[obase]\n\t"
    "buffer_load_dwordx4 v142, %[srdws], s46 offen sc1 lds\n\t"
    "buffer_load_dwordx4 v142, %[srdws], s46 offen offset:2048 sc1 lds\n\t"
    "s_waitcnt vmcnt(6)\n\t"
    "s_barrier\n\t"
    "s_add_u32 m0, s47, 0x24000\n\t"
    "s_bfe_u32 s46, %[htab], 0x30014\n\t"
    "s_lshl_b32 s46, s46, 17\n\t"
    "s_or_b32 s46, s46, 0x10000\n\t"
    "buffer_load_dwordx4 v134, %[srdwo], s46 offen lds\n\t"
    "buffer_load_dwordx4 v135, %[srdwo], s46 offen offset:1024 lds\n\t"
    "buffer_load_dwordx4 v136, %[srdwo], s46 offen offset:2048 lds\n\t"
    "buffer_load_dwordx4 v137, %[srdwo], s46 offen offset:3072 lds\n\t"
    "s_add_u32 m0, s47, 0xc000\n\t"
    "s_xor_b32 s46, s43, 0x30000\n\t"
    "s_lshr_b32 s46, s46, 2\n\t"
    "s_add_u32 s46, s46, %[obase]\n\t"
    "buffer_load_dwordx4 v142, %[srdws], s46 offen sc1 lds\n\t"
    "buffer_load_dwordx4 v142, %[srdws], s46 offen offset:2048 sc1 lds\n\t"
    "s_add_u32 m0, s47, 0x1c000\n\t"
    "s_bfe_u32 s46, %[htab], 0x30018\n\t"
    "s_lshl_b32 s46, s46, 17\n\t"
    "buffer_load_dwordx4 v134, %[srdwo], s46 offen lds\n\t"
    "buffer_load_dwordx4 v135, %[srdwo], s46 offen offset:1024 lds\n\t"
    "buffer_load_dwordx4 v136, %[srdwo], s46 offen offset:2048 lds\n\t"
    "buffer_load_dwordx4 v137, %[srdwo], s46 offen offset:3072 lds\n\t"
    "s_add_u32 m0, s47, 0x14000\n\t"
    "s_xor_b32 s46, s43, 0x40000\n\t"
    "s_lshr_b32 s46, s46, 2\n\t"
    "s_add_u32 s46, s46, %[obase]\n\t"
    "buffer_load_dwordx4 v142, %[srdws], s46 offen sc1 lds\n\t"
    "buffer_load_dwordx4 v142, %[srdws], s46 offen offset:2048 sc1 lds\n\t"
    "s_waitcnt vmcnt(6)\n\t"
    "s_barrier\n\t"
    "s_add_u32 m0, s47, 0x20000\n\t"
    "s_bfe_u32 s46, %[htab], 0x30018\n\t"
    "s_lshl_b32 s46, s46, 17\n\t"
    "s_or_b32 s46, s46, 0x10000\n\t"
    "buffer_load_dwordx4 v134, %[srdwo], s46 offen lds\n\t"
    "buffer_load_dwordx4 v135, %[srdwo], s46 offen offset:1024 lds\n\t"
    "buffer_load_dwordx4 v136, %[srdwo], s46 offen offset:2048 lds\n\t"
    "buffer_load_dwordx4 v137, %[srdwo], s46 offen offset:3072 lds\n\t"
    "s_add_u32 m0, s47, 0x18000\n\t"
    "s_xor_b32 s46, s43, 0x50000\n\t"
    "s_lshr_b32 s46, s46, 2\n\t"
    "s_add_u32 s46, s46, %[obase]\n\t"
    "buffer_load_dwordx4 v142, %[srdws], s46 offen sc1 lds\n\t"
    "buffer_load_dwordx4 v142, %[srdws], s46 offen offset:2048 sc1 lds\n\t"
    "s_add_u32 m0, s47, 0x8000\n\t"
    "s_bfe_u32 s46, %[htab], 0x3001c\n\t"
    "s_lshl_b32 s46, s46, 17\n\t"
    "buffer_load_dwordx4 v134, %[srdwo], s46 offen lds\n\t"
    "buffer_load_dwordx4 v135, %[srdwo], s46 offen offset:1024 lds\n\t"
    "buffer_load_dwordx4 v136, %[srdwo], s46 offen offset:2048 lds\n\t"
    "buffer_load_dwordx4 v137, %[srdwo], s46 offen offset:3072 lds\n\t"
    "s_add_u32 m0, s47, 0x4000\n\t"
    "s_xor_b32 s46, s43, 0x60000\n\t"
    "s_lshr_b32 s46, s46, 2\n\t"
    "s_add_u32 s46, s46, %[obase]\n\t"
    "buffer_load_dwordx4 v142, %[srdws], s46 offen sc1 lds\n\t"
    "buffer_load_dwordx4 v142, %[srdws], s46 offen offset:2048 sc1 lds\n\t"
    "s_waitcnt vmcnt(6)\n\t"
    "s_barrier\n\t"
    "s_add_u32 m0, s47, 0x10000\n\t"
    "s_bfe_u32 s46, %[htab], 0x3001c\n\t"
    "s_lshl_b32 s46, s46, 17\n\t"
    "s_or_b32 s46, s46, 0x10000\n\t"
    "buffer_load_dwordx4 v134, %[srdwo], s46 offen lds\n\t"
    "buffer_load_dwordx4 v135, %[srdwo], s46 offen offset:1024 lds\n\t"
    "buffer_load_dwordx4 v136, %[srdwo], s46 offen offset:2048 lds\n\t"
    "buffer_load_dwordx4 v137, %[srdwo], s46 offen offset:3072 lds\n\t"
    "s_add_u32 m0, s47, 0x0\n\t"
    "s_xor_b32 s46, s43, 0x70000\n\t"
    "s_lshr_b32 s46, s46, 2\n\t"
    "s_add_u32 s46, s46, %[obase]\n\t"
    "buffer_load_dwordx4 v142, %[srdws], s46 offen sc1 lds\n\t"
    "buffer_load_dwordx4 v142, %[srdws], s46 offen offset:2048 sc1 lds\n\t"
    "s_waitcnt vmcnt(0)\n\t"
    "s_branch END_%=\n\t"
    "FB%=:\n\t"
    "s_nop 15\n\t"
    "v_mov_b32 v144, 0x400\n\t"
    "v_bfe_u32 v254, v253, 0, 4\n\t"
    "v_lshl_add_u32 v144, v254, 8, v144\n\t"
    "v_bfe_u32 v254, v253, 4, 1\n\t"
    "v_lshl_add_u32 v144, v254, 5, v144\n\t"
    "v_bfe_u32 v254, v253, 5, 1\n\t"
    "v_lshl_add_u32 v144, v254, 4, v144\n\t"
    "v_add_u32 v145, 0x1000, v144\n\t"
    "s_cmp_eq_u32 s51, 0\n\t"
    "s_cselect_b32 s53, s48, s49\n\t"
    "v_mov_b32 v192, 0xff800000\n\t"
    "v_mov_b32 v193, 0xff800000\n\t"
    "s_mov_b32 s56, 0\n\t"
    "FB1%=:\n\t"
    "s_lshl_b32 s55, s56, 10\n\t"
    "s_add_u32 s55, s55, s59\n\t"
    "s_and_b32 s55, s55, 0x1ffff\n\t"
    "s_or_b32 s55, s55, s53\n\t"
    "buffer_load_dwordx4 v[200:203], v144, %[srdk], s55 offen\n\t"
    "buffer_load_dwordx4 v[204:207], v144, %[srdk], s55 offen offset:64\n\t"
    "buffer_load_dwordx4 v[208:211], v144, %[srdk], s55 offen offset:128\n\t"
    "buffer_load_dwordx4 v[212:215], v144, %[srdk], s55 offen offset:192\n\t"
    "s_waitcnt vmcnt(0)\n\t"
    "v_mfma_f32_16x16x32_bf16 v[128:131], v[200:203], v[152:155], 0\n\t"
    "v_mfma_f32_16x16x32_bf16 v[136:139], v[200:203], v[156:159], 0\n\t"
    "v_mfma_f32_16x16x32_bf16 v[128:131], v[204:207], v[160:163], v[128:131]\n\t"
    "v_mfma_f32_16x16x32_bf16 v[136:139], v[204:207], v[164:167], v[136:139]\n\t"
    "v_mfma_f32_16x16x32_bf16 v[128:131], v[208:211], v[168:171], v[128:131]\n\t"
    "v_mfma_f32_16x16x32_bf16 v[136:139], v[208:211], v[172:175], v[136:139]\n\t"
    "v_mfma_f32_16x16x32_bf16 v[128:131], v[212:215], v[176:179], v[128:131]\n\t"
    "v_mfma_f32_16x16x32_bf16 v[136:139], v[212:215], v[180:183], v[136:139]\n\t"
    "buffer_load_dwordx4 v[200:203], v145, %[srdk], s55 offen\n\t"
    "buffer_load_dwordx4 v[204:207], v145, %[srdk], s55 offen offset:64\n\t"
    "buffer_load_dwordx4 v[208:211], v145, %[srdk], s55 offen offset:128\n\t"
    "buffer_load_dwordx4 v[212:215], v145, %[srdk], s55 offen offset:192\n\t"
    "s_waitcnt vmcnt(0)\n\t"
    "v_mfma_f32_16x16x32_bf16 v[132:135], v[200:203], v[152:155], 0\n\t"
    "v_mfma_f32_16x16x32_bf16 v[140:143], v[200:203], v[156:159], 0\n\t"
    "v_mfma_f32_16x16x32_bf16 v[132:135], v[204:207], v[160:163], v[132:135]\n\t"
    "v_mfma_f32_16x16x32_bf16 v[140:143], v[204:207], v[164:167], v[140:143]\n\t"
    "v_mfma_f32_16x16x32_bf16 v[132:135], v[208:211], v[168:171], v[132:135]\n\t"
    "v_mfma_f32_16x16x32_bf16 v[140:143], v[208:211], v[172:175], v[140:143]\n\t"
    "v_mfma_f32_16x16x32_bf16 v[132:135], v[212:215], v[176:179], v[132:135]\n\t"
    "v_mfma_f32_16x16x32_bf16 v[140:143], v[212:215], v[180:183], v[140:143]\n\t"
    "s_nop 15\n\t"
    "v_max3_f32 v192, v192, v128, v129\n\t"
    "v_max3_f32 v192, v192, v130, v131\n\t"
    "v_max3_f32 v192, v192, v132, v133\n\t"
    "v_max3_f32 v192, v192, v134, v135\n\t"
    "v_max3_f32 v193, v193, v136, v137\n\t"
    "v_max3_f32 v193, v193, v138, v139\n\t"
    "v_max3_f32 v193, v193, v140, v141\n\t"
    "v_max3_f32 v193, v193, v142, v143\n\t"
    "s_add_u32 s56, s56, 8\n\t"
    "s_cmp_lt_u32 s56, 0x80\n\t"
    "s_cbranch_scc1 FB1%=\n\t"
    "v_mov_b32 v194, v192\n\t"
    "v_mov_b32 v195, v193\n\t"
    "s_nop 0\n\t"
    "v_permlane32_swap_b32 v192, v194\n\t"
    "v_permlane32_swap_b32 v193, v195\n\t"
    "v_max_f32 v192, v192, v194\n\t"
    "v_max_f32 v193, v193, v195\n\t"
    "v_mov_b32 v194, v192\n\t"
    "v_mov_b32 v195, v193\n\t"
    "s_nop 0\n\t"
    "v_permlane16_swap_b32 v192, v194\n\t"
    "v_permlane16_swap_b32 v193, v195\n\t"
    "v_max_f32 v192, v192, v194\n\t"
    "v_max_f32 v193, v193, v195\n\t"
    "v_sub_f32 v184, 0, v192\n\t"
    "v_sub_f32 v185, 0, v192\n\t"
    "v_sub_f32 v186, 0, v192\n\t"
    "v_sub_f32 v187, 0, v192\n\t"
    "v_sub_f32 v188, 0, v193\n\t"
    "v_sub_f32 v189, 0, v193\n\t"
    "v_sub_f32 v190, 0, v193\n\t"
    "v_sub_f32 v191, 0, v193\n\t"
    "v_mov_b32 v251, 0\n\t"
    "v_mov_b32 v252, 0\n\t"
    "s_mov_b32 s56, 0\n\t"
    "FB2%=:\n\t"
    "s_lshl_b32 s55, s56, 10\n\t"
    "s_add_u32 s55, s55, s59\n\t"
    "s_and_b32 s55, s55, 0x1ffff\n\t"
    "s_or_b32 s55, s55, s53\n\t"
    "buffer_load_dwordx4 v[200:203], v144, %[srdk], s55 offen\n\t"
    "buffer_load_dwordx4 v[204:207], v144, %[srdk], s55 offen offset:64\n\t"
    "buffer_load_dwordx4 v[208:211], v144, %[srdk], s55 offen offset:128\n\t"
    "buffer_load_dwordx4 v[212:215], v144, %[srdk], s55 offen offset:192\n\t"
    "s_waitcnt vmcnt(0)\n\t"
    "v_mfma_f32_16x16x32_bf16 v[128:131], v[200:203], v[152:155], v[184:187]\n\t"
    "v_mfma_f32_16x16x32_bf16 v[136:139], v[200:203], v[156:159], v[188:191]\n\t"
    "v_mfma_f32_16x16x32_bf16 v[128:131], v[204:207], v[160:163], v[128:131]\n\t"
    "v_mfma_f32_16x16x32_bf16 v[136:139], v[204:207], v[164:167], v[136:139]\n\t"
    "v_mfma_f32_16x16x32_bf16 v[128:131], v[208:211], v[168:171], v[128:131]\n\t"
    "v_mfma_f32_16x16x32_bf16 v[136:139], v[208:211], v[172:175], v[136:139]\n\t"
    "v_mfma_f32_16x16x32_bf16 v[128:131], v[212:215], v[176:179], v[128:131]\n\t"
    "v_mfma_f32_16x16x32_bf16 v[136:139], v[212:215], v[180:183], v[136:139]\n\t"
    "buffer_load_dwordx4 v[200:203], v145, %[srdk], s55 offen\n\t"
    "buffer_load_dwordx4 v[204:207], v145, %[srdk], s55 offen offset:64\n\t"
    "buffer_load_dwordx4 v[208:211], v145, %[srdk], s55 offen offset:128\n\t"
    "buffer_load_dwordx4 v[212:215], v145, %[srdk], s55 offen offset:192\n\t"
    "s_waitcnt vmcnt(0)\n\t"
    "v_mfma_f32_16x16x32_bf16 v[132:135], v[200:203], v[152:155], v[184:187]\n\t"
    "v_mfma_f32_16x16x32_bf16 v[140:143], v[200:203], v[156:159], v[188:191]\n\t"
    "v_mfma_f32_16x16x32_bf16 v[132:135], v[204:207], v[160:163], v[132:135]\n\t"
    "v_mfma_f32_16x16x32_bf16 v[140:143], v[204:207], v[164:167], v[140:143]\n\t"
    "v_mfma_f32_16x16x32_bf16 v[132:135], v[208:211], v[168:171], v[132:135]\n\t"
    "v_mfma_f32_16x16x32_bf16 v[140:143], v[208:211], v[172:175], v[140:143]\n\t"
    "v_mfma_f32_16x16x32_bf16 v[132:135], v[212:215], v[176:179], v[132:135]\n\t"
    "v_mfma_f32_16x16x32_bf16 v[140:143], v[212:215], v[180:183], v[140:143]\n\t"
    "s_nop 15\n\t"
    "v_exp_f32 v128, v128\n\t"
    "v_exp_f32 v129, v129\n\t"
    "v_exp_f32 v130, v130\n\t"
    "v_exp_f32 v131, v131\n\t"
    "v_exp_f32 v132, v132\n\t"
    "v_exp_f32 v133, v133\n\t"
    "v_exp_f32 v134, v134\n\t"
    "v_exp_f32 v135, v135\n\t"
    "v_exp_f32 v136, v136\n\t"
    "v_exp_f32 v137, v137\n\t"
    "v_exp_f32 v138, v138\n\t"
    "v_exp_f32 v139, v139\n\t"
    "v_exp_f32 v140, v140\n\t"
    "v_exp_f32 v141, v141\n\t"
    "v_exp_f32 v142, v142\n\t"
    "v_exp_f32 v143, v143\n\t"
    "v_add_f32 v251, v251, v128\n\t"
    "v_add_f32 v251, v251, v129\n\t"
    "v_add_f32 v251, v251, v130\n\t"
    "v_add_f32 v251, v251, v131\n\t"
    "v_add_f32 v251, v251, v132\n\t"
    "v_add_f32 v251, v251, v133\n\t"
    "v_add_f32 v251, v251, v134\n\t"
    "v_add_f32 v251, v251, v135\n\t"
    "v_add_f32 v252, v252, v136\n\t"
    "v_add_f32 v252, v252, v137\n\t"
    "v_add_f32 v252, v252, v138\n\t"
    "v_add_f32 v252, v252, v139\n\t"
    "v_add_f32 v252, v252, v140\n\t"
    "v_add_f32 v252, v252, v141\n\t"
    "v_add_f32 v252, v252, v142\n\t"
    "v_add_f32 v252, v252, v143\n\t"
    "v_cvt_pk_bf16_f32 v128, v128, v129\n\t"
    "v_cvt_pk_bf16_f32 v129, v130, v131\n\t"
    "v_cvt_pk_bf16_f32 v130, v132, v133\n\t"
    "v_cvt_pk_bf16_f32 v131, v134, v135\n\t"
    "v_cvt_pk_bf16_f32 v132, v136, v137\n\t"
    "v_cvt_pk_bf16_f32 v133, v138, v139\n\t"
    "v_cvt_pk_bf16_f32 v134, v140, v141\n\t"
    "v_cvt_pk_bf16_f32 v135, v142, v143\n\t"
    "s_set_gpr_idx_on s56, gpr_idx(DST)\n\t"
    "s_nop 1\n\t"
    "v_mov_b32 v0, v128\n\t"
    "v_mov_b32 v1, v129\n\t"
    "v_mov_b32 v2, v130\n\t"
    "v_mov_b32 v3, v131\n\t"
    "v_mov_b32 v4, v132\n\t"
    "v_mov_b32 v5, v133\n\t"
    "v_mov_b32 v6, v134\n\t"
    "v_mov_b32 v7, v135\n\t"
    "s_set_gpr_idx_off\n\t"
    "s_nop 1\n\t"
    "s_add_u32 s56, s56, 8\n\t"
    "s_cmp_lt_u32 s56, 0x80\n\t"
    "s_cbranch_scc1 FB2%=\n\t"
    "v_mov_b32 v194, v251\n\t"
    "v_mov_b32 v195, v252\n\t"
    "s_nop 0\n\t"
    "v_permlane32_swap_b32 v251, v194\n\t"
    "v_permlane32_swap_b32 v252, v195\n\t"
    "v_add_f32 v251, v251, v194\n\t"
    "v_add_f32 v252, v252, v195\n\t"
    "v_mov_b32 v194, v251\n\t"
    "v_mov_b32 v195, v252\n\t"
    "s_nop 0\n\t"
    "v_permlane16_swap_b32 v251, v194\n\t"
    "v_permlane16_swap_b32 v252, v195\n\t"
    "v_add_f32 v251, v251, v194\n\t"
    "v_add_f32 v252, v252, v195\n\t"
    "s_nop 7\n\t"
    "s_cmp_eq_u32 %[wid], 3\n\t"
    "s_cbranch_scc1 FBRET2_%=\n\t"
    "s_cmp_eq_u32 %[wid], 7\n\t"
    "s_cbranch_scc1 FBRET3_%=\n\t"
    "s_cmp_eq_u32 s51, 0\n\t"
    "s_cbranch_scc1 FBRET0_%=\n\t"
    "s_branch FBRET1_%=\n\t"
    "END_%=:\n\t"
    :
    : [srdx] "s"(srdx), [srdk] "s"(srdk), [srdv] "s"(srdv), [srdwq] "s"(srdwq), [srdwo] "s"(srdwo), [srdws] "s"(srdws), [srdout] "s"(srdout),
      [obase] "s"(obase), [xbase] "s"(xbase), [f1base] "s"(f1base), [f2base] "s"(f2base), [eptr] "s"(eptr), [lds] "s"(ldsb), [wid] "s"(wid), [oct] "s"(oct), [qi] "s"(qi), [rot] "s"(rot), [rotq] "s"(rotq), [wj] "s"(j), [htab] "s"(htab), [cbo] "s"(cbo), [octo] "s"(octo), [sdn] "s"(sdn), [vsel] "s"(vsel)
    : "v0", "v1", "v2", "v3", "v4", "v5", "v6", "v7", "v8", "v9", "v10", "v11", "v12", "v13", "v14", "v15", "v16", "v17", "v18", "v19", "v20", "v21", "v22", "v23", "v24", "v25", "v26", "v27", "v28", "v29", "v30", "v31", "v32", "v33", "v34", "v35", "v36", "v37", "v38", "v39", "v40", "v41", "v42", "v43", "v44", "v45", "v46", "v47", "v48", "v49", "v50", "v51", "v52", "v53", "v54", "v55", "v56", "v57", "v58", "v59", "v60", "v61", "v62", "v63", "v64", "v65", "v66", "v67", "v68", "v69", "v70", "v71", "v72", "v73", "v74", "v75", "v76", "v77", "v78", "v79", "v80", "v81", "v82", "v83", "v84", "v85", "v86", "v87", "v88", "v89", "v90", "v91", "v92", "v93", "v94", "v95", "v96", "v97", "v98", "v99", "v100", "v101", "v102", "v103", "v104", "v105", "v106", "v107", "v108", "v109", "v110", "v111", "v112", "v113", "v114", "v115", "v116", "v117", "v118", "v119", "v120", "v121", "v122", "v123", "v124", "v125", "v126", "v127", "v128", "v129", "v130", "v131", "v132", "v133", "v134", "v135", "v136", "v137", "v138", "v139", "v140", "v141", "v142", "v143", "v144", "v145", "v146", "v147", "v148", "v149", "v150", "v151", "v152", "v153", "v154", "v155", "v156", "v157", "v158", "v159", "v160", "v161", "v162", "v163", "v164", "v165", "v166", "v167", "v168", "v169", "v170", "v171", "v172", "v173", "v174", "v175", "v176", "v177", "v178", "v179", "v180", "v181", "v182", "v183", "v184", "v185", "v186", "v187", "v188", "v189", "v190", "v191", "v192", "v193", "v194", "v195", "v196", "v197", "v198", "v199", "v200", "v201", "v202", "v203", "v204", "v205", "v206", "v207", "v208", "v209", "v210", "v211", "v212", "v213", "v214", "v215", "v216", "v217", "v218", "v219", "v220", "v221", "v222", "v223", "v224", "v225", "v226", "v227", "v228", "v229", "v230", "v231", "v232", "v233", "v234", "v235", "v236", "v237", "v238", "v239", "v240", "v241", "v242", "v243", "v244", "v245", "v246", "v247", "v248", "v249", "v250", "v251", "v252", "v253", "v254", "v255", "s42", "s43", "s44", "s45", "s46", "s47", "s48", "s49", "s50", "s51", "s52", "s53", "s54", "s55", "s56", "s57", "s58", "s59", "s60", "s61", "s62", "s63", "vcc", "scc", "memory");
}

extern "C" void kernel_launch(void* const* d_in, const int* in_sizes, int n_in, void* d_out, int out_size, void* d_ws, size_t ws_size,
                              hipStream_t stream) {
  FusedArgs a;
  for (int i = 0; i < 2; ++i) {
    a.x[i] = (const char*)d_in[5 * i]; a.k[i] = (const char*)d_in[5 * i + 1]; a.v[i] = (const char*)d_in[5 * i + 2];
    a.wq[i] = (const char*)d_in[5 * i + 3]; a.wo[i] = (const char*)d_in[5 * i + 4];
  }
  a.ws = (char*)d_ws; a.out = (char*)d_out;
  hipLaunchKernelGGL(fused_mha_kernel, dim3(256), dim3(512), 0, stream, a);
}
```
